```python
import torch

def _softmax(x, axis=-1):
    return torch.softmax(x, dim=axis)

def _gelu(x):
    return torch.nn.functional.gelu(x, approximate='tanh')

def _silu(x):
    return torch.nn.functional.silu(x)

def _sigmoid(x):
    return torch.sigmoid(x)

def _layernorm(x, axis=-1, eps=1e-05):
    m = x.mean(dim=axis, keepdim=True)
    v = x.var(dim=axis, keepdim=True, unbiased=False)
    return (x - m) / torch.sqrt(v + eps)

def _scatter_add(tgt, idx, src, axis=0):
    return tgt.clone().index_add_(axis, idx, src)

def _segment_sum(src, seg, n):
    out = torch.zeros((n,) + src.shape[1:], dtype=src.dtype)
    return out.index_add_(0, seg, src)

def _pad(x, pw):
    flat = tuple((v for pair in reversed(pw) for v in pair))
    return torch.nn.functional.pad(x, flat)
kernel_name = 'gated_mix__memory_aug_recurrent_mem_tr__glu_gated_swiglu__prompt'

def setup_inputs():

    _w = lambda a: (lambda t: t.to(torch.bfloat16) if t.is_floating_point() else t)(torch.as_tensor(a))

    def _A_setup():
        B, S, Dm = (1, 2048, 640)
        H, Dh = (32, 128)
        M, seg = (8, 32)
        seg = min(seg, S)
        S = S // seg * seg
        return {'x': _w((lambda *s: torch.randn(*s, dtype=torch.bfloat16) * 0.05)(B, S, Dm)), 'mem0': _w((lambda *s: torch.randn(*s, dtype=torch.bfloat16) * 0.8)(B, M, Dm)), 'Wq': _w((lambda *s: torch.randn(*s, dtype=torch.bfloat16) * 1.6)(Dm, H, Dh) * Dm ** (-0.5)), 'Wk': _w((lambda *s: torch.randn(*s, dtype=torch.bfloat16) * 1.6)(Dm, H, Dh) * Dm ** (-0.5)), 'Wv': _w((lambda *s: torch.randn(*s, dtype=torch.bfloat16) * 0.8)(Dm, H, Dh) * Dm ** (-0.5)), 'Wo': _w((lambda *s: torch.randn(*s, dtype=torch.bfloat16) * 0.8)(H, Dh, Dm) * (H * Dh) ** (-0.5)), 'seg_len': seg}
    _A_in = _A_setup()

    def _B_setup():
        B, S, D, F = (1, 2048, 640, 1728)
        return {'x': _w((lambda *s: torch.randn(*s, dtype=torch.bfloat16) * 0.05)(B, S, D)), 'w1': _w((lambda *s: torch.randn(*s, dtype=torch.bfloat16) * 3.2)(D, F) * (2.0 / D) ** 0.5), 'w3': _w((lambda *s: torch.randn(*s, dtype=torch.bfloat16) * 3.2)(D, F) * (2.0 / D) ** 0.5), 'w2': _w((lambda *s: torch.randn(*s, dtype=torch.bfloat16) * 3.2)(F, D) * (2.0 / F) ** 0.5)}
    _B_in = _B_setup()
    return {'A_x': _A_in['x'], 'A_mem0': _A_in['mem0'], 'A_Wq': _A_in['Wq'], 'A_Wk': _A_in['Wk'], 'A_Wv': _A_in['Wv'], 'A_Wo': _A_in['Wo'], 'A_seg_len': _A_in['seg_len'], 'B_x': _B_in['x'], 'B_w1': _B_in['w1'], 'B_w3': _B_in['w3'], 'B_w2': _B_in['w2'], 'gate_w': _w((lambda *s: torch.randn(*s, dtype=torch.bfloat16) * 0.05)(640) * 16.0)}

def reference(A_x, A_mem0, A_Wq, A_Wk, A_Wv, A_Wo, A_seg_len, B_x, B_w1, B_w3, B_w2, gate_w):

    def _A_ref(x, mem0, Wq, Wk, Wv, Wo, seg_len):
        B, S, Dm = x.shape
        H, Dh = (Wq.shape[1], Wq.shape[2])
        M = mem0.shape[1]
        scale = Dh ** (-0.5)
        n_seg = S // seg_len
        L = M + seg_len
        idx = torch.arange(L)
        mask = (idx[:, None] < M) | (idx[:, None] >= idx[None, :])
        mem = mem0
        outs = []
        for s in range(n_seg):
            xs = x[:, s * seg_len:(s + 1) * seg_len, :].clone()
            h = torch.concatenate([mem, xs], axis=1)
            q = torch.einsum('btd,dhe->bhte', h, Wq)
            k = torch.einsum('btd,dhe->bhte', h, Wk)
            v = torch.einsum('btd,dhe->bhte', h, Wv)
            scores = torch.einsum('bhqe,bhke->bhqk', q, k) * scale
            scores = torch.where(mask[None, None], scores, -1000000000.0)
            p = _softmax(scores, axis=-1)
            o = torch.einsum('bhqk,bhke->bhqe', p, v)
            o = torch.einsum('bhte,hed->btd', o, Wo)
            mem = o[:, :M, :].clone()
            outs.append(o[:, M:, :])
        return torch.concatenate(outs, axis=1)

    def _B_ref(x, w1, w3, w2):
        gate = torch.einsum('bsd,df->bsf', x, w1)
        up = torch.einsum('bsd,df->bsf', x, w3)
        h = _silu(gate) * up
        out = torch.einsum('bsf,fd->bsd', h, w2)
        return out
    x = A_x
    g = _sigmoid((x * gate_w).sum(-1, keepdims=True))
    a = _A_ref(x, A_mem0, A_Wq, A_Wk, A_Wv, A_Wo, A_seg_len)
    b = _B_ref(x, B_w1, B_w3, B_w2)
    return g * a + (1 - g) * b
```

```cpp
#include <hip/hip_runtime.h>
#include <hip/hip_bf16.h>
typedef unsigned short u16;
typedef __attribute__((ext_vector_type(8))) short bf8;
typedef __attribute__((ext_vector_type(4))) float f4;
__device__ __forceinline__ float b2f(u16 x) { return __uint_as_float((unsigned)x << 16); }
__device__ __forceinline__ u16 f2b(float f) { unsigned u = __float_as_uint(f); return (u16)((u + 0x7FFF + ((u >> 16) & 1)) >> 16); }
__device__ __forceinline__ float rb(float f) { return b2f(f2b(f)); }

__global__ void gemm(const u16* A, int lda, const u16* B, int ldb, u16* C, int ldc, int M, int N, int K) {
    int lane = threadIdx.x & 63, wave = threadIdx.x >> 6;
    int tn = blockIdx.x * 4 + wave, tm = blockIdx.y;
    if (tn * 16 >= N) return;
    f4 acc = {0, 0, 0, 0};
    int r = lane & 15, g = lane >> 4;
    int arow = tm * 16 + r; if (arow >= M) arow = M - 1;
    for (int k0 = 0; k0 < K; k0 += 32) {
        bf8 a = *(const bf8*)(A + (size_t)arow * lda + k0 + g * 8);
        bf8 b;
        for (int j = 0; j < 8; ++j) b[j] = B[(size_t)(k0 + g * 8 + j) * ldb + tn * 16 + r];
        acc = __builtin_amdgcn_mfma_f32_16x16x32_bf16((__attribute__((ext_vector_type(8))) __bf16)a, (__attribute__((ext_vector_type(8))) __bf16)b, acc, 0, 0, 0);
    }
    for (int j = 0; j < 4; ++j) { int row = tm * 16 + g * 4 + j; if (row < M) C[(size_t)row * ldc + tn * 16 + r] = f2b(acc[j]); }
}
__global__ void attn(const u16* qm, const u16* km, const u16* vm, const u16* Q, const u16* K, const u16* V, u16* om, u16* O, int seg, int memq) {
    int h = blockIdx.x, i = blockIdx.y, lane = threadIdx.x;
    if (!memq) { seg = blockIdx.z; km += (size_t)seg * 8 * 4096; vm += (size_t)seg * 8 * 4096; }
    const u16* q = memq ? qm + i * 4096 + h * 128 : Q + (size_t)(seg * 32 + i) * 4096 + h * 128;
    __shared__ float p[40];
    float s = -1e9f;
    if (lane < 40) {
        const u16* k = lane < 8 ? km + lane * 4096 + h * 128 : K + (size_t)(seg * 32 + lane - 8) * 4096 + h * 128;
        float a = 0; for (int e = 0; e < 128; ++e) a += b2f(q[e]) * b2f(k[e]);
        s = rb(rb(a) * 0.08838834764831845f);
        if (!memq && lane >= 8 && lane - 8 > i) s = -1e9f;
    }
    float m = lane < 40 ? s : -3e38f;
    for (int o = 32; o; o >>= 1) m = fmaxf(m, __shfl_xor(m, o));
    float e = lane < 40 ? __expf(s - m) : 0.f;
    float t = e; for (int o = 32; o; o >>= 1) t += __shfl_xor(t, o);
    if (lane < 40) p[lane] = rb(e / t);
    __syncthreads();
    for (int c = lane; c < 128; c += 64) {
        float a = 0;
        for (int j = 0; j < 40; ++j) { const u16* v = j < 8 ? vm + j * 4096 + h * 128 : V + (size_t)(seg * 32 + j - 8) * 4096 + h * 128; a += p[j] * b2f(v[c]); }
        if (memq) om[i * 4096 + h * 128 + c] = f2b(a); else O[(size_t)(seg * 32 + i) * 4096 + h * 128 + c] = f2b(a);
    }
}
__global__ void swiglu(const u16* G, const u16* U, u16* H, int n) {
    int i = blockIdx.x * 256 + threadIdx.x; if (i >= n) return;
    float g = b2f(G[i]); float s = rb(g / (1.f + __expf(-g))); H[i] = f2b(s * b2f(U[i]));
}
__global__ void fin(const u16* x, const u16* gw, const u16* a, const u16* b, u16* out) {
    int row = blockIdx.x, lane = threadIdx.x;
    float s = 0; for (int c = lane; c < 640; c += 64) s += rb(b2f(x[row * 640 + c]) * b2f(gw[c]));
    for (int o = 32; o; o >>= 1) s += __shfl_xor(s, o);
    s = rb(s); float g = rb(1.f / (1.f + __expf(-s))); float og = rb(1.f - g);
    for (int c = lane; c < 640; c += 64) out[row * 640 + c] = f2b(rb(g * b2f(a[row * 640 + c])) + rb(og * b2f(b[row * 640 + c])));
}
extern "C" void kernel_launch(void* const* d_in, const int* in_sizes, int n_in, void* d_out, int out_size, void* d_ws, size_t ws_size, hipStream_t stream) {
    const u16 *x = (const u16*)d_in[0], *mem0 = (const u16*)d_in[1], *Wq = (const u16*)d_in[2], *Wk = (const u16*)d_in[3], *Wv = (const u16*)d_in[4], *Wo = (const u16*)d_in[5];
    const u16 *w1 = (const u16*)d_in[8], *w3 = (const u16*)d_in[9], *w2 = (const u16*)d_in[10], *gw = (const u16*)d_in[11];
    u16* ws = (u16*)d_ws;
    size_t T = 2048ull * 4096;
    u16 *Q = ws, *K = Q + T, *V = K + T, *O = V + T, *G = O + T, *U = G + 2048 * 1728, *H = U + 2048 * 1728, *A = H + 2048 * 1728, *B = A + 2048 * 640;
    u16 *mem = B + 2048 * 640, *km = mem + 64 * 8 * 640, *vm = km + 64 * 8 * 4096, *qm = vm + 64 * 8 * 4096, *om = qm + 8 * 4096;
    gemm<<<dim3(64, 128), 256, 0, stream>>>(x, 640, Wq, 4096, Q, 4096, 2048, 4096, 640);
    gemm<<<dim3(64, 128), 256, 0, stream>>>(x, 640, Wk, 4096, K, 4096, 2048, 4096, 640);
    gemm<<<dim3(64, 128), 256, 0, stream>>>(x, 640, Wv, 4096, V, 4096, 2048, 4096, 640);
    gemm<<<dim3(27, 128), 256, 0, stream>>>(x, 640, w1, 1728, G, 1728, 2048, 1728, 640);
    gemm<<<dim3(27, 128), 256, 0, stream>>>(x, 640, w3, 1728, U, 1728, 2048, 1728, 640);
    swiglu<<<(2048 * 1728 + 255) / 256, 256, 0, stream>>>(G, U, H, 2048 * 1728);
    gemm<<<dim3(10, 128), 256, 0, stream>>>(H, 1728, w2, 640, B, 640, 2048, 640, 1728);
    hipMemcpyAsync(mem, mem0, 8 * 640 * 2, hipMemcpyDeviceToDevice, stream);
    for (int s = 0; s < 64; ++s) {
        const u16* m = mem + s * 8 * 640;
        gemm<<<dim3(64, 1), 256, 0, stream>>>(m, 640, Wk, 4096, km + (size_t)s * 8 * 4096, 4096, 8, 4096, 640);
        gemm<<<dim3(64, 1), 256, 0, stream>>>(m, 640, Wv, 4096, vm + (size_t)s * 8 * 4096, 4096, 8, 4096, 640);
        if (s == 63) break;
        gemm<<<dim3(64, 1), 256, 0, stream>>>(m, 640, Wq, 4096, qm, 4096, 8, 4096, 640);
        attn<<<dim3(32, 8), 64, 0, stream>>>(qm, km + (size_t)s * 8 * 4096, vm + (size_t)s * 8 * 4096, Q, K, V, om, O, s, 1);
        gemm<<<dim3(10, 1), 256, 0, stream>>>(om, 4096, Wo, 640, mem + (s + 1) * 8 * 640, 640, 8, 640, 4096);
    }
    attn<<<dim3(32, 32, 64), 64, 0, stream>>>(qm, km, vm, Q, K, V, om, O, 0, 0);
    gemm<<<dim3(10, 128), 256, 0, stream>>>(O, 4096, Wo, 640, A, 640, 2048, 640, 4096);
    fin<<<2048, 64, 0, stream>>>(x, gw, A, B, (u16*)d_out);
}
```

```cpp
#include <hip/hip_runtime.h>

typedef unsigned short u16;
typedef unsigned u32;
typedef __attribute__((ext_vector_type(4))) u32 u4;
typedef __attribute__((ext_vector_type(2))) u32 u2;
typedef __attribute__((ext_vector_type(8))) u32 u8v;
typedef __attribute__((ext_vector_type(2))) __bf16 bf2;
typedef __attribute__((ext_vector_type(8))) __bf16 bf8;
typedef __attribute__((ext_vector_type(16))) __bf16 bf16v;
typedef __attribute__((ext_vector_type(2))) float f2;
typedef __attribute__((ext_vector_type(4))) float f4;
typedef __attribute__((ext_vector_type(16))) float f16v;
typedef __attribute__((ext_vector_type(4))) short s4;
typedef __attribute__((address_space(3))) s4* lds_s4;
typedef __attribute__((address_space(3))) u4* lds_u4;
typedef __attribute__((address_space(3))) u2* lds_u2;
typedef __attribute__((address_space(3))) u32* lds_u1;
typedef __attribute__((address_space(3))) f4* lds_f4;
typedef __attribute__((address_space(3))) f2* lds_f2;
typedef __attribute__((address_space(3))) float* lds_f1;
typedef __amdgpu_buffer_rsrc_t rsrc_t;
#define DI __device__ __forceinline__

#define PLACE 1

constexpr u32 WIN0 = 0, WIN1 = 2u << 20;
constexpr u32 UM_STR = 327680u * 2, P_STR = 24576u * 2, M_STR = 16384u * 2;
constexpr u32 P_OFF = WIN1, M_OFF = WIN1 + 4 * P_STR, CAL0 = WIN0 + 3 * UM_STR, CAL1 = M_OFF + 4 * M_STR;
constexpr u32 FLG = 4u << 20;
constexpr u32 F_QK = FLG, F_B = FLG + 8192, F_H = FLG + 16384, F_F = FLG + 24576, F_G = FLG + 28672;
constexpr u32 F_I1 = FLG + 36864, F_IU = F_I1 + 128, F_I2 = F_I1 + 256, F_PAR = F_I1 + 512, FLG_BYTES = 37888;
constexpr u32 QF = 5u << 20, KF = 21u << 20, PX = 37u << 20, HF = 43u << 20, BF = 50u << 20, BX = 53u << 20;
constexpr u32 TS = 140u << 20;
constexpr u32 SENT = 0xFFFFFFFFu;
constexpr int SPIN = 200000;
constexpr int LDS_BYTES = 98304;

struct Params {
    const u16 *x, *mem0, *wq, *wk, *wv, *wo, *w1, *w3, *w2, *gw;
    u16* out;
    char* ws;
};

DI u32 pk(float a, float b) { f2 v = {a, b}; return __builtin_bit_cast(u32, __builtin_convertvector(v, bf2)); }
DI float lo2f(u32 x) { return __uint_as_float(x << 16); }
DI float hi2f(u32 x) { return __uint_as_float(x & 0xFFFF0000u); }
DI float rbf(float a) { return hi2f(pk(a, a)); }
DI u32 nos(u32 x) { return x < 0xFFFFFFFEu ? x : 0xFFFFFFFEu; }
DI u32 amap(u32 off) { return ((off >> 13) << 14) | (off & 8191u); }
DI u4 ldc(rsrc_t r, u32 v, u32 s) { return __builtin_amdgcn_raw_buffer_load_b128(r, v, s, 16); }
DI u2 ldc2(rsrc_t r, u32 v, u32 s) { return __builtin_amdgcn_raw_buffer_load_b64(r, v, s, 16); }
DI u32 ldc1(rsrc_t r, u32 v, u32 s) { return __builtin_amdgcn_raw_buffer_load_b32(r, v, s, 16); }
DI void stc(u4 d, rsrc_t r, u32 v, u32 s) { __builtin_amdgcn_raw_buffer_store_b128(d, r, v, s, 16); }
DI void stc1(u32 d, rsrc_t r, u32 v, u32 s) { __builtin_amdgcn_raw_buffer_store_b32(d, r, v, s, 16); }
DI u32 mx4(u4 v) { return max(max(v.x, v.y), max(v.z, v.w)); }
DI bf8 asb(u4 v) { return __builtin_bit_cast(bf8, v); }
DI u2 trr(u32 a) { return __builtin_bit_cast(u2, __builtin_amdgcn_ds_read_tr16_b64_v4i16((lds_s4)a)); }
DI u4 cat(u2 a, u2 b) { return __builtin_shufflevector(a, b, 0, 1, 2, 3); }
DI bf16v cat8(u4 a, u4 b) { return __builtin_bit_cast(bf16v, __builtin_shufflevector(a, b, 0, 1, 2, 3, 4, 5, 6, 7)); }
DI f4 smf(u4 a, bf16v b, f4 c, int idx) { return __builtin_amdgcn_smfmac_f32_16x16x64_bf16(asb(a), b, c, idx, 0, 0); }
DI f4 m16(u4 a, u4 b, f4 c) { return __builtin_amdgcn_mfma_f32_16x16x32_bf16(asb(a), asb(b), c, 0, 0, 0); }
DI f16v m32(u4 a, u4 b, f16v c) { return __builtin_amdgcn_mfma_f32_32x32x16_bf16(asb(a), asb(b), c, 0, 0, 0); }
DI float sw32add(float a, float b) { auto r = __builtin_amdgcn_permlane32_swap(__float_as_uint(a), __float_as_uint(b), false, false); return __uint_as_float(r[0]) + __uint_as_float(r[1]); }
DI float xmax(float m) {
    auto r = __builtin_amdgcn_permlane16_swap(__float_as_uint(m), __float_as_uint(m), false, false);
    m = fmaxf(__uint_as_float(r[0]), __uint_as_float(r[1]));
    r = __builtin_amdgcn_permlane32_swap(__float_as_uint(m), __float_as_uint(m), false, false);
    return fmaxf(__uint_as_float(r[0]), __uint_as_float(r[1]));
}
DI float xsum(float m) {
    auto r = __builtin_amdgcn_permlane16_swap(__float_as_uint(m), __float_as_uint(m), false, false);
    m = __uint_as_float(r[0]) + __uint_as_float(r[1]);
    r = __builtin_amdgcn_permlane32_swap(__float_as_uint(m), __float_as_uint(m), false, false);
    return __uint_as_float(r[0]) + __uint_as_float(r[1]);
}
#define BAR() asm volatile("s_waitcnt lgkmcnt(0)\n\ts_barrier" ::: "memory")
#define DRAIN() asm volatile("s_waitcnt vmcnt(0)" ::: "memory")
#define FENCE() asm volatile("" ::: "memory")
#define SB() __builtin_amdgcn_sched_barrier(0)
#define PIN_A(x) asm volatile("" : "+a"(x))

#define STAMP(slot) do {} while (0)
#define CSTAMP(slot) do {} while (0)

DI void wait_flags(rsrc_t rs, u32 off, int n, int lane, bool& dead) {
    u32 v = off + (lane < n ? lane : 0) * 4;
    for (int i = 0; !dead; ++i) {
        FENCE();
        u32 f = ldc1(rs, v, 0);
        if (__all(f != SENT)) break;
        if (i > SPIN) dead = true;
        __builtin_amdgcn_s_sleep(1);
    }
    FENCE();
}
template <int N>
DI void gather(u4 (&v)[N], rsrc_t rs, const u32 (&off)[N], u32 so, bool& dead) {
    for (int i = 0;; ++i) {
        FENCE();
#pragma unroll
        for (int j = 0; j < N; ++j) v[j] = ldc(rs, off[j], so);
        u32 m = 0;
#pragma unroll
        for (int j = 0; j < N; ++j) m = max(m, mx4(v[j]));
        if (__all(m != SENT) || dead) break;
        if (i > SPIN) dead = true;
    }
    FENCE();
}

template <int NCH, class GA, class LP, class RD>
DI void stage(GA ga, LP lp, RD rd) {
    u4 g[3][8];
#pragma unroll
    for (int c = 0; c < 3; ++c)
        if (c < NCH) {
#pragma unroll
            for (int i = 0; i < 8; ++i) g[c][i] = *ga(c, i);
        }
#pragma unroll
    for (int c = 0; c < NCH; ++c) {
        const u32 base = (c & 1) * 32768;
#pragma unroll
        for (int i = 0; i < 8; ++i) *(lds_u4)(base + lp(i)) = g[c % 3][i];
        if (c + 3 < NCH) {
#pragma unroll
            for (int i = 0; i < 8; ++i) g[c % 3][i] = *ga(c + 3, i);
        }
        BAR();
        rd(c, base);
    }
    BAR();
}
DI u32 fsw(u32 r) { return 2 * ((r & 1) | ((r >> 1) & 2) | ((r >> 1) & 4)); }

DI void softmax12(f4 sm, f4 s1, f4 s2, u4& xch, u4& mch) {
    const float c = 0.08838834764831845f * 1.4426950408889634f;
    float m = fmaxf(fmaxf(fmaxf(sm[0], sm[1]), fmaxf(sm[2], sm[3])), fmaxf(fmaxf(fmaxf(s1[0], s1[1]), fmaxf(s1[2], s1[3])), fmaxf(fmaxf(s2[0], s2[1]), fmaxf(s2[2], s2[3]))));
    m = xmax(m);
    const float mc = -m * c;
    float em[4], e1[4], e2[4];
#pragma unroll
    for (int j = 0; j < 4; ++j) {
        em[j] = __builtin_amdgcn_exp2f(fmaf(sm[j], c, mc));
        e1[j] = __builtin_amdgcn_exp2f(fmaf(s1[j], c, mc));
        e2[j] = __builtin_amdgcn_exp2f(fmaf(s2[j], c, mc));
    }
    float t = ((e1[0] + e1[1]) + (e1[2] + e1[3])) + ((e2[0] + e2[1]) + (e2[2] + e2[3])) + 0.5f * ((em[0] + em[1]) + (em[2] + em[3]));
    t = xsum(t);
    const float r = __builtin_amdgcn_rcpf(t);
    xch = (u4){pk(e1[0] * r, e1[1] * r), pk(e1[2] * r, e1[3] * r), pk(e2[0] * r, e2[1] * r), pk(e2[2] * r, e2[3] * r)};
    u32 a = pk(em[0] * r, em[1] * r), b = pk(em[2] * r, em[3] * r);
    auto ra = __builtin_amdgcn_permlane16_swap(a, a, false, false);
    auto rb = __builtin_amdgcn_permlane16_swap(b, b, false, false);
    mch = (u4){ra[0], rb[0], ra[1], rb[1]};
}

DI u32 get_par(rsrc_t rs, int lane, bool& dead, int which) {
    wait_flags(rs, F_PAR, 2, lane, dead);
    u32 z = ldc1(rs, F_PAR + which * 4, 0);
    return (__builtin_amdgcn_readfirstlane(z) & 1) << 13;
}

DI void role_r1(const Params& p, rsrc_t rs, int h, int tid, int wave, int lane) {
    bool dead = false;
    if (tid < 160) { int q = tid / 40, ch = tid % 40; stc((u4)(SENT), rs, amap(h * 640 + ch * 16) + (q & 1) * 8192 + (q >> 1) * P_STR, P_OFF); }
    bf16v B[10][4];
    {
        const u16* src = p.wq + (size_t)(tid >> 4) * 4096 + h * 128 + (tid & 15) * 8;
        const ptrdiff_t dk = p.wk - p.wq;
        const u32 lw = (tid >> 4) * 256 + (((tid & 15) ^ fsw(tid >> 4)) << 4);
        const u32 gb = lane >> 4, rho = (lane >> 2) & 3, c4 = lane & 3;
        const u32 rl = 16 * (gb >> 1) + 4 * (gb & 1) + (rho & 1) + 8 * (rho >> 1);
        u32 ra[2];
#pragma unroll
        for (int u = 0; u < 2; ++u) ra[u] = rl * 256 + (((4 * wave + 2 * u + (c4 >> 1)) ^ fsw(rl)) << 4) + (c4 & 1) * 8;
        stage<10>([&](int c, int i) { return (const u4*)(src + (i >= 4 ? dk : 0) + (size_t)(64 * c + 16 * (i & 3)) * 4096); },
                  [&](int i) { return lw + i * 4096; },
                  [&](int c, u32 base) {
#pragma unroll
                      for (int t = 0; t < 4; ++t) {
                          u32 a = base + ra[t & 1] + (t >> 1) * 16384;
                          B[c][t] = cat8(cat(trr(a), trr(a + 512)), cat(trr(a + 8192), trr(a + 8192 + 512)));
                          if (c * 4 + t < 28) PIN_A(B[c][t]);
                      }
                  });
    }
    DRAIN(); BAR();
    if (tid == 0) stc1(0, rs, F_I1 + h * 4, 0);
    const u32 par = get_par(rs, lane, dead, 1);
    const int idx = (lane & 8) ? 0xEEEE : 0x4444;
    const u32 g = lane >> 4, col = lane & 15, gam = col >> 2, jj = col & 3;
    u32 moff[3], mlds[3];
#pragma unroll
    for (int j = 0; j < 3; ++j) { u32 ch = wave * 160 + (j == 2 ? 128 + (lane & 31) : j * 64 + lane); moff[j] = amap(ch * 16) + par; mlds[j] = ch * 16; }
    const u32 IMG = 10240;
    const u32 qa = IMG + (g >> 1) * 512 + ((lane & 1) * 16 + 4 * (g & 1) + ((lane >> 2) & 3)) * 8;
    const u32 poff = amap(h * 640 + (col < 8 ? (1 + g) * 128 + col * 16 : (col - 8) * 16)) + par;
    const u32 pinit = amap(h * 640 + (lane < 40 ? lane : 0) * 16) + par;
    const int tt = wave - 1;
    const u32 tok1 = wave == 0 ? 16 * (gam >> 1) + 2 * (gam & 1) + (jj & 1) + 8 * (jj >> 1) : 16 * (gam >> 1) + 4 * (gam & 1) + jj;
    const u32 kxo = h * 8192 + g * 512 + tok1 * 16, kx2 = wave == 0 ? 64 : 128;
    const u32 qxo = h * 8192 + g * 512 + (16 * tt + col) * 16;
    u4 kx[2][4], qx[4];
    f4 sx1 = {0, 0, 0, 0}, sx2 = {0, 0, 0, 0};
    auto prefetch = [&](int s) {
        if (wave < 3) {
            wait_flags(rs, F_QK + (s * 32 + h) * 4, 1, lane, dead);
#pragma unroll
            for (int ks = 0; ks < 4; ++ks) { kx[0][ks] = ldc(rs, kxo + ks * 2048, KF + s * 262144); kx[1][ks] = ldc(rs, kxo + kx2 + ks * 2048, KF + s * 262144); }
            if (wave > 0) {
#pragma unroll
                for (int ks = 0; ks < 4; ++ks) qx[ks] = ldc(rs, qxo + ks * 2048, QF + s * 262144);
                sx1 = (f4){0, 0, 0, 0}; sx2 = sx1;
#pragma unroll
                for (int ks = 0; ks < 4; ++ks) { sx1 = m16(kx[0][ks], qx[ks], sx1); sx2 = m16(kx[1][ks], qx[ks], sx2); }
                const int tok = 16 * tt + col, key = 16 * (g >> 1) + 4 * (g & 1);
#pragma unroll
                for (int j = 0; j < 4; ++j) { if (key + j > tok) sx1[j] = -INFINITY; if (key + 8 + j > tok) sx2[j] = -INFINITY; }
            }
        }
    };
    prefetch(0);
    wait_flags(rs, F_I2, 40, lane, dead);
    for (int s = 0; s < 64; ++s) {
        u4 v[3];
        gather<3>(v, rs, moff, M_OFF + (s & 3) * M_STR, dead);
        STAMP(0);
#pragma unroll
        for (int j = 0; j < 3; ++j) *(lds_u4)mlds[j] = v[j];
        BAR();
        u4 a[10];
#pragma unroll
        for (int kb = 0; kb < 10; ++kb) a[kb] = *(lds_u4)(kb * 1024 + lane * 16);
        SB();
        f4 acc[4] = {};
#pragma unroll
        for (int kb = 0; kb < 10; ++kb)
#pragma unroll
            for (int t = 0; t < 4; ++t) acc[t] = smf(a[kb], B[kb][t], acc[t], idx);
#pragma unroll
        for (int u = 0; u < 2; ++u) {
            float s0 = sw32add(acc[u][0], acc[2 + u][0]), s1 = sw32add(acc[u][1], acc[2 + u][1]), s2 = sw32add(acc[u][2], acc[2 + u][2]), s3 = sw32add(acc[u][3], acc[2 + u][3]);
            *(lds_u2)(IMG + (wave * 2 + u) * 512 + lane * 8) = (u2){pk(s0, s1), pk(s2, s3)};
        }
        BAR();
        STAMP(1);
        if (wave == 0) {
            u4 qf[4], kf[4];
#pragma unroll
            for (int ks = 0; ks < 4; ++ks) qf[ks] = cat(trr(qa + ks * 1024), trr(qa + ks * 1024 + 64));
#pragma unroll
            for (int ks = 0; ks < 4; ++ks) kf[ks] = cat(trr(qa + ks * 1024 + 256), trr(qa + ks * 1024 + 256 + 64));
            SB();
            f4 sm = {0, 0, 0, 0}, s1 = sm, s2 = sm;
#pragma unroll
            for (int ks = 0; ks < 4; ++ks) { s1 = m16(kx[0][ks], qf[ks], s1); s2 = m16(kx[1][ks], qf[ks], s2); }
#pragma unroll
            for (int ks = 0; ks < 4; ++ks) sm = m16(kf[ks], qf[ks], sm);
            u4 xch, mch;
            softmax12(sm, s1, s2, xch, mch);
            u4 d = col < 8 ? xch : mch;
            d = (u4){nos(d.x), nos(d.y), nos(d.z), nos(d.w)};
            if (col < 8 || g == 0) stc(d, rs, poff, P_OFF + (s & 3) * P_STR);
            STAMP(2);
            if (lane < 40) stc((u4)(SENT), rs, pinit, P_OFF + ((s + 2) & 3) * P_STR);
        } else if (wave < 3) {
            u4 kf[4];
#pragma unroll
            for (int ks = 0; ks < 4; ++ks) kf[ks] = cat(trr(qa + ks * 1024 + 256), trr(qa + ks * 1024 + 256 + 64));
            f4 sm = {0, 0, 0, 0};
#pragma unroll
            for (int ks = 0; ks < 4; ++ks) sm = m16(kf[ks], qx[ks], sm);
            u4 xch, mch;
            softmax12(sm, sx1, sx2, xch, mch);
            const u32 pb = PX + (s * 32 + h) * 2560;
            stc(xch, rs, g * 512 + (16 * tt + col) * 16, pb);
            if (g == 0) stc(mch, rs, 2048 + (16 * tt + col) * 16, pb);
        }
        if (s < 63) prefetch(s + 1);
        STAMP(3);
    }
    DRAIN(); BAR();
    if (wave == 0 && lane < 40) stc((u4)(0u), rs, pinit, P_OFF);
}

DI void role_ru(const Params& p, rsrc_t rs, int h, int tid, int wave, int lane) {
    bool dead = false;
#pragma unroll
    for (int i = 0; i < 10; ++i) { u32 n = i * 256 + tid, q = n / 640, r = n % 640; stc((u4)(SENT), rs, (r >> 4) * 16384 + h * 256 + (r & 15) * 16 + (q & 1) * 8192 + (q >> 1) * UM_STR, WIN0); }
    bf16v BV[10][2], BO[5][2][2];
    {
        const u16* sv = p.wv + (size_t)(tid >> 4) * 4096 + h * 128 + (tid & 15) * 8;
        const u16* so = p.wo + (size_t)(h * 128 + (tid >> 4)) * 640 + (tid & 15) * 8;
        const u32 lw = (tid >> 4) * 256 + (((tid & 15) ^ fsw(tid >> 4)) << 4);
        const u32 gb = lane >> 4, rho = (lane >> 2) & 3, c4 = lane & 3;
        const u32 rl = 16 * (gb >> 1) + 4 * (gb & 1) + (rho & 1) + 8 * (rho >> 1);
        u32 ra[2];
#pragma unroll
        for (int u = 0; u < 2; ++u) ra[u] = rl * 256 + (((4 * wave + 2 * u + (c4 >> 1)) ^ fsw(rl)) << 4) + (c4 & 1) * 8;
        stage<10>([&](int c, int i) { return c < 5 ? (const u4*)(sv + (size_t)(128 * c + 16 * i) * 4096) : (const u4*)(so + (size_t)(16 * i) * 640 + (c - 5) * 128); },
                  [&](int i) { return lw + i * 4096; },
                  [&](int c, u32 base) {
#pragma unroll
                      for (int kk = 0; kk < 2; ++kk)
#pragma unroll
                          for (int u = 0; u < 2; ++u) {
                              u32 a = base + ra[u] + kk * 16384;
                              bf16v f = cat8(cat(trr(a), trr(a + 512)), cat(trr(a + 8192), trr(a + 8192 + 512)));
                              if (c < 5) { BV[2 * c + kk][u] = f; PIN_A(BV[2 * c + kk][u]); }
                              else { BO[c - 5][u][kk] = f; if (c == 5) PIN_A(BO[c - 5][u][kk]); }
                          }
                  });
    }
    DRAIN(); BAR();
    if (tid == 0) stc1(0, rs, F_IU + h * 4, 0);
    const u32 par1 = get_par(rs, lane, dead, 1), par0 = get_par(rs, lane, dead, 0);
    const int idx = (lane & 8) ? 0xEEEE : 0x4444;
    const u32 g = lane >> 4, col = lane & 15;
    u32 moff[3], mlds[3];
#pragma unroll
    for (int j = 0; j < 3; ++j) { u32 ch = wave * 160 + (j == 2 ? 128 + (lane & 31) : j * 64 + lane); moff[j] = amap(ch * 16) + par1; mlds[j] = ch * 16; }
    const u32 IMG = 10240;
    const u32 vw = IMG + wave * 512 + (g * 16 + (col ^ (4 * (g >> 1)))) * 8;
    u32 vr[2];
    {
        const u32 rho = (lane >> 2) & 3, c4 = lane & 3, t = g & 1;
#pragma unroll
        for (int z = 0; z < 2; ++z) vr[z] = IMG + (g >> 1) * 512 + ((2 * t + (c4 & 1)) * 16 + ((8 * (c4 >> 1) + 4 * z + rho) ^ (4 * t))) * 8;
    }
    u32 uo[3];
    uo[0] = (8 * (g & 1) + 2 * wave + (g >> 1)) * 16384 + h * 256 + col * 16 + par0;
    uo[1] = uo[0] + 16 * 16384;
    uo[2] = (32 + 2 * wave + (g >> 1)) * 16384 + h * 256 + col * 16 + par0;
    wait_flags(rs, F_I2, 40, lane, dead);
    u32 us = 0;
    for (int s = 0; s < 64; ++s) {
        u4 v[3];
        gather<3>(v, rs, moff, M_OFF + (s & 3) * M_STR, dead);
        STAMP(0);
#pragma unroll
        for (int j = 0; j < 3; ++j) *(lds_u4)mlds[j] = v[j];
        BAR();
        u4 a[10];
#pragma unroll
        for (int kb = 0; kb < 10; ++kb) a[kb] = *(lds_u4)(kb * 1024 + lane * 16);
        SB();
        f4 av[2] = {};
#pragma unroll
        for (int kb = 0; kb < 10; ++kb)
#pragma unroll
            for (int u = 0; u < 2; ++u) av[u] = smf(a[kb], BV[kb][u], av[u], idx);
        {
            float s0 = sw32add(av[0][0], av[1][0]), s1 = sw32add(av[0][1], av[1][1]), s2 = sw32add(av[0][2], av[1][2]), s3 = sw32add(av[0][3], av[1][3]);
            *(lds_u2)vw = (u2){pk(s0, s1), pk(s2, s3)};
        }
        BAR();
        STAMP(1);
        u4 af[2];
#pragma unroll
        for (int kb = 0; kb < 2; ++kb) af[kb] = cat(trr(vr[0] + kb * 1024), trr(vr[1] + kb * 1024));
        f4 ao[5][2];
#pragma unroll
        for (int j = 0; j < 5; ++j)
#pragma unroll
            for (int u = 0; u < 2; ++u) { ao[j][u] = smf(af[0], BO[j][u][0], (f4){0, 0, 0, 0}, idx); ao[j][u] = smf(af[1], BO[j][u][1], ao[j][u], idx); }
        u2 R[5];
#pragma unroll
        for (int j = 0; j < 5; ++j) R[j] = (u2){nos(pk(sw32add(ao[j][0][0], ao[j][1][0]), sw32add(ao[j][0][1], ao[j][1][1]))), nos(pk(sw32add(ao[j][0][2], ao[j][1][2]), sw32add(ao[j][0][3], ao[j][1][3])))};
        const u32 ub = WIN0 + us * UM_STR;
#pragma unroll
        for (int G = 0; G < 3; ++G) {
            u2 x = R[2 * G], y = R[G == 2 ? 4 : 2 * G + 1];
            auto r0 = __builtin_amdgcn_permlane16_swap(x.x, y.x, false, false);
            auto r1 = __builtin_amdgcn_permlane16_swap(x.y, y.y, false, false);
            if (G < 2 || !(g & 1)) stc((u4){r0[0], r1[0], r0[1], r1[1]}, rs, uo[G], ub);
        }
        STAMP(2);
        const u32 un = us == 0 ? 2 : us - 1;
#pragma unroll
        for (int G = 0; G < 3; ++G)
            if (G < 2 || !(g & 1)) stc((u4)(SENT), rs, uo[G], WIN0 + un * UM_STR);
        us = us == 2 ? 0 : us + 1;
    }
}

DI void role_r2(const Params& p, rsrc_t rs, int c, int tid, int wave, int lane) {
    bool dead = false;
    if (tid < 96) {
        int j = tid & 15, q = tid >> 4, slot = q >> 1;
        u4 d = (u4)(SENT);
        if (slot == 0) { d = *(const u4*)(p.mem0 + (j & 7) * 640 + 16 * c + 8 * (j >> 3)); d = (u4){nos(d.x), nos(d.y), nos(d.z), nos(d.w)}; }
        stc(d, rs, amap(c * 256 + j * 16) + (q & 1) * 8192 + slot * M_STR, M_OFF);
    }
    DRAIN(); BAR();
    if (tid == 0) stc1(0, rs, F_I2 + c * 4, 0);
    if (c == 0 && (wave == 1 || wave == 2)) {
        const u32 cal = wave == 1 ? CAL0 : CAL1;
        u32 best[2] = {~0u, ~0u};
        for (int rep = 0; rep < 10; ++rep)
#pragma unroll
            for (int q = 0; q < 2; ++q) {
                DRAIN();
                u32 t0 = (u32)__builtin_amdgcn_s_memtime();
                SB();
                if (lane == 0) stc((u4)(0u), rs, cal + q * 8192 + rep * 128, 0);
                DRAIN();
                SB();
                u32 t1 = (u32)__builtin_amdgcn_s_memtime();
                best[q] = min(best[q], t1 - t0);
            }
        if (lane == 0) stc1(best[1] < best[0] ? 1 : 0, rs, F_PAR + (wave - 1) * 4, 0);
    }
    const u32 par1 = get_par(rs, lane, dead, 1), par0 = get_par(rs, lane, dead, 0);
    const int idx = (lane & 8) ? 0xEEEE : 0x4444;
    const u32 g = lane >> 4, col = lane & 15;
    u32 uoff[2], poff[6];
#pragma unroll
    for (int mq = 0; mq < 2; ++mq) {
        uoff[mq] = c * 16384 + (8 * wave + 4 * mq + g) * 256 + col * 16 + par0;
        poff[4 + mq] = col < 8 ? amap((8 * wave + 4 * mq + g) * 640 + col * 16) + par1 : 0xF0000000u;
    }
#pragma unroll
    for (int i = 0; i < 4; ++i) poff[i] = amap((8 * wave + 4 * (i >> 1) + g) * 640 + (1 + 2 * (i & 1) + (col >> 3)) * 128 + (col & 7) * 16) + par1;
    const u32 bxo = (8 * wave + (g >> 1)) * 1024 + (g & 1) * 256 + col * 16;
    const u32 pxo = (8 * wave + (g >> 1)) * 2560 + (g & 1) * 512 + col * 16;
    const u32 pxm = (8 * wave + g) * 2560 + 2048 + col * 16;
    const u32 mo = amap(c * 256 + ((lane >> 2) & 1) * 128 + (lane >> 3) * 16) + par1;
    const u32 mi = amap(c * 256 + col * 16) + par1;
    const u32 rw = (wave * 8 + 2 * (g >> 1) + (g & 1)) * 64 + col * 4;
    const u32 rr = (((lane >> 3) & 3) * 2 + (lane >> 5)) * 64 + (lane & 7) * 8;
    const u32 RED2 = 2048;
    u4 bx[4][2], um[2], pbx[4][2], pum[2];
    auto load_bx = [&](int s, u4 (&d)[4][2]) {
#pragma unroll
        for (int i = 0; i < 4; ++i)
#pragma unroll
            for (int z = 0; z < 2; ++z) d[i][z] = ldc(rs, bxo + (i >> 1) * 4096 + z * 2048 + (i & 1) * 512, BX + (s * 40 + c) * 32768);
    };
    auto outx = [&](int t, int snext) {
        u4 px[4][2][2], pm[2][2], nb[4][2];
        const u32 pb = PX + t * 81920;
        u32 fl = ldc1(rs, lane < 32 ? F_B + (snext * 32 + lane) * 4 : F_F + (t * 16 + (c >> 2)) * 4, 0);
#pragma unroll
        for (int i = 0; i < 4; ++i)
#pragma unroll
            for (int z = 0; z < 2; ++z)
#pragma unroll
                for (int tt = 0; tt < 2; ++tt) px[i][z][tt] = ldc(rs, pxo + (i >> 1) * 10240 + z * 5120 + (i & 1) * 1024 + tt * 256, pb);
#pragma unroll
        for (int mq = 0; mq < 2; ++mq)
#pragma unroll
            for (int tt = 0; tt < 2; ++tt) pm[mq][tt] = ldc(rs, pxm + mq * 10240 + tt * 256, pb);
        SB();
        if (!__all(fl != SENT)) {
            wait_flags(rs, F_B + snext * 128, 32, lane, dead);
            wait_flags(rs, F_F + (t * 16 + (c >> 2)) * 4, 1, lane, dead);
        }
        FENCE();
        load_bx(snext, nb);
        const u32 tok = 16 * (wave & 1) + col;
        u2 bb = ldc2(rs, ((g & 1) * 32 + tok) * 16 + (g >> 1) * 8, BF + (t * 40 + c) * 1024);
        u32 gv = ldc1(rs, F_G + (t * 32 + tok) * 4, 0);
        SB();
        f4 o[2] = {};
#pragma unroll
        for (int tt = 0; tt < 2; ++tt) {
#pragma unroll
            for (int i = 0; i < 4; ++i)
#pragma unroll
                for (int z = 0; z < 2; ++z) o[tt] = m16(pbx[i][z], px[i][z][tt], o[tt]);
#pragma unroll
            for (int mq = 0; mq < 2; ++mq) o[tt] = m16(pum[mq], pm[mq][tt], o[tt]);
            *(lds_f4)(RED2 + ((wave * 2 + tt) * 64 + lane) * 16) = o[tt];
        }
        BAR();
        if (wave < 2) {
            f4 a = *(lds_f4)(RED2 + (wave * 64 + lane) * 16);
#pragma unroll
            for (int w = 1; w < 4; ++w) a += *(lds_f4)(RED2 + ((w * 2 + wave) * 64 + lane) * 16);
            for (int i = 0; __any(gv == SENT) && !dead; ++i) { FENCE(); gv = ldc1(rs, F_G + (t * 32 + tok) * 4, 0); if (i > SPIN) dead = true; }
            const float gf = __uint_as_float(gv), og = rbf(1.f - gf);
            float r0 = rbf(gf * rbf(a[0])) + rbf(og * lo2f(bb.x)), r1 = rbf(gf * rbf(a[1])) + rbf(og * hi2f(bb.x));
            float r2 = rbf(gf * rbf(a[2])) + rbf(og * lo2f(bb.y)), r3 = rbf(gf * rbf(a[3])) + rbf(og * hi2f(bb.y));
            *(u2*)(p.out + (size_t)(t * 32 + tok) * 640 + 16 * c + 4 * g) = (u2){pk(r0, r1), pk(r2, r3)};
        }
#pragma unroll
        for (int i = 0; i < 4; ++i)
#pragma unroll
            for (int z = 0; z < 2; ++z) { pbx[i][z] = bx[i][z]; bx[i][z] = nb[i][z]; }
        pum[0] = um[0]; pum[1] = um[1];
    };
    wait_flags(rs, F_B, 32, lane, dead);
    load_bx(0, bx);
    wait_flags(rs, F_I1, 32, lane, dead);
    wait_flags(rs, F_IU, 32, lane, dead);
    u32 us = 0;
    for (int s = 0; s < 64; ++s) {
        gather<2>(um, rs, uoff, WIN0 + us * UM_STR, dead);
        STAMP(0);
        u4 pa[6];
        gather<6>(pa, rs, poff, P_OFF + (s & 3) * P_STR, dead);
        STAMP(1);
        {
            f4 a0 = m16(pa[4], um[0], (f4){0, 0, 0, 0});
            f4 a1 = smf(pa[0], cat8(bx[0][0], bx[0][1]), (f4){0, 0, 0, 0}, idx);
            f4 a2 = smf(pa[1], cat8(bx[1][0], bx[1][1]), (f4){0, 0, 0, 0}, idx);
            a0 = m16(pa[5], um[1], a0);
            a1 = smf(pa[2], cat8(bx[2][0], bx[2][1]), a1, idx);
            a2 = smf(pa[3], cat8(bx[3][0], bx[3][1]), a2, idx);
            f4 d = (a0 + a1) + a2;
            *(lds_f1)rw = sw32add(d[0], d[1]);
            *(lds_f1)(rw + 256) = sw32add(d[2], d[3]);
            BAR();
            if (wave == 0 && s < 63) {
                f2 t = (*(lds_f2)rr + *(lds_f2)(rr + 512)) + (*(lds_f2)(rr + 1024) + *(lds_f2)(rr + 1536));
                u32 w = nos(pk(t[0], t[1]));
                u4 o = {(u32)__builtin_amdgcn_update_dpp(0u, w, 0x00, 0xf, 0xf, true), (u32)__builtin_amdgcn_update_dpp(0u, w, 0x55, 0xf, 0xf, true),
                        (u32)__builtin_amdgcn_update_dpp(0u, w, 0xAA, 0xf, 0xf, true), (u32)__builtin_amdgcn_update_dpp(0u, w, 0xFF, 0xf, 0xf, true)};
                if ((lane & 3) == 0) stc(o, rs, mo, M_OFF + ((s + 1) & 3) * M_STR);
                STAMP(2);
                if (lane < 16) stc((u4)(SENT), rs, mi, M_OFF + ((s + 3) & 3) * M_STR);
            }
        }
        if (s == 0) {
            wait_flags(rs, F_B + 128, 32, lane, dead);
#pragma unroll
            for (int i = 0; i < 4; ++i)
#pragma unroll
                for (int z = 0; z < 2; ++z) pbx[i][z] = bx[i][z];
            pum[0] = um[0]; pum[1] = um[1];
            load_bx(1, bx);
            BAR();
        } else {
            outx(s - 1, s < 63 ? s + 1 : 63);
        }
        STAMP(3);
        us = us == 2 ? 0 : us + 1;
    }
    {
        u4 pa[6];
        gather<6>(pa, rs, poff, P_OFF, dead);
        BAR();
        outx(63, 63);
    }
}

struct XStage {
    const u4* xs; u32 lw[10]; u4 xg[10]; u32 rd;
    DI void init(const u16* x, int tid, int lane) {
        xs = (const u4*)x + tid;
#pragma unroll
        for (int i = 0; i < 10; ++i) { u32 n = i * 256 + tid, t = n / 80, ch = n % 80; lw[i] = t * 1280 + ((ch & ~15u) | ((ch ^ t) & 15u)) * 16; }
        rd = (lane & 31) * 1280 + (((lane >> 5) ^ (lane & 15)) << 4);
    }
    DI void load(int s) {
#pragma unroll
        for (int i = 0; i < 10; ++i) xg[i] = xs[s * 2560 + i * 256];
    }
    DI void write(int s) {
        const u32 b = (s & 1) * 40960;
#pragma unroll
        for (int i = 0; i < 10; ++i) *(lds_u4)(b + lw[i]) = xg[i];
    }
    DI u4 frag(int s, int ks) const { return *(lds_u4)((s & 1) * 40960 + (rd ^ (32 * (ks & 7))) + (ks >> 3) * 256); }
};
template <class F>
DI void kloop(const XStage& X, int s, F f) {
    u4 b[40];
#pragma unroll
    for (int k = 0; k < 4; ++k) b[k] = X.frag(s, k);
#pragma unroll
    for (int k = 0; k < 40; ++k) {
        if (k + 4 < 40) b[k + 4] = X.frag(s, k + 4);
        SB();
        f(k, b[k]);
        SB();
    }
}
DI u4 pk8(const f16v& a, int p) { return (u4){pk(a[8 * p], a[8 * p + 1]), pk(a[8 * p + 2], a[8 * p + 3]), pk(a[8 * p + 4], a[8 * p + 5]), pk(a[8 * p + 6], a[8 * p + 7])}; }

DI void role_wqk(const Params& p, rsrc_t rs, int h, int tid, int wave, int lane) {
    XStage X; X.init(p.x, tid, lane); X.load(0);
    u4 A[2][40];
    {
        const u16* src = p.wq + (size_t)(tid >> 4) * 4096 + h * 128 + (tid & 15) * 8;
        const ptrdiff_t dk = p.wk - p.wq;
        const u32 lw = (tid >> 4) * 256 + (((tid & 15) ^ (4 * ((tid >> 4) & 3))) << 4);
        const u32 rho = (lane >> 2) & 3, c4 = lane & 3;
        const u32 ra = (8 * (lane >> 5) + rho) * 256 + (((4 * wave + 2 * ((lane >> 4) & 1) + (c4 >> 1)) ^ (4 * rho)) << 4) + (c4 & 1) * 8;
        stage<10>([&](int c, int i) { return (const u4*)(src + (i >= 4 ? dk : 0) + (size_t)(64 * c + 16 * (i & 3)) * 4096); },
                  [&](int i) { return lw + i * 4096; },
                  [&](int c, u32 base) {
#pragma unroll
                      for (int m = 0; m < 2; ++m)
#pragma unroll
                          for (int kk = 0; kk < 4; ++kk) {
                              u32 a = base + ra + m * 16384 + kk * 4096;
                              A[m][4 * c + kk] = cat(trr(a), trr(a + 1024));
                              if (c < 7) PIN_A(A[m][4 * c + kk]);
                          }
                  });
    }
    for (int s = 0; s < 64; ++s) {
        CSTAMP(1);
        X.write(s);
        CSTAMP(2);
        DRAIN(); BAR();
        CSTAMP(3);
        if (s > 0 && tid == 0) stc1(0, rs, F_QK + ((s - 1) * 32 + h) * 4, 0);
        if (s < 63) X.load(s + 1);
        f16v q = {}, k = {};
        kloop(X, s, [&](int ks, u4 b) { q = m32(A[0][ks], b, q); k = m32(A[1][ks], b, k); });
        CSTAMP(4);
        const u32 o = h * 8192 + wave * 2048 + lane * 16;
        stc(pk8(q, 0), rs, o, QF + s * 262144); stc(pk8(q, 1), rs, o + 1024, QF + s * 262144);
        stc(pk8(k, 0), rs, o, KF + s * 262144); stc(pk8(k, 1), rs, o + 1024, KF + s * 262144);
        STAMP(0);
    }
    DRAIN(); BAR();
    if (tid == 0) stc1(0, rs, F_QK + (63 * 32 + h) * 4, 0);
}

DI void role_wvb(const Params& p, rsrc_t rs, int h, int tid, int wave, int lane) {
    XStage X; X.init(p.x, tid, lane); X.load(0);
    u4 AV[40], BO[5][8];
    {
        const u16* sv = p.wv + (size_t)(tid >> 4) * 4096 + h * 128 + (tid & 15) * 8;
        const u16* so = p.wo + (size_t)(h * 128 + (tid >> 4)) * 640 + (tid & 15) * 8;
        const u32 lw = (tid >> 4) * 256 + (((tid & 15) ^ (4 * ((tid >> 4) & 3))) << 4);
        const u32 rho = (lane >> 2) & 3, c4 = lane & 3;
        const u32 cx = (((4 * wave + 2 * ((lane >> 4) & 1) + (c4 >> 1)) ^ (4 * rho)) << 4) + (c4 & 1) * 8;
        const u32 ra = (8 * (lane >> 5) + rho) * 256 + cx, rb = (4 * (lane >> 5) + rho) * 256 + cx;
        stage<10>([&](int c, int i) { return c < 5 ? (const u4*)(sv + (size_t)(128 * c + 16 * i) * 4096) : (const u4*)(so + (size_t)(16 * i) * 640 + (c - 5) * 128); },
                  [&](int i) { return lw + i * 4096; },
                  [&](int c, u32 base) {
#pragma unroll
                      for (int kk = 0; kk < 8; ++kk) {
                          if (c < 5) { u32 a = base + ra + kk * 4096; AV[8 * c + kk] = cat(trr(a), trr(a + 1024)); PIN_A(AV[8 * c + kk]); }
                          else { u32 a = base + rb + kk * 4096; BO[c - 5][kk] = cat(trr(a), trr(a + 2048)); if (c == 5) PIN_A(BO[c - 5][kk]); }
                      }
                  });
    }
    const u32 VX = 81920;
    const u32 bo = (2 * wave + ((lane >> 4) & 1)) * 32768 + h * 1024 + (lane >> 5) * 256 + (lane & 15) * 16;
    for (int s = 0; s < 64; ++s) {
        X.write(s);
        DRAIN(); BAR();
        if (s > 0 && tid == 0) stc1(0, rs, F_B + ((s - 1) * 32 + h) * 4, 0);
        if (s < 63) X.load(s + 1);
        f16v v = {};
        kloop(X, s, [&](int ks, u4 b) { v = m32(AV[ks], b, v); });
        *(lds_u4)(VX + (2 * wave) * 1024 + lane * 16) = pk8(v, 0);
        *(lds_u4)(VX + (2 * wave + 1) * 1024 + lane * 16) = pk8(v, 1);
        BAR();
        u4 vf[8];
#pragma unroll
        for (int k = 0; k < 8; ++k) vf[k] = *(lds_u4)(VX + k * 1024 + lane * 16);
        const u32 sb = BX + s * 40 * 32768;
#pragma unroll
        for (int j = 0; j < 5; ++j) {
            f16v d = {};
#pragma unroll
            for (int k = 0; k < 8; ++k) d = m32(vf[k], BO[j][k], d);
            stc(pk8(d, 0), rs, bo + j * 8 * 32768, sb);
            stc(pk8(d, 1), rs, bo + j * 8 * 32768 + 512, sb);
        }
        STAMP(0);
    }
    DRAIN(); BAR();
    if (tid == 0) stc1(0, rs, F_B + (63 * 32 + h) * 4, 0);
}

DI void role_f1(const Params& p, rsrc_t rs, int f, int tid, int wave, int lane) {
    XStage X; X.init(p.x, tid, lane); X.load(0);
    u4 A[40];
    {
        const u16* src = ((tid & 8) ? p.w3 : p.w1) + (size_t)(tid >> 4) * 1728 + 64 * f + (tid & 7) * 8;
        const u32 lw = (tid >> 4) * 256 + (((tid & 15) ^ (2 * ((tid >> 4) & 3))) << 4);
        const u32 rho = (lane >> 2) & 3, c4 = lane & 3;
        const u32 ra = (8 * (lane >> 5) + rho) * 256 + (((8 * ((lane >> 4) & 1) + 2 * wave + (c4 >> 1)) ^ (2 * rho)) << 4) + (c4 & 1) * 8;
        stage<5>([&](int c, int i) { return (const u4*)(src + (size_t)(128 * c + 16 * i) * 1728); },
                 [&](int i) { return lw + i * 4096; },
                 [&](int c, u32 base) {
#pragma unroll
                     for (int kk = 0; kk < 8; ++kk) { u32 a = base + ra + kk * 4096; A[8 * c + kk] = cat(trr(a), trr(a + 1024)); PIN_A(A[8 * c + kk]); }
                 });
    }
    for (int s = 0; s < 64; ++s) {
        X.write(s);
        DRAIN(); BAR();
        if (s > 0 && tid == 0) stc1(0, rs, F_H + ((s - 1) * 32 + f) * 4, 0);
        if (s < 63) X.load(s + 1);
        f16v d = {};
        kloop(X, s, [&](int ks, u4 b) { d = m32(A[ks], b, d); });
        float hv[8];
#pragma unroll
        for (int j = 0; j < 8; ++j) {
            float gt = rbf(d[j]), up = rbf(d[8 + j]);
            float sl = rbf(gt * __builtin_amdgcn_rcpf(1.f + __builtin_amdgcn_exp2f(-1.4426950408889634f * gt)));
            hv[j] = sl * up;
        }
        stc((u4){pk(hv[0], hv[1]), pk(hv[2], hv[3]), pk(hv[4], hv[5]), pk(hv[6], hv[7])}, rs, (4 * f + wave) * 1024 + lane * 16, HF + s * 110592);
        STAMP(0);
    }
    DRAIN(); BAR();
    if (tid == 0) stc1(0, rs, F_H + (63 * 32 + f) * 4, 0);
}

DI void role_f2(const Params& p, rsrc_t rs, int n, int tid, int wave, int lane) {
    bool dead = false;
    u4 A[2][27];
    {
        const u16* src = p.w2 + 64 * n + (tid & 7) * 8;
        const u32 r0 = tid >> 3;
        const u32 lw = r0 * 128 + (((tid & 7) ^ (4 * ((r0 >> 1) & 1))) << 4);
        const u32 rho = (lane >> 2) & 3, c4 = lane & 3;
        u32 ra[2];
#pragma unroll
        for (int u = 0; u < 2; ++u) ra[u] = (wave * 64 + 4 * (lane >> 5) + rho) * 128 + (((4 * u + 2 * ((lane >> 4) & 1) + (c4 >> 1)) ^ (4 * (rho >> 1))) << 4) + (c4 & 1) * 8;
        stage<7>([&](int c, int i) { u32 rr = 64 * c + 32 * (i & 1) + r0; rr = rr < 431 ? rr : 431; return (const u4*)(src + (size_t)(432 * (i >> 1) + rr) * 640); },
                 [&](int i) { return lw + i * 4096; },
                 [&](int c, u32 base) {
#pragma unroll
                     for (int kk = 0; kk < 4; ++kk)
#pragma unroll
                         for (int u = 0; u < 2; ++u)
                             if (4 * c + kk < 27) { u32 a = base + ra[u] + kk * 2048; A[u][4 * c + kk] = cat(trr(a), trr(a + 1024)); PIN_A(A[u][4 * c + kk]); }
                 });
    }
    for (int s = 0; s < 64; ++s) {
        CSTAMP(1);
        wait_flags(rs, F_H + s * 128, 27, lane, dead);
        CSTAMP(2);
        u4 hb[27];
#pragma unroll
        for (int k = 0; k < 27; ++k) hb[k] = ldc(rs, (27 * wave + k) * 1024 + lane * 16, HF + s * 110592);
        SB();
        f16v d[2] = {};
#pragma unroll
        for (int k = 0; k < 27; ++k) { d[0] = m32(A[0][k], hb[k], d[0]); d[1] = m32(A[1][k], hb[k], d[1]); }
        CSTAMP(3);
#pragma unroll
        for (int u = 0; u < 2; ++u)
#pragma unroll
            for (int i = 0; i < 4; ++i) *(lds_f4)((((wave * 2 + u) * 4 + i) * 64 + lane) * 16) = (f4){d[u][4 * i], d[u][4 * i + 1], d[u][4 * i + 2], d[u][4 * i + 3]};
        BAR();
        f4 r[2];
#pragma unroll
        for (int z = 0; z < 2; ++z) {
            r[z] = *(lds_f4)(((2 * wave + z) * 64 + lane) * 16);
#pragma unroll
            for (int w = 1; w < 4; ++w) r[z] += *(lds_f4)(((w * 8 + 2 * wave + z) * 64 + lane) * 16);
        }
        stc((u4){pk(r[0][0], r[0][1]), pk(r[0][2], r[0][3]), pk(r[1][0], r[1][1]), pk(r[1][2], r[1][3])}, rs, (4 * n + wave) * 1024 + lane * 16, BF + s * 40960);
        CSTAMP(4);
        DRAIN(); BAR();
        if (tid == 0) stc1(0, rs, F_F + (s * 16 + n) * 4, 0);
        STAMP(0);
    }
}

DI void role_g(const Params& p, rsrc_t rs, int gi, int tid, int wave, int lane) {
    u4 w[10];
#pragma unroll
    for (int i = 0; i < 10; ++i) w[i] = *(const u4*)(p.gw + ((lane & 7) + 8 * i) * 8);
    for (int s = gi; s < 64; s += 2) {
        const int tok = s * 32 + 8 * wave + (lane >> 3);
        u4 x[10];
#pragma unroll
        for (int i = 0; i < 10; ++i) x[i] = *(const u4*)(p.x + (size_t)tok * 640 + ((lane & 7) + 8 * i) * 8);
        float a = 0;
#pragma unroll
        for (int i = 0; i < 10; ++i)
#pragma unroll
            for (int j = 0; j < 4; ++j) { u32 t = pk(lo2f(x[i][j]) * lo2f(w[i][j]), hi2f(x[i][j]) * hi2f(w[i][j])); a += lo2f(t) + hi2f(t); }
        a += __shfl_xor(a, 1); a += __shfl_xor(a, 2); a += __shfl_xor(a, 4);
        a = rbf(a);
        float gt = rbf(__builtin_amdgcn_rcpf(1.f + __builtin_amdgcn_exp2f(-1.4426950408889634f * a)));
        if ((lane & 7) == 0) stc1(nos(__float_as_uint(gt)), rs, F_G + tok * 4, 0);
    }
}

__global__ void __launch_bounds__(256, 1) fused(Params p) {
    const int tid = threadIdx.x, lane = tid & 63, wave = __builtin_amdgcn_readfirstlane(tid >> 6);
    const int xcd = blockIdx.x & 7, k = blockIdx.x >> 3;
    const rsrc_t rs = __builtin_amdgcn_make_buffer_rsrc(p.ws, 0, 0x0F000000, 0x00020000);
    int role = -1, id = 0;
    if (xcd >= 4) {
        int j = xcd - 4 + 4 * k;
        if (j < 32) { role = 3; id = j; } else if (j < 64) { role = 4; id = j - 32; } else if (j < 91) { role = 5; id = j - 64; }
    } else {
        if (xcd == 0) { role = 0; id = k; } else if (xcd == 1) { role = 1; id = k; }
        else if (k < 20) { role = 2; id = (xcd - 2) * 20 + k; } else if (k < 25) { role = 6; id = (xcd - 2) * 5 + k - 20; } else if (k == 25) { role = 7; id = xcd - 2; }
    }
    if (role == 0) role_r1(p, rs, id, tid, wave, lane);
    else if (role == 1) role_ru(p, rs, id, tid, wave, lane);
    else if (role == 2) role_r2(p, rs, id, tid, wave, lane);
    else if (role == 3) role_wqk(p, rs, id, tid, wave, lane);
    else if (role == 4) role_wvb(p, rs, id, tid, wave, lane);
    else if (role == 5) role_f1(p, rs, id, tid, wave, lane);
    else if (role == 6) role_f2(p, rs, id, tid, wave, lane);
    else if (role == 7) role_g(p, rs, id, tid, wave, lane);
}

extern "C" void kernel_launch(void* const* d_in, const int* in_sizes, int n_in, void* d_out, int out_size, void* d_ws, size_t ws_size, hipStream_t stream) {
    Params p;
    p.x = (const u16*)d_in[0]; p.mem0 = (const u16*)d_in[1]; p.wq = (const u16*)d_in[2]; p.wk = (const u16*)d_in[3]; p.wv = (const u16*)d_in[4]; p.wo = (const u16*)d_in[5];
    p.w1 = (const u16*)d_in[8]; p.w3 = (const u16*)d_in[9]; p.w2 = (const u16*)d_in[10]; p.gw = (const u16*)d_in[11];
    p.out = (u16*)d_out;
    p.ws = (char*)(((size_t)d_ws + (2u << 20) - 1) & ~(size_t)((2u << 20) - 1));
    (void)hipFuncSetAttribute((const void*)fused, hipFuncAttributeMaxDynamicSharedMemorySize, LDS_BYTES);
    (void)hipMemsetAsync(p.ws + FLG, 0xFF, FLG_BYTES, stream);
    hipLaunchKernelGGL(fused, dim3(256), dim3(256), LDS_BYTES, stream, p);
}
```

```cpp
#include <hip/hip_runtime.h>

typedef unsigned short u16;
typedef unsigned u32;
typedef __attribute__((ext_vector_type(4))) u32 u4;
typedef __attribute__((ext_vector_type(2))) u32 u2;
typedef __attribute__((ext_vector_type(8))) u32 u8v;
typedef __attribute__((ext_vector_type(2))) __bf16 bf2;
typedef __attribute__((ext_vector_type(8))) __bf16 hv8;
typedef __attribute__((ext_vector_type(16))) __bf16 bf16v;
typedef __attribute__((ext_vector_type(2))) float f2;
typedef __attribute__((ext_vector_type(4))) float f4;
typedef __attribute__((ext_vector_type(16))) float f16v;
typedef __attribute__((ext_vector_type(4))) short s4;
typedef __attribute__((address_space(3))) s4* lds_s4;
typedef __attribute__((address_space(3))) u4* lds_u4;
typedef __attribute__((address_space(3))) u2* lds_u2;
typedef __attribute__((address_space(3))) u32* lds_u1;
typedef __attribute__((address_space(3))) f4* lds_f4;
typedef __attribute__((address_space(3))) f2* lds_f2;
typedef __attribute__((address_space(3))) float* lds_f1;
typedef __amdgpu_buffer_rsrc_t rsrc_t;
#define DI __device__ __forceinline__

constexpr u32 WIN0 = 0, WIN1 = 2u << 20;
constexpr u32 UM_STR = 327680u * 2, P_STR = 24576u * 2, M_STR = 16384u * 2;
constexpr u32 P_OFF = WIN1, M_OFF = WIN1 + 4 * P_STR, CAL0 = WIN0 + 3 * UM_STR, CAL1 = M_OFF + 4 * M_STR;
constexpr u32 HN = WIN1 + 512 * 1024, WSH = 2, PT = WIN1 + 1536 * 1024;
constexpr u32 FLG = 4u << 20;
constexpr u32 F_QK = FLG, F_B = FLG + 32768, F_H = FLG + 65536, F_F = FLG + 98304, F_G = FLG + 102400;
constexpr u32 F_I1 = FLG + 110592, F_IU = F_I1 + 128, F_I2 = F_I1 + 256, F_PAR = F_I1 + 512, F_D = FLG + 111616, FLG_BYTES = 0 ? 131072 : 115712;
constexpr u32 HS_M = FLG + 131072, HS_R = FLG + 139264;
constexpr u32 QF = 5u << 20, KF = 21u << 20, PX = 37u << 20, HF = 43u << 20, BF = 50u << 20, BX = 53u << 20;
constexpr u32 TS = 180u << 20;
constexpr u32 SENT = 0xFFFFFFFFu;
constexpr int SPIN = 200000;
constexpr int LDS_BYTES = 163840;

struct Params {
    const u16 *x, *mem0, *wq, *wk, *wv, *wo, *w1, *w3, *w2, *gw;
    u16* out;
    char* ws;
    u32 tune[10];
};

DI u32 pk(float a, float b) { f2 v = {a, b}; return __builtin_bit_cast(u32, __builtin_convertvector(v, bf2)); }
DI float lo2f(u32 x) { return __uint_as_float(x << 16); }
DI float hi2f(u32 x) { return __uint_as_float(x & 0xFFFF0000u); }
DI float rbf(float a) { return hi2f(pk(a, a)); }
DI u32 nos(u32 x) { return x < 0xFFFFFFFEu ? x : 0xFFFFFFFEu; }
DI u32 ppos(u32 h, u32 ch) { return 1 ? (ch < 8 ? 16384 + ((h >> 3) * 4 + (h & 3)) * 256 + (4 * (ch >> 1) + 2 * ((h >> 2) & 1) + (ch & 1)) * 16 : h * 512 + (ch - 8) * 16) : h * 640 + ch * 16; }
DI u32 amap(u32 off) { return ((off >> 13) << 14) | (off & 8191u); }
DI u4 ldc(rsrc_t r, u32 v, u32 s) { return __builtin_amdgcn_raw_buffer_load_b128(r, v, s, 16); }
DI u2 ldc2(rsrc_t r, u32 v, u32 s) { return __builtin_amdgcn_raw_buffer_load_b64(r, v, s, 16); }
DI u32 ldc1(rsrc_t r, u32 v, u32 s) { return __builtin_amdgcn_raw_buffer_load_b32(r, v, s, 16); }
DI void stc(u4 d, rsrc_t r, u32 v, u32 s) { __builtin_amdgcn_raw_buffer_store_b128(d, r, v, s, 16); }
DI void stc1(u32 d, rsrc_t r, u32 v, u32 s) { __builtin_amdgcn_raw_buffer_store_b32(d, r, v, s, 16); }
DI u32 mx4(u4 v) { return max(max(v.x, v.y), max(v.z, v.w)); }
DI hv8 asb(u4 v) { return __builtin_bit_cast(hv8, v); }
DI u2 trr(u32 a) { return __builtin_bit_cast(u2, __builtin_amdgcn_ds_read_tr16_b64_v4i16((lds_s4)a)); }
DI u4 cat(u2 a, u2 b) { return __builtin_shufflevector(a, b, 0, 1, 2, 3); }
DI bf16v cat8(u4 a, u4 b) { return __builtin_bit_cast(bf16v, __builtin_shufflevector(a, b, 0, 1, 2, 3, 4, 5, 6, 7)); }
DI f4 smf(u4 a, bf16v b, f4 c, int idx) { return __builtin_amdgcn_smfmac_f32_16x16x64_bf16(asb(a), b, c, idx, 0, 0); }
DI f4 m16(u4 a, u4 b, f4 c) { return __builtin_amdgcn_mfma_f32_16x16x32_bf16(asb(a), asb(b), c, 0, 0, 0); }
DI f16v m32(u4 a, u4 b, f16v c) { return __builtin_amdgcn_mfma_f32_32x32x16_bf16(asb(a), asb(b), c, 0, 0, 0); }
DI float sw32add(float a, float b) { auto r = __builtin_amdgcn_permlane32_swap(__float_as_uint(a), __float_as_uint(b), false, false); return __uint_as_float(r[0]) + __uint_as_float(r[1]); }
DI float xmax(float m) {
    auto r = __builtin_amdgcn_permlane16_swap(__float_as_uint(m), __float_as_uint(m), false, false);
    m = fmaxf(__uint_as_float(r[0]), __uint_as_float(r[1]));
    r = __builtin_amdgcn_permlane32_swap(__float_as_uint(m), __float_as_uint(m), false, false);
    return fmaxf(__uint_as_float(r[0]), __uint_as_float(r[1]));
}
DI float xsum(float m) {
    auto r = __builtin_amdgcn_permlane16_swap(__float_as_uint(m), __float_as_uint(m), false, false);
    m = __uint_as_float(r[0]) + __uint_as_float(r[1]);
    r = __builtin_amdgcn_permlane32_swap(__float_as_uint(m), __float_as_uint(m), false, false);
    return __uint_as_float(r[0]) + __uint_as_float(r[1]);
}
DI int lane_id() { int l; asm volatile("v_mbcnt_lo_u32_b32 %0, -1, 0\n\tv_mbcnt_hi_u32_b32 %0, -1, %0" : "=v"(l)); __builtin_assume(l >= 0 && l < 64); return l; }
#define BAR() asm volatile("s_waitcnt lgkmcnt(0)\n\ts_barrier" ::: "memory")
#define DRAIN() asm volatile("s_waitcnt vmcnt(0)" ::: "memory")
#define FENCE() asm volatile("" ::: "memory")
#define SB() __builtin_amdgcn_sched_barrier(0)
#define PIN_A(x) asm volatile("" : "+a"(x))

#define STAMP(slot) do {} while (0)
#define CSTAMP(slot) do {} while (0)

template <int NAP = 1>
DI void wait_flags(rsrc_t rs, u32 off, int n, int lane, bool& dead) {
    u32 v = off + (lane < n ? lane : 0) * 4;
    for (int i = 0; !dead; ++i) {
        FENCE();
        u32 f = ldc1(rs, v, 0);
        if (__all(f != SENT)) break;
        if (i > SPIN) dead = true;
        __builtin_amdgcn_s_sleep(NAP);
    }
    FENCE();
}
DI void wait_flags4(rsrc_t rs, u32 off, int lane, bool& dead) {
    u32 v = off + (lane & 31) * 16;
    for (int i = 0; !dead; ++i) {
        FENCE();
        u32 f = mx4(ldc(rs, v, 0));
        if (__all(f != SENT)) break;
        if (i > SPIN) dead = true;
        __builtin_amdgcn_s_sleep(1);
    }
    FENCE();
}
DI u32 hello_ask(rsrc_t rs, int wave, int lane) {
    const u32 o = (blockIdx.x * 4 + wave) * 4;
    const u32 n = ldc1(rs, o, HS_R) + 1;
    if (lane == 0) stc1(n, rs, o, HS_M);
    return n;
}
DI void hello_wait(rsrc_t rs, int wave, int lane, u32 n, bool& dead) {
    const u32 o = (blockIdx.x * 4 + wave) * 4;
    for (int i = 0; !dead; ++i) {
        FENCE();
        if (__all(ldc1(rs, o, HS_R) == n)) break;
        if (i > SPIN) dead = true;
        __builtin_amdgcn_s_sleep(2);
    }
    FENCE();
    if (lane == 0) stc1(0, rs, o, F_D);
}
template <int N>
DI void gather(u4 (&v)[N], rsrc_t rs, const u32 (&off)[N], u32 so, bool& dead) {
    for (int i = 0;; ++i) {
        FENCE();
#pragma unroll
        for (int j = 0; j < N; ++j) v[j] = ldc(rs, off[j], so);
        u32 m = 0;
#pragma unroll
        for (int j = 0; j < N; ++j) m = max(m, mx4(v[j]));
        if (__all(m != SENT) || dead) break;
        if (i > SPIN) dead = true;
    }
    FENCE();
}

template <int NCH, class GA, class LP, class RD>
DI void stage(GA ga, LP lp, RD rd) {
    u4 g[3][8];
#pragma unroll
    for (int c = 0; c < 3; ++c)
        if (c < NCH) {
#pragma unroll
            for (int i = 0; i < 8; ++i) g[c][i] = *ga(c, i);
        }
#pragma unroll
    for (int c = 0; c < NCH; ++c) {
        const u32 base = (c & 1) * 32768;
#pragma unroll
        for (int i = 0; i < 8; ++i) *(lds_u4)(base + lp(i)) = g[c % 3][i];
        if (c + 3 < NCH) {
#pragma unroll
            for (int i = 0; i < 8; ++i) g[c % 3][i] = *ga(c + 3, i);
        }
        BAR();
        rd(c, base);
    }
    BAR();
}
DI u32 fsw(u32 r) { return 2 * ((r & 1) | ((r >> 1) & 2) | ((r >> 1) & 4)); }

typedef __attribute__((ext_vector_type(16))) u32 u32x16;
typedef __attribute__((ext_vector_type(32))) u32 u32x32;
#define LO(q) (u32)(size_t)(q)
#define HI(q) (u32)((size_t)(q) >> 32)
#define ROLE_OPS(A, B) : "+{v[0:31]}"(ad) : [r0] "s"(LO(p.ws)), [r1] "s"(HI(p.ws)), [wave] "s"(wave), [h] "s"(h), [ts] "s"((u32)(TS + (blockIdx.x * 4 + wave) * 2560)), [hs] "s"((blockIdx.x * 4 + wave) * 4), \
    [t0] "s"(p.tune[0]), [t1] "s"(p.tune[1]), [t2] "s"(p.tune[2]), [t3] "s"(p.tune[3]), [t4] "s"(p.tune[4]), [t6] "s"(p.tune[6]), [t7] "s"(p.tune[7]), [t8] "s"(p.tune[8]), [t9] "s"(p.tune[9]), [x0] "s"(LO(p.x)), [x1] "s"(HI(p.x)), [a0] "s"(LO(A)), [a1] "s"(HI(A)), [b0] "s"(LO(B)), [b1] "s"(HI(B))
#define HEAD_OPS(A, B) : : [v24] "v"(((u32)tid >> 4) * 8192 + (((tid & 15) ^ fsw((u32)tid >> 4)) << 4)), [v25] "v"(((u32)tid >> 4) * 1280 + (((tid & 15) ^ fsw((u32)tid >> 4)) << 4)), [wave] "s"(wave), [h] "s"(h), \
    [a0] "s"(LO(A)), [a1] "s"(HI(A)), [b0] "s"(LO(B)), [b1] "s"(HI(B)) : "s58", "s65", "s71", "s78", "s79", "s80", "s81", "s82", "s83", "s84", "s85", "s86", "s87", "m0", "scc", "memory"
DI void sparse_stage_addr(u32x32& ad, int tid, int wave, int lane) {
    const u32 r = tid >> 4, ch = ((tid & 15) ^ fsw(r)) << 4;
    ad[24] = r * 8192 + ch; ad[25] = r * 1280 + ch;
    const u32 gb = lane >> 4, rho = (lane >> 2) & 3, c4 = lane & 3;
    const u32 rl = 16 * (gb >> 1) + 4 * (gb & 1) + (rho & 1) + 8 * (rho >> 1);
#pragma unroll
    for (int u = 0; u < 2; ++u) ad[26 + u] = rl * 256 + (((4 * wave + 2 * u + (c4 >> 1)) ^ fsw(rl)) << 4) + (c4 & 1) * 8;
}

DI u32 get_par(rsrc_t rs, int lane, bool& dead, int which) {
    wait_flags(rs, F_PAR, 2, lane, dead);
    u32 z = ldc1(rs, F_PAR + which * 4, 0);
    return (__builtin_amdgcn_readfirstlane(z) & 1) << 13;
}

DI void warm_slow(const Params& p, u32 wg, int wave) {
    const u32 tid = wave * 64 + lane_id();
    u4 v[12];
#pragma unroll
    for (int it = 0; it < 12; ++it) {
        u32 i = min(it * 58 + wg, 647u), pr = i / 216, r = i % 216, j = r % 9, q = r / 9;
        const char* base = (const char*)(q < 16 ? (q & 1 ? p.wk : p.wq) : p.wv);
        const u32 row = q < 16 ? 64 * (2 + pr + 5 * ((q >> 1) & 1)) + 16 * (q >> 2) : 128 * (2 + pr) + 16 * (q - 16);
        const u32 b8 = (u32)((size_t)base >> 8), h = j < 8 ? ((1 - b8) & 3) + 4 * j : (31 - b8) & 31;
        v[it] = *(const u4*)(base + (size_t)(row + (tid >> 4)) * 8192 + h * 256 + (tid & 15) * 16);
    }
#pragma unroll
    for (int it = 0; it < 12; ++it) asm volatile("" :: "v"(v[it]));
}

DI u32 skew(u32 j) { return 0 ? (j & ~3u) | ((j + (j >> 2)) & 3u) : j; }
template <int NL, int L0, int L1, int L2, int L3>
DI void warm_some(const Params& p, u32 o, u32 h) {
    const u32 tid = threadIdx.x, r = tid >> 4, cb = (tid & 15) * 16;
    constexpr int L[4] = {L0, L1, L2, L3};
    u4 v[NL][8];
#pragma unroll
    for (int k = 0; k < NL; ++k)
#pragma unroll
        for (int i = 0; i < 8; ++i) {
            const int c = L[k];
            const char* a;
            if (o < 32) a = (const char*)(i < 4 ? p.wq : p.wk) + (size_t)(64 * c + 16 * (i & 3) + r) * 8192 + h * 256 + cb;
            else if (c < 5) a = (const char*)p.wv + (size_t)(128 * c + 16 * i + r) * 8192 + h * 256 + cb;
            else a = (const char*)p.wo + (size_t)(h * 128 + 16 * i + r) * 1280 + (c - 5) * 256 + cb;
            v[k][i] = 0 ? __builtin_nontemporal_load((const u4*)a) : *(const u4*)a;
        }
#pragma unroll
    for (int k = 0; k < NL; ++k)
#pragma unroll
        for (int i = 0; i < 8; ++i) asm volatile("" :: "v"(v[k][i]));
}
DI void warm_slab(const Params& p, u32 o) {
    const u32 h = skew(o) & 31;
    warm_some<3, 2, 3, 4, 0>(p, o, h);
}

DI void warm_x(const Params& p, int id, int wave) {
    if (id < 2 * 6) {
        const u4* src = (const u4*)p.x + 2560 + id * 1280 + wave * 64 + lane_id();
        u4 v[5];
#pragma unroll
        for (int i = 0; i < 5; ++i) v[i] = src[i * 256];
#pragma unroll
        for (int i = 0; i < 5; ++i) asm volatile("" :: "v"(v[i]));
    }
}

DI void warm_helper(const Params& p, int wave) { const int id = 40 + (0 ? (blockIdx.x & 7) * 6 + (blockIdx.x >> 3) - 26 : ((blockIdx.x & 7) - 2) * 12 + (blockIdx.x >> 3) - 20); warm_slow(p, id, wave); warm_x(p, id - 40, wave); }

DI void role_r1(const Params& p, rsrc_t rs, int h, int wave) {
    const int lane = lane_id(), tid = wave * 64 + lane;
    asm volatile(
        "s_mov_b32 s58, %[h]\n"
        "s_lshl_b32 s71, %[wave], 10\n"
        "s_mov_b32 s80, %[a0]\n"
        "s_mov_b32 s81, %[a1]\n"
        "s_mov_b32 s82, 5242880\n"
        "s_mov_b32 s83, 0x00020000\n"
        "s_mov_b32 s84, %[b0]\n"
        "s_mov_b32 s85, %[b1]\n"
        "s_mov_b32 s86, s82\n"
        "s_mov_b32 s87, 0x00020000\n"
        "s_lshl_b32 s78, s58, 8\n"
        "s_mul_i32 s79, s58, 163840\n"
        "s_add_u32 m0, s71, 40960\n"
        "s_add_u32 s65, s78, 2621440\n"
        "buffer_load_dwordx4 %[v24], s[80:83], s65 offen lds\n"
        "s_add_u32 m0, s71, 45056\n"
        "s_add_u32 s65, s78, 2752512\n"
        "buffer_load_dwordx4 %[v24], s[80:83], s65 offen lds\n"
        "s_add_u32 m0, s71, 49152\n"
        "s_add_u32 s65, s78, 2883584\n"
        "buffer_load_dwordx4 %[v24], s[80:83], s65 offen lds\n"
        "s_add_u32 m0, s71, 53248\n"
        "s_add_u32 s65, s78, 3014656\n"
        "buffer_load_dwordx4 %[v24], s[80:83], s65 offen lds\n"
        "s_add_u32 m0, s71, 57344\n"
        "s_add_u32 s65, s78, 2621440\n"
        "buffer_load_dwordx4 %[v24], s[84:87], s65 offen lds\n"
        "s_add_u32 m0, s71, 61440\n"
        "s_add_u32 s65, s78, 2752512\n"
        "buffer_load_dwordx4 %[v24], s[84:87], s65 offen lds\n"
        "s_add_u32 m0, s71, 65536\n"
        "s_add_u32 s65, s78, 2883584\n"
        "buffer_load_dwordx4 %[v24], s[84:87], s65 offen lds\n"
        "s_add_u32 m0, s71, 69632\n"
        "s_add_u32 s65, s78, 3014656\n"
        "buffer_load_dwordx4 %[v24], s[84:87], s65 offen lds\n"
        "s_add_u32 m0, s71, 73728\n"
        "s_add_u32 s65, s78, 3145728\n"
        "buffer_load_dwordx4 %[v24], s[80:83], s65 offen lds\n"
        "s_add_u32 m0, s71, 77824\n"
        "s_add_u32 s65, s78, 3276800\n"
        "buffer_load_dwordx4 %[v24], s[80:83], s65 offen lds\n"
        "s_add_u32 m0, s71, 81920\n"
        "s_add_u32 s65, s78, 3407872\n"
        "buffer_load_dwordx4 %[v24], s[80:83], s65 offen lds\n"
        "s_add_u32 m0, s71, 86016\n"
        "s_add_u32 s65, s78, 3538944\n"
        "buffer_load_dwordx4 %[v24], s[80:83], s65 offen lds\n"
        "s_add_u32 m0, s71, 90112\n"
        "s_add_u32 s65, s78, 3145728\n"
        "buffer_load_dwordx4 %[v24], s[84:87], s65 offen lds\n"
        "s_add_u32 m0, s71, 94208\n"
        "s_add_u32 s65, s78, 3276800\n"
        "buffer_load_dwordx4 %[v24], s[84:87], s65 offen lds\n"
        "s_add_u32 m0, s71, 98304\n"
        "s_add_u32 s65, s78, 3407872\n"
        "buffer_load_dwordx4 %[v24], s[84:87], s65 offen lds\n"
        "s_add_u32 m0, s71, 102400\n"
        "s_add_u32 s65, s78, 3538944\n"
        "buffer_load_dwordx4 %[v24], s[84:87], s65 offen lds\n"
        HEAD_OPS(p.wq, p.wk));
    SB();
    { int q = tid / 40, ch = tid % 40; stc((u4)(SENT), rs, tid < 160 ? amap(ppos(h, ch)) + (q & 1) * 8192 + (q >> 1) * P_STR : 0xF0000000u, P_OFF); }
    stc((u4)(SENT), rs, tid < 160 ? h * 2560 + tid * 16 : 0xF0000000u, PX + 63 * 81920);
    const u32 g = lane >> 4, col = lane & 15, gam = col >> 2, jj = col & 3;
    const u32 IMG = 10240;
    const int tt = wave - 1;
    const u32 tok1 = wave == 0 ? 16 * (gam >> 1) + 2 * (gam & 1) + (jj & 1) + 8 * (jj >> 1) : 16 * (gam >> 1) + 4 * (gam & 1) + jj;
    u32x32 ad = {};
    sparse_stage_addr(ad, tid, wave, lane);
    ad[0] = lane * 16;
    ad[1] = (wave * 160 + lane) * 16;
    ad[2] = 1 ? (wave * 160 + 128) * 16 + lane * 8 : (wave * 160 + 128 + (lane & 31)) * 16;
    if (3) { ad[1] = (wave < 3 ? 3 * wave : 8) * 1024 + lane * 16; ad[2] = ad[1] + 2048; }
    ad[3] = amap(ad[1]); ad[4] = amap(ad[1] + 1024); ad[5] = amap(ad[2]);
    ad[6] = IMG + wave * 512 + (1 ? ((g >> 1) * 16 + col) * 8 + (g & 1) * 4 : lane * 8);
    ad[7] = IMG + ((g >> 1) * 32 + (lane & 1) * 16 + 4 * (g & 1) + ((lane >> 2) & 3)) * 8;
    ad[8] = wave == 0 ? amap(ppos(h, col < 8 ? (1 ? 8 + (g >> 1) * 16 + 4 * (col >> 1) + 2 * (g & 1) + (col & 1) : (1 + g) * 8 + col) : col - 8)) : g * 512 + (16 * tt + col) * 16;
    ad[9] = wave == 0 ? (2 ? amap(ppos(h, col & 7) + (g & 1) * 8 + (2 == 2 ? (g >> 1) * 4 : 0)) : ad[8]) : 2048 + (16 * tt + col) * 16;
    ad[10] = (lane & (1 ? 2 : 8)) ? 0xEEEE : 0x4444;
    ad[11] = 32768 + g * 512 + tok1 * 16;
    ad[12] = ad[11] + (wave == 0 ? 64 : 128);
    ad[13] = 32768 + 8192 + g * 512 + (16 * (tt & 1) + col) * 16;
    ad[14] = amap(ppos(h, lane < 40 ? lane : 0));
    ad[15] = 16 * tt + col - (16 * (g >> 1) + 4 * (g & 1));
    asm volatile(
        "s_mov_b32 s60, %[r0]\n"
        "s_mov_b32 s61, %[r1]\n"
        "s_mov_b32 s62, 0x0f000000\n"
        "s_mov_b32 s63, 0x00020000\n"
        "s_mov_b32 s67, %[wave]\n"
        "s_mov_b32 s58, %[h]\n"
        "s_mov_b32 s59, %[ts]\n"
        "s_mov_b32 s66, 0x100000\n"
        "s_mov_b32 s64, 0\n"
        "s_lshl_b32 s71, s67, 10\n"
        "s_mov_b32 s80, %[a0]\n"
        "s_mov_b32 s81, %[a1]\n"
        "s_mov_b32 s82, 5242880\n"
        "s_mov_b32 s83, 0x00020000\n"
        "s_mov_b32 s84, %[b0]\n"
        "s_mov_b32 s85, %[b1]\n"
        "s_mov_b32 s86, s82\n"
        "s_mov_b32 s87, 0x00020000\n"
        "s_lshl_b32 s78, s58, 8\n"
        "s_mul_i32 s79, s58, 163840\n"
        "s_add_u32 s88, %[hs], 4333568\n"
        "s_add_u32 s89, %[hs], 4325376\n"
        "s_add_u32 s90, %[hs], 4305920\n"
        "buffer_load_dword v188, off, s[60:63], s88 sc1\n"
        "s_add_u32 m0, s71, 106496\n"
        "s_add_u32 s65, s78, 3670016\n"
        "buffer_load_dwordx4 v24, s[80:83], s65 offen lds\n"
        "s_add_u32 m0, s71, 110592\n"
        "s_add_u32 s65, s78, 3801088\n"
        "buffer_load_dwordx4 v24, s[80:83], s65 offen lds\n"
        "s_add_u32 m0, s71, 114688\n"
        "s_add_u32 s65, s78, 3932160\n"
        "buffer_load_dwordx4 v24, s[80:83], s65 offen lds\n"
        "s_add_u32 m0, s71, 118784\n"
        "s_add_u32 s65, s78, 4063232\n"
        "buffer_load_dwordx4 v24, s[80:83], s65 offen lds\n"
        "s_add_u32 m0, s71, 122880\n"
        "s_add_u32 s65, s78, 3670016\n"
        "buffer_load_dwordx4 v24, s[84:87], s65 offen lds\n"
        "s_add_u32 m0, s71, 126976\n"
        "s_add_u32 s65, s78, 3801088\n"
        "buffer_load_dwordx4 v24, s[84:87], s65 offen lds\n"
        "s_add_u32 m0, s71, 131072\n"
        "s_add_u32 s65, s78, 3932160\n"
        "buffer_load_dwordx4 v24, s[84:87], s65 offen lds\n"
        "s_add_u32 m0, s71, 135168\n"
        "s_add_u32 s65, s78, 4063232\n"
        "buffer_load_dwordx4 v24, s[84:87], s65 offen lds\n"
        "v_add_u32 v28, 40960, v26\n"
        "v_add_u32 v29, 40960, v27\n"
        "s_waitcnt vmcnt(19)\n"
        "s_barrier\n"
        "ds_read_b64_tr_b16 a[160:161], v28 offset:0\n"
        "ds_read_b64_tr_b16 a[162:163], v28 offset:512\n"
        "ds_read_b64_tr_b16 a[164:165], v28 offset:8192\n"
        "ds_read_b64_tr_b16 a[166:167], v28 offset:8704\n"
        "ds_read_b64_tr_b16 a[168:169], v29 offset:0\n"
        "ds_read_b64_tr_b16 a[170:171], v29 offset:512\n"
        "ds_read_b64_tr_b16 a[172:173], v29 offset:8192\n"
        "ds_read_b64_tr_b16 a[174:175], v29 offset:8704\n"
        "ds_read_b64_tr_b16 a[176:177], v28 offset:16384\n"
        "ds_read_b64_tr_b16 a[178:179], v28 offset:16896\n"
        "ds_read_b64_tr_b16 a[180:181], v28 offset:24576\n"
        "ds_read_b64_tr_b16 a[182:183], v28 offset:25088\n"
        "ds_read_b64_tr_b16 a[184:185], v29 offset:16384\n"
        "ds_read_b64_tr_b16 a[186:187], v29 offset:16896\n"
        "ds_read_b64_tr_b16 a[188:189], v29 offset:24576\n"
        "ds_read_b64_tr_b16 a[190:191], v29 offset:25088\n"
        "s_waitcnt lgkmcnt(0)\n"
        "s_barrier\n"
        "s_add_u32 m0, s71, 40960\n"
        "s_add_u32 s65, s78, 4194304\n"
        "buffer_load_dwordx4 v24, s[80:83], s65 offen lds\n"
        "s_add_u32 m0, s71, 45056\n"
        "s_add_u32 s65, s78, 4325376\n"
        "buffer_load_dwordx4 v24, s[80:83], s65 offen lds\n"
        "s_add_u32 m0, s71, 49152\n"
        "s_add_u32 s65, s78, 4456448\n"
        "buffer_load_dwordx4 v24, s[80:83], s65 offen lds\n"
        "s_add_u32 m0, s71, 53248\n"
        "s_add_u32 s65, s78, 4587520\n"
        "buffer_load_dwordx4 v24, s[80:83], s65 offen lds\n"
        "s_add_u32 m0, s71, 57344\n"
        "s_add_u32 s65, s78, 4194304\n"
        "buffer_load_dwordx4 v24, s[84:87], s65 offen lds\n"
        "s_add_u32 m0, s71, 61440\n"
        "s_add_u32 s65, s78, 4325376\n"
        "buffer_load_dwordx4 v24, s[84:87], s65 offen lds\n"
        "s_add_u32 m0, s71, 65536\n"
        "s_add_u32 s65, s78, 4456448\n"
        "buffer_load_dwordx4 v24, s[84:87], s65 offen lds\n"
        "s_add_u32 m0, s71, 69632\n"
        "s_add_u32 s65, s78, 4587520\n"
        "buffer_load_dwordx4 v24, s[84:87], s65 offen lds\n"
        "v_add_u32 v28, 73728, v26\n"
        "v_add_u32 v29, 73728, v27\n"
        "s_waitcnt vmcnt(19)\n"
        "s_barrier\n"
        "ds_read_b64_tr_b16 a[192:193], v28 offset:0\n"
        "ds_read_b64_tr_b16 a[194:195], v28 offset:512\n"
        "ds_read_b64_tr_b16 a[196:197], v28 offset:8192\n"
        "ds_read_b64_tr_b16 a[198:199], v28 offset:8704\n"
        "ds_read_b64_tr_b16 a[200:201], v29 offset:0\n"
        "ds_read_b64_tr_b16 a[202:203], v29 offset:512\n"
        "ds_read_b64_tr_b16 a[204:205], v29 offset:8192\n"
        "ds_read_b64_tr_b16 a[206:207], v29 offset:8704\n"
        "ds_read_b64_tr_b16 a[208:209], v28 offset:16384\n"
        "ds_read_b64_tr_b16 a[210:211], v28 offset:16896\n"
        "ds_read_b64_tr_b16 a[212:213], v28 offset:24576\n"
        "ds_read_b64_tr_b16 a[214:215], v28 offset:25088\n"
        "ds_read_b64_tr_b16 a[216:217], v29 offset:16384\n"
        "ds_read_b64_tr_b16 a[218:219], v29 offset:16896\n"
        "ds_read_b64_tr_b16 a[220:221], v29 offset:24576\n"
        "ds_read_b64_tr_b16 a[222:223], v29 offset:25088\n"
        "s_waitcnt lgkmcnt(0)\n"
        "s_barrier\n"
        "s_add_u32 m0, s71, 73728\n"
        "s_add_u32 s65, s78, 4718592\n"
        "buffer_load_dwordx4 v24, s[80:83], s65 offen lds\n"
        "s_add_u32 m0, s71, 77824\n"
        "s_add_u32 s65, s78, 4849664\n"
        "buffer_load_dwordx4 v24, s[80:83], s65 offen lds\n"
        "s_add_u32 m0, s71, 81920\n"
        "s_add_u32 s65, s78, 4980736\n"
        "buffer_load_dwordx4 v24, s[80:83], s65 offen lds\n"
        "s_add_u32 m0, s71, 86016\n"
        "s_add_u32 s65, s78, 5111808\n"
        "buffer_load_dwordx4 v24, s[80:83], s65 offen lds\n"
        "s_add_u32 m0, s71, 90112\n"
        "s_add_u32 s65, s78, 4718592\n"
        "buffer_load_dwordx4 v24, s[84:87], s65 offen lds\n"
        "s_add_u32 m0, s71, 94208\n"
        "s_add_u32 s65, s78, 4849664\n"
        "buffer_load_dwordx4 v24, s[84:87], s65 offen lds\n"
        "s_add_u32 m0, s71, 98304\n"
        "s_add_u32 s65, s78, 4980736\n"
        "buffer_load_dwordx4 v24, s[84:87], s65 offen lds\n"
        "s_add_u32 m0, s71, 102400\n"
        "s_add_u32 s65, s78, 5111808\n"
        "buffer_load_dwordx4 v24, s[84:87], s65 offen lds\n"
        "v_add_u32 v28, 106496, v26\n"
        "v_add_u32 v29, 106496, v27\n"
        "s_waitcnt vmcnt(16)\n"
        "s_barrier\n"
        "ds_read_b64_tr_b16 a[224:225], v28 offset:0\n"
        "ds_read_b64_tr_b16 a[226:227], v28 offset:512\n"
        "ds_read_b64_tr_b16 a[228:229], v28 offset:8192\n"
        "ds_read_b64_tr_b16 a[230:231], v28 offset:8704\n"
        "ds_read_b64_tr_b16 a[232:233], v29 offset:0\n"
        "ds_read_b64_tr_b16 a[234:235], v29 offset:512\n"
        "ds_read_b64_tr_b16 a[236:237], v29 offset:8192\n"
        "ds_read_b64_tr_b16 a[238:239], v29 offset:8704\n"
        "ds_read_b64_tr_b16 a[240:241], v28 offset:16384\n"
        "ds_read_b64_tr_b16 a[242:243], v28 offset:16896\n"
        "ds_read_b64_tr_b16 a[244:245], v28 offset:24576\n"
        "ds_read_b64_tr_b16 a[246:247], v28 offset:25088\n"
        "ds_read_b64_tr_b16 a[248:249], v29 offset:16384\n"
        "ds_read_b64_tr_b16 a[250:251], v29 offset:16896\n"
        "ds_read_b64_tr_b16 a[252:253], v29 offset:24576\n"
        "ds_read_b64_tr_b16 a[254:255], v29 offset:25088\n"
        "s_waitcnt lgkmcnt(0)\n"
        "s_barrier\n"
        "s_add_u32 m0, s71, 106496\n"
        "s_add_u32 s65, s78, 0\n"
        "buffer_load_dwordx4 v24, s[80:83], s65 offen lds\n"
        "s_add_u32 m0, s71, 110592\n"
        "s_add_u32 s65, s78, 131072\n"
        "buffer_load_dwordx4 v24, s[80:83], s65 offen lds\n"
        "s_add_u32 m0, s71, 114688\n"
        "s_add_u32 s65, s78, 262144\n"
        "buffer_load_dwordx4 v24, s[80:83], s65 offen lds\n"
        "s_add_u32 m0, s71, 118784\n"
        "s_add_u32 s65, s78, 393216\n"
        "buffer_load_dwordx4 v24, s[80:83], s65 offen lds\n"
        "s_add_u32 m0, s71, 122880\n"
        "s_add_u32 s65, s78, 0\n"
        "buffer_load_dwordx4 v24, s[84:87], s65 offen lds\n"
        "s_add_u32 m0, s71, 126976\n"
        "s_add_u32 s65, s78, 131072\n"
        "buffer_load_dwordx4 v24, s[84:87], s65 offen lds\n"
        "s_add_u32 m0, s71, 131072\n"
        "s_add_u32 s65, s78, 262144\n"
        "buffer_load_dwordx4 v24, s[84:87], s65 offen lds\n"
        "s_add_u32 m0, s71, 135168\n"
        "s_add_u32 s65, s78, 393216\n"
        "buffer_load_dwordx4 v24, s[84:87], s65 offen lds\n"
        "v_add_u32 v188, 1, v188\n"
        "s_mov_b64 exec, 1\n"
        "buffer_store_dword v188, off, s[60:63], s89 sc1\n"
        "s_mov_b64 exec, -1\n"
        "v_add_u32 v28, 40960, v26\n"
        "v_add_u32 v29, 40960, v27\n"
        "s_waitcnt vmcnt(17)\n"
        "s_barrier\n"
        "ds_read_b64_tr_b16 v[192:193], v28 offset:0\n"
        "ds_read_b64_tr_b16 v[194:195], v28 offset:512\n"
        "ds_read_b64_tr_b16 v[196:197], v28 offset:8192\n"
        "ds_read_b64_tr_b16 v[198:199], v28 offset:8704\n"
        "ds_read_b64_tr_b16 v[200:201], v29 offset:0\n"
        "ds_read_b64_tr_b16 v[202:203], v29 offset:512\n"
        "ds_read_b64_tr_b16 v[204:205], v29 offset:8192\n"
        "ds_read_b64_tr_b16 v[206:207], v29 offset:8704\n"
        "ds_read_b64_tr_b16 v[208:209], v28 offset:16384\n"
        "ds_read_b64_tr_b16 v[210:211], v28 offset:16896\n"
        "ds_read_b64_tr_b16 v[212:213], v28 offset:24576\n"
        "ds_read_b64_tr_b16 v[214:215], v28 offset:25088\n"
        "ds_read_b64_tr_b16 v[216:217], v29 offset:16384\n"
        "ds_read_b64_tr_b16 v[218:219], v29 offset:16896\n"
        "ds_read_b64_tr_b16 v[220:221], v29 offset:24576\n"
        "ds_read_b64_tr_b16 v[222:223], v29 offset:25088\n"
        "s_waitcnt lgkmcnt(0)\n"
        "s_barrier\n"
        "s_add_u32 m0, s71, 40960\n"
        "s_add_u32 s65, s78, 524288\n"
        "buffer_load_dwordx4 v24, s[80:83], s65 offen lds\n"
        "s_add_u32 m0, s71, 45056\n"
        "s_add_u32 s65, s78, 655360\n"
        "buffer_load_dwordx4 v24, s[80:83], s65 offen lds\n"
        "s_add_u32 m0, s71, 49152\n"
        "s_add_u32 s65, s78, 786432\n"
        "buffer_load_dwordx4 v24, s[80:83], s65 offen lds\n"
        "s_add_u32 m0, s71, 53248\n"
        "s_add_u32 s65, s78, 917504\n"
        "buffer_load_dwordx4 v24, s[80:83], s65 offen lds\n"
        "s_add_u32 m0, s71, 57344\n"
        "s_add_u32 s65, s78, 524288\n"
        "buffer_load_dwordx4 v24, s[84:87], s65 offen lds\n"
        "s_add_u32 m0, s71, 61440\n"
        "s_add_u32 s65, s78, 655360\n"
        "buffer_load_dwordx4 v24, s[84:87], s65 offen lds\n"
        "s_add_u32 m0, s71, 65536\n"
        "s_add_u32 s65, s78, 786432\n"
        "buffer_load_dwordx4 v24, s[84:87], s65 offen lds\n"
        "s_add_u32 m0, s71, 69632\n"
        "s_add_u32 s65, s78, 917504\n"
        "buffer_load_dwordx4 v24, s[84:87], s65 offen lds\n"
        "v_add_u32 v28, 73728, v26\n"
        "v_add_u32 v29, 73728, v27\n"
        "s_waitcnt vmcnt(17)\n"
        "s_barrier\n"
        "ds_read_b64_tr_b16 v[224:225], v28 offset:0\n"
        "ds_read_b64_tr_b16 v[226:227], v28 offset:512\n"
        "ds_read_b64_tr_b16 v[228:229], v28 offset:8192\n"
        "ds_read_b64_tr_b16 v[230:231], v28 offset:8704\n"
        "ds_read_b64_tr_b16 v[232:233], v29 offset:0\n"
        "ds_read_b64_tr_b16 v[234:235], v29 offset:512\n"
        "ds_read_b64_tr_b16 v[236:237], v29 offset:8192\n"
        "ds_read_b64_tr_b16 v[238:239], v29 offset:8704\n"
        "ds_read_b64_tr_b16 v[240:241], v28 offset:16384\n"
        "ds_read_b64_tr_b16 v[242:243], v28 offset:16896\n"
        "ds_read_b64_tr_b16 v[244:245], v28 offset:24576\n"
        "ds_read_b64_tr_b16 v[246:247], v28 offset:25088\n"
        "ds_read_b64_tr_b16 v[248:249], v29 offset:16384\n"
        "ds_read_b64_tr_b16 v[250:251], v29 offset:16896\n"
        "ds_read_b64_tr_b16 v[252:253], v29 offset:24576\n"
        "ds_read_b64_tr_b16 v[254:255], v29 offset:25088\n"
        "s_waitcnt lgkmcnt(0)\n"
        "s_barrier\n"
        "s_add_u32 m0, s71, 73728\n"
        "s_add_u32 s65, s78, 1048576\n"
        "buffer_load_dwordx4 v24, s[80:83], s65 offen lds\n"
        "s_add_u32 m0, s71, 77824\n"
        "s_add_u32 s65, s78, 1179648\n"
        "buffer_load_dwordx4 v24, s[80:83], s65 offen lds\n"
        "s_add_u32 m0, s71, 81920\n"
        "s_add_u32 s65, s78, 1310720\n"
        "buffer_load_dwordx4 v24, s[80:83], s65 offen lds\n"
        "s_add_u32 m0, s71, 86016\n"
        "s_add_u32 s65, s78, 1441792\n"
        "buffer_load_dwordx4 v24, s[80:83], s65 offen lds\n"
        "s_add_u32 m0, s71, 90112\n"
        "s_add_u32 s65, s78, 1048576\n"
        "buffer_load_dwordx4 v24, s[84:87], s65 offen lds\n"
        "s_add_u32 m0, s71, 94208\n"
        "s_add_u32 s65, s78, 1179648\n"
        "buffer_load_dwordx4 v24, s[84:87], s65 offen lds\n"
        "s_add_u32 m0, s71, 98304\n"
        "s_add_u32 s65, s78, 1310720\n"
        "buffer_load_dwordx4 v24, s[84:87], s65 offen lds\n"
        "s_add_u32 m0, s71, 102400\n"
        "s_add_u32 s65, s78, 1441792\n"
        "buffer_load_dwordx4 v24, s[84:87], s65 offen lds\n"
        "v_add_u32 v28, 106496, v26\n"
        "v_add_u32 v29, 106496, v27\n"
        "s_waitcnt vmcnt(17)\n"
        "s_barrier\n"
        "ds_read_b64_tr_b16 a[0:1], v28 offset:0\n"
        "ds_read_b64_tr_b16 a[2:3], v28 offset:512\n"
        "ds_read_b64_tr_b16 a[4:5], v28 offset:8192\n"
        "ds_read_b64_tr_b16 a[6:7], v28 offset:8704\n"
        "ds_read_b64_tr_b16 a[8:9], v29 offset:0\n"
        "ds_read_b64_tr_b16 a[10:11], v29 offset:512\n"
        "ds_read_b64_tr_b16 a[12:13], v29 offset:8192\n"
        "ds_read_b64_tr_b16 a[14:15], v29 offset:8704\n"
        "ds_read_b64_tr_b16 a[16:17], v28 offset:16384\n"
        "ds_read_b64_tr_b16 a[18:19], v28 offset:16896\n"
        "ds_read_b64_tr_b16 a[20:21], v28 offset:24576\n"
        "ds_read_b64_tr_b16 a[22:23], v28 offset:25088\n"
        "ds_read_b64_tr_b16 a[24:25], v29 offset:16384\n"
        "ds_read_b64_tr_b16 a[26:27], v29 offset:16896\n"
        "ds_read_b64_tr_b16 a[28:29], v29 offset:24576\n"
        "ds_read_b64_tr_b16 a[30:31], v29 offset:25088\n"
        "s_waitcnt lgkmcnt(0)\n"
        "s_barrier\n"
        "buffer_load_dword v189, off, s[60:63], s88 sc1\n"
        "s_add_u32 m0, s71, 106496\n"
        "s_add_u32 s65, s78, 1572864\n"
        "buffer_load_dwordx4 v24, s[80:83], s65 offen lds\n"
        "s_add_u32 m0, s71, 110592\n"
        "s_add_u32 s65, s78, 1703936\n"
        "buffer_load_dwordx4 v24, s[80:83], s65 offen lds\n"
        "s_add_u32 m0, s71, 114688\n"
        "s_add_u32 s65, s78, 1835008\n"
        "buffer_load_dwordx4 v24, s[80:83], s65 offen lds\n"
        "s_add_u32 m0, s71, 118784\n"
        "s_add_u32 s65, s78, 1966080\n"
        "buffer_load_dwordx4 v24, s[80:83], s65 offen lds\n"
        "s_add_u32 m0, s71, 122880\n"
        "s_add_u32 s65, s78, 1572864\n"
        "buffer_load_dwordx4 v24, s[84:87], s65 offen lds\n"
        "s_add_u32 m0, s71, 126976\n"
        "s_add_u32 s65, s78, 1703936\n"
        "buffer_load_dwordx4 v24, s[84:87], s65 offen lds\n"
        "s_add_u32 m0, s71, 131072\n"
        "s_add_u32 s65, s78, 1835008\n"
        "buffer_load_dwordx4 v24, s[84:87], s65 offen lds\n"
        "s_add_u32 m0, s71, 135168\n"
        "s_add_u32 s65, s78, 1966080\n"
        "buffer_load_dwordx4 v24, s[84:87], s65 offen lds\n"
        "v_add_u32 v28, 40960, v26\n"
        "v_add_u32 v29, 40960, v27\n"
        "s_waitcnt vmcnt(17)\n"
        "s_barrier\n"
        "ds_read_b64_tr_b16 a[32:33], v28 offset:0\n"
        "ds_read_b64_tr_b16 a[34:35], v28 offset:512\n"
        "ds_read_b64_tr_b16 a[36:37], v28 offset:8192\n"
        "ds_read_b64_tr_b16 a[38:39], v28 offset:8704\n"
        "ds_read_b64_tr_b16 a[40:41], v29 offset:0\n"
        "ds_read_b64_tr_b16 a[42:43], v29 offset:512\n"
        "ds_read_b64_tr_b16 a[44:45], v29 offset:8192\n"
        "ds_read_b64_tr_b16 a[46:47], v29 offset:8704\n"
        "ds_read_b64_tr_b16 a[48:49], v28 offset:16384\n"
        "ds_read_b64_tr_b16 a[50:51], v28 offset:16896\n"
        "ds_read_b64_tr_b16 a[52:53], v28 offset:24576\n"
        "ds_read_b64_tr_b16 a[54:55], v28 offset:25088\n"
        "ds_read_b64_tr_b16 a[56:57], v29 offset:16384\n"
        "ds_read_b64_tr_b16 a[58:59], v29 offset:16896\n"
        "ds_read_b64_tr_b16 a[60:61], v29 offset:24576\n"
        "ds_read_b64_tr_b16 a[62:63], v29 offset:25088\n"
        "s_waitcnt lgkmcnt(0)\n"
        "s_barrier\n"
        "s_getpc_b64 s[92:93]\n"
        "CPR13_loop:\n"
        "s_add_u32 s92, s92, R13_loop-CPR13_loop\n"
        "s_addc_u32 s93, s93, 0\n"
        "s_mov_b32 s94, R1_fin-R13_loop\n"
        "s_mov_b32 s95, 0x00020000\n"
        "v_mbcnt_lo_u32_b32 v186, -1, 0\n"
        "v_mbcnt_hi_u32_b32 v186, -1, v186\n"
        "v_lshlrev_b32 v186, 7, v186\n"
        "s_lshl_b32 s65, s67, 13\n"
        "v_add_u32 v186, s65, v186\n"
        "buffer_load_dword v186, v186, s[92:95], 0 offen\n"
        "s_add_u32 m0, s71, 40960\n"
        "s_add_u32 s65, s78, 2097152\n"
        "buffer_load_dwordx4 v24, s[80:83], s65 offen lds\n"
        "s_add_u32 m0, s71, 45056\n"
        "s_add_u32 s65, s78, 2228224\n"
        "buffer_load_dwordx4 v24, s[80:83], s65 offen lds\n"
        "s_add_u32 m0, s71, 49152\n"
        "s_add_u32 s65, s78, 2359296\n"
        "buffer_load_dwordx4 v24, s[80:83], s65 offen lds\n"
        "s_add_u32 m0, s71, 53248\n"
        "s_add_u32 s65, s78, 2490368\n"
        "buffer_load_dwordx4 v24, s[80:83], s65 offen lds\n"
        "s_add_u32 m0, s71, 57344\n"
        "s_add_u32 s65, s78, 2097152\n"
        "buffer_load_dwordx4 v24, s[84:87], s65 offen lds\n"
        "s_add_u32 m0, s71, 61440\n"
        "s_add_u32 s65, s78, 2228224\n"
        "buffer_load_dwordx4 v24, s[84:87], s65 offen lds\n"
        "s_add_u32 m0, s71, 65536\n"
        "s_add_u32 s65, s78, 2359296\n"
        "buffer_load_dwordx4 v24, s[84:87], s65 offen lds\n"
        "s_add_u32 m0, s71, 69632\n"
        "s_add_u32 s65, s78, 2490368\n"
        "buffer_load_dwordx4 v24, s[84:87], s65 offen lds\n"
        "v_add_u32 v28, 73728, v26\n"
        "v_add_u32 v29, 73728, v27\n"
        "s_waitcnt vmcnt(17)\n"
        "s_barrier\n"
        "ds_read_b64_tr_b16 a[64:65], v28 offset:0\n"
        "ds_read_b64_tr_b16 a[66:67], v28 offset:512\n"
        "ds_read_b64_tr_b16 a[68:69], v28 offset:8192\n"
        "ds_read_b64_tr_b16 a[70:71], v28 offset:8704\n"
        "ds_read_b64_tr_b16 a[72:73], v29 offset:0\n"
        "ds_read_b64_tr_b16 a[74:75], v29 offset:512\n"
        "ds_read_b64_tr_b16 a[76:77], v29 offset:8192\n"
        "ds_read_b64_tr_b16 a[78:79], v29 offset:8704\n"
        "ds_read_b64_tr_b16 a[80:81], v28 offset:16384\n"
        "ds_read_b64_tr_b16 a[82:83], v28 offset:16896\n"
        "ds_read_b64_tr_b16 a[84:85], v28 offset:24576\n"
        "ds_read_b64_tr_b16 a[86:87], v28 offset:25088\n"
        "ds_read_b64_tr_b16 a[88:89], v29 offset:16384\n"
        "ds_read_b64_tr_b16 a[90:91], v29 offset:16896\n"
        "ds_read_b64_tr_b16 a[92:93], v29 offset:24576\n"
        "ds_read_b64_tr_b16 a[94:95], v29 offset:25088\n"
        "s_waitcnt lgkmcnt(0)\n"
        "s_barrier\n"
        "v_cmp_ne_u32 vcc, v188, v189\n"
        "s_lshl_b32 s65, s58, 2\n"
        "s_add_u32 s65, s65, 4304896\n"
        "s_cmp_eq_u32 s67, 0\n"
        "s_cselect_b32 s65, s65, 0xf0000000\n"
        "s_cmp_lg_u64 vcc, 0\n"
        "s_cselect_b32 s91, 1, 0\n"
        "s_cselect_b32 s65, 0xf0000000, s65\n"
        "s_cselect_b32 s69, 0xf0000000, s90\n"
        "v_mov_b32 v20, 0\n"
        "s_mov_b64 exec, 1\n"
        "buffer_store_dword v20, off, s[60:63], s65 sc1\n"
        "buffer_store_dword v20, off, s[60:63], s69 sc1\n"
        "s_mov_b64 exec, -1\n"
        "v_mbcnt_lo_u32_b32 v21, -1, 0\n"
        "v_mbcnt_hi_u32_b32 v21, -1, v21\n"
        "v_and_b32 v187, 3, v21\n"
        "v_lshlrev_b32 v187, 2, v187\n"
        "v_min_u32 v21, 39, v21\n"
        "v_lshlrev_b32 v21, 2, v21\n"
        "v_add_u32 v28, 106496, v26\n"
        "v_add_u32 v29, 106496, v27\n"
        "s_waitcnt vmcnt(11)\n"
        "s_barrier\n"
        "ds_read_b64_tr_b16 a[96:97], v28 offset:0\n"
        "ds_read_b64_tr_b16 a[98:99], v28 offset:512\n"
        "ds_read_b64_tr_b16 a[100:101], v28 offset:8192\n"
        "ds_read_b64_tr_b16 a[102:103], v28 offset:8704\n"
        "ds_read_b64_tr_b16 a[104:105], v29 offset:0\n"
        "ds_read_b64_tr_b16 a[106:107], v29 offset:512\n"
        "ds_read_b64_tr_b16 a[108:109], v29 offset:8192\n"
        "ds_read_b64_tr_b16 a[110:111], v29 offset:8704\n"
        "ds_read_b64_tr_b16 a[112:113], v28 offset:16384\n"
        "ds_read_b64_tr_b16 a[114:115], v28 offset:16896\n"
        "ds_read_b64_tr_b16 a[116:117], v28 offset:24576\n"
        "ds_read_b64_tr_b16 a[118:119], v28 offset:25088\n"
        "ds_read_b64_tr_b16 a[120:121], v29 offset:16384\n"
        "ds_read_b64_tr_b16 a[122:123], v29 offset:16896\n"
        "ds_read_b64_tr_b16 a[124:125], v29 offset:24576\n"
        "ds_read_b64_tr_b16 a[126:127], v29 offset:25088\n"
        "s_waitcnt lgkmcnt(0)\n"
        "s_barrier\n"
        "s_mov_b32 s65, 4305152\n"
        "s_mov_b32 s69, 4305408\n"
        "buffer_load_dword v22, v21, s[60:63], s65 offen sc1\n"
        "buffer_load_dwordx2 v[24:25], off, s[60:63], s69 sc1\n"
        "s_lshl_b32 s57, s58, 4\n"
        "s_add_u32 s57, s57, 4194304\n"
        "buffer_load_dword v190, v187, s[60:63], s57 offen sc1\n"
        "v_add_u32 v28, 40960, v26\n"
        "v_add_u32 v29, 40960, v27\n"
        "s_waitcnt vmcnt(5)\n"
        "s_barrier\n"
        "ds_read_b64_tr_b16 a[128:129], v28 offset:0\n"
        "ds_read_b64_tr_b16 a[130:131], v28 offset:512\n"
        "ds_read_b64_tr_b16 a[132:133], v28 offset:8192\n"
        "ds_read_b64_tr_b16 a[134:135], v28 offset:8704\n"
        "ds_read_b64_tr_b16 a[136:137], v29 offset:0\n"
        "ds_read_b64_tr_b16 a[138:139], v29 offset:512\n"
        "ds_read_b64_tr_b16 a[140:141], v29 offset:8192\n"
        "ds_read_b64_tr_b16 a[142:143], v29 offset:8704\n"
        "ds_read_b64_tr_b16 a[144:145], v28 offset:16384\n"
        "ds_read_b64_tr_b16 a[146:147], v28 offset:16896\n"
        "ds_read_b64_tr_b16 a[148:149], v28 offset:24576\n"
        "ds_read_b64_tr_b16 a[150:151], v28 offset:25088\n"
        "ds_read_b64_tr_b16 a[152:153], v29 offset:16384\n"
        "ds_read_b64_tr_b16 a[154:155], v29 offset:16896\n"
        "ds_read_b64_tr_b16 a[156:157], v29 offset:24576\n"
        "ds_read_b64_tr_b16 a[158:159], v29 offset:25088\n"
        "s_waitcnt lgkmcnt(0)\n"
        "s_barrier\n"
        "s_waitcnt vmcnt(0)\n"
        "s_cmp_eq_u32 s91, 0\n"
        "s_cbranch_scc1 HS480_qkd\n"
        "HS480_qka:\n"
        "buffer_load_dword v189, off, s[60:63], s88 sc1\n"
        "s_waitcnt vmcnt(0)\n"
        "v_cmp_ne_u32 vcc, v188, v189\n"
        "s_cbranch_vccz HS480_qkb\n"
        "s_sleep 1\n"
        "s_cmp_eq_u32 s66, 0\n"
        "s_cbranch_scc1 HS480_qkb\n"
        "s_sub_u32 s66, s66, 1\n"
        "s_branch HS480_qka\n"
        "HS480_qkb:\n"
        "s_lshl_b32 s65, s58, 2\n"
        "s_add_u32 s65, s65, 4304896\n"
        "s_cmp_eq_u32 s67, 0\n"
        "s_cselect_b32 s65, s65, 0xf0000000\n"
        "s_mov_b64 exec, 1\n"
        "buffer_store_dword v20, off, s[60:63], s65 sc1\n"
        "buffer_store_dword v20, off, s[60:63], s90 sc1\n"
        "s_mov_b64 exec, -1\n"
        "v_mov_b32 v22, -1\n"
        "v_mov_b32 v190, -1\n"
        "HS480_qkd:\n"
        "s_mov_b32 s65, 4305152\n"
        "s_mov_b32 s69, 4305408\n"
        "s_branch R1_c3\n"
        "R1_c1:\n"
        "buffer_load_dword v22, v21, s[60:63], s65 offen sc1\n"
        "buffer_load_dwordx2 v[24:25], off, s[60:63], s69 sc1\n"
        "s_waitcnt vmcnt(0)\n"
        "R1_c3:\n"
        "v_max3_u32 v22, v22, v24, v25\n"
        "v_cmp_eq_u32 vcc, -1, v22\n"
        "s_cbranch_vccz R1_c2\n"
        "s_sleep 1\n"
        "s_cmp_eq_u32 s66, 0\n"
        "s_cbranch_scc1 R1_c2\n"
        "s_sub_u32 s66, s66, 1\n"
        "s_branch R1_c1\n"
        "R1_c2:\n"
        "v_and_b32 v24, 1, v24\n"
        "v_and_b32 v25, 1, v25\n"
        "v_lshlrev_b32 v24, 13, v24\n"
        "v_lshlrev_b32 v25, 13, v25\n"
        "v_add_u32 v3, v3, v25\n"
        "v_add_u32 v4, v4, v25\n"
        "v_add_u32 v5, v5, v25\n"
        "v_readfirstlane_b32 s49, v25\n"
        "s_nop 1\n"
        "s_add_u32 s49, s49, 2621440\n"
        "v_mov_b32 v16, -1\n"
        "v_mov_b32 v17, -1\n"
        "v_mov_b32 v18, -1\n"
        "v_mov_b32 v19, -1\n"
        "s_mov_b32 s46, %[t0]\n"
        "s_mov_b32 s45, %[t4]\n"
        "s_memtime s[10:11]\n"
        "s_memrealtime s[14:15]\n"
        "s_nop 2\n"
        "s_memrealtime s[16:17]\n"
        "s_nop 2\n"
        "s_memrealtime s[18:19]\n"
        "s_nop 2\n"
        "s_memrealtime s[20:21]\n"
        "s_nop 2\n"
        "s_memrealtime s[22:23]\n"
        "s_nop 2\n"
        "s_memrealtime s[24:25]\n"
        "s_nop 1\n"
        "s_memtime s[12:13]\n"
        "s_memrealtime s[26:27]\n"
        "s_waitcnt lgkmcnt(0)\n"
        "s_mul_i32 s14, s14, 24\n"
        "s_mul_i32 s16, s16, 24\n"
        "s_sub_u32 s16, s16, 17\n"
        "s_max_i32 s14, s14, s16\n"
        "s_mul_i32 s18, s18, 24\n"
        "s_sub_u32 s18, s18, 34\n"
        "s_max_i32 s14, s14, s18\n"
        "s_mul_i32 s20, s20, 24\n"
        "s_sub_u32 s20, s20, 51\n"
        "s_max_i32 s14, s14, s20\n"
        "s_mul_i32 s22, s22, 24\n"
        "s_sub_u32 s22, s22, 68\n"
        "s_max_i32 s14, s14, s22\n"
        "s_mul_i32 s24, s24, 24\n"
        "s_sub_u32 s24, s24, 85\n"
        "s_max_i32 s14, s14, s24\n"
        "s_mul_i32 s26, s26, 24\n"
        "s_sub_u32 s26, s26, 102\n"
        "s_max_i32 s14, s14, s26\n"
        "s_sub_u32 s14, s14, s10\n"
        "s_mov_b32 s42, s14\n"
        "s_mov_b32 s0, s10\n"
        "s_mov_b32 s1, 4000\n"
        "s_add_u32 s44, s10, s42\n"
        "s_sub_u32 s44, s44, 0x1000000\n"
        "s_lshr_b32 s36, s45, 8\n"
        "s_lshl_b32 s35, s36, 1\n"
        "s_mul_i32 s34, s36, 3\n"
        "s_mov_b32 s6, s46\n"
        "s_mov_b32 s2, 1600\n"
        "s_mov_b32 s5, 1350\n"
        "s_mov_b32 s7, 3072\n"
        "s_cmp_eq_u32 s67, 0\n"
        "s_cselect_b32 s8, 15, 0\n"
        "s_mov_b32 s9, 0\n"
        "s_lshr_b32 s47, s58, 2\n"
        "s_lshl_b32 s47, s47, 7\n"
        "s_and_b32 s65, s58, 3\n"
        "s_lshl_b32 s65, s65, 2\n"
        "s_add_u32 s47, s47, s65\n"
        "s_add_u32 s47, s47, s49\n"
        "v_mbcnt_lo_u32_b32 v191, -1, 0\n"
        "v_lshlrev_b32 v191, 10, v191\n"
        "v_add_u32 v14, v14, v25\n"
        "v_mbcnt_lo_u32_b32 v20, -1, 0\n"
        "v_mbcnt_hi_u32_b32 v20, -1, v20\n"
        "v_and_b32 v21, 15, v20\n"
        "v_cmp_gt_u32 s[70:71], 8, v21\n"
        "v_cmp_gt_u32 s[72:73], 16, v20\n"
        "v_cmp_gt_u32 s[74:75], 40, v20\n"
        "v_cmp_gt_i32 s[76:77], 0, v15\n"
        "v_cmp_gt_i32 s[84:85], 8, v15\n"
        "v_cmp_gt_i32 s[78:79], 1, v15\n"
        "v_cmp_gt_i32 s[86:87], 9, v15\n"
        "v_cmp_gt_i32 s[80:81], 2, v15\n"
        "v_cmp_gt_i32 s[88:89], 10, v15\n"
        "v_cmp_gt_i32 s[82:83], 3, v15\n"
        "v_cmp_gt_i32 s[90:91], 11, v15\n"
        "v_mbcnt_lo_u32_b32 v109, -1, 0\n"
        "v_mbcnt_hi_u32_b32 v109, -1, v109\n"
        "v_lshlrev_b32 v110, 5, v109\n"
        "v_and_b32 v109, 3, v109\n"
        "v_and_b32 v110, 0x780, v110\n"
        "v_lshl_add_u32 v109, v109, 2, v110\n"
        "v_add_u32 v109, s7, v109\n"
        "v_mov_b32 v2, v109\n"
        "s_cmp_eq_u32 s67, 0\n"
        "s_cbranch_scc0 R1_i1\n"
        "v_add_u32 v8, v8, v25\n"
        "v_add_u32 v9, v9, v25\n"
        "s_mov_b32 s76, 0\n"
        "s_mov_b32 s77, -1\n"
        "s_branch R10_loop\n"
        "R1_i1:\n"
        "s_mov_b64 s[70:71], -1\n"
        "v_mov_b32 v16, 0xf149f2ca\n"
        "s_cmp_eq_u32 s67, 1\n"
        "s_cbranch_scc1 R11_loop\n"
        "s_cmp_eq_u32 s67, 2\n"
        "s_cbranch_scc1 R12_loop\n"
        "v_lshlrev_b32 v15, 4, v20\n"
        "s_lshl_b32 s65, s58, 13\n"
        "v_add_u32 v15, s65, v15\n"
        "v_and_b32 v17, 3, v20\n"
        "v_lshlrev_b32 v17, 2, v17\n"
        "v_mov_b32 v18, v190\n"
        "s_lshl_b32 s57, s58, 4\n"
        "s_add_u32 s57, s57, 4194304\n"
        "s_mov_b32 s56, 22020096\n"
        "s_mov_b32 s55, 32768\n"
        "v_cmp_eq_u32 vcc, -1, v18\n"
        "s_cbranch_vccz R1_s0b\n"
        "R1_s0a:\n"
        "buffer_load_dword v18, v17, s[60:63], s57 offen sc1\n"
        "s_waitcnt vmcnt(0)\n"
        "v_cmp_eq_u32 vcc, -1, v18\n"
        "s_cbranch_vccz R1_s0b\n"
        "s_sleep 1\n"
        "s_cmp_eq_u32 s66, 0\n"
        "s_cbranch_scc1 R1_s0b\n"
        "s_sub_u32 s66, s66, 1\n"
        "s_branch R1_s0a\n"
        "R1_s0b:\n"
        "s_add_u32 m0, s55, 0\n"
        "s_add_u32 s65, s56, 0\n"
        "buffer_load_dwordx4 v15, s[60:63], s65 offen offset:0 sc1 lds\n"
        "buffer_load_dwordx4 v15, s[60:63], s65 offen offset:1024 sc1 lds\n"
        "buffer_load_dwordx4 v15, s[60:63], s65 offen offset:2048 sc1 lds\n"
        "buffer_load_dwordx4 v15, s[60:63], s65 offen offset:3072 sc1 lds\n"
        "s_add_u32 m0, s55, 4096\n"
        "s_add_u32 s65, s56, 4096\n"
        "buffer_load_dwordx4 v15, s[60:63], s65 offen offset:0 sc1 lds\n"
        "buffer_load_dwordx4 v15, s[60:63], s65 offen offset:1024 sc1 lds\n"
        "buffer_load_dwordx4 v15, s[60:63], s65 offen offset:2048 sc1 lds\n"
        "buffer_load_dwordx4 v15, s[60:63], s65 offen offset:3072 sc1 lds\n"
        "s_add_u32 m0, s55, 8192\n"
        "s_sub_u32 s65, s56, 16777216\n"
        "buffer_load_dwordx4 v15, s[60:63], s65 offen offset:0 sc1 lds\n"
        "buffer_load_dwordx4 v15, s[60:63], s65 offen offset:1024 sc1 lds\n"
        "buffer_load_dwordx4 v15, s[60:63], s65 offen offset:2048 sc1 lds\n"
        "buffer_load_dwordx4 v15, s[60:63], s65 offen offset:3072 sc1 lds\n"
        "s_add_u32 m0, s55, 12288\n"
        "s_sub_u32 s65, s56, 16773120\n"
        "buffer_load_dwordx4 v15, s[60:63], s65 offen offset:0 sc1 lds\n"
        "buffer_load_dwordx4 v15, s[60:63], s65 offen offset:1024 sc1 lds\n"
        "buffer_load_dwordx4 v15, s[60:63], s65 offen offset:2048 sc1 lds\n"
        "buffer_load_dwordx4 v15, s[60:63], s65 offen offset:3072 sc1 lds\n"
        "s_add_u32 s57, s57, 512\n"
        "buffer_load_dword v18, v17, s[60:63], s57 offen sc1\n"
        "s_xor_b32 s55, s55, 16384\n"
        "s_add_u32 s56, s56, 262144\n"
        ".p2align 6\n"
        "s_nop 0\n"
        "s_nop 0\n"
        "s_nop 0\n"
        "s_nop 0\n"
        "s_nop 0\n"
        "s_nop 0\n"
        "R13_loop:\n"
        "v_mov_b32 v104, 0\n"
        "v_mov_b32 v105, 0\n"
        "v_mov_b32 v106, 0\n"
        "v_mov_b32 v107, 0\n"
        "v_mov_b32 v108, 0\n"
        "v_mov_b32 v109, 0\n"
        "v_mov_b32 v110, 0\n"
        "v_mov_b32 v111, 0\n"
        "v_mov_b32 v112, 0\n"
        "v_mov_b32 v113, 0\n"
        "v_mov_b32 v114, 0\n"
        "v_mov_b32 v115, 0\n"
        "v_mov_b32 v116, 0\n"
        "v_mov_b32 v117, 0\n"
        "v_mov_b32 v118, 0\n"
        "v_mov_b32 v119, 0\n"
        "s_lshl_b32 s69, s64, 14\n"
        "s_add_u32 s69, s69, s49\n"
        "s_and_b32 s65, s64, 3\n"
        "s_lshl_b32 s65, s65, 15\n"
        "s_add_u32 s65, s65, 2293760\n"
        "s_add_u32 s48, s44, s46\n"
        "s_sub_u32 s48, s48, s42\n"
        "s_sub_u32 s48, s48, s0\n"
        "s_max_i32 s48, s48, 0\n"
        "s_min_i32 s48, s48, 0x10000\n"
        "s_mul_i32 s40, s48, s1\n"
        "s_lshr_b32 s40, s40, 16\n"
        "s_sub_u32 s41, s48, s40\n"
        "s_mul_i32 s40, s41, s1\n"
        "s_lshr_b32 s40, s40, 16\n"
        "s_sub_u32 s48, s48, s40\n"
        "s_add_u32 s48, s48, s0\n"
        "s_sub_u32 s48, s48, 620\n"
        "s_memtime s[50:51]\n"
        "s_waitcnt lgkmcnt(0)\n"
        "s_sub_u32 s41, s50, s0\n"
        "s_sub_u32 s50, s50, s48\n"
        "s_cmp_gt_u32 s41, 12000\n"
        "s_cselect_b32 s50, 0, s50\n"
        "s_add_u32 s45, s48, 544\n"
        "s_cmp_gt_i32 s50, 150\n"
        "s_cselect_b32 s48, s45, s48\n"
        "s_cbranch_scc1 R13_twk\n"
        "s_cmp_gt_i32 s50, -60\n"
        "s_cbranch_scc1 R13_twy\n"
        "R13_twt:\n"
        "s_sleep 1\n"
        "s_memtime s[50:51]\n"
        "s_waitcnt lgkmcnt(0)\n"
        "s_sub_u32 s50, s50, s48\n"
        "s_cmp_lt_i32 s50, 0\n"
        "s_cbranch_scc1 R13_twt\n"
        "R13_twy:\n"
        "s_memtime s[10:11]\n"
        "s_memrealtime s[14:15]\n"
        "s_nop 2\n"
        "s_memrealtime s[16:17]\n"
        "s_nop 2\n"
        "s_memrealtime s[18:19]\n"
        "s_nop 2\n"
        "s_memrealtime s[20:21]\n"
        "s_nop 2\n"
        "s_memrealtime s[22:23]\n"
        "s_nop 2\n"
        "s_memrealtime s[24:25]\n"
        "s_nop 1\n"
        "s_memtime s[12:13]\n"
        "s_memrealtime s[26:27]\n"
        "s_waitcnt lgkmcnt(0)\n"
        "s_mul_i32 s14, s14, 24\n"
        "s_mul_i32 s16, s16, 24\n"
        "s_sub_u32 s16, s16, 17\n"
        "s_max_i32 s14, s14, s16\n"
        "s_mul_i32 s18, s18, 24\n"
        "s_sub_u32 s18, s18, 34\n"
        "s_max_i32 s14, s14, s18\n"
        "s_mul_i32 s20, s20, 24\n"
        "s_sub_u32 s20, s20, 51\n"
        "s_max_i32 s14, s14, s20\n"
        "s_mul_i32 s22, s22, 24\n"
        "s_sub_u32 s22, s22, 68\n"
        "s_max_i32 s14, s14, s22\n"
        "s_mul_i32 s24, s24, 24\n"
        "s_sub_u32 s24, s24, 85\n"
        "s_max_i32 s14, s14, s24\n"
        "s_mul_i32 s26, s26, 24\n"
        "s_sub_u32 s26, s26, 102\n"
        "s_max_i32 s14, s14, s26\n"
        "s_sub_u32 s14, s14, s10\n"
        "s_sub_u32 s41, s10, s0\n"
        "s_mov_b32 s0, s10\n"
        "s_mul_i32 s41, s41, s1\n"
        "s_lshr_b32 s41, s41, 16\n"
        "s_add_u32 s42, s42, s41\n"
        "s_sub_u32 s40, s12, s10\n"
        "s_sub_u32 s41, s14, s42\n"
        "s_sub_u32 s40, s40, 88\n"
        "s_cmp_gt_u32 s40, 32\n"
        "s_cselect_b32 s41, 0, s41\n"
        "s_add_u32 s42, s42, s41\n"
        "s_min_i32 s41, s41, 120\n"
        "s_max_i32 s41, s41, -120\n"
        "s_mul_i32 s41, s41, 7\n"
        "s_add_u32 s1, s1, s41\n"
        "s_max_i32 s1, s1, 0\n"
        "s_add_u32 s48, s44, s46\n"
        "s_sub_u32 s48, s48, s42\n"
        "s_sub_u32 s48, s48, s0\n"
        "s_max_i32 s48, s48, 0\n"
        "s_min_i32 s48, s48, 0x10000\n"
        "s_mul_i32 s40, s48, s1\n"
        "s_lshr_b32 s40, s40, 16\n"
        "s_sub_u32 s41, s48, s40\n"
        "s_mul_i32 s40, s41, s1\n"
        "s_lshr_b32 s40, s40, 16\n"
        "s_sub_u32 s48, s48, s40\n"
        "s_add_u32 s48, s48, s0\n"
        "s_sub_u32 s48, s48, 76\n"
        "R13_twk:\n"
        "R13_twsa:\n"
        "s_memtime s[50:51]\n"
        "s_getpc_b64 s[40:41]\n"
        "R13_twsb:\n"
        "s_waitcnt lgkmcnt(0)\n"
        "s_sub_u32 s50, s48, s50\n"
        "s_cmp_gt_i32 s50, 96\n"
        "s_cbranch_scc1 R13_twsa\n"
        "s_max_i32 s50, s50, 0\n"
        "s_and_b32 s50, s50, -4\n"
        "s_sub_u32 s50, R13_twse-R13_twsb, s50\n"
        "s_add_u32 s40, s40, s50\n"
        "s_addc_u32 s41, s41, 0\n"
        "s_setpc_b64 s[40:41]\n"
        "s_nop 0\n"
        "s_nop 0\n"
        "s_nop 0\n"
        "s_nop 0\n"
        "s_nop 0\n"
        "s_nop 0\n"
        "s_nop 0\n"
        "s_nop 0\n"
        "s_nop 0\n"
        "s_nop 0\n"
        "s_nop 0\n"
        "s_nop 0\n"
        "s_nop 0\n"
        "s_nop 0\n"
        "s_nop 0\n"
        "s_nop 0\n"
        "s_nop 0\n"
        "s_nop 0\n"
        "s_nop 0\n"
        "s_nop 0\n"
        "s_nop 0\n"
        "s_nop 0\n"
        "s_nop 0\n"
        "s_nop 0\n"
        "R13_twse:\n"
        "R13_poll:\n"
        "buffer_load_dwordx4 v[20:23], v3, s[60:63], s65 offen sc1\n"
        "buffer_load_dwordx4 v[24:27], v4, s[60:63], s65 offen sc1\n"
        "s_waitcnt vmcnt(1)\n"
        "ds_write_b128 v1, v[20:23]\n"
        "v_max3_u32 v32, v20, v21, v22\n"
        "s_waitcnt vmcnt(0)\n"
        "ds_write_b128 v1, v[24:27] offset:1024\n"
        "v_max3_u32 v32, v32, v23, v24\n"
        "v_max3_u32 v32, v32, v25, v26\n"
        "v_max_u32 v32, v32, v27\n"
        "v_cmp_eq_u32 vcc, -1, v32\n"
        "v_smfmac_f32_16x16x64_bf16 v[104:107], v[20:23], v[192:199], v10\n"
        "v_smfmac_f32_16x16x64_bf16 v[108:111], v[20:23], v[200:207], v10\n"
        "v_smfmac_f32_16x16x64_bf16 v[104:107], v[24:27], v[224:231], v10\n"
        "v_smfmac_f32_16x16x64_bf16 v[108:111], v[24:27], v[232:239], v10\n"
        "s_cbranch_vccnz R13_miss\n"
        "R13_got:\n"
        "s_waitcnt lgkmcnt(0)\n"
        "s_barrier\n"
        "ds_read_b128 v[32:35], v0 offset:0\n"
        "v_smfmac_f32_16x16x64_bf16 v[112:115], v[20:23], v[208:215], v10\n"
        "ds_read_b128 v[36:39], v0 offset:1024\n"
        "ds_read_b128 v[40:43], v0 offset:2048\n"
        "v_smfmac_f32_16x16x64_bf16 v[116:119], v[20:23], v[216:223], v10\n"
        "ds_read_b128 v[44:47], v0 offset:3072\n"
        "ds_read_b128 v[48:51], v0 offset:4096\n"
        "v_smfmac_f32_16x16x64_bf16 v[112:115], v[24:27], v[240:247], v10\n"
        "ds_read_b128 v[52:55], v0 offset:5120\n"
        "ds_read_b128 v[56:59], v0 offset:6144\n"
        "v_smfmac_f32_16x16x64_bf16 v[116:119], v[24:27], v[248:255], v10\n"
        "ds_read_b128 v[60:63], v0 offset:7168\n"
        "s_waitcnt lgkmcnt(7)\n"
        "v_smfmac_f32_16x16x64_bf16 v[104:107], v[32:35], a[0:7], v10\n"
        "v_smfmac_f32_16x16x64_bf16 v[108:111], v[32:35], a[8:15], v10\n"
        "s_waitcnt lgkmcnt(6)\n"
        "v_smfmac_f32_16x16x64_bf16 v[104:107], v[36:39], a[32:39], v10\n"
        "v_smfmac_f32_16x16x64_bf16 v[108:111], v[36:39], a[40:47], v10\n"
        "s_waitcnt lgkmcnt(5)\n"
        "v_smfmac_f32_16x16x64_bf16 v[104:107], v[40:43], a[64:71], v10\n"
        "v_smfmac_f32_16x16x64_bf16 v[108:111], v[40:43], a[72:79], v10\n"
        "s_waitcnt lgkmcnt(4)\n"
        "v_smfmac_f32_16x16x64_bf16 v[104:107], v[44:47], a[96:103], v10\n"
        "v_smfmac_f32_16x16x64_bf16 v[108:111], v[44:47], a[104:111], v10\n"
        "s_waitcnt lgkmcnt(3)\n"
        "v_smfmac_f32_16x16x64_bf16 v[104:107], v[48:51], a[128:135], v10\n"
        "v_smfmac_f32_16x16x64_bf16 v[108:111], v[48:51], a[136:143], v10\n"
        "s_waitcnt lgkmcnt(2)\n"
        "v_smfmac_f32_16x16x64_bf16 v[104:107], v[52:55], a[160:167], v10\n"
        "v_smfmac_f32_16x16x64_bf16 v[108:111], v[52:55], a[168:175], v10\n"
        "s_waitcnt lgkmcnt(1)\n"
        "v_smfmac_f32_16x16x64_bf16 v[104:107], v[56:59], a[192:199], v10\n"
        "v_smfmac_f32_16x16x64_bf16 v[108:111], v[56:59], a[200:207], v10\n"
        "s_waitcnt lgkmcnt(0)\n"
        "v_smfmac_f32_16x16x64_bf16 v[104:107], v[60:63], a[224:231], v10\n"
        "v_smfmac_f32_16x16x64_bf16 v[108:111], v[60:63], a[232:239], v10\n"
        "v_smfmac_f32_16x16x64_bf16 v[112:115], v[32:35], a[16:23], v10\n"
        "v_smfmac_f32_16x16x64_bf16 v[116:119], v[32:35], a[24:31], v10\n"
        "v_pk_add_f32 v[104:105], v[104:105], v[106:107]\n"
        "v_pk_add_f32 v[108:109], v[108:109], v[110:111]\n"
        "v_cvt_pk_bf16_f32 v104, v104, v105\n"
        "v_cvt_pk_bf16_f32 v105, v108, v109\n"
        "ds_write2st64_b32 v6, v104, v105 offset0:0 offset1:1\n"
        "v_smfmac_f32_16x16x64_bf16 v[112:115], v[36:39], a[48:55], v10\n"
        "v_smfmac_f32_16x16x64_bf16 v[116:119], v[36:39], a[56:63], v10\n"
        "v_smfmac_f32_16x16x64_bf16 v[112:115], v[40:43], a[80:87], v10\n"
        "v_smfmac_f32_16x16x64_bf16 v[116:119], v[40:43], a[88:95], v10\n"
        "v_smfmac_f32_16x16x64_bf16 v[112:115], v[44:47], a[112:119], v10\n"
        "s_waitcnt lgkmcnt(0)\n"
        "s_barrier\n"
        "v_smfmac_f32_16x16x64_bf16 v[116:119], v[44:47], a[120:127], v10\n"
        "v_smfmac_f32_16x16x64_bf16 v[112:115], v[48:51], a[144:151], v10\n"
        "v_smfmac_f32_16x16x64_bf16 v[116:119], v[48:51], a[152:159], v10\n"
        "v_smfmac_f32_16x16x64_bf16 v[112:115], v[52:55], a[176:183], v10\n"
        "v_smfmac_f32_16x16x64_bf16 v[116:119], v[52:55], a[184:191], v10\n"
        "v_smfmac_f32_16x16x64_bf16 v[112:115], v[56:59], a[208:215], v10\n"
        "v_smfmac_f32_16x16x64_bf16 v[116:119], v[56:59], a[216:223], v10\n"
        "v_smfmac_f32_16x16x64_bf16 v[112:115], v[60:63], a[240:247], v10\n"
        "v_smfmac_f32_16x16x64_bf16 v[116:119], v[60:63], a[248:255], v10\n"
        "s_nop 7\n"
        "v_pk_add_f32 v[112:113], v[112:113], v[114:115]\n"
        "v_pk_add_f32 v[116:117], v[116:117], v[118:119]\n"
        "v_cvt_pk_bf16_f32 v112, v112, v113\n"
        "v_cvt_pk_bf16_f32 v113, v116, v117\n"
        "ds_write2st64_b32 v6, v112, v113 offset0:8 offset1:9\n"
        "s_waitcnt lgkmcnt(0)\n"
        "s_barrier\n"
        "buffer_load_dword v104, v2, s[60:63], s69 offen sc1\n"
        "s_memtime s[10:11]\n"
        "s_memrealtime s[14:15]\n"
        "s_nop 2\n"
        "s_memrealtime s[16:17]\n"
        "s_nop 2\n"
        "s_memrealtime s[18:19]\n"
        "s_nop 2\n"
        "s_memrealtime s[20:21]\n"
        "s_nop 2\n"
        "s_memrealtime s[22:23]\n"
        "s_nop 2\n"
        "s_memrealtime s[24:25]\n"
        "s_nop 1\n"
        "s_memtime s[12:13]\n"
        "s_memrealtime s[26:27]\n"
        "s_waitcnt lgkmcnt(0)\n"
        "s_mul_i32 s14, s14, 24\n"
        "s_mul_i32 s16, s16, 24\n"
        "s_sub_u32 s16, s16, 17\n"
        "s_max_i32 s14, s14, s16\n"
        "s_mul_i32 s18, s18, 24\n"
        "s_sub_u32 s18, s18, 34\n"
        "s_max_i32 s14, s14, s18\n"
        "s_mul_i32 s20, s20, 24\n"
        "s_sub_u32 s20, s20, 51\n"
        "s_max_i32 s14, s14, s20\n"
        "s_mul_i32 s22, s22, 24\n"
        "s_sub_u32 s22, s22, 68\n"
        "s_max_i32 s14, s14, s22\n"
        "s_mul_i32 s24, s24, 24\n"
        "s_sub_u32 s24, s24, 85\n"
        "s_max_i32 s14, s14, s24\n"
        "s_mul_i32 s26, s26, 24\n"
        "s_sub_u32 s26, s26, 102\n"
        "s_max_i32 s14, s14, s26\n"
        "s_sub_u32 s14, s14, s10\n"
        "s_sub_u32 s41, s10, s0\n"
        "s_mov_b32 s0, s10\n"
        "s_mul_i32 s41, s41, s1\n"
        "s_lshr_b32 s41, s41, 16\n"
        "s_add_u32 s42, s42, s41\n"
        "s_sub_u32 s40, s12, s10\n"
        "s_sub_u32 s41, s14, s42\n"
        "s_sub_u32 s40, s40, 88\n"
        "s_cmp_gt_u32 s40, 32\n"
        "s_cselect_b32 s41, 0, s41\n"
        "s_add_u32 s42, s42, s41\n"
        "s_min_i32 s41, s41, 120\n"
        "s_max_i32 s41, s41, -120\n"
        "s_mul_i32 s41, s41, 7\n"
        "s_add_u32 s1, s1, s41\n"
        "s_max_i32 s1, s1, 0\n"
        "s_barrier\n"
        "s_cmp_lt_u32 s64, 63\n"
        "s_cbranch_scc0 R13_next\n"
        "s_sleep 2\n"
        "s_waitcnt vmcnt(0)\n"
        "v_cmp_eq_u32 vcc, -1, v18\n"
        "s_cbranch_vccz R1_s1b\n"
        "R1_s1a:\n"
        "buffer_load_dword v18, v17, s[60:63], s57 offen sc1\n"
        "s_waitcnt vmcnt(0)\n"
        "v_cmp_eq_u32 vcc, -1, v18\n"
        "s_cbranch_vccz R1_s1b\n"
        "s_sleep 1\n"
        "s_cmp_eq_u32 s66, 0\n"
        "s_cbranch_scc1 R1_s1b\n"
        "s_sub_u32 s66, s66, 1\n"
        "s_branch R1_s1a\n"
        "R1_s1b:\n"
        "s_add_u32 m0, s55, 0\n"
        "s_add_u32 s65, s56, 0\n"
        "buffer_load_dwordx4 v15, s[60:63], s65 offen offset:0 sc1 lds\n"
        "buffer_load_dwordx4 v15, s[60:63], s65 offen offset:1024 sc1 lds\n"
        "buffer_load_dwordx4 v15, s[60:63], s65 offen offset:2048 sc1 lds\n"
        "buffer_load_dwordx4 v15, s[60:63], s65 offen offset:3072 sc1 lds\n"
        "s_add_u32 m0, s55, 4096\n"
        "s_add_u32 s65, s56, 4096\n"
        "buffer_load_dwordx4 v15, s[60:63], s65 offen offset:0 sc1 lds\n"
        "buffer_load_dwordx4 v15, s[60:63], s65 offen offset:1024 sc1 lds\n"
        "buffer_load_dwordx4 v15, s[60:63], s65 offen offset:2048 sc1 lds\n"
        "buffer_load_dwordx4 v15, s[60:63], s65 offen offset:3072 sc1 lds\n"
        "s_add_u32 m0, s55, 8192\n"
        "s_sub_u32 s65, s56, 16777216\n"
        "buffer_load_dwordx4 v15, s[60:63], s65 offen offset:0 sc1 lds\n"
        "buffer_load_dwordx4 v15, s[60:63], s65 offen offset:1024 sc1 lds\n"
        "buffer_load_dwordx4 v15, s[60:63], s65 offen offset:2048 sc1 lds\n"
        "buffer_load_dwordx4 v15, s[60:63], s65 offen offset:3072 sc1 lds\n"
        "s_add_u32 m0, s55, 12288\n"
        "s_sub_u32 s65, s56, 16773120\n"
        "buffer_load_dwordx4 v15, s[60:63], s65 offen offset:0 sc1 lds\n"
        "buffer_load_dwordx4 v15, s[60:63], s65 offen offset:1024 sc1 lds\n"
        "buffer_load_dwordx4 v15, s[60:63], s65 offen offset:2048 sc1 lds\n"
        "buffer_load_dwordx4 v15, s[60:63], s65 offen offset:3072 sc1 lds\n"
        "s_add_u32 s57, s57, 512\n"
        "buffer_load_dword v18, v17, s[60:63], s57 offen sc1\n"
        "s_xor_b32 s55, s55, 16384\n"
        "s_add_u32 s56, s56, 262144\n"
        "R13_next:\n"
        "s_cmp_eq_u32 s64, 63\n"
        "s_cbranch_scc1 R13_h2\n"
        "R13_h0:\n"
        "s_nop 1\n"
        "v_max_u32_dpp v104, v104, v104 quad_perm:[1,0,3,2] row_mask:0xf bank_mask:0xf\n"
        "s_nop 1\n"
        "v_max_u32_dpp v104, v104, v104 quad_perm:[2,3,0,1] row_mask:0xf bank_mask:0xf\n"
        "s_nop 1\n"
        "v_max_u32_dpp v104, v104, v104 row_half_mirror row_mask:0xf bank_mask:0xf\n"
        "s_nop 1\n"
        "v_readlane_b32 s10, v104, 0\n"
        "v_readlane_b32 s11, v104, 8\n"
        "v_readlane_b32 s12, v104, 16\n"
        "v_readlane_b32 s13, v104, 24\n"
        "v_readlane_b32 s14, v104, 32\n"
        "v_readlane_b32 s15, v104, 40\n"
        "v_readlane_b32 s16, v104, 48\n"
        "v_readlane_b32 s17, v104, 56\n"
        "s_nop 1\n"
        "s_max_u32 s14, s14, s15\n"
        "s_max_u32 s16, s16, s17\n"
        "s_max_u32 s41, s14, s16\n"
        "s_cmp_eq_u32 s41, -1\n"
        "s_cselect_b32 s48, 0, s44\n"
        "s_sub_u32 s41, s41, s48\n"
        "s_min_u32 s14, s10, s11\n"
        "s_max_u32 s11, s10, s11\n"
        "s_min_u32 s10, s12, s13\n"
        "s_max_u32 s13, s12, s13\n"
        "s_min_u32 s12, s14, s10\n"
        "s_max_u32 s10, s14, s10\n"
        "s_min_u32 s14, s11, s13\n"
        "s_max_u32 s13, s11, s13\n"
        "s_min_u32 s11, s14, s10\n"
        "s_max_u32 s10, s14, s10\n"
        "s_add_u32 s12, s12, s34\n"
        "s_add_u32 s11, s11, s35\n"
        "s_add_u32 s10, s10, s36\n"
        "s_max_u32 s12, s12, s11\n"
        "s_max_u32 s10, s10, s13\n"
        "s_max_u32 s40, s12, s10\n"
        "s_cmp_lg_u32 s13, -1\n"
        "s_cselect_b32 s44, s40, s44\n"
        "s_cbranch_scc1 R13_h1\n"
        "s_cmp_eq_u32 s66, 0\n"
        "s_cbranch_scc1 R13_h1\n"
        "s_sub_u32 s66, s66, 1\n"
        "buffer_load_dword v104, v2, s[60:63], s69 offen sc1\n"
        "s_waitcnt vmcnt(0)\n"
        "s_branch R13_h0\n"
        "R13_h1:\n"
        "s_add_u32 s5, s5, 6\n"
        "s_min_u32 s5, s5, s41\n"
        "s_add_u32 s46, s5, s6\n"
        "R13_h2:\n"
        "s_add_u32 s64, s64, 1\n"
        "s_cmp_lt_u32 s64, 64\n"
        "s_cbranch_scc1 R13_loop\n"
        "s_endpgm\n"
        ".p2align 6\n"
        "s_nop 0\n"
        "s_nop 0\n"
        "s_nop 0\n"
        "s_nop 0\n"
        "s_nop 0\n"
        "s_nop 0\n"
        "s_nop 0\n"
        "s_nop 0\n"
        "s_nop 0\n"
        "s_nop 0\n"
        "s_nop 0\n"
        "s_nop 0\n"
        "s_nop 0\n"
        "R10_loop:\n"
        "v_mov_b32 v104, 0\n"
        "v_mov_b32 v105, 0\n"
        "v_mov_b32 v106, 0\n"
        "v_mov_b32 v107, 0\n"
        "v_mov_b32 v108, 0\n"
        "v_mov_b32 v109, 0\n"
        "v_mov_b32 v110, 0\n"
        "v_mov_b32 v111, 0\n"
        "v_mov_b32 v112, 0\n"
        "v_mov_b32 v113, 0\n"
        "v_mov_b32 v114, 0\n"
        "v_mov_b32 v115, 0\n"
        "v_mov_b32 v116, 0\n"
        "v_mov_b32 v117, 0\n"
        "v_mov_b32 v118, 0\n"
        "v_mov_b32 v119, 0\n"
        "s_and_b32 s68, s64, 3\n"
        "s_mul_i32 s68, s68, 49152\n"
        "s_add_u32 s68, s68, 2097152\n"
        "s_lshl_b32 s69, s64, 14\n"
        "s_add_u32 s69, s69, s49\n"
        "s_and_b32 s65, s64, 3\n"
        "s_lshl_b32 s65, s65, 15\n"
        "s_add_u32 s65, s65, 2293760\n"
        "s_add_u32 s48, s44, s46\n"
        "s_sub_u32 s48, s48, s42\n"
        "s_sub_u32 s48, s48, s0\n"
        "s_max_i32 s48, s48, 0\n"
        "s_min_i32 s48, s48, 0x10000\n"
        "s_mul_i32 s40, s48, s1\n"
        "s_lshr_b32 s40, s40, 16\n"
        "s_sub_u32 s41, s48, s40\n"
        "s_mul_i32 s40, s41, s1\n"
        "s_lshr_b32 s40, s40, 16\n"
        "s_sub_u32 s48, s48, s40\n"
        "s_add_u32 s48, s48, s0\n"
        "s_sub_u32 s48, s48, 620\n"
        "s_memtime s[50:51]\n"
        "s_waitcnt lgkmcnt(0)\n"
        "s_sub_u32 s41, s50, s0\n"
        "s_sub_u32 s50, s50, s48\n"
        "s_cmp_gt_u32 s41, 12000\n"
        "s_cselect_b32 s50, 0, s50\n"
        "s_add_u32 s45, s48, 544\n"
        "s_cmp_gt_i32 s50, 150\n"
        "s_cselect_b32 s48, s45, s48\n"
        "s_cbranch_scc1 R10_twk\n"
        "s_cmp_gt_i32 s50, -60\n"
        "s_cbranch_scc1 R10_twy\n"
        "R10_twt:\n"
        "s_sleep 1\n"
        "s_memtime s[50:51]\n"
        "s_waitcnt lgkmcnt(0)\n"
        "s_sub_u32 s50, s50, s48\n"
        "s_cmp_lt_i32 s50, 0\n"
        "s_cbranch_scc1 R10_twt\n"
        "R10_twy:\n"
        "s_memtime s[10:11]\n"
        "s_memrealtime s[14:15]\n"
        "s_nop 2\n"
        "s_memrealtime s[16:17]\n"
        "s_nop 2\n"
        "s_memrealtime s[18:19]\n"
        "s_nop 2\n"
        "s_memrealtime s[20:21]\n"
        "s_nop 2\n"
        "s_memrealtime s[22:23]\n"
        "s_nop 2\n"
        "s_memrealtime s[24:25]\n"
        "s_nop 1\n"
        "s_memtime s[12:13]\n"
        "s_memrealtime s[26:27]\n"
        "s_waitcnt lgkmcnt(0)\n"
        "s_mul_i32 s14, s14, 24\n"
        "s_mul_i32 s16, s16, 24\n"
        "s_sub_u32 s16, s16, 17\n"
        "s_max_i32 s14, s14, s16\n"
        "s_mul_i32 s18, s18, 24\n"
        "s_sub_u32 s18, s18, 34\n"
        "s_max_i32 s14, s14, s18\n"
        "s_mul_i32 s20, s20, 24\n"
        "s_sub_u32 s20, s20, 51\n"
        "s_max_i32 s14, s14, s20\n"
        "s_mul_i32 s22, s22, 24\n"
        "s_sub_u32 s22, s22, 68\n"
        "s_max_i32 s14, s14, s22\n"
        "s_mul_i32 s24, s24, 24\n"
        "s_sub_u32 s24, s24, 85\n"
        "s_max_i32 s14, s14, s24\n"
        "s_mul_i32 s26, s26, 24\n"
        "s_sub_u32 s26, s26, 102\n"
        "s_max_i32 s14, s14, s26\n"
        "s_sub_u32 s14, s14, s10\n"
        "s_sub_u32 s41, s10, s0\n"
        "s_mov_b32 s0, s10\n"
        "s_mul_i32 s41, s41, s1\n"
        "s_lshr_b32 s41, s41, 16\n"
        "s_add_u32 s42, s42, s41\n"
        "s_sub_u32 s40, s12, s10\n"
        "s_sub_u32 s41, s14, s42\n"
        "s_sub_u32 s40, s40, 88\n"
        "s_cmp_gt_u32 s40, 32\n"
        "s_cselect_b32 s41, 0, s41\n"
        "s_add_u32 s42, s42, s41\n"
        "s_min_i32 s41, s41, 120\n"
        "s_max_i32 s41, s41, -120\n"
        "s_mul_i32 s41, s41, 7\n"
        "s_add_u32 s1, s1, s41\n"
        "s_max_i32 s1, s1, 0\n"
        "s_add_u32 s48, s44, s46\n"
        "s_sub_u32 s48, s48, s42\n"
        "s_sub_u32 s48, s48, s0\n"
        "s_max_i32 s48, s48, 0\n"
        "s_min_i32 s48, s48, 0x10000\n"
        "s_mul_i32 s40, s48, s1\n"
        "s_lshr_b32 s40, s40, 16\n"
        "s_sub_u32 s41, s48, s40\n"
        "s_mul_i32 s40, s41, s1\n"
        "s_lshr_b32 s40, s40, 16\n"
        "s_sub_u32 s48, s48, s40\n"
        "s_add_u32 s48, s48, s0\n"
        "s_sub_u32 s48, s48, 76\n"
        "R10_twk:\n"
        "R10_twsa:\n"
        "s_memtime s[50:51]\n"
        "s_getpc_b64 s[40:41]\n"
        "R10_twsb:\n"
        "s_waitcnt lgkmcnt(0)\n"
        "s_sub_u32 s50, s48, s50\n"
        "s_cmp_gt_i32 s50, 96\n"
        "s_cbranch_scc1 R10_twsa\n"
        "s_max_i32 s50, s50, 0\n"
        "s_and_b32 s50, s50, -4\n"
        "s_sub_u32 s50, R10_twse-R10_twsb, s50\n"
        "s_add_u32 s40, s40, s50\n"
        "s_addc_u32 s41, s41, 0\n"
        "s_setpc_b64 s[40:41]\n"
        "s_nop 0\n"
        "s_nop 0\n"
        "s_nop 0\n"
        "s_nop 0\n"
        "s_nop 0\n"
        "s_nop 0\n"
        "s_nop 0\n"
        "s_nop 0\n"
        "s_nop 0\n"
        "s_nop 0\n"
        "s_nop 0\n"
        "s_nop 0\n"
        "s_nop 0\n"
        "s_nop 0\n"
        "s_nop 0\n"
        "s_nop 0\n"
        "s_nop 0\n"
        "s_nop 0\n"
        "s_nop 0\n"
        "s_nop 0\n"
        "s_nop 0\n"
        "s_nop 0\n"
        "s_nop 0\n"
        "s_nop 0\n"
        "R10_twse:\n"
        "R10_poll:\n"
        "buffer_load_dwordx4 v[20:23], v3, s[60:63], s65 offen sc1\n"
        "buffer_load_dwordx4 v[24:27], v4, s[60:63], s65 offen sc1\n"
        "buffer_load_dwordx4 v[28:31], v5, s[60:63], s65 offen sc1\n"
        "s_waitcnt vmcnt(2)\n"
        "ds_write_b128 v1, v[20:23]\n"
        "v_max3_u32 v32, v20, v21, v22\n"
        "s_waitcnt vmcnt(1)\n"
        "ds_write_b128 v1, v[24:27] offset:1024\n"
        "v_max3_u32 v32, v32, v23, v24\n"
        "v_max3_u32 v32, v32, v25, v26\n"
        "s_waitcnt vmcnt(0)\n"
        "ds_write_b128 v1, v[28:31] offset:2048\n"
        "v_max3_u32 v32, v32, v27, v28\n"
        "v_max3_u32 v32, v32, v29, v30\n"
        "v_max_u32 v32, v32, v31\n"
        "v_cmp_eq_u32 vcc, -1, v32\n"
        "v_smfmac_f32_16x16x64_bf16 v[104:107], v[20:23], a[0:7], v10\n"
        "v_smfmac_f32_16x16x64_bf16 v[108:111], v[20:23], a[8:15], v10\n"
        "v_smfmac_f32_16x16x64_bf16 v[104:107], v[24:27], a[32:39], v10\n"
        "v_smfmac_f32_16x16x64_bf16 v[108:111], v[24:27], a[40:47], v10\n"
        "s_cbranch_vccnz R10_miss\n"
        "R10_got:\n"
        "s_waitcnt lgkmcnt(0)\n"
        "s_barrier\n"
        "s_memtime s[50:51]\n"
        "ds_read_b128 v[44:47], v0 offset:3072\n"
        "v_smfmac_f32_16x16x64_bf16 v[104:107], v[28:31], a[64:71], v10\n"
        "ds_read_b128 v[48:51], v0 offset:4096\n"
        "ds_read_b128 v[52:55], v0 offset:5120\n"
        "v_smfmac_f32_16x16x64_bf16 v[108:111], v[28:31], a[72:79], v10\n"
        "ds_read_b128 v[56:59], v0 offset:6144\n"
        "ds_read_b128 v[60:63], v0 offset:7168\n"
        "v_smfmac_f32_16x16x64_bf16 v[112:115], v[20:23], a[16:23], v10\n"
        "ds_read_b128 v[64:67], v0 offset:8192\n"
        "ds_read_b128 v[68:71], v0 offset:9216\n"
        "v_smfmac_f32_16x16x64_bf16 v[116:119], v[20:23], a[24:31], v10\n"
        "v_smfmac_f32_16x16x64_bf16 v[112:115], v[24:27], a[48:55], v10\n"
        "s_waitcnt lgkmcnt(6)\n"
        "v_smfmac_f32_16x16x64_bf16 v[104:107], v[44:47], a[96:103], v10\n"
        "s_mov_b32 s3, s50\n"
        "s_add_u32 s50, s50, s2\n"
        "v_smfmac_f32_16x16x64_bf16 v[108:111], v[44:47], a[104:111], v10\n"
        "s_sub_u32 s41, s50, s0\n"
        "s_waitcnt lgkmcnt(5)\n"
        "v_smfmac_f32_16x16x64_bf16 v[104:107], v[48:51], a[128:135], v10\n"
        "s_mul_i32 s41, s41, s1\n"
        "s_ashr_i32 s41, s41, 16\n"
        "v_smfmac_f32_16x16x64_bf16 v[108:111], v[48:51], a[136:143], v10\n"
        "s_add_u32 s50, s50, s42\n"
        "s_waitcnt lgkmcnt(4)\n"
        "v_smfmac_f32_16x16x64_bf16 v[104:107], v[52:55], a[160:167], v10\n"
        "s_add_u32 s50, s50, s41\n"
        "s_min_u32 s50, s50, -2\n"
        "v_smfmac_f32_16x16x64_bf16 v[108:111], v[52:55], a[168:175], v10\n"
        "v_mov_b32 v32, s50\n"
        "s_waitcnt lgkmcnt(3)\n"
        "v_smfmac_f32_16x16x64_bf16 v[104:107], v[56:59], a[192:199], v10\n"
        "s_mov_b64 exec, s[8:9]\n"
        "buffer_store_dword v32, v191, s[60:63], s47 offen sc1\n"
        "v_smfmac_f32_16x16x64_bf16 v[108:111], v[56:59], a[200:207], v10\n"
        "s_mov_b64 exec, -1\n"
        "s_waitcnt lgkmcnt(2)\n"
        "v_smfmac_f32_16x16x64_bf16 v[104:107], v[60:63], a[224:231], v10\n"
        "s_add_u32 s47, s47, 16384\n"
        "ds_read_b128 v[120:123], v11 offset:0\n"
        "v_smfmac_f32_16x16x64_bf16 v[108:111], v[60:63], a[232:239], v10\n"
        "ds_read_b128 v[136:139], v12 offset:0\n"
        "s_waitcnt lgkmcnt(3)\n"
        "v_smfmac_f32_16x16x64_bf16 v[104:107], v[64:67], v[192:199], v10\n"
        "ds_read_b128 v[124:127], v11 offset:2048\n"
        "v_smfmac_f32_16x16x64_bf16 v[108:111], v[64:67], v[200:207], v10\n"
        "ds_read_b128 v[140:143], v12 offset:2048\n"
        "s_waitcnt lgkmcnt(4)\n"
        "v_smfmac_f32_16x16x64_bf16 v[104:107], v[68:71], v[224:231], v10\n"
        "ds_read_b128 v[128:131], v11 offset:4096\n"
        "v_smfmac_f32_16x16x64_bf16 v[108:111], v[68:71], v[232:239], v10\n"
        "ds_read_b128 v[144:147], v12 offset:4096\n"
        "v_smfmac_f32_16x16x64_bf16 v[116:119], v[24:27], a[56:63], v10\n"
        "ds_read_b128 v[132:135], v11 offset:6144\n"
        "ds_read_b128 v[148:151], v12 offset:6144\n"
        "v_smfmac_f32_16x16x64_bf16 v[112:115], v[28:31], a[80:87], v10\n"
        "v_pk_add_f32 v[104:105], v[104:105], v[106:107]\n"
        "v_pk_add_f32 v[108:109], v[108:109], v[110:111]\n"
        "v_cvt_pk_bf16_f32 v104, v104, v105\n"
        "v_cvt_pk_bf16_f32 v105, v108, v109\n"
        "ds_write2st64_b32 v6, v104, v105 offset0:0 offset1:1\n"
        "v_smfmac_f32_16x16x64_bf16 v[116:119], v[28:31], a[88:95], v10\n"
        "v_smfmac_f32_16x16x64_bf16 v[112:115], v[44:47], a[112:119], v10\n"
        "ds_read_b64_tr_b16 v[72:73], v7 offset:0\n"
        "ds_read_b64_tr_b16 v[74:75], v7 offset:64\n"
        "v_smfmac_f32_16x16x64_bf16 v[116:119], v[44:47], a[120:127], v10\n"
        "v_smfmac_f32_16x16x64_bf16 v[112:115], v[48:51], a[144:151], v10\n"
        "v_smfmac_f32_16x16x64_bf16 v[116:119], v[48:51], a[152:159], v10\n"
        "v_smfmac_f32_16x16x64_bf16 v[112:115], v[52:55], a[176:183], v10\n"
        "s_barrier\n"
        "ds_read_b64_tr_b16 v[76:77], v7 offset:512\n"
        "ds_read_b64_tr_b16 v[78:79], v7 offset:576\n"
        "v_smfmac_f32_16x16x64_bf16 v[116:119], v[52:55], a[184:191], v10\n"
        "ds_read_b64_tr_b16 v[80:81], v7 offset:1024\n"
        "ds_read_b64_tr_b16 v[82:83], v7 offset:1088\n"
        "v_smfmac_f32_16x16x64_bf16 v[112:115], v[56:59], a[208:215], v10\n"
        "ds_read_b64_tr_b16 v[84:85], v7 offset:1536\n"
        "ds_read_b64_tr_b16 v[86:87], v7 offset:1600\n"
        "v_smfmac_f32_16x16x64_bf16 v[116:119], v[56:59], a[216:223], v10\n"
        "v_smfmac_f32_16x16x64_bf16 v[112:115], v[60:63], a[240:247], v10\n"
        "v_smfmac_f32_16x16x64_bf16 v[116:119], v[60:63], a[248:255], v10\n"
        "v_smfmac_f32_16x16x64_bf16 v[112:115], v[64:67], v[208:215], v10\n"
        "v_smfmac_f32_16x16x64_bf16 v[116:119], v[64:67], v[216:223], v10\n"
        "v_smfmac_f32_16x16x64_bf16 v[112:115], v[68:71], v[240:247], v10\n"
        "v_smfmac_f32_16x16x64_bf16 v[116:119], v[68:71], v[248:255], v10\n"
        "s_waitcnt lgkmcnt(6)\n"
        "v_mfma_f32_16x16x32_bf16 v[168:171], v[120:123], v[72:75], 0\n"
        "v_mfma_f32_16x16x32_bf16 v[172:175], v[136:139], v[72:75], 0\n"
        "v_pk_add_f32 v[112:113], v[112:113], v[114:115]\n"
        "s_waitcnt lgkmcnt(4)\n"
        "v_mfma_f32_16x16x32_bf16 v[168:171], v[124:127], v[76:79], v[168:171]\n"
        "v_pk_add_f32 v[116:117], v[116:117], v[118:119]\n"
        "v_mfma_f32_16x16x32_bf16 v[172:175], v[140:143], v[76:79], v[172:175]\n"
        "v_cvt_pk_bf16_f32 v112, v112, v113\n"
        "v_cvt_pk_bf16_f32 v113, v116, v117\n"
        "s_waitcnt lgkmcnt(2)\n"
        "v_mfma_f32_16x16x32_bf16 v[168:171], v[128:131], v[80:83], v[168:171]\n"
        "ds_write2st64_b32 v6, v112, v113 offset0:8 offset1:9\n"
        "v_mfma_f32_16x16x32_bf16 v[172:175], v[144:147], v[80:83], v[172:175]\n"
        "ds_read_b64_tr_b16 v[88:89], v7 offset:2048\n"
        "ds_read_b64_tr_b16 v[90:91], v7 offset:2112\n"
        "s_waitcnt lgkmcnt(3)\n"
        "v_mfma_f32_16x16x32_bf16 v[168:171], v[132:135], v[84:87], v[168:171]\n"
        "s_cmp_eq_u32 s64, 63\n"
        "v_mfma_f32_16x16x32_bf16 v[172:175], v[148:151], v[84:87], v[172:175]\n"
        "s_barrier\n"
        "s_cbranch_scc1 R10_last\n"
        "ds_read_b64_tr_b16 v[92:93], v7 offset:2560\n"
        "ds_read_b64_tr_b16 v[94:95], v7 offset:2624\n"
        "ds_read_b64_tr_b16 v[96:97], v7 offset:3072\n"
        "ds_read_b64_tr_b16 v[98:99], v7 offset:3136\n"
        "ds_read_b64_tr_b16 v[100:101], v7 offset:3584\n"
        "ds_read_b64_tr_b16 v[102:103], v7 offset:3648\n"
        "buffer_load_dword v104, v2, s[60:63], s69 offen sc1\n"
        "v_max3_f32 v188, v168, v169, v170\n"
        "v_max3_f32 v190, v171, v172, v173\n"
        "v_max3_f32 v188, v188, v174, v175\n"
        "v_max_f32 v188, v188, v190\n"
        "v_mul_f32 v190, 0xbe0293ee, v188\n"
        "v_fmamk_f32 v180, v168, 0x3e0293ee, v190\n"
        "v_fmamk_f32 v181, v169, 0x3e0293ee, v190\n"
        "v_fmamk_f32 v182, v170, 0x3e0293ee, v190\n"
        "v_fmamk_f32 v183, v171, 0x3e0293ee, v190\n"
        "v_fmamk_f32 v184, v172, 0x3e0293ee, v190\n"
        "v_fmamk_f32 v185, v173, 0x3e0293ee, v190\n"
        "v_fmamk_f32 v186, v174, 0x3e0293ee, v190\n"
        "v_fmamk_f32 v187, v175, 0x3e0293ee, v190\n"
        "s_waitcnt lgkmcnt(6)\n"
        "v_mfma_f32_16x16x32_bf16 v[176:179], v[88:91], v[72:75], 0\n"
        "v_exp_f32 v180, v180\n"
        "s_waitcnt lgkmcnt(4)\n"
        "v_mfma_f32_16x16x32_bf16 v[176:179], v[92:95], v[76:79], v[176:179]\n"
        "v_exp_f32 v181, v181\n"
        "s_waitcnt lgkmcnt(2)\n"
        "v_mfma_f32_16x16x32_bf16 v[176:179], v[96:99], v[80:83], v[176:179]\n"
        "v_exp_f32 v182, v182\n"
        "s_waitcnt lgkmcnt(0)\n"
        "v_mfma_f32_16x16x32_bf16 v[176:179], v[100:103], v[84:87], v[176:179]\n"
        "v_exp_f32 v183, v183\n"
        "v_exp_f32 v184, v184\n"
        "v_exp_f32 v185, v185\n"
        "v_exp_f32 v186, v186\n"
        "v_exp_f32 v187, v187\n"
        "v_pk_add_f32 v[26:27], v[180:181], v[182:183]\n"
        "v_pk_add_f32 v[28:29], v[184:185], v[186:187]\n"
        "v_pk_add_f32 v[26:27], v[26:27], v[28:29]\n"
        "v_cndmask_b32 v176, v176, v178, s[76:77]\n"
        "v_cndmask_b32 v177, v177, v179, s[76:77]\n"
        "v_max3_f32 v20, v176, v177, v188\n"
        "v_mov_b32 v21, v20\n"
        "v_add_f32 v189, v26, v27\n"
        "s_nop 0\n"
        "v_permlane16_swap_b32 v20, v21\n"
        "v_max_f32 v20, v20, v21\n"
        "v_mov_b32 v21, v20\n"
        "s_nop 1\n"
        "v_permlane32_swap_b32 v20, v21\n"
        "v_max_f32 v20, v20, v21\n"
        "v_mul_f32 v20, 0xbe0293ee, v20\n"
        "v_fmamk_f32 v22, v176, 0x3e0293ee, v20\n"
        "v_fmamk_f32 v23, v177, 0x3e0293ee, v20\n"
        "v_fmamk_f32 v26, v188, 0x3e0293ee, v20\n"
        "v_exp_f32 v22, v22\n"
        "v_exp_f32 v23, v23\n"
        "v_exp_f32 v26, v26\n"
        "v_add_f32 v27, v22, v23\n"
        "v_fmac_f32 v27, v26, v189\n"
        "v_mov_b32 v28, v27\n"
        "s_nop 1\n"
        "v_permlane16_swap_b32 v27, v28\n"
        "v_add_f32 v27, v27, v28\n"
        "v_mov_b32 v28, v27\n"
        "s_nop 1\n"
        "v_permlane32_swap_b32 v27, v28\n"
        "v_add_f32 v27, v27, v28\n"
        "v_rcp_f32 v28, v27\n"
        "s_mov_b64 exec, s[70:71]\n"
        "v_mul_f32 v30, v28, v26\n"
        "v_pk_mul_f32 v[180:181], v[180:181], v[30:31] op_sel_hi:[1,0] clamp\n"
        "v_pk_mul_f32 v[182:183], v[182:183], v[30:31] op_sel_hi:[1,0] clamp\n"
        "v_pk_mul_f32 v[184:185], v[184:185], v[30:31] op_sel_hi:[1,0] clamp\n"
        "v_pk_mul_f32 v[186:187], v[186:187], v[30:31] op_sel_hi:[1,0] clamp\n"
        "v_pk_mul_f32 v[22:23], v[22:23], v[28:29] op_sel_hi:[1,0] clamp\n"
        "v_cvt_pk_bf16_f32 v32, v180, v181\n"
        "v_cvt_pk_bf16_f32 v33, v182, v183\n"
        "v_cvt_pk_bf16_f32 v34, v184, v185\n"
        "v_cvt_pk_bf16_f32 v35, v186, v187\n"
        "buffer_store_dwordx4 v[32:35], v8, s[60:63], s68 offen sc1\n"
        "v_cvt_pk_bf16_f32 v36, v22, v23\n"
        "buffer_store_dword v36, v9, s[60:63], s68 offen sc1\n"
        "s_mov_b64 exec, -1\n"
        "s_memtime s[50:51]\n"
        "s_barrier\n"
        "s_add_u32 s65, s64, 2\n"
        "s_and_b32 s65, s65, 3\n"
        "s_mul_i32 s65, s65, 49152\n"
        "s_add_u32 s65, s65, 2097152\n"
        "s_mov_b64 exec, s[74:75]\n"
        "buffer_store_dwordx4 v[16:19], v14, s[60:63], s65 offen sc1\n"
        "s_mov_b64 exec, -1\n"
        "v_xor_b32 v11, 16384, v11\n"
        "v_xor_b32 v12, 16384, v12\n"
        "s_waitcnt vmcnt(3) lgkmcnt(0)\n"
        "s_sub_u32 s2, s50, s3\n"
        "s_min_u32 s2, s2, 1400\n"
        "R10_next:\n"
        "s_cmp_eq_u32 s64, 63\n"
        "s_cbranch_scc1 R10_h2\n"
        "R10_h0:\n"
        "s_nop 1\n"
        "v_max_u32_dpp v104, v104, v104 quad_perm:[1,0,3,2] row_mask:0xf bank_mask:0xf\n"
        "s_nop 1\n"
        "v_max_u32_dpp v104, v104, v104 quad_perm:[2,3,0,1] row_mask:0xf bank_mask:0xf\n"
        "s_nop 1\n"
        "v_max_u32_dpp v104, v104, v104 row_half_mirror row_mask:0xf bank_mask:0xf\n"
        "s_nop 1\n"
        "v_readlane_b32 s10, v104, 0\n"
        "v_readlane_b32 s11, v104, 8\n"
        "v_readlane_b32 s12, v104, 16\n"
        "v_readlane_b32 s13, v104, 24\n"
        "v_readlane_b32 s14, v104, 32\n"
        "v_readlane_b32 s15, v104, 40\n"
        "v_readlane_b32 s16, v104, 48\n"
        "v_readlane_b32 s17, v104, 56\n"
        "s_nop 1\n"
        "s_max_u32 s14, s14, s15\n"
        "s_max_u32 s16, s16, s17\n"
        "s_max_u32 s41, s14, s16\n"
        "s_cmp_eq_u32 s41, -1\n"
        "s_cselect_b32 s48, 0, s44\n"
        "s_sub_u32 s41, s41, s48\n"
        "s_min_u32 s14, s10, s11\n"
        "s_max_u32 s11, s10, s11\n"
        "s_min_u32 s10, s12, s13\n"
        "s_max_u32 s13, s12, s13\n"
        "s_min_u32 s12, s14, s10\n"
        "s_max_u32 s10, s14, s10\n"
        "s_min_u32 s14, s11, s13\n"
        "s_max_u32 s13, s11, s13\n"
        "s_min_u32 s11, s14, s10\n"
        "s_max_u32 s10, s14, s10\n"
        "s_add_u32 s12, s12, s34\n"
        "s_add_u32 s11, s11, s35\n"
        "s_add_u32 s10, s10, s36\n"
        "s_max_u32 s12, s12, s11\n"
        "s_max_u32 s10, s10, s13\n"
        "s_max_u32 s40, s12, s10\n"
        "s_cmp_lg_u32 s13, -1\n"
        "s_cselect_b32 s44, s40, s44\n"
        "s_cbranch_scc1 R10_h1\n"
        "s_cmp_eq_u32 s66, 0\n"
        "s_cbranch_scc1 R10_h1\n"
        "s_sub_u32 s66, s66, 1\n"
        "buffer_load_dword v104, v2, s[60:63], s69 offen sc1\n"
        "s_waitcnt vmcnt(0)\n"
        "s_branch R10_h0\n"
        "R10_h1:\n"
        "s_add_u32 s5, s5, 6\n"
        "s_min_u32 s5, s5, s41\n"
        "s_add_u32 s46, s5, s6\n"
        "R10_h2:\n"
        "s_add_u32 s64, s64, 1\n"
        "s_cmp_lt_u32 s64, 64\n"
        "s_cbranch_scc1 R10_loop\n"
        "s_endpgm\n"
        "R10_last:\n"
        "s_waitcnt lgkmcnt(0)\n"
        "s_barrier\n"
        "s_branch R10_h2\n"
        ".p2align 6\n"
        "s_nop 0\n"
        "s_nop 0\n"
        "s_nop 0\n"
        "s_nop 0\n"
        "s_nop 0\n"
        "s_nop 0\n"
        "R11_loop:\n"
        "v_mov_b32 v104, 0\n"
        "v_mov_b32 v105, 0\n"
        "v_mov_b32 v106, 0\n"
        "v_mov_b32 v107, 0\n"
        "v_mov_b32 v108, 0\n"
        "v_mov_b32 v109, 0\n"
        "v_mov_b32 v110, 0\n"
        "v_mov_b32 v111, 0\n"
        "v_mov_b32 v112, 0\n"
        "v_mov_b32 v113, 0\n"
        "v_mov_b32 v114, 0\n"
        "v_mov_b32 v115, 0\n"
        "v_mov_b32 v116, 0\n"
        "v_mov_b32 v117, 0\n"
        "v_mov_b32 v118, 0\n"
        "v_mov_b32 v119, 0\n"
        "s_lshl_b32 s68, s64, 5\n"
        "s_add_u32 s68, s68, s58\n"
        "s_mul_i32 s68, s68, 2560\n"
        "s_add_u32 s68, s68, 38797312\n"
        "s_lshl_b32 s69, s64, 14\n"
        "s_add_u32 s69, s69, s49\n"
        "s_and_b32 s65, s64, 3\n"
        "s_lshl_b32 s65, s65, 15\n"
        "s_add_u32 s65, s65, 2293760\n"
        "s_add_u32 s48, s44, s46\n"
        "s_sub_u32 s48, s48, s42\n"
        "s_sub_u32 s48, s48, s0\n"
        "s_max_i32 s48, s48, 0\n"
        "s_min_i32 s48, s48, 0x10000\n"
        "s_mul_i32 s40, s48, s1\n"
        "s_lshr_b32 s40, s40, 16\n"
        "s_sub_u32 s41, s48, s40\n"
        "s_mul_i32 s40, s41, s1\n"
        "s_lshr_b32 s40, s40, 16\n"
        "s_sub_u32 s48, s48, s40\n"
        "s_add_u32 s48, s48, s0\n"
        "s_sub_u32 s48, s48, 620\n"
        "s_memtime s[50:51]\n"
        "s_waitcnt lgkmcnt(0)\n"
        "s_sub_u32 s41, s50, s0\n"
        "s_sub_u32 s50, s50, s48\n"
        "s_cmp_gt_u32 s41, 12000\n"
        "s_cselect_b32 s50, 0, s50\n"
        "s_add_u32 s45, s48, 544\n"
        "s_cmp_gt_i32 s50, 150\n"
        "s_cselect_b32 s48, s45, s48\n"
        "s_cbranch_scc1 R11_twk\n"
        "s_cmp_gt_i32 s50, -60\n"
        "s_cbranch_scc1 R11_twy\n"
        "R11_twt:\n"
        "s_sleep 1\n"
        "s_memtime s[50:51]\n"
        "s_waitcnt lgkmcnt(0)\n"
        "s_sub_u32 s50, s50, s48\n"
        "s_cmp_lt_i32 s50, 0\n"
        "s_cbranch_scc1 R11_twt\n"
        "R11_twy:\n"
        "s_memtime s[10:11]\n"
        "s_memrealtime s[14:15]\n"
        "s_nop 2\n"
        "s_memrealtime s[16:17]\n"
        "s_nop 2\n"
        "s_memrealtime s[18:19]\n"
        "s_nop 2\n"
        "s_memrealtime s[20:21]\n"
        "s_nop 2\n"
        "s_memrealtime s[22:23]\n"
        "s_nop 2\n"
        "s_memrealtime s[24:25]\n"
        "s_nop 1\n"
        "s_memtime s[12:13]\n"
        "s_memrealtime s[26:27]\n"
        "s_waitcnt lgkmcnt(0)\n"
        "s_mul_i32 s14, s14, 24\n"
        "s_mul_i32 s16, s16, 24\n"
        "s_sub_u32 s16, s16, 17\n"
        "s_max_i32 s14, s14, s16\n"
        "s_mul_i32 s18, s18, 24\n"
        "s_sub_u32 s18, s18, 34\n"
        "s_max_i32 s14, s14, s18\n"
        "s_mul_i32 s20, s20, 24\n"
        "s_sub_u32 s20, s20, 51\n"
        "s_max_i32 s14, s14, s20\n"
        "s_mul_i32 s22, s22, 24\n"
        "s_sub_u32 s22, s22, 68\n"
        "s_max_i32 s14, s14, s22\n"
        "s_mul_i32 s24, s24, 24\n"
        "s_sub_u32 s24, s24, 85\n"
        "s_max_i32 s14, s14, s24\n"
        "s_mul_i32 s26, s26, 24\n"
        "s_sub_u32 s26, s26, 102\n"
        "s_max_i32 s14, s14, s26\n"
        "s_sub_u32 s14, s14, s10\n"
        "s_sub_u32 s41, s10, s0\n"
        "s_mov_b32 s0, s10\n"
        "s_mul_i32 s41, s41, s1\n"
        "s_lshr_b32 s41, s41, 16\n"
        "s_add_u32 s42, s42, s41\n"
        "s_sub_u32 s40, s12, s10\n"
        "s_sub_u32 s41, s14, s42\n"
        "s_sub_u32 s40, s40, 88\n"
        "s_cmp_gt_u32 s40, 32\n"
        "s_cselect_b32 s41, 0, s41\n"
        "s_add_u32 s42, s42, s41\n"
        "s_min_i32 s41, s41, 120\n"
        "s_max_i32 s41, s41, -120\n"
        "s_mul_i32 s41, s41, 7\n"
        "s_add_u32 s1, s1, s41\n"
        "s_max_i32 s1, s1, 0\n"
        "s_add_u32 s48, s44, s46\n"
        "s_sub_u32 s48, s48, s42\n"
        "s_sub_u32 s48, s48, s0\n"
        "s_max_i32 s48, s48, 0\n"
        "s_min_i32 s48, s48, 0x10000\n"
        "s_mul_i32 s40, s48, s1\n"
        "s_lshr_b32 s40, s40, 16\n"
        "s_sub_u32 s41, s48, s40\n"
        "s_mul_i32 s40, s41, s1\n"
        "s_lshr_b32 s40, s40, 16\n"
        "s_sub_u32 s48, s48, s40\n"
        "s_add_u32 s48, s48, s0\n"
        "s_sub_u32 s48, s48, 76\n"
        "R11_twk:\n"
        "R11_twsa:\n"
        "s_memtime s[50:51]\n"
        "s_getpc_b64 s[40:41]\n"
        "R11_twsb:\n"
        "s_waitcnt lgkmcnt(0)\n"
        "s_sub_u32 s50, s48, s50\n"
        "s_cmp_gt_i32 s50, 96\n"
        "s_cbranch_scc1 R11_twsa\n"
        "s_max_i32 s50, s50, 0\n"
        "s_and_b32 s50, s50, -4\n"
        "s_sub_u32 s50, R11_twse-R11_twsb, s50\n"
        "s_add_u32 s40, s40, s50\n"
        "s_addc_u32 s41, s41, 0\n"
        "s_setpc_b64 s[40:41]\n"
        "s_nop 0\n"
        "s_nop 0\n"
        "s_nop 0\n"
        "s_nop 0\n"
        "s_nop 0\n"
        "s_nop 0\n"
        "s_nop 0\n"
        "s_nop 0\n"
        "s_nop 0\n"
        "s_nop 0\n"
        "s_nop 0\n"
        "s_nop 0\n"
        "s_nop 0\n"
        "s_nop 0\n"
        "s_nop 0\n"
        "s_nop 0\n"
        "s_nop 0\n"
        "s_nop 0\n"
        "s_nop 0\n"
        "s_nop 0\n"
        "s_nop 0\n"
        "s_nop 0\n"
        "s_nop 0\n"
        "s_nop 0\n"
        "R11_twse:\n"
        "R11_poll:\n"
        "buffer_load_dwordx4 v[20:23], v3, s[60:63], s65 offen sc1\n"
        "buffer_load_dwordx4 v[24:27], v4, s[60:63], s65 offen sc1\n"
        "buffer_load_dwordx4 v[28:31], v5, s[60:63], s65 offen sc1\n"
        "s_waitcnt vmcnt(2)\n"
        "ds_write_b128 v1, v[20:23]\n"
        "v_max3_u32 v32, v20, v21, v22\n"
        "s_waitcnt vmcnt(1)\n"
        "ds_write_b128 v1, v[24:27] offset:1024\n"
        "v_max3_u32 v32, v32, v23, v24\n"
        "v_max3_u32 v32, v32, v25, v26\n"
        "s_waitcnt vmcnt(0)\n"
        "ds_write_b128 v1, v[28:31] offset:2048\n"
        "v_max3_u32 v32, v32, v27, v28\n"
        "v_max3_u32 v32, v32, v29, v30\n"
        "v_max_u32 v32, v32, v31\n"
        "v_cmp_eq_u32 vcc, -1, v32\n"
        "v_smfmac_f32_16x16x64_bf16 v[104:107], v[20:23], a[96:103], v10\n"
        "v_smfmac_f32_16x16x64_bf16 v[108:111], v[20:23], a[104:111], v10\n"
        "v_smfmac_f32_16x16x64_bf16 v[104:107], v[24:27], a[128:135], v10\n"
        "v_smfmac_f32_16x16x64_bf16 v[108:111], v[24:27], a[136:143], v10\n"
        "s_cbranch_vccnz R11_miss\n"
        "R11_got:\n"
        "s_waitcnt lgkmcnt(0)\n"
        "s_barrier\n"
        "ds_read_b128 v[32:35], v0 offset:0\n"
        "v_smfmac_f32_16x16x64_bf16 v[104:107], v[28:31], a[160:167], v10\n"
        "ds_read_b128 v[36:39], v0 offset:1024\n"
        "ds_read_b128 v[40:43], v0 offset:2048\n"
        "v_smfmac_f32_16x16x64_bf16 v[108:111], v[28:31], a[168:175], v10\n"
        "ds_read_b128 v[56:59], v0 offset:6144\n"
        "ds_read_b128 v[60:63], v0 offset:7168\n"
        "v_smfmac_f32_16x16x64_bf16 v[112:115], v[20:23], a[112:119], v10\n"
        "ds_read_b128 v[64:67], v0 offset:8192\n"
        "ds_read_b128 v[68:71], v0 offset:9216\n"
        "v_smfmac_f32_16x16x64_bf16 v[116:119], v[20:23], a[120:127], v10\n"
        "v_smfmac_f32_16x16x64_bf16 v[112:115], v[24:27], a[144:151], v10\n"
        "v_smfmac_f32_16x16x64_bf16 v[116:119], v[24:27], a[152:159], v10\n"
        "s_waitcnt lgkmcnt(6)\n"
        "v_smfmac_f32_16x16x64_bf16 v[104:107], v[32:35], a[0:7], v10\n"
        "v_smfmac_f32_16x16x64_bf16 v[108:111], v[32:35], a[8:15], v10\n"
        "s_waitcnt lgkmcnt(5)\n"
        "v_smfmac_f32_16x16x64_bf16 v[104:107], v[36:39], a[32:39], v10\n"
        "v_smfmac_f32_16x16x64_bf16 v[108:111], v[36:39], a[40:47], v10\n"
        "s_waitcnt lgkmcnt(4)\n"
        "v_smfmac_f32_16x16x64_bf16 v[104:107], v[40:43], a[64:71], v10\n"
        "v_smfmac_f32_16x16x64_bf16 v[108:111], v[40:43], a[72:79], v10\n"
        "s_waitcnt lgkmcnt(3)\n"
        "v_smfmac_f32_16x16x64_bf16 v[104:107], v[56:59], a[192:199], v10\n"
        "v_smfmac_f32_16x16x64_bf16 v[108:111], v[56:59], a[200:207], v10\n"
        "s_waitcnt lgkmcnt(2)\n"
        "v_smfmac_f32_16x16x64_bf16 v[104:107], v[60:63], a[224:231], v10\n"
        "v_smfmac_f32_16x16x64_bf16 v[108:111], v[60:63], a[232:239], v10\n"
        "s_waitcnt lgkmcnt(1)\n"
        "v_smfmac_f32_16x16x64_bf16 v[104:107], v[64:67], v[192:199], v10\n"
        "v_smfmac_f32_16x16x64_bf16 v[108:111], v[64:67], v[200:207], v10\n"
        "s_waitcnt lgkmcnt(0)\n"
        "v_smfmac_f32_16x16x64_bf16 v[104:107], v[68:71], v[224:231], v10\n"
        "v_smfmac_f32_16x16x64_bf16 v[108:111], v[68:71], v[232:239], v10\n"
        "v_smfmac_f32_16x16x64_bf16 v[112:115], v[28:31], a[176:183], v10\n"
        "v_smfmac_f32_16x16x64_bf16 v[116:119], v[28:31], a[184:191], v10\n"
        "v_pk_add_f32 v[104:105], v[104:105], v[106:107]\n"
        "v_pk_add_f32 v[108:109], v[108:109], v[110:111]\n"
        "v_cvt_pk_bf16_f32 v104, v104, v105\n"
        "v_cvt_pk_bf16_f32 v105, v108, v109\n"
        "ds_write2st64_b32 v6, v104, v105 offset0:0 offset1:1\n"
        "v_smfmac_f32_16x16x64_bf16 v[112:115], v[32:35], a[16:23], v10\n"
        "v_smfmac_f32_16x16x64_bf16 v[116:119], v[32:35], a[24:31], v10\n"
        "v_smfmac_f32_16x16x64_bf16 v[112:115], v[36:39], a[48:55], v10\n"
        "v_smfmac_f32_16x16x64_bf16 v[116:119], v[36:39], a[56:63], v10\n"
        "v_smfmac_f32_16x16x64_bf16 v[112:115], v[40:43], a[80:87], v10\n"
        "s_waitcnt lgkmcnt(0)\n"
        "s_barrier\n"
        "v_smfmac_f32_16x16x64_bf16 v[116:119], v[40:43], a[88:95], v10\n"
        "v_smfmac_f32_16x16x64_bf16 v[112:115], v[56:59], a[208:215], v10\n"
        "v_smfmac_f32_16x16x64_bf16 v[116:119], v[56:59], a[216:223], v10\n"
        "v_smfmac_f32_16x16x64_bf16 v[112:115], v[60:63], a[240:247], v10\n"
        "v_smfmac_f32_16x16x64_bf16 v[116:119], v[60:63], a[248:255], v10\n"
        "v_smfmac_f32_16x16x64_bf16 v[112:115], v[64:67], v[208:215], v10\n"
        "v_smfmac_f32_16x16x64_bf16 v[116:119], v[64:67], v[216:223], v10\n"
        "v_smfmac_f32_16x16x64_bf16 v[112:115], v[68:71], v[240:247], v10\n"
        "v_smfmac_f32_16x16x64_bf16 v[116:119], v[68:71], v[248:255], v10\n"
        "s_nop 7\n"
        "v_pk_add_f32 v[112:113], v[112:113], v[114:115]\n"
        "v_pk_add_f32 v[116:117], v[116:117], v[118:119]\n"
        "v_cvt_pk_bf16_f32 v112, v112, v113\n"
        "v_cvt_pk_bf16_f32 v113, v116, v117\n"
        "ds_write2st64_b32 v6, v112, v113 offset0:8 offset1:9\n"
        "s_waitcnt lgkmcnt(0)\n"
        "s_barrier\n"
        "buffer_load_dword v104, v2, s[60:63], s69 offen sc1\n"
        "s_sleep 2\n"
        "ds_read_b128 v[152:155], v13 offset:0\n"
        "ds_read_b128 v[120:123], v11 offset:0\n"
        "ds_read_b128 v[136:139], v12 offset:0\n"
        "ds_read_b128 v[156:159], v13 offset:2048\n"
        "ds_read_b128 v[124:127], v11 offset:2048\n"
        "ds_read_b128 v[140:143], v12 offset:2048\n"
        "ds_read_b128 v[160:163], v13 offset:4096\n"
        "ds_read_b128 v[128:131], v11 offset:4096\n"
        "ds_read_b128 v[144:147], v12 offset:4096\n"
        "ds_read_b128 v[164:167], v13 offset:6144\n"
        "ds_read_b128 v[132:135], v11 offset:6144\n"
        "ds_read_b128 v[148:151], v12 offset:6144\n"
        "v_xor_b32 v11, 16384, v11\n"
        "v_xor_b32 v12, 16384, v12\n"
        "v_xor_b32 v13, 16384, v13\n"
        "s_waitcnt lgkmcnt(9)\n"
        "v_mfma_f32_16x16x32_bf16 v[168:171], v[120:123], v[152:155], 0\n"
        "v_mfma_f32_16x16x32_bf16 v[172:175], v[136:139], v[152:155], 0\n"
        "s_waitcnt lgkmcnt(6)\n"
        "v_mfma_f32_16x16x32_bf16 v[168:171], v[124:127], v[156:159], v[168:171]\n"
        "v_mfma_f32_16x16x32_bf16 v[172:175], v[140:143], v[156:159], v[172:175]\n"
        "s_waitcnt lgkmcnt(3)\n"
        "v_mfma_f32_16x16x32_bf16 v[168:171], v[128:131], v[160:163], v[168:171]\n"
        "v_mfma_f32_16x16x32_bf16 v[172:175], v[144:147], v[160:163], v[172:175]\n"
        "s_waitcnt lgkmcnt(0)\n"
        "v_mfma_f32_16x16x32_bf16 v[168:171], v[132:135], v[164:167], v[168:171]\n"
        "v_mfma_f32_16x16x32_bf16 v[172:175], v[148:151], v[164:167], v[172:175]\n"
        "s_nop 7\n"
        "v_cndmask_b32 v168, v168, v16, s[76:77]\n"
        "v_cndmask_b32 v169, v169, v16, s[78:79]\n"
        "v_cndmask_b32 v170, v170, v16, s[80:81]\n"
        "v_cndmask_b32 v171, v171, v16, s[82:83]\n"
        "v_cndmask_b32 v172, v172, v16, s[84:85]\n"
        "v_cndmask_b32 v173, v173, v16, s[86:87]\n"
        "v_cndmask_b32 v174, v174, v16, s[88:89]\n"
        "v_cndmask_b32 v175, v175, v16, s[90:91]\n"
        "v_max3_f32 v188, v168, v169, v170\n"
        "v_max3_f32 v190, v171, v172, v173\n"
        "v_max3_f32 v188, v188, v174, v175\n"
        "v_max_f32 v188, v188, v190\n"
        "v_mul_f32 v190, 0xbe0293ee, v188\n"
        "v_fmamk_f32 v180, v168, 0x3e0293ee, v190\n"
        "v_fmamk_f32 v181, v169, 0x3e0293ee, v190\n"
        "v_fmamk_f32 v182, v170, 0x3e0293ee, v190\n"
        "v_fmamk_f32 v183, v171, 0x3e0293ee, v190\n"
        "v_fmamk_f32 v184, v172, 0x3e0293ee, v190\n"
        "v_fmamk_f32 v185, v173, 0x3e0293ee, v190\n"
        "v_fmamk_f32 v186, v174, 0x3e0293ee, v190\n"
        "v_fmamk_f32 v187, v175, 0x3e0293ee, v190\n"
        "v_exp_f32 v180, v180\n"
        "v_exp_f32 v181, v181\n"
        "v_exp_f32 v182, v182\n"
        "v_exp_f32 v183, v183\n"
        "v_exp_f32 v184, v184\n"
        "v_exp_f32 v185, v185\n"
        "v_exp_f32 v186, v186\n"
        "v_exp_f32 v187, v187\n"
        "s_nop 0\n"
        "v_pk_add_f32 v[20:21], v[180:181], v[182:183]\n"
        "v_pk_add_f32 v[20:21], v[20:21], v[184:185]\n"
        "v_pk_add_f32 v[20:21], v[20:21], v[186:187]\n"
        "v_add_f32 v189, v20, v21\n"
        "s_barrier\n"
        "ds_read_b64_tr_b16 v[88:89], v7 offset:2048\n"
        "ds_read_b64_tr_b16 v[90:91], v7 offset:2112\n"
        "ds_read_b64_tr_b16 v[92:93], v7 offset:2560\n"
        "ds_read_b64_tr_b16 v[94:95], v7 offset:2624\n"
        "ds_read_b64_tr_b16 v[96:97], v7 offset:3072\n"
        "ds_read_b64_tr_b16 v[98:99], v7 offset:3136\n"
        "ds_read_b64_tr_b16 v[100:101], v7 offset:3584\n"
        "ds_read_b64_tr_b16 v[102:103], v7 offset:3648\n"
        "s_waitcnt lgkmcnt(0)\n"
        "v_mfma_f32_16x16x32_bf16 v[176:179], v[88:91], v[152:155], 0\n"
        "v_mfma_f32_16x16x32_bf16 v[176:179], v[92:95], v[156:159], v[176:179]\n"
        "v_mfma_f32_16x16x32_bf16 v[176:179], v[96:99], v[160:163], v[176:179]\n"
        "v_mfma_f32_16x16x32_bf16 v[176:179], v[100:103], v[164:167], v[176:179]\n"
        "s_nop 7\n"
        "v_max3_f32 v20, v176, v177, v178\n"
        "v_max3_f32 v20, v20, v179, v188\n"
        "v_mov_b32 v21, v20\n"
        "s_nop 1\n"
        "v_permlane16_swap_b32 v20, v21\n"
        "v_max_f32 v20, v20, v21\n"
        "v_mov_b32 v21, v20\n"
        "s_nop 1\n"
        "v_permlane32_swap_b32 v20, v21\n"
        "v_max_f32 v20, v20, v21\n"
        "v_mul_f32 v20, 0xbe0293ee, v20\n"
        "v_fmamk_f32 v22, v176, 0x3e0293ee, v20\n"
        "v_fmamk_f32 v23, v177, 0x3e0293ee, v20\n"
        "v_fmamk_f32 v24, v178, 0x3e0293ee, v20\n"
        "v_fmamk_f32 v25, v179, 0x3e0293ee, v20\n"
        "v_fmamk_f32 v26, v188, 0x3e0293ee, v20\n"
        "v_exp_f32 v22, v22\n"
        "v_exp_f32 v23, v23\n"
        "v_exp_f32 v24, v24\n"
        "v_exp_f32 v25, v25\n"
        "v_exp_f32 v26, v26\n"
        "v_pk_add_f32 v[28:29], v[22:23], v[24:25]\n"
        "v_mul_f32 v27, v26, v189\n"
        "v_add_f32 v28, v28, v29\n"
        "v_fmac_f32 v27, 0.5, v28\n"
        "v_mov_b32 v28, v27\n"
        "s_nop 1\n"
        "v_permlane16_swap_b32 v27, v28\n"
        "v_add_f32 v27, v27, v28\n"
        "v_mov_b32 v28, v27\n"
        "s_nop 1\n"
        "v_permlane32_swap_b32 v27, v28\n"
        "v_add_f32 v27, v27, v28\n"
        "v_rcp_f32 v28, v27\n"
        "s_nop 0\n"
        "v_mul_f32 v30, v28, v26\n"
        "v_pk_mul_f32 v[22:23], v[22:23], v[28:29] op_sel_hi:[1,0] clamp\n"
        "v_pk_mul_f32 v[24:25], v[24:25], v[28:29] op_sel_hi:[1,0] clamp\n"
        "v_pk_mul_f32 v[180:181], v[180:181], v[30:31] op_sel_hi:[1,0] clamp\n"
        "v_pk_mul_f32 v[182:183], v[182:183], v[30:31] op_sel_hi:[1,0] clamp\n"
        "v_pk_mul_f32 v[184:185], v[184:185], v[30:31] op_sel_hi:[1,0] clamp\n"
        "v_pk_mul_f32 v[186:187], v[186:187], v[30:31] op_sel_hi:[1,0] clamp\n"
        "v_cvt_pk_bf16_f32 v36, v22, v23\n"
        "v_cvt_pk_bf16_f32 v37, v24, v25\n"
        "v_cvt_pk_bf16_f32 v38, v22, v23\n"
        "v_cvt_pk_bf16_f32 v39, v24, v25\n"
        "v_cvt_pk_bf16_f32 v32, v180, v181\n"
        "v_cvt_pk_bf16_f32 v33, v182, v183\n"
        "v_cvt_pk_bf16_f32 v34, v184, v185\n"
        "v_cvt_pk_bf16_f32 v35, v186, v187\n"
        "v_permlane16_swap_b32 v36, v38\n"
        "v_permlane16_swap_b32 v37, v39\n"
        "s_mov_b64 exec, s[70:71]\n"
        "buffer_store_dwordx4 v[32:35], v8, s[60:63], s68 offen sc1\n"
        "s_mov_b64 exec, s[72:73]\n"
        "buffer_store_dwordx4 v[36:39], v9, s[60:63], s68 offen sc1\n"
        "s_mov_b64 exec, -1\n"
        "s_waitcnt vmcnt(2)\n"
        "R11_next:\n"
        "s_cmp_eq_u32 s64, 63\n"
        "s_cbranch_scc1 R11_h2\n"
        "R11_h0:\n"
        "s_nop 1\n"
        "v_max_u32_dpp v104, v104, v104 quad_perm:[1,0,3,2] row_mask:0xf bank_mask:0xf\n"
        "s_nop 1\n"
        "v_max_u32_dpp v104, v104, v104 quad_perm:[2,3,0,1] row_mask:0xf bank_mask:0xf\n"
        "s_nop 1\n"
        "v_max_u32_dpp v104, v104, v104 row_half_mirror row_mask:0xf bank_mask:0xf\n"
        "s_nop 1\n"
        "v_readlane_b32 s10, v104, 0\n"
        "v_readlane_b32 s11, v104, 8\n"
        "v_readlane_b32 s12, v104, 16\n"
        "v_readlane_b32 s13, v104, 24\n"
        "v_readlane_b32 s14, v104, 32\n"
        "v_readlane_b32 s15, v104, 40\n"
        "v_readlane_b32 s16, v104, 48\n"
        "v_readlane_b32 s17, v104, 56\n"
        "s_nop 1\n"
        "s_max_u32 s14, s14, s15\n"
        "s_max_u32 s16, s16, s17\n"
        "s_max_u32 s41, s14, s16\n"
        "s_cmp_eq_u32 s41, -1\n"
        "s_cselect_b32 s48, 0, s44\n"
        "s_sub_u32 s41, s41, s48\n"
        "s_min_u32 s14, s10, s11\n"
        "s_max_u32 s11, s10, s11\n"
        "s_min_u32 s10, s12, s13\n"
        "s_max_u32 s13, s12, s13\n"
        "s_min_u32 s12, s14, s10\n"
        "s_max_u32 s10, s14, s10\n"
        "s_min_u32 s14, s11, s13\n"
        "s_max_u32 s13, s11, s13\n"
        "s_min_u32 s11, s14, s10\n"
        "s_max_u32 s10, s14, s10\n"
        "s_add_u32 s12, s12, s34\n"
        "s_add_u32 s11, s11, s35\n"
        "s_add_u32 s10, s10, s36\n"
        "s_max_u32 s12, s12, s11\n"
        "s_max_u32 s10, s10, s13\n"
        "s_max_u32 s40, s12, s10\n"
        "s_cmp_lg_u32 s13, -1\n"
        "s_cselect_b32 s44, s40, s44\n"
        "s_cbranch_scc1 R11_h1\n"
        "s_cmp_eq_u32 s66, 0\n"
        "s_cbranch_scc1 R11_h1\n"
        "s_sub_u32 s66, s66, 1\n"
        "buffer_load_dword v104, v2, s[60:63], s69 offen sc1\n"
        "s_waitcnt vmcnt(0)\n"
        "s_branch R11_h0\n"
        "R11_h1:\n"
        "s_add_u32 s5, s5, 6\n"
        "s_min_u32 s5, s5, s41\n"
        "s_add_u32 s46, s5, s6\n"
        "R11_h2:\n"
        "s_add_u32 s64, s64, 1\n"
        "s_cmp_lt_u32 s64, 64\n"
        "s_cbranch_scc1 R11_loop\n"
        "s_endpgm\n"
        ".p2align 6\n"
        "s_nop 0\n"
        "s_nop 0\n"
        "s_nop 0\n"
        "s_nop 0\n"
        "s_nop 0\n"
        "s_nop 0\n"
        "R12_loop:\n"
        "v_mov_b32 v104, 0\n"
        "v_mov_b32 v105, 0\n"
        "v_mov_b32 v106, 0\n"
        "v_mov_b32 v107, 0\n"
        "v_mov_b32 v108, 0\n"
        "v_mov_b32 v109, 0\n"
        "v_mov_b32 v110, 0\n"
        "v_mov_b32 v111, 0\n"
        "v_mov_b32 v112, 0\n"
        "v_mov_b32 v113, 0\n"
        "v_mov_b32 v114, 0\n"
        "v_mov_b32 v115, 0\n"
        "v_mov_b32 v116, 0\n"
        "v_mov_b32 v117, 0\n"
        "v_mov_b32 v118, 0\n"
        "v_mov_b32 v119, 0\n"
        "s_lshl_b32 s68, s64, 5\n"
        "s_add_u32 s68, s68, s58\n"
        "s_mul_i32 s68, s68, 2560\n"
        "s_add_u32 s68, s68, 38797312\n"
        "s_lshl_b32 s69, s64, 14\n"
        "s_add_u32 s69, s69, s49\n"
        "s_and_b32 s65, s64, 3\n"
        "s_lshl_b32 s65, s65, 15\n"
        "s_add_u32 s65, s65, 2293760\n"
        "s_add_u32 s48, s44, s46\n"
        "s_sub_u32 s48, s48, s42\n"
        "s_sub_u32 s48, s48, s0\n"
        "s_max_i32 s48, s48, 0\n"
        "s_min_i32 s48, s48, 0x10000\n"
        "s_mul_i32 s40, s48, s1\n"
        "s_lshr_b32 s40, s40, 16\n"
        "s_sub_u32 s41, s48, s40\n"
        "s_mul_i32 s40, s41, s1\n"
        "s_lshr_b32 s40, s40, 16\n"
        "s_sub_u32 s48, s48, s40\n"
        "s_add_u32 s48, s48, s0\n"
        "s_sub_u32 s48, s48, 620\n"
        "s_memtime s[50:51]\n"
        "s_waitcnt lgkmcnt(0)\n"
        "s_sub_u32 s41, s50, s0\n"
        "s_sub_u32 s50, s50, s48\n"
        "s_cmp_gt_u32 s41, 12000\n"
        "s_cselect_b32 s50, 0, s50\n"
        "s_add_u32 s45, s48, 544\n"
        "s_cmp_gt_i32 s50, 150\n"
        "s_cselect_b32 s48, s45, s48\n"
        "s_cbranch_scc1 R12_twk\n"
        "s_cmp_gt_i32 s50, -60\n"
        "s_cbranch_scc1 R12_twy\n"
        "R12_twt:\n"
        "s_sleep 1\n"
        "s_memtime s[50:51]\n"
        "s_waitcnt lgkmcnt(0)\n"
        "s_sub_u32 s50, s50, s48\n"
        "s_cmp_lt_i32 s50, 0\n"
        "s_cbranch_scc1 R12_twt\n"
        "R12_twy:\n"
        "s_memtime s[10:11]\n"
        "s_memrealtime s[14:15]\n"
        "s_nop 2\n"
        "s_memrealtime s[16:17]\n"
        "s_nop 2\n"
        "s_memrealtime s[18:19]\n"
        "s_nop 2\n"
        "s_memrealtime s[20:21]\n"
        "s_nop 2\n"
        "s_memrealtime s[22:23]\n"
        "s_nop 2\n"
        "s_memrealtime s[24:25]\n"
        "s_nop 1\n"
        "s_memtime s[12:13]\n"
        "s_memrealtime s[26:27]\n"
        "s_waitcnt lgkmcnt(0)\n"
        "s_mul_i32 s14, s14, 24\n"
        "s_mul_i32 s16, s16, 24\n"
        "s_sub_u32 s16, s16, 17\n"
        "s_max_i32 s14, s14, s16\n"
        "s_mul_i32 s18, s18, 24\n"
        "s_sub_u32 s18, s18, 34\n"
        "s_max_i32 s14, s14, s18\n"
        "s_mul_i32 s20, s20, 24\n"
        "s_sub_u32 s20, s20, 51\n"
        "s_max_i32 s14, s14, s20\n"
        "s_mul_i32 s22, s22, 24\n"
        "s_sub_u32 s22, s22, 68\n"
        "s_max_i32 s14, s14, s22\n"
        "s_mul_i32 s24, s24, 24\n"
        "s_sub_u32 s24, s24, 85\n"
        "s_max_i32 s14, s14, s24\n"
        "s_mul_i32 s26, s26, 24\n"
        "s_sub_u32 s26, s26, 102\n"
        "s_max_i32 s14, s14, s26\n"
        "s_sub_u32 s14, s14, s10\n"
        "s_sub_u32 s41, s10, s0\n"
        "s_mov_b32 s0, s10\n"
        "s_mul_i32 s41, s41, s1\n"
        "s_lshr_b32 s41, s41, 16\n"
        "s_add_u32 s42, s42, s41\n"
        "s_sub_u32 s40, s12, s10\n"
        "s_sub_u32 s41, s14, s42\n"
        "s_sub_u32 s40, s40, 88\n"
        "s_cmp_gt_u32 s40, 32\n"
        "s_cselect_b32 s41, 0, s41\n"
        "s_add_u32 s42, s42, s41\n"
        "s_min_i32 s41, s41, 120\n"
        "s_max_i32 s41, s41, -120\n"
        "s_mul_i32 s41, s41, 7\n"
        "s_add_u32 s1, s1, s41\n"
        "s_max_i32 s1, s1, 0\n"
        "s_add_u32 s48, s44, s46\n"
        "s_sub_u32 s48, s48, s42\n"
        "s_sub_u32 s48, s48, s0\n"
        "s_max_i32 s48, s48, 0\n"
        "s_min_i32 s48, s48, 0x10000\n"
        "s_mul_i32 s40, s48, s1\n"
        "s_lshr_b32 s40, s40, 16\n"
        "s_sub_u32 s41, s48, s40\n"
        "s_mul_i32 s40, s41, s1\n"
        "s_lshr_b32 s40, s40, 16\n"
        "s_sub_u32 s48, s48, s40\n"
        "s_add_u32 s48, s48, s0\n"
        "s_sub_u32 s48, s48, 76\n"
        "R12_twk:\n"
        "R12_twsa:\n"
        "s_memtime s[50:51]\n"
        "s_getpc_b64 s[40:41]\n"
        "R12_twsb:\n"
        "s_waitcnt lgkmcnt(0)\n"
        "s_sub_u32 s50, s48, s50\n"
        "s_cmp_gt_i32 s50, 96\n"
        "s_cbranch_scc1 R12_twsa\n"
        "s_max_i32 s50, s50, 0\n"
        "s_and_b32 s50, s50, -4\n"
        "s_sub_u32 s50, R12_twse-R12_twsb, s50\n"
        "s_add_u32 s40, s40, s50\n"
        "s_addc_u32 s41, s41, 0\n"
        "s_setpc_b64 s[40:41]\n"
        "s_nop 0\n"
        "s_nop 0\n"
        "s_nop 0\n"
        "s_nop 0\n"
        "s_nop 0\n"
        "s_nop 0\n"
        "s_nop 0\n"
        "s_nop 0\n"
        "s_nop 0\n"
        "s_nop 0\n"
        "s_nop 0\n"
        "s_nop 0\n"
        "s_nop 0\n"
        "s_nop 0\n"
        "s_nop 0\n"
        "s_nop 0\n"
        "s_nop 0\n"
        "s_nop 0\n"
        "s_nop 0\n"
        "s_nop 0\n"
        "s_nop 0\n"
        "s_nop 0\n"
        "s_nop 0\n"
        "s_nop 0\n"
        "R12_twse:\n"
        "R12_poll:\n"
        "buffer_load_dwordx4 v[20:23], v3, s[60:63], s65 offen sc1\n"
        "buffer_load_dwordx4 v[24:27], v4, s[60:63], s65 offen sc1\n"
        "s_waitcnt vmcnt(1)\n"
        "ds_write_b128 v1, v[20:23]\n"
        "v_max3_u32 v32, v20, v21, v22\n"
        "s_waitcnt vmcnt(0)\n"
        "ds_write_b128 v1, v[24:27] offset:1024\n"
        "v_max3_u32 v32, v32, v23, v24\n"
        "v_max3_u32 v32, v32, v25, v26\n"
        "v_max_u32 v32, v32, v27\n"
        "v_cmp_eq_u32 vcc, -1, v32\n"
        "v_smfmac_f32_16x16x64_bf16 v[104:107], v[20:23], a[192:199], v10\n"
        "v_smfmac_f32_16x16x64_bf16 v[108:111], v[20:23], a[200:207], v10\n"
        "v_smfmac_f32_16x16x64_bf16 v[104:107], v[24:27], a[224:231], v10\n"
        "v_smfmac_f32_16x16x64_bf16 v[108:111], v[24:27], a[232:239], v10\n"
        "s_cbranch_vccnz R12_miss\n"
        "R12_got:\n"
        "s_waitcnt lgkmcnt(0)\n"
        "s_barrier\n"
        "ds_read_b128 v[32:35], v0 offset:0\n"
        "v_smfmac_f32_16x16x64_bf16 v[112:115], v[20:23], a[208:215], v10\n"
        "ds_read_b128 v[36:39], v0 offset:1024\n"
        "ds_read_b128 v[40:43], v0 offset:2048\n"
        "v_smfmac_f32_16x16x64_bf16 v[116:119], v[20:23], a[216:223], v10\n"
        "ds_read_b128 v[44:47], v0 offset:3072\n"
        "ds_read_b128 v[48:51], v0 offset:4096\n"
        "v_smfmac_f32_16x16x64_bf16 v[112:115], v[24:27], a[240:247], v10\n"
        "ds_read_b128 v[52:55], v0 offset:5120\n"
        "ds_read_b128 v[64:67], v0 offset:8192\n"
        "v_smfmac_f32_16x16x64_bf16 v[116:119], v[24:27], a[248:255], v10\n"
        "ds_read_b128 v[68:71], v0 offset:9216\n"
        "s_waitcnt lgkmcnt(7)\n"
        "v_smfmac_f32_16x16x64_bf16 v[104:107], v[32:35], a[0:7], v10\n"
        "v_smfmac_f32_16x16x64_bf16 v[108:111], v[32:35], a[8:15], v10\n"
        "s_waitcnt lgkmcnt(6)\n"
        "v_smfmac_f32_16x16x64_bf16 v[104:107], v[36:39], a[32:39], v10\n"
        "v_smfmac_f32_16x16x64_bf16 v[108:111], v[36:39], a[40:47], v10\n"
        "s_waitcnt lgkmcnt(5)\n"
        "v_smfmac_f32_16x16x64_bf16 v[104:107], v[40:43], a[64:71], v10\n"
        "v_smfmac_f32_16x16x64_bf16 v[108:111], v[40:43], a[72:79], v10\n"
        "s_waitcnt lgkmcnt(4)\n"
        "v_smfmac_f32_16x16x64_bf16 v[104:107], v[44:47], a[96:103], v10\n"
        "v_smfmac_f32_16x16x64_bf16 v[108:111], v[44:47], a[104:111], v10\n"
        "s_waitcnt lgkmcnt(3)\n"
        "v_smfmac_f32_16x16x64_bf16 v[104:107], v[48:51], a[128:135], v10\n"
        "v_smfmac_f32_16x16x64_bf16 v[108:111], v[48:51], a[136:143], v10\n"
        "s_waitcnt lgkmcnt(2)\n"
        "v_smfmac_f32_16x16x64_bf16 v[104:107], v[52:55], a[160:167], v10\n"
        "v_smfmac_f32_16x16x64_bf16 v[108:111], v[52:55], a[168:175], v10\n"
        "s_waitcnt lgkmcnt(1)\n"
        "v_smfmac_f32_16x16x64_bf16 v[104:107], v[64:67], v[192:199], v10\n"
        "v_smfmac_f32_16x16x64_bf16 v[108:111], v[64:67], v[200:207], v10\n"
        "s_waitcnt lgkmcnt(0)\n"
        "v_smfmac_f32_16x16x64_bf16 v[104:107], v[68:71], v[224:231], v10\n"
        "v_smfmac_f32_16x16x64_bf16 v[108:111], v[68:71], v[232:239], v10\n"
        "v_smfmac_f32_16x16x64_bf16 v[112:115], v[32:35], a[16:23], v10\n"
        "v_smfmac_f32_16x16x64_bf16 v[116:119], v[32:35], a[24:31], v10\n"
        "v_pk_add_f32 v[104:105], v[104:105], v[106:107]\n"
        "v_pk_add_f32 v[108:109], v[108:109], v[110:111]\n"
        "v_cvt_pk_bf16_f32 v104, v104, v105\n"
        "v_cvt_pk_bf16_f32 v105, v108, v109\n"
        "ds_write2st64_b32 v6, v104, v105 offset0:0 offset1:1\n"
        "v_smfmac_f32_16x16x64_bf16 v[112:115], v[36:39], a[48:55], v10\n"
        "v_smfmac_f32_16x16x64_bf16 v[116:119], v[36:39], a[56:63], v10\n"
        "v_smfmac_f32_16x16x64_bf16 v[112:115], v[40:43], a[80:87], v10\n"
        "v_smfmac_f32_16x16x64_bf16 v[116:119], v[40:43], a[88:95], v10\n"
        "v_smfmac_f32_16x16x64_bf16 v[112:115], v[44:47], a[112:119], v10\n"
        "s_waitcnt lgkmcnt(0)\n"
        "s_barrier\n"
        "v_smfmac_f32_16x16x64_bf16 v[116:119], v[44:47], a[120:127], v10\n"
        "v_smfmac_f32_16x16x64_bf16 v[112:115], v[48:51], a[144:151], v10\n"
        "v_smfmac_f32_16x16x64_bf16 v[116:119], v[48:51], a[152:159], v10\n"
        "v_smfmac_f32_16x16x64_bf16 v[112:115], v[52:55], a[176:183], v10\n"
        "v_smfmac_f32_16x16x64_bf16 v[116:119], v[52:55], a[184:191], v10\n"
        "v_smfmac_f32_16x16x64_bf16 v[112:115], v[64:67], v[208:215], v10\n"
        "v_smfmac_f32_16x16x64_bf16 v[116:119], v[64:67], v[216:223], v10\n"
        "v_smfmac_f32_16x16x64_bf16 v[112:115], v[68:71], v[240:247], v10\n"
        "v_smfmac_f32_16x16x64_bf16 v[116:119], v[68:71], v[248:255], v10\n"
        "s_nop 7\n"
        "v_pk_add_f32 v[112:113], v[112:113], v[114:115]\n"
        "v_pk_add_f32 v[116:117], v[116:117], v[118:119]\n"
        "v_cvt_pk_bf16_f32 v112, v112, v113\n"
        "v_cvt_pk_bf16_f32 v113, v116, v117\n"
        "ds_write2st64_b32 v6, v112, v113 offset0:8 offset1:9\n"
        "s_waitcnt lgkmcnt(0)\n"
        "s_barrier\n"
        "buffer_load_dword v104, v2, s[60:63], s69 offen sc1\n"
        "s_sleep 2\n"
        "ds_read_b128 v[152:155], v13 offset:0\n"
        "ds_read_b128 v[120:123], v11 offset:0\n"
        "ds_read_b128 v[136:139], v12 offset:0\n"
        "ds_read_b128 v[156:159], v13 offset:2048\n"
        "ds_read_b128 v[124:127], v11 offset:2048\n"
        "ds_read_b128 v[140:143], v12 offset:2048\n"
        "ds_read_b128 v[160:163], v13 offset:4096\n"
        "ds_read_b128 v[128:131], v11 offset:4096\n"
        "ds_read_b128 v[144:147], v12 offset:4096\n"
        "ds_read_b128 v[164:167], v13 offset:6144\n"
        "ds_read_b128 v[132:135], v11 offset:6144\n"
        "ds_read_b128 v[148:151], v12 offset:6144\n"
        "v_xor_b32 v11, 16384, v11\n"
        "v_xor_b32 v12, 16384, v12\n"
        "v_xor_b32 v13, 16384, v13\n"
        "s_waitcnt lgkmcnt(9)\n"
        "v_mfma_f32_16x16x32_bf16 v[168:171], v[120:123], v[152:155], 0\n"
        "v_mfma_f32_16x16x32_bf16 v[172:175], v[136:139], v[152:155], 0\n"
        "s_waitcnt lgkmcnt(6)\n"
        "v_mfma_f32_16x16x32_bf16 v[168:171], v[124:127], v[156:159], v[168:171]\n"
        "v_mfma_f32_16x16x32_bf16 v[172:175], v[140:143], v[156:159], v[172:175]\n"
        "s_waitcnt lgkmcnt(3)\n"
        "v_mfma_f32_16x16x32_bf16 v[168:171], v[128:131], v[160:163], v[168:171]\n"
        "v_mfma_f32_16x16x32_bf16 v[172:175], v[144:147], v[160:163], v[172:175]\n"
        "s_waitcnt lgkmcnt(0)\n"
        "v_mfma_f32_16x16x32_bf16 v[168:171], v[132:135], v[164:167], v[168:171]\n"
        "v_mfma_f32_16x16x32_bf16 v[172:175], v[148:151], v[164:167], v[172:175]\n"
        "s_nop 7\n"
        "v_cndmask_b32 v168, v168, v16, s[76:77]\n"
        "v_cndmask_b32 v169, v169, v16, s[78:79]\n"
        "v_cndmask_b32 v170, v170, v16, s[80:81]\n"
        "v_cndmask_b32 v171, v171, v16, s[82:83]\n"
        "v_cndmask_b32 v172, v172, v16, s[84:85]\n"
        "v_cndmask_b32 v173, v173, v16, s[86:87]\n"
        "v_cndmask_b32 v174, v174, v16, s[88:89]\n"
        "v_cndmask_b32 v175, v175, v16, s[90:91]\n"
        "v_max3_f32 v188, v168, v169, v170\n"
        "v_max3_f32 v190, v171, v172, v173\n"
        "v_max3_f32 v188, v188, v174, v175\n"
        "v_max_f32 v188, v188, v190\n"
        "v_mul_f32 v190, 0xbe0293ee, v188\n"
        "v_fmamk_f32 v180, v168, 0x3e0293ee, v190\n"
        "v_fmamk_f32 v181, v169, 0x3e0293ee, v190\n"
        "v_fmamk_f32 v182, v170, 0x3e0293ee, v190\n"
        "v_fmamk_f32 v183, v171, 0x3e0293ee, v190\n"
        "v_fmamk_f32 v184, v172, 0x3e0293ee, v190\n"
        "v_fmamk_f32 v185, v173, 0x3e0293ee, v190\n"
        "v_fmamk_f32 v186, v174, 0x3e0293ee, v190\n"
        "v_fmamk_f32 v187, v175, 0x3e0293ee, v190\n"
        "v_exp_f32 v180, v180\n"
        "v_exp_f32 v181, v181\n"
        "v_exp_f32 v182, v182\n"
        "v_exp_f32 v183, v183\n"
        "v_exp_f32 v184, v184\n"
        "v_exp_f32 v185, v185\n"
        "v_exp_f32 v186, v186\n"
        "v_exp_f32 v187, v187\n"
        "s_nop 0\n"
        "v_pk_add_f32 v[20:21], v[180:181], v[182:183]\n"
        "v_pk_add_f32 v[20:21], v[20:21], v[184:185]\n"
        "v_pk_add_f32 v[20:21], v[20:21], v[186:187]\n"
        "v_add_f32 v189, v20, v21\n"
        "s_barrier\n"
        "ds_read_b64_tr_b16 v[88:89], v7 offset:2048\n"
        "ds_read_b64_tr_b16 v[90:91], v7 offset:2112\n"
        "ds_read_b64_tr_b16 v[92:93], v7 offset:2560\n"
        "ds_read_b64_tr_b16 v[94:95], v7 offset:2624\n"
        "ds_read_b64_tr_b16 v[96:97], v7 offset:3072\n"
        "ds_read_b64_tr_b16 v[98:99], v7 offset:3136\n"
        "ds_read_b64_tr_b16 v[100:101], v7 offset:3584\n"
        "ds_read_b64_tr_b16 v[102:103], v7 offset:3648\n"
        "s_waitcnt lgkmcnt(0)\n"
        "v_mfma_f32_16x16x32_bf16 v[176:179], v[88:91], v[152:155], 0\n"
        "v_mfma_f32_16x16x32_bf16 v[176:179], v[92:95], v[156:159], v[176:179]\n"
        "v_mfma_f32_16x16x32_bf16 v[176:179], v[96:99], v[160:163], v[176:179]\n"
        "v_mfma_f32_16x16x32_bf16 v[176:179], v[100:103], v[164:167], v[176:179]\n"
        "s_nop 7\n"
        "v_max3_f32 v20, v176, v177, v178\n"
        "v_max3_f32 v20, v20, v179, v188\n"
        "v_mov_b32 v21, v20\n"
        "s_nop 1\n"
        "v_permlane16_swap_b32 v20, v21\n"
        "v_max_f32 v20, v20, v21\n"
        "v_mov_b32 v21, v20\n"
        "s_nop 1\n"
        "v_permlane32_swap_b32 v20, v21\n"
        "v_max_f32 v20, v20, v21\n"
        "v_mul_f32 v20, 0xbe0293ee, v20\n"
        "v_fmamk_f32 v22, v176, 0x3e0293ee, v20\n"
        "v_fmamk_f32 v23, v177, 0x3e0293ee, v20\n"
        "v_fmamk_f32 v24, v178, 0x3e0293ee, v20\n"
        "v_fmamk_f32 v25, v179, 0x3e0293ee, v20\n"
        "v_fmamk_f32 v26, v188, 0x3e0293ee, v20\n"
        "v_exp_f32 v22, v22\n"
        "v_exp_f32 v23, v23\n"
        "v_exp_f32 v24, v24\n"
        "v_exp_f32 v25, v25\n"
        "v_exp_f32 v26, v26\n"
        "v_pk_add_f32 v[28:29], v[22:23], v[24:25]\n"
        "v_mul_f32 v27, v26, v189\n"
        "v_add_f32 v28, v28, v29\n"
        "v_fmac_f32 v27, 0.5, v28\n"
        "v_mov_b32 v28, v27\n"
        "s_nop 1\n"
        "v_permlane16_swap_b32 v27, v28\n"
        "v_add_f32 v27, v27, v28\n"
        "v_mov_b32 v28, v27\n"
        "s_nop 1\n"
        "v_permlane32_swap_b32 v27, v28\n"
        "v_add_f32 v27, v27, v28\n"
        "v_rcp_f32 v28, v27\n"
        "s_nop 0\n"
        "v_mul_f32 v30, v28, v26\n"
        "v_pk_mul_f32 v[22:23], v[22:23], v[28:29] op_sel_hi:[1,0] clamp\n"
        "v_pk_mul_f32 v[24:25], v[24:25], v[28:29] op_sel_hi:[1,0] clamp\n"
        "v_pk_mul_f32 v[180:181], v[180:181], v[30:31] op_sel_hi:[1,0] clamp\n"
        "v_pk_mul_f32 v[182:183], v[182:183], v[30:31] op_sel_hi:[1,0] clamp\n"
        "v_pk_mul_f32 v[184:185], v[184:185], v[30:31] op_sel_hi:[1,0] clamp\n"
        "v_pk_mul_f32 v[186:187], v[186:187], v[30:31] op_sel_hi:[1,0] clamp\n"
        "v_cvt_pk_bf16_f32 v36, v22, v23\n"
        "v_cvt_pk_bf16_f32 v37, v24, v25\n"
        "v_cvt_pk_bf16_f32 v38, v22, v23\n"
        "v_cvt_pk_bf16_f32 v39, v24, v25\n"
        "v_cvt_pk_bf16_f32 v32, v180, v181\n"
        "v_cvt_pk_bf16_f32 v33, v182, v183\n"
        "v_cvt_pk_bf16_f32 v34, v184, v185\n"
        "v_cvt_pk_bf16_f32 v35, v186, v187\n"
        "v_permlane16_swap_b32 v36, v38\n"
        "v_permlane16_swap_b32 v37, v39\n"
        "s_mov_b64 exec, s[70:71]\n"
        "buffer_store_dwordx4 v[32:35], v8, s[60:63], s68 offen sc1\n"
        "s_mov_b64 exec, s[72:73]\n"
        "buffer_store_dwordx4 v[36:39], v9, s[60:63], s68 offen sc1\n"
        "s_mov_b64 exec, -1\n"
        "s_waitcnt vmcnt(2)\n"
        "R12_next:\n"
        "s_cmp_eq_u32 s64, 63\n"
        "s_cbranch_scc1 R12_h2\n"
        "R12_h0:\n"
        "s_nop 1\n"
        "v_max_u32_dpp v104, v104, v104 quad_perm:[1,0,3,2] row_mask:0xf bank_mask:0xf\n"
        "s_nop 1\n"
        "v_max_u32_dpp v104, v104, v104 quad_perm:[2,3,0,1] row_mask:0xf bank_mask:0xf\n"
        "s_nop 1\n"
        "v_max_u32_dpp v104, v104, v104 row_half_mirror row_mask:0xf bank_mask:0xf\n"
        "s_nop 1\n"
        "v_readlane_b32 s10, v104, 0\n"
        "v_readlane_b32 s11, v104, 8\n"
        "v_readlane_b32 s12, v104, 16\n"
        "v_readlane_b32 s13, v104, 24\n"
        "v_readlane_b32 s14, v104, 32\n"
        "v_readlane_b32 s15, v104, 40\n"
        "v_readlane_b32 s16, v104, 48\n"
        "v_readlane_b32 s17, v104, 56\n"
        "s_nop 1\n"
        "s_max_u32 s14, s14, s15\n"
        "s_max_u32 s16, s16, s17\n"
        "s_max_u32 s41, s14, s16\n"
        "s_cmp_eq_u32 s41, -1\n"
        "s_cselect_b32 s48, 0, s44\n"
        "s_sub_u32 s41, s41, s48\n"
        "s_min_u32 s14, s10, s11\n"
        "s_max_u32 s11, s10, s11\n"
        "s_min_u32 s10, s12, s13\n"
        "s_max_u32 s13, s12, s13\n"
        "s_min_u32 s12, s14, s10\n"
        "s_max_u32 s10, s14, s10\n"
        "s_min_u32 s14, s11, s13\n"
        "s_max_u32 s13, s11, s13\n"
        "s_min_u32 s11, s14, s10\n"
        "s_max_u32 s10, s14, s10\n"
        "s_add_u32 s12, s12, s34\n"
        "s_add_u32 s11, s11, s35\n"
        "s_add_u32 s10, s10, s36\n"
        "s_max_u32 s12, s12, s11\n"
        "s_max_u32 s10, s10, s13\n"
        "s_max_u32 s40, s12, s10\n"
        "s_cmp_lg_u32 s13, -1\n"
        "s_cselect_b32 s44, s40, s44\n"
        "s_cbranch_scc1 R12_h1\n"
        "s_cmp_eq_u32 s66, 0\n"
        "s_cbranch_scc1 R12_h1\n"
        "s_sub_u32 s66, s66, 1\n"
        "buffer_load_dword v104, v2, s[60:63], s69 offen sc1\n"
        "s_waitcnt vmcnt(0)\n"
        "s_branch R12_h0\n"
        "R12_h1:\n"
        "s_add_u32 s5, s5, 6\n"
        "s_min_u32 s5, s5, s41\n"
        "s_add_u32 s46, s5, s6\n"
        "R12_h2:\n"
        "s_add_u32 s64, s64, 1\n"
        "s_cmp_lt_u32 s64, 64\n"
        "s_cbranch_scc1 R12_loop\n"
        "s_endpgm\n"
        "s_endpgm\n"
        "R13_miss:\n"
        "s_waitcnt lgkmcnt(0)\n"
        "s_cmp_eq_u32 s66, 0\n"
        "s_cbranch_scc1 R13_got\n"
        "s_sub_u32 s66, s66, 1\n"
        "s_nop 7\n"
        "v_mov_b32 v104, 0\n"
        "v_mov_b32 v105, 0\n"
        "v_mov_b32 v106, 0\n"
        "v_mov_b32 v107, 0\n"
        "v_mov_b32 v108, 0\n"
        "v_mov_b32 v109, 0\n"
        "v_mov_b32 v110, 0\n"
        "v_mov_b32 v111, 0\n"
        "s_branch R13_poll\n"
        "R10_miss:\n"
        "s_waitcnt lgkmcnt(0)\n"
        "s_cmp_eq_u32 s66, 0\n"
        "s_cbranch_scc1 R10_got\n"
        "s_sub_u32 s66, s66, 1\n"
        "s_nop 7\n"
        "v_mov_b32 v104, 0\n"
        "v_mov_b32 v105, 0\n"
        "v_mov_b32 v106, 0\n"
        "v_mov_b32 v107, 0\n"
        "v_mov_b32 v108, 0\n"
        "v_mov_b32 v109, 0\n"
        "v_mov_b32 v110, 0\n"
        "v_mov_b32 v111, 0\n"
        "s_branch R10_poll\n"
        "R11_miss:\n"
        "s_waitcnt lgkmcnt(0)\n"
        "s_cmp_eq_u32 s66, 0\n"
        "s_cbranch_scc1 R11_got\n"
        "s_sub_u32 s66, s66, 1\n"
        "s_nop 7\n"
        "v_mov_b32 v104, 0\n"
        "v_mov_b32 v105, 0\n"
        "v_mov_b32 v106, 0\n"
        "v_mov_b32 v107, 0\n"
        "v_mov_b32 v108, 0\n"
        "v_mov_b32 v109, 0\n"
        "v_mov_b32 v110, 0\n"
        "v_mov_b32 v111, 0\n"
        "s_branch R11_poll\n"
        "R12_miss:\n"
        "s_waitcnt lgkmcnt(0)\n"
        "s_cmp_eq_u32 s66, 0\n"
        "s_cbranch_scc1 R12_got\n"
        "s_sub_u32 s66, s66, 1\n"
        "s_nop 7\n"
        "v_mov_b32 v104, 0\n"
        "v_mov_b32 v105, 0\n"
        "v_mov_b32 v106, 0\n"
        "v_mov_b32 v107, 0\n"
        "v_mov_b32 v108, 0\n"
        "v_mov_b32 v109, 0\n"
        "v_mov_b32 v110, 0\n"
        "v_mov_b32 v111, 0\n"
        "s_branch R12_poll\n"
        "R1_fin:\n"
        "s_endpgm\n"
        ROLE_OPS(p.wq, p.wk) : "v32", "v33", "v34", "v35", "v36", "v37", "v38", "v39", "v40", "v41", "v42", "v43", "v44", "v45", "v46", "v47", "v48", "v49", "v50", "v51", "v52", "v53", "v54", "v55", "v56", "v57", "v58", "v59", "v60", "v61", "v62", "v63", "v64", "v65", "v66", "v67", "v68", "v69", "v70", "v71", "v72", "v73", "v74", "v75", "v76", "v77", "v78", "v79", "v80", "v81", "v82", "v83", "v84", "v85", "v86", "v87", "v88", "v89", "v90", "v91", "v92", "v93", "v94", "v95", "v96", "v97", "v98", "v99", "v100", "v101", "v102", "v103", "v104", "v105", "v106", "v107", "v108", "v109", "v110", "v111", "v112", "v113", "v114", "v115", "v116", "v117", "v118", "v119", "v120", "v121", "v122", "v123", "v124", "v125", "v126", "v127", "v128", "v129", "v130", "v131", "v132", "v133", "v134", "v135", "v136", "v137", "v138", "v139", "v140", "v141", "v142", "v143", "v144", "v145", "v146", "v147", "v148", "v149", "v150", "v151", "v152", "v153", "v154", "v155", "v156", "v157", "v158", "v159", "v160", "v161", "v162", "v163", "v164", "v165", "v166", "v167", "v168", "v169", "v170", "v171", "v172", "v173", "v174", "v175", "v176", "v177", "v178", "v179", "v180", "v181", "v182", "v183", "v184", "v185", "v186", "v187", "v188", "v189", "v190", "v192", "v193", "v194", "v195", "v196", "v197", "v198", "v199", "v200", "v201", "v202", "v203", "v204", "v205", "v206", "v207", "v208", "v209", "v210", "v211", "v212", "v213", "v214", "v215", "v216", "v217", "v218", "v219", "v220", "v221", "v222", "v223", "v224", "v225", "v226", "v227", "v228", "v229", "v230", "v231", "v232", "v233", "v234", "v235", "v236", "v237", "v238", "v239", "v240", "v241", "v242", "v243", "v244", "v245", "v246", "v247", "v248", "v249", "v250", "v251", "v252", "v253", "v254", "v255", "s38", "s39", "s40", "s41", "s42", "s43", "s44", "s45", "s46", "s47", "s48", "s49", "s50", "s51", "s52", "s53", "s54", "s55", "s56", "s57", "s58", "s59", "s60", "s61", "s62", "s63", "s64", "s65", "s66", "s67", "s68", "s69", "s70", "s71", "s72", "s73", "s74", "s75", "s76", "s77", "s78", "s79", "s80", "s81", "s82", "s83", "s84", "s85", "s86", "s87", "s88", "s89", "s90", "s91", "s92", "s93", "s94", "s95", "s96", "s97", "s98", "s99", "vcc", "scc", "memory", "m0", "a0", "a1", "a2", "a3", "a4", "a5", "a6", "a7", "a8", "a9", "a10", "a11", "a12", "a13", "a14", "a15", "a16", "a17", "a18", "a19", "a20", "a21", "a22", "a23", "a24", "a25", "a26", "a27", "a28", "a29", "a30", "a31", "a32", "a33", "a34", "a35", "a36", "a37", "a38", "a39", "a40", "a41", "a42", "a43", "a44", "a45", "a46", "a47", "a48", "a49", "a50", "a51", "a52", "a53", "a54", "a55", "a56", "a57", "a58", "a59", "a60", "a61", "a62", "a63", "a64", "a65", "a66", "a67", "a68", "a69", "a70", "a71", "a72", "a73", "a74", "a75", "a76", "a77", "a78", "a79", "a80", "a81", "a82", "a83", "a84", "a85", "a86", "a87", "a88", "a89", "a90", "a91", "a92", "a93", "a94", "a95", "a96", "a97", "a98", "a99", "a100", "a101", "a102", "a103", "a104", "a105", "a106", "a107", "a108", "a109", "a110", "a111", "a112", "a113", "a114", "a115", "a116", "a117", "a118", "a119", "a120", "a121", "a122", "a123", "a124", "a125", "a126", "a127", "a128", "a129", "a130", "a131", "a132", "a133", "a134", "a135", "a136", "a137", "a138", "a139", "a140", "a141", "a142", "a143", "a144", "a145", "a146", "a147", "a148", "a149", "a150", "a151", "a152", "a153", "a154", "a155", "a156", "a157", "a158", "a159", "a160", "a161", "a162", "a163", "a164", "a165", "a166", "a167", "a168", "a169", "a170", "a171", "a172", "a173", "a174", "a175", "a176", "a177", "a178", "a179", "a180", "a181", "a182", "a183", "a184", "a185", "a186", "a187", "a188", "a189", "a190", "a191", "a192", "a193", "a194", "a195", "a196", "a197", "a198", "a199", "a200", "a201", "a202", "a203", "a204", "a205", "a206", "a207", "a208", "a209", "a210", "a211", "a212", "a213", "a214", "a215", "a216", "a217", "a218", "a219", "a220", "a221", "a222", "a223", "a224", "a225", "a226", "a227", "a228", "a229", "a230", "a231", "a232", "a233", "a234", "a235", "a236", "a237", "a238", "a239", "a240", "a241", "a242", "a243", "a244", "a245", "a246", "a247", "a248", "a249", "a250", "a251", "a252", "a253", "a254", "a255");
    __builtin_unreachable();
}

DI void role_ru(const Params& p, rsrc_t rs, int h, int wave) {
    const int lane = lane_id(), tid = wave * 64 + lane;
    asm volatile(
        "s_mov_b32 s58, %[h]\n"
        "s_lshl_b32 s71, %[wave], 10\n"
        "s_mov_b32 s80, %[a0]\n"
        "s_mov_b32 s81, %[a1]\n"
        "s_mov_b32 s82, 5242880\n"
        "s_mov_b32 s83, 0x00020000\n"
        "s_mov_b32 s84, %[b0]\n"
        "s_mov_b32 s85, %[b1]\n"
        "s_mov_b32 s86, s82\n"
        "s_mov_b32 s87, 0x00020000\n"
        "s_lshl_b32 s78, s58, 8\n"
        "s_mul_i32 s79, s58, 163840\n"
        "s_add_u32 m0, s71, 40960\n"
        "s_add_u32 s65, s79, 0\n"
        "buffer_load_dwordx4 %[v25], s[84:87], s65 offen lds\n"
        "s_add_u32 m0, s71, 45056\n"
        "s_add_u32 s65, s79, 20480\n"
        "buffer_load_dwordx4 %[v25], s[84:87], s65 offen lds\n"
        "s_add_u32 m0, s71, 49152\n"
        "s_add_u32 s65, s79, 40960\n"
        "buffer_load_dwordx4 %[v25], s[84:87], s65 offen lds\n"
        "s_add_u32 m0, s71, 53248\n"
        "s_add_u32 s65, s79, 61440\n"
        "buffer_load_dwordx4 %[v25], s[84:87], s65 offen lds\n"
        "s_add_u32 m0, s71, 57344\n"
        "s_add_u32 s65, s79, 81920\n"
        "buffer_load_dwordx4 %[v25], s[84:87], s65 offen lds\n"
        "s_add_u32 m0, s71, 61440\n"
        "s_add_u32 s65, s79, 102400\n"
        "buffer_load_dwordx4 %[v25], s[84:87], s65 offen lds\n"
        "s_add_u32 m0, s71, 65536\n"
        "s_add_u32 s65, s79, 122880\n"
        "buffer_load_dwordx4 %[v25], s[84:87], s65 offen lds\n"
        "s_add_u32 m0, s71, 69632\n"
        "s_add_u32 s65, s79, 143360\n"
        "buffer_load_dwordx4 %[v25], s[84:87], s65 offen lds\n"
        "s_add_u32 m0, s71, 73728\n"
        "s_add_u32 s65, s79, 256\n"
        "buffer_load_dwordx4 %[v25], s[84:87], s65 offen lds\n"
        "s_add_u32 m0, s71, 77824\n"
        "s_add_u32 s65, s79, 20736\n"
        "buffer_load_dwordx4 %[v25], s[84:87], s65 offen lds\n"
        "s_add_u32 m0, s71, 81920\n"
        "s_add_u32 s65, s79, 41216\n"
        "buffer_load_dwordx4 %[v25], s[84:87], s65 offen lds\n"
        "s_add_u32 m0, s71, 86016\n"
        "s_add_u32 s65, s79, 61696\n"
        "buffer_load_dwordx4 %[v25], s[84:87], s65 offen lds\n"
        "s_add_u32 m0, s71, 90112\n"
        "s_add_u32 s65, s79, 82176\n"
        "buffer_load_dwordx4 %[v25], s[84:87], s65 offen lds\n"
        "s_add_u32 m0, s71, 94208\n"
        "s_add_u32 s65, s79, 102656\n"
        "buffer_load_dwordx4 %[v25], s[84:87], s65 offen lds\n"
        "s_add_u32 m0, s71, 98304\n"
        "s_add_u32 s65, s79, 123136\n"
        "buffer_load_dwordx4 %[v25], s[84:87], s65 offen lds\n"
        "s_add_u32 m0, s71, 102400\n"
        "s_add_u32 s65, s79, 143616\n"
        "buffer_load_dwordx4 %[v25], s[84:87], s65 offen lds\n"
        HEAD_OPS(p.wv, p.wo));
    SB();
#pragma unroll
    for (int i = 0; i < 10; ++i) { u32 n = i * 256 + tid, q = n / 640, r = n % 640; stc((u4)(SENT), rs, (r >> 4) * 16384 + h * 256 + (r & 15) * 16 + (q & 1) * 8192 + (q >> 1) * UM_STR, WIN0); }
    const u32 g = lane >> 4, col = lane & 15;
    const u32 IMG = 10240;
    const u32 rho = (lane >> 2) & 3, c4 = lane & 3, t = g & 1;
    u32x32 ad = {};
    sparse_stage_addr(ad, tid, wave, lane);
    ad[0] = lane * 16;
    ad[1] = (wave * 160 + lane) * 16;
    ad[2] = 1 ? (wave * 160 + 128) * 16 + lane * 8 : (wave * 160 + 128 + (lane & 31)) * 16;
    if (3 > 1) { ad[1] = (wave > 0 ? 9 - 3 * wave : 8) * 1024 + lane * 16; ad[2] = ad[1] + 2048; }
    ad[3] = amap(ad[1]); ad[4] = amap(ad[1] + 1024); ad[5] = amap(ad[2]);
    ad[6] = IMG + wave * 512 + (g * 16 + (col ^ (4 * (g >> 1)))) * 8;
    ad[7] = IMG + ((2 * (g >> 1) + (c4 & 1)) * 16 + ((8 * t + rho) ^ (4 * (g >> 1)))) * 8;
    ad[8] = IMG + ((2 * (g >> 1) + (c4 & 1)) * 16 + ((8 * t + 4 + rho) ^ (4 * (g >> 1)))) * 8;
    const u32 ro = 8 * (g >> 1) + 4 * t + (rho & 1);
    ad[23] = ro * 256 + (((4 * wave + 2 * (rho >> 1) + (c4 >> 1)) ^ fsw(ro)) << 4) + (c4 & 1) * 8;
    ad[9] = (8 * (g & 1) + 2 * wave + (g >> 1)) * 16384 + h * 256 + col * 16;
    ad[10] = ad[9] + 16 * 16384;
    ad[11] = (32 + 2 * wave + (g >> 1)) * 16384 + h * 256 + col * 16;
    ad[12] = (lane & 8) ? 0xEEEE : 0x4444;
    if (1) {
        ad[13] = (lane & 2) ? 0xEEEE : 0x4444;
        ad[6] = IMG + wave * 512 + ((g >> 1) * 16 + col) * 8 + (g & 1) * 4;
        ad[14] = IMG + wave * 512 + ((2 + (g >> 1)) * 16 + (col ^ 4)) * 8 + (g & 1) * 4;
    }
    asm volatile(
        "s_mov_b32 s60, %[r0]\n"
        "s_mov_b32 s61, %[r1]\n"
        "s_mov_b32 s62, 0x0f000000\n"
        "s_mov_b32 s63, 0x00020000\n"
        "s_mov_b32 s67, %[wave]\n"
        "s_mov_b32 s58, %[h]\n"
        "s_mov_b32 s59, %[ts]\n"
        "s_mov_b32 s66, 0x100000\n"
        "s_mov_b32 s64, 0\n"
        "s_lshl_b32 s71, s67, 10\n"
        "s_mov_b32 s80, %[a0]\n"
        "s_mov_b32 s81, %[a1]\n"
        "s_mov_b32 s82, 5242880\n"
        "s_mov_b32 s83, 0x00020000\n"
        "s_mov_b32 s84, %[b0]\n"
        "s_mov_b32 s85, %[b1]\n"
        "s_mov_b32 s86, s82\n"
        "s_mov_b32 s87, 0x00020000\n"
        "s_lshl_b32 s78, s58, 8\n"
        "s_mul_i32 s79, s58, 163840\n"
        "s_add_u32 s88, %[hs], 4333568\n"
        "s_add_u32 s89, %[hs], 4325376\n"
        "s_add_u32 s90, %[hs], 4305920\n"
        "buffer_load_dword v188, off, s[60:63], s88 sc1\n"
        "s_add_u32 m0, s71, 106496\n"
        "s_add_u32 s65, s79, 512\n"
        "buffer_load_dwordx4 v25, s[84:87], s65 offen lds\n"
        "s_add_u32 m0, s71, 110592\n"
        "s_add_u32 s65, s79, 20992\n"
        "buffer_load_dwordx4 v25, s[84:87], s65 offen lds\n"
        "s_add_u32 m0, s71, 114688\n"
        "s_add_u32 s65, s79, 41472\n"
        "buffer_load_dwordx4 v25, s[84:87], s65 offen lds\n"
        "s_add_u32 m0, s71, 118784\n"
        "s_add_u32 s65, s79, 61952\n"
        "buffer_load_dwordx4 v25, s[84:87], s65 offen lds\n"
        "s_add_u32 m0, s71, 122880\n"
        "s_add_u32 s65, s79, 82432\n"
        "buffer_load_dwordx4 v25, s[84:87], s65 offen lds\n"
        "s_add_u32 m0, s71, 126976\n"
        "s_add_u32 s65, s79, 102912\n"
        "buffer_load_dwordx4 v25, s[84:87], s65 offen lds\n"
        "s_add_u32 m0, s71, 131072\n"
        "s_add_u32 s65, s79, 123392\n"
        "buffer_load_dwordx4 v25, s[84:87], s65 offen lds\n"
        "s_add_u32 m0, s71, 135168\n"
        "s_add_u32 s65, s79, 143872\n"
        "buffer_load_dwordx4 v25, s[84:87], s65 offen lds\n"
        "v_add_u32 v28, 40960, v23\n"
        "v_add_u32 v29, 40960, v27\n"
        "s_waitcnt vmcnt(27)\n"
        "s_barrier\n"
        "ds_read_b64_tr_b16 a[160:161], v28 offset:0\n"
        "ds_read_b64_tr_b16 a[162:163], v28 offset:512\n"
        "ds_read_b64_tr_b16 a[164:165], v28 offset:4096\n"
        "ds_read_b64_tr_b16 a[166:167], v28 offset:4608\n"
        "ds_read_b64_tr_b16 a[168:169], v28 offset:8192\n"
        "ds_read_b64_tr_b16 a[170:171], v28 offset:8704\n"
        "ds_read_b64_tr_b16 a[172:173], v28 offset:12288\n"
        "ds_read_b64_tr_b16 a[174:175], v28 offset:12800\n"
        "ds_read_b64_tr_b16 a[176:177], v28 offset:16384\n"
        "ds_read_b64_tr_b16 a[178:179], v28 offset:16896\n"
        "ds_read_b64_tr_b16 a[180:181], v28 offset:20480\n"
        "ds_read_b64_tr_b16 a[182:183], v28 offset:20992\n"
        "ds_read_b64_tr_b16 a[184:185], v28 offset:24576\n"
        "ds_read_b64_tr_b16 a[186:187], v28 offset:25088\n"
        "ds_read_b64_tr_b16 a[188:189], v28 offset:28672\n"
        "ds_read_b64_tr_b16 a[190:191], v28 offset:29184\n"
        "s_waitcnt lgkmcnt(0)\n"
        "s_barrier\n"
        "s_add_u32 m0, s71, 40960\n"
        "s_add_u32 s65, s79, 768\n"
        "buffer_load_dwordx4 v25, s[84:87], s65 offen lds\n"
        "s_add_u32 m0, s71, 45056\n"
        "s_add_u32 s65, s79, 21248\n"
        "buffer_load_dwordx4 v25, s[84:87], s65 offen lds\n"
        "s_add_u32 m0, s71, 49152\n"
        "s_add_u32 s65, s79, 41728\n"
        "buffer_load_dwordx4 v25, s[84:87], s65 offen lds\n"
        "s_add_u32 m0, s71, 53248\n"
        "s_add_u32 s65, s79, 62208\n"
        "buffer_load_dwordx4 v25, s[84:87], s65 offen lds\n"
        "s_add_u32 m0, s71, 57344\n"
        "s_add_u32 s65, s79, 82688\n"
        "buffer_load_dwordx4 v25, s[84:87], s65 offen lds\n"
        "s_add_u32 m0, s71, 61440\n"
        "s_add_u32 s65, s79, 103168\n"
        "buffer_load_dwordx4 v25, s[84:87], s65 offen lds\n"
        "s_add_u32 m0, s71, 65536\n"
        "s_add_u32 s65, s79, 123648\n"
        "buffer_load_dwordx4 v25, s[84:87], s65 offen lds\n"
        "s_add_u32 m0, s71, 69632\n"
        "s_add_u32 s65, s79, 144128\n"
        "buffer_load_dwordx4 v25, s[84:87], s65 offen lds\n"
        "v_add_u32 v28, 73728, v23\n"
        "v_add_u32 v29, 73728, v27\n"
        "s_waitcnt vmcnt(27)\n"
        "s_barrier\n"
        "ds_read_b64_tr_b16 a[192:193], v28 offset:0\n"
        "ds_read_b64_tr_b16 a[194:195], v28 offset:512\n"
        "ds_read_b64_tr_b16 a[196:197], v28 offset:4096\n"
        "ds_read_b64_tr_b16 a[198:199], v28 offset:4608\n"
        "ds_read_b64_tr_b16 a[200:201], v28 offset:8192\n"
        "ds_read_b64_tr_b16 a[202:203], v28 offset:8704\n"
        "ds_read_b64_tr_b16 a[204:205], v28 offset:12288\n"
        "ds_read_b64_tr_b16 a[206:207], v28 offset:12800\n"
        "ds_read_b64_tr_b16 a[208:209], v28 offset:16384\n"
        "ds_read_b64_tr_b16 a[210:211], v28 offset:16896\n"
        "ds_read_b64_tr_b16 a[212:213], v28 offset:20480\n"
        "ds_read_b64_tr_b16 a[214:215], v28 offset:20992\n"
        "ds_read_b64_tr_b16 a[216:217], v28 offset:24576\n"
        "ds_read_b64_tr_b16 a[218:219], v28 offset:25088\n"
        "ds_read_b64_tr_b16 a[220:221], v28 offset:28672\n"
        "ds_read_b64_tr_b16 a[222:223], v28 offset:29184\n"
        "s_waitcnt lgkmcnt(0)\n"
        "s_barrier\n"
        "s_add_u32 m0, s71, 73728\n"
        "s_add_u32 s65, s79, 1024\n"
        "buffer_load_dwordx4 v25, s[84:87], s65 offen lds\n"
        "s_add_u32 m0, s71, 77824\n"
        "s_add_u32 s65, s79, 21504\n"
        "buffer_load_dwordx4 v25, s[84:87], s65 offen lds\n"
        "s_add_u32 m0, s71, 81920\n"
        "s_add_u32 s65, s79, 41984\n"
        "buffer_load_dwordx4 v25, s[84:87], s65 offen lds\n"
        "s_add_u32 m0, s71, 86016\n"
        "s_add_u32 s65, s79, 62464\n"
        "buffer_load_dwordx4 v25, s[84:87], s65 offen lds\n"
        "s_add_u32 m0, s71, 90112\n"
        "s_add_u32 s65, s79, 82944\n"
        "buffer_load_dwordx4 v25, s[84:87], s65 offen lds\n"
        "s_add_u32 m0, s71, 94208\n"
        "s_add_u32 s65, s79, 103424\n"
        "buffer_load_dwordx4 v25, s[84:87], s65 offen lds\n"
        "s_add_u32 m0, s71, 98304\n"
        "s_add_u32 s65, s79, 123904\n"
        "buffer_load_dwordx4 v25, s[84:87], s65 offen lds\n"
        "s_add_u32 m0, s71, 102400\n"
        "s_add_u32 s65, s79, 144384\n"
        "buffer_load_dwordx4 v25, s[84:87], s65 offen lds\n"
        "v_add_u32 v28, 106496, v23\n"
        "v_add_u32 v29, 106496, v27\n"
        "s_waitcnt vmcnt(16)\n"
        "s_barrier\n"
        "ds_read_b64_tr_b16 a[224:225], v28 offset:0\n"
        "ds_read_b64_tr_b16 a[226:227], v28 offset:512\n"
        "ds_read_b64_tr_b16 a[228:229], v28 offset:4096\n"
        "ds_read_b64_tr_b16 a[230:231], v28 offset:4608\n"
        "ds_read_b64_tr_b16 a[232:233], v28 offset:8192\n"
        "ds_read_b64_tr_b16 a[234:235], v28 offset:8704\n"
        "ds_read_b64_tr_b16 a[236:237], v28 offset:12288\n"
        "ds_read_b64_tr_b16 a[238:239], v28 offset:12800\n"
        "ds_read_b64_tr_b16 a[240:241], v28 offset:16384\n"
        "ds_read_b64_tr_b16 a[242:243], v28 offset:16896\n"
        "ds_read_b64_tr_b16 a[244:245], v28 offset:20480\n"
        "ds_read_b64_tr_b16 a[246:247], v28 offset:20992\n"
        "ds_read_b64_tr_b16 a[248:249], v28 offset:24576\n"
        "ds_read_b64_tr_b16 a[250:251], v28 offset:25088\n"
        "ds_read_b64_tr_b16 a[252:253], v28 offset:28672\n"
        "ds_read_b64_tr_b16 a[254:255], v28 offset:29184\n"
        "s_waitcnt lgkmcnt(0)\n"
        "s_barrier\n"
        "s_add_u32 m0, s71, 106496\n"
        "s_add_u32 s65, s78, 0\n"
        "buffer_load_dwordx4 v24, s[80:83], s65 offen lds\n"
        "s_add_u32 m0, s71, 110592\n"
        "s_add_u32 s65, s78, 131072\n"
        "buffer_load_dwordx4 v24, s[80:83], s65 offen lds\n"
        "s_add_u32 m0, s71, 114688\n"
        "s_add_u32 s65, s78, 262144\n"
        "buffer_load_dwordx4 v24, s[80:83], s65 offen lds\n"
        "s_add_u32 m0, s71, 118784\n"
        "s_add_u32 s65, s78, 393216\n"
        "buffer_load_dwordx4 v24, s[80:83], s65 offen lds\n"
        "s_add_u32 m0, s71, 122880\n"
        "s_add_u32 s65, s78, 524288\n"
        "buffer_load_dwordx4 v24, s[80:83], s65 offen lds\n"
        "s_add_u32 m0, s71, 126976\n"
        "s_add_u32 s65, s78, 655360\n"
        "buffer_load_dwordx4 v24, s[80:83], s65 offen lds\n"
        "s_add_u32 m0, s71, 131072\n"
        "s_add_u32 s65, s78, 786432\n"
        "buffer_load_dwordx4 v24, s[80:83], s65 offen lds\n"
        "s_add_u32 m0, s71, 135168\n"
        "s_add_u32 s65, s78, 917504\n"
        "buffer_load_dwordx4 v24, s[80:83], s65 offen lds\n"
        "v_add_u32 v188, 1, v188\n"
        "s_mov_b64 exec, 1\n"
        "buffer_store_dword v188, off, s[60:63], s89 sc1\n"
        "s_mov_b64 exec, -1\n"
        "v_add_u32 v28, 40960, v23\n"
        "v_add_u32 v29, 40960, v27\n"
        "s_waitcnt vmcnt(17)\n"
        "s_barrier\n"
        "ds_read_b64_tr_b16 v[192:193], v28 offset:0\n"
        "ds_read_b64_tr_b16 v[194:195], v28 offset:512\n"
        "ds_read_b64_tr_b16 v[196:197], v28 offset:4096\n"
        "ds_read_b64_tr_b16 v[198:199], v28 offset:4608\n"
        "ds_read_b64_tr_b16 v[200:201], v28 offset:8192\n"
        "ds_read_b64_tr_b16 v[202:203], v28 offset:8704\n"
        "ds_read_b64_tr_b16 v[204:205], v28 offset:12288\n"
        "ds_read_b64_tr_b16 v[206:207], v28 offset:12800\n"
        "ds_read_b64_tr_b16 v[208:209], v28 offset:16384\n"
        "ds_read_b64_tr_b16 v[210:211], v28 offset:16896\n"
        "ds_read_b64_tr_b16 v[212:213], v28 offset:20480\n"
        "ds_read_b64_tr_b16 v[214:215], v28 offset:20992\n"
        "ds_read_b64_tr_b16 v[216:217], v28 offset:24576\n"
        "ds_read_b64_tr_b16 v[218:219], v28 offset:25088\n"
        "ds_read_b64_tr_b16 v[220:221], v28 offset:28672\n"
        "ds_read_b64_tr_b16 v[222:223], v28 offset:29184\n"
        "s_waitcnt lgkmcnt(0)\n"
        "s_barrier\n"
        "s_add_u32 m0, s71, 40960\n"
        "s_add_u32 s65, s78, 1048576\n"
        "buffer_load_dwordx4 v24, s[80:83], s65 offen lds\n"
        "s_add_u32 m0, s71, 45056\n"
        "s_add_u32 s65, s78, 1179648\n"
        "buffer_load_dwordx4 v24, s[80:83], s65 offen lds\n"
        "s_add_u32 m0, s71, 49152\n"
        "s_add_u32 s65, s78, 1310720\n"
        "buffer_load_dwordx4 v24, s[80:83], s65 offen lds\n"
        "s_add_u32 m0, s71, 53248\n"
        "s_add_u32 s65, s78, 1441792\n"
        "buffer_load_dwordx4 v24, s[80:83], s65 offen lds\n"
        "s_add_u32 m0, s71, 57344\n"
        "s_add_u32 s65, s78, 1572864\n"
        "buffer_load_dwordx4 v24, s[80:83], s65 offen lds\n"
        "s_add_u32 m0, s71, 61440\n"
        "s_add_u32 s65, s78, 1703936\n"
        "buffer_load_dwordx4 v24, s[80:83], s65 offen lds\n"
        "s_add_u32 m0, s71, 65536\n"
        "s_add_u32 s65, s78, 1835008\n"
        "buffer_load_dwordx4 v24, s[80:83], s65 offen lds\n"
        "s_add_u32 m0, s71, 69632\n"
        "s_add_u32 s65, s78, 1966080\n"
        "buffer_load_dwordx4 v24, s[80:83], s65 offen lds\n"
        "v_add_u32 v28, 73728, v23\n"
        "v_add_u32 v29, 73728, v27\n"
        "s_waitcnt vmcnt(17)\n"
        "s_barrier\n"
        "ds_read_b64_tr_b16 v[224:225], v28 offset:0\n"
        "ds_read_b64_tr_b16 v[226:227], v28 offset:512\n"
        "ds_read_b64_tr_b16 v[228:229], v28 offset:4096\n"
        "ds_read_b64_tr_b16 v[230:231], v28 offset:4608\n"
        "ds_read_b64_tr_b16 v[232:233], v28 offset:8192\n"
        "ds_read_b64_tr_b16 v[234:235], v28 offset:8704\n"
        "ds_read_b64_tr_b16 v[236:237], v28 offset:12288\n"
        "ds_read_b64_tr_b16 v[238:239], v28 offset:12800\n"
        "ds_read_b64_tr_b16 v[240:241], v28 offset:16384\n"
        "ds_read_b64_tr_b16 v[242:243], v28 offset:16896\n"
        "ds_read_b64_tr_b16 v[244:245], v28 offset:20480\n"
        "ds_read_b64_tr_b16 v[246:247], v28 offset:20992\n"
        "ds_read_b64_tr_b16 v[248:249], v28 offset:24576\n"
        "ds_read_b64_tr_b16 v[250:251], v28 offset:25088\n"
        "ds_read_b64_tr_b16 v[252:253], v28 offset:28672\n"
        "ds_read_b64_tr_b16 v[254:255], v28 offset:29184\n"
        "s_waitcnt lgkmcnt(0)\n"
        "s_barrier\n"
        "s_add_u32 m0, s71, 73728\n"
        "s_add_u32 s65, s78, 2097152\n"
        "buffer_load_dwordx4 v24, s[80:83], s65 offen lds\n"
        "s_add_u32 m0, s71, 77824\n"
        "s_add_u32 s65, s78, 2228224\n"
        "buffer_load_dwordx4 v24, s[80:83], s65 offen lds\n"
        "s_add_u32 m0, s71, 81920\n"
        "s_add_u32 s65, s78, 2359296\n"
        "buffer_load_dwordx4 v24, s[80:83], s65 offen lds\n"
        "s_add_u32 m0, s71, 86016\n"
        "s_add_u32 s65, s78, 2490368\n"
        "buffer_load_dwordx4 v24, s[80:83], s65 offen lds\n"
        "s_add_u32 m0, s71, 90112\n"
        "s_add_u32 s65, s78, 2621440\n"
        "buffer_load_dwordx4 v24, s[80:83], s65 offen lds\n"
        "s_add_u32 m0, s71, 94208\n"
        "s_add_u32 s65, s78, 2752512\n"
        "buffer_load_dwordx4 v24, s[80:83], s65 offen lds\n"
        "s_add_u32 m0, s71, 98304\n"
        "s_add_u32 s65, s78, 2883584\n"
        "buffer_load_dwordx4 v24, s[80:83], s65 offen lds\n"
        "s_add_u32 m0, s71, 102400\n"
        "s_add_u32 s65, s78, 3014656\n"
        "buffer_load_dwordx4 v24, s[80:83], s65 offen lds\n"
        "v_add_u32 v28, 106496, v26\n"
        "v_add_u32 v29, 106496, v27\n"
        "s_waitcnt vmcnt(17)\n"
        "s_barrier\n"
        "ds_read_b64_tr_b16 a[0:1], v28 offset:0\n"
        "ds_read_b64_tr_b16 a[2:3], v28 offset:512\n"
        "ds_read_b64_tr_b16 a[4:5], v28 offset:8192\n"
        "ds_read_b64_tr_b16 a[6:7], v28 offset:8704\n"
        "ds_read_b64_tr_b16 a[8:9], v29 offset:0\n"
        "ds_read_b64_tr_b16 a[10:11], v29 offset:512\n"
        "ds_read_b64_tr_b16 a[12:13], v29 offset:8192\n"
        "ds_read_b64_tr_b16 a[14:15], v29 offset:8704\n"
        "ds_read_b64_tr_b16 a[16:17], v28 offset:16384\n"
        "ds_read_b64_tr_b16 a[18:19], v28 offset:16896\n"
        "ds_read_b64_tr_b16 a[20:21], v28 offset:24576\n"
        "ds_read_b64_tr_b16 a[22:23], v28 offset:25088\n"
        "ds_read_b64_tr_b16 a[24:25], v29 offset:16384\n"
        "ds_read_b64_tr_b16 a[26:27], v29 offset:16896\n"
        "ds_read_b64_tr_b16 a[28:29], v29 offset:24576\n"
        "ds_read_b64_tr_b16 a[30:31], v29 offset:25088\n"
        "s_waitcnt lgkmcnt(0)\n"
        "s_barrier\n"
        "buffer_load_dword v189, off, s[60:63], s88 sc1\n"
        "s_add_u32 m0, s71, 106496\n"
        "s_add_u32 s65, s78, 3145728\n"
        "buffer_load_dwordx4 v24, s[80:83], s65 offen lds\n"
        "s_add_u32 m0, s71, 110592\n"
        "s_add_u32 s65, s78, 3276800\n"
        "buffer_load_dwordx4 v24, s[80:83], s65 offen lds\n"
        "s_add_u32 m0, s71, 114688\n"
        "s_add_u32 s65, s78, 3407872\n"
        "buffer_load_dwordx4 v24, s[80:83], s65 offen lds\n"
        "s_add_u32 m0, s71, 118784\n"
        "s_add_u32 s65, s78, 3538944\n"
        "buffer_load_dwordx4 v24, s[80:83], s65 offen lds\n"
        "s_add_u32 m0, s71, 122880\n"
        "s_add_u32 s65, s78, 3670016\n"
        "buffer_load_dwordx4 v24, s[80:83], s65 offen lds\n"
        "s_add_u32 m0, s71, 126976\n"
        "s_add_u32 s65, s78, 3801088\n"
        "buffer_load_dwordx4 v24, s[80:83], s65 offen lds\n"
        "s_add_u32 m0, s71, 131072\n"
        "s_add_u32 s65, s78, 3932160\n"
        "buffer_load_dwordx4 v24, s[80:83], s65 offen lds\n"
        "s_add_u32 m0, s71, 135168\n"
        "s_add_u32 s65, s78, 4063232\n"
        "buffer_load_dwordx4 v24, s[80:83], s65 offen lds\n"
        "v_add_u32 v28, 40960, v26\n"
        "v_add_u32 v29, 40960, v27\n"
        "s_waitcnt vmcnt(17)\n"
        "s_barrier\n"
        "ds_read_b64_tr_b16 a[32:33], v28 offset:0\n"
        "ds_read_b64_tr_b16 a[34:35], v28 offset:512\n"
        "ds_read_b64_tr_b16 a[36:37], v28 offset:8192\n"
        "ds_read_b64_tr_b16 a[38:39], v28 offset:8704\n"
        "ds_read_b64_tr_b16 a[40:41], v29 offset:0\n"
        "ds_read_b64_tr_b16 a[42:43], v29 offset:512\n"
        "ds_read_b64_tr_b16 a[44:45], v29 offset:8192\n"
        "ds_read_b64_tr_b16 a[46:47], v29 offset:8704\n"
        "ds_read_b64_tr_b16 a[48:49], v28 offset:16384\n"
        "ds_read_b64_tr_b16 a[50:51], v28 offset:16896\n"
        "ds_read_b64_tr_b16 a[52:53], v28 offset:24576\n"
        "ds_read_b64_tr_b16 a[54:55], v28 offset:25088\n"
        "ds_read_b64_tr_b16 a[56:57], v29 offset:16384\n"
        "ds_read_b64_tr_b16 a[58:59], v29 offset:16896\n"
        "ds_read_b64_tr_b16 a[60:61], v29 offset:24576\n"
        "ds_read_b64_tr_b16 a[62:63], v29 offset:25088\n"
        "s_waitcnt lgkmcnt(0)\n"
        "s_barrier\n"
        "s_getpc_b64 s[92:93]\n"
        "CPRU0_loop:\n"
        "s_add_u32 s92, s92, RU0_loop-CPRU0_loop\n"
        "s_addc_u32 s93, s93, 0\n"
        "s_mov_b32 s94, RU_fin-RU0_loop\n"
        "s_mov_b32 s95, 0x00020000\n"
        "v_mbcnt_lo_u32_b32 v186, -1, 0\n"
        "v_mbcnt_hi_u32_b32 v186, -1, v186\n"
        "v_lshlrev_b32 v186, 7, v186\n"
        "s_lshl_b32 s65, s67, 13\n"
        "v_add_u32 v186, s65, v186\n"
        "buffer_load_dword v186, v186, s[92:95], 0 offen\n"
        "s_add_u32 m0, s71, 40960\n"
        "s_add_u32 s65, s78, 4194304\n"
        "buffer_load_dwordx4 v24, s[80:83], s65 offen lds\n"
        "s_add_u32 m0, s71, 45056\n"
        "s_add_u32 s65, s78, 4325376\n"
        "buffer_load_dwordx4 v24, s[80:83], s65 offen lds\n"
        "s_add_u32 m0, s71, 49152\n"
        "s_add_u32 s65, s78, 4456448\n"
        "buffer_load_dwordx4 v24, s[80:83], s65 offen lds\n"
        "s_add_u32 m0, s71, 53248\n"
        "s_add_u32 s65, s78, 4587520\n"
        "buffer_load_dwordx4 v24, s[80:83], s65 offen lds\n"
        "s_add_u32 m0, s71, 57344\n"
        "s_add_u32 s65, s78, 4718592\n"
        "buffer_load_dwordx4 v24, s[80:83], s65 offen lds\n"
        "s_add_u32 m0, s71, 61440\n"
        "s_add_u32 s65, s78, 4849664\n"
        "buffer_load_dwordx4 v24, s[80:83], s65 offen lds\n"
        "s_add_u32 m0, s71, 65536\n"
        "s_add_u32 s65, s78, 4980736\n"
        "buffer_load_dwordx4 v24, s[80:83], s65 offen lds\n"
        "s_add_u32 m0, s71, 69632\n"
        "s_add_u32 s65, s78, 5111808\n"
        "buffer_load_dwordx4 v24, s[80:83], s65 offen lds\n"
        "v_add_u32 v28, 73728, v26\n"
        "v_add_u32 v29, 73728, v27\n"
        "s_waitcnt vmcnt(17)\n"
        "s_barrier\n"
        "ds_read_b64_tr_b16 a[64:65], v28 offset:0\n"
        "ds_read_b64_tr_b16 a[66:67], v28 offset:512\n"
        "ds_read_b64_tr_b16 a[68:69], v28 offset:8192\n"
        "ds_read_b64_tr_b16 a[70:71], v28 offset:8704\n"
        "ds_read_b64_tr_b16 a[72:73], v29 offset:0\n"
        "ds_read_b64_tr_b16 a[74:75], v29 offset:512\n"
        "ds_read_b64_tr_b16 a[76:77], v29 offset:8192\n"
        "ds_read_b64_tr_b16 a[78:79], v29 offset:8704\n"
        "ds_read_b64_tr_b16 a[80:81], v28 offset:16384\n"
        "ds_read_b64_tr_b16 a[82:83], v28 offset:16896\n"
        "ds_read_b64_tr_b16 a[84:85], v28 offset:24576\n"
        "ds_read_b64_tr_b16 a[86:87], v28 offset:25088\n"
        "ds_read_b64_tr_b16 a[88:89], v29 offset:16384\n"
        "ds_read_b64_tr_b16 a[90:91], v29 offset:16896\n"
        "ds_read_b64_tr_b16 a[92:93], v29 offset:24576\n"
        "ds_read_b64_tr_b16 a[94:95], v29 offset:25088\n"
        "s_waitcnt lgkmcnt(0)\n"
        "s_barrier\n"
        "v_cmp_ne_u32 vcc, v188, v189\n"
        "s_lshl_b32 s65, s58, 2\n"
        "s_add_u32 s65, s65, 4305024\n"
        "s_cmp_eq_u32 s67, 0\n"
        "s_cselect_b32 s65, s65, 0xf0000000\n"
        "s_cmp_lg_u64 vcc, 0\n"
        "s_cselect_b32 s91, 1, 0\n"
        "s_cselect_b32 s65, 0xf0000000, s65\n"
        "s_cselect_b32 s69, 0xf0000000, s90\n"
        "v_mov_b32 v20, 0\n"
        "s_mov_b64 exec, 1\n"
        "buffer_store_dword v20, off, s[60:63], s65 sc1\n"
        "buffer_store_dword v20, off, s[60:63], s69 sc1\n"
        "s_mov_b64 exec, -1\n"
        "v_mbcnt_lo_u32_b32 v21, -1, 0\n"
        "v_mbcnt_hi_u32_b32 v21, -1, v21\n"
        "v_and_b32 v187, 3, v21\n"
        "v_lshlrev_b32 v187, 2, v187\n"
        "v_min_u32 v21, 39, v21\n"
        "v_lshlrev_b32 v21, 2, v21\n"
        "v_add_u32 v28, 106496, v26\n"
        "v_add_u32 v29, 106496, v27\n"
        "s_waitcnt vmcnt(11)\n"
        "s_barrier\n"
        "ds_read_b64_tr_b16 a[96:97], v28 offset:0\n"
        "ds_read_b64_tr_b16 a[98:99], v28 offset:512\n"
        "ds_read_b64_tr_b16 a[100:101], v28 offset:8192\n"
        "ds_read_b64_tr_b16 a[102:103], v28 offset:8704\n"
        "ds_read_b64_tr_b16 a[104:105], v29 offset:0\n"
        "ds_read_b64_tr_b16 a[106:107], v29 offset:512\n"
        "ds_read_b64_tr_b16 a[108:109], v29 offset:8192\n"
        "ds_read_b64_tr_b16 a[110:111], v29 offset:8704\n"
        "ds_read_b64_tr_b16 a[112:113], v28 offset:16384\n"
        "ds_read_b64_tr_b16 a[114:115], v28 offset:16896\n"
        "ds_read_b64_tr_b16 a[116:117], v28 offset:24576\n"
        "ds_read_b64_tr_b16 a[118:119], v28 offset:25088\n"
        "ds_read_b64_tr_b16 a[120:121], v29 offset:16384\n"
        "ds_read_b64_tr_b16 a[122:123], v29 offset:16896\n"
        "ds_read_b64_tr_b16 a[124:125], v29 offset:24576\n"
        "ds_read_b64_tr_b16 a[126:127], v29 offset:25088\n"
        "s_waitcnt lgkmcnt(0)\n"
        "s_barrier\n"
        "s_mov_b32 s65, 4305152\n"
        "s_mov_b32 s69, 4305408\n"
        "buffer_load_dword v22, v21, s[60:63], s65 offen sc1\n"
        "buffer_load_dwordx2 v[24:25], off, s[60:63], s69 sc1\n"
        "v_add_u32 v28, 40960, v26\n"
        "v_add_u32 v29, 40960, v27\n"
        "s_waitcnt vmcnt(4)\n"
        "s_barrier\n"
        "ds_read_b64_tr_b16 a[128:129], v28 offset:0\n"
        "ds_read_b64_tr_b16 a[130:131], v28 offset:512\n"
        "ds_read_b64_tr_b16 a[132:133], v28 offset:8192\n"
        "ds_read_b64_tr_b16 a[134:135], v28 offset:8704\n"
        "ds_read_b64_tr_b16 a[136:137], v29 offset:0\n"
        "ds_read_b64_tr_b16 a[138:139], v29 offset:512\n"
        "ds_read_b64_tr_b16 a[140:141], v29 offset:8192\n"
        "ds_read_b64_tr_b16 a[142:143], v29 offset:8704\n"
        "ds_read_b64_tr_b16 a[144:145], v28 offset:16384\n"
        "ds_read_b64_tr_b16 a[146:147], v28 offset:16896\n"
        "ds_read_b64_tr_b16 a[148:149], v28 offset:24576\n"
        "ds_read_b64_tr_b16 a[150:151], v28 offset:25088\n"
        "ds_read_b64_tr_b16 a[152:153], v29 offset:16384\n"
        "ds_read_b64_tr_b16 a[154:155], v29 offset:16896\n"
        "ds_read_b64_tr_b16 a[156:157], v29 offset:24576\n"
        "ds_read_b64_tr_b16 a[158:159], v29 offset:25088\n"
        "s_waitcnt lgkmcnt(0)\n"
        "s_barrier\n"
        "s_waitcnt vmcnt(0)\n"
        "s_cmp_eq_u32 s91, 0\n"
        "s_cbranch_scc1 HS477_vod\n"
        "HS477_voa:\n"
        "buffer_load_dword v189, off, s[60:63], s88 sc1\n"
        "s_waitcnt vmcnt(0)\n"
        "v_cmp_ne_u32 vcc, v188, v189\n"
        "s_cbranch_vccz HS477_vob\n"
        "s_sleep 1\n"
        "s_cmp_eq_u32 s66, 0\n"
        "s_cbranch_scc1 HS477_vob\n"
        "s_sub_u32 s66, s66, 1\n"
        "s_branch HS477_voa\n"
        "HS477_vob:\n"
        "s_lshl_b32 s65, s58, 2\n"
        "s_add_u32 s65, s65, 4305024\n"
        "s_cmp_eq_u32 s67, 0\n"
        "s_cselect_b32 s65, s65, 0xf0000000\n"
        "s_mov_b64 exec, 1\n"
        "buffer_store_dword v20, off, s[60:63], s65 sc1\n"
        "buffer_store_dword v20, off, s[60:63], s90 sc1\n"
        "s_mov_b64 exec, -1\n"
        "v_mov_b32 v22, -1\n"
        "v_mov_b32 v190, -1\n"
        "HS477_vod:\n"
        "s_mov_b32 s65, 4305152\n"
        "s_mov_b32 s69, 4305408\n"
        "s_branch RU_c3\n"
        "RU_c1:\n"
        "buffer_load_dword v22, v21, s[60:63], s65 offen sc1\n"
        "buffer_load_dwordx2 v[24:25], off, s[60:63], s69 sc1\n"
        "s_waitcnt vmcnt(0)\n"
        "RU_c3:\n"
        "v_max3_u32 v22, v22, v24, v25\n"
        "v_cmp_eq_u32 vcc, -1, v22\n"
        "s_cbranch_vccz RU_c2\n"
        "s_sleep 1\n"
        "s_cmp_eq_u32 s66, 0\n"
        "s_cbranch_scc1 RU_c2\n"
        "s_sub_u32 s66, s66, 1\n"
        "s_branch RU_c1\n"
        "RU_c2:\n"
        "v_and_b32 v24, 1, v24\n"
        "v_and_b32 v25, 1, v25\n"
        "v_lshlrev_b32 v24, 13, v24\n"
        "v_lshlrev_b32 v25, 13, v25\n"
        "v_add_u32 v3, v3, v25\n"
        "v_add_u32 v4, v4, v25\n"
        "v_add_u32 v5, v5, v25\n"
        "v_readfirstlane_b32 s49, v25\n"
        "s_nop 1\n"
        "s_add_u32 s49, s49, 2621440\n"
        "v_mov_b32 v16, -1\n"
        "v_mov_b32 v17, -1\n"
        "v_mov_b32 v18, -1\n"
        "v_mov_b32 v19, -1\n"
        "s_mov_b32 s46, %[t1]\n"
        "s_mov_b32 s45, %[t4]\n"
        "s_memtime s[10:11]\n"
        "s_memrealtime s[14:15]\n"
        "s_nop 2\n"
        "s_memrealtime s[16:17]\n"
        "s_nop 2\n"
        "s_memrealtime s[18:19]\n"
        "s_nop 2\n"
        "s_memrealtime s[20:21]\n"
        "s_nop 2\n"
        "s_memrealtime s[22:23]\n"
        "s_nop 2\n"
        "s_memrealtime s[24:25]\n"
        "s_nop 1\n"
        "s_memtime s[12:13]\n"
        "s_memrealtime s[26:27]\n"
        "s_waitcnt lgkmcnt(0)\n"
        "s_mul_i32 s14, s14, 24\n"
        "s_mul_i32 s16, s16, 24\n"
        "s_sub_u32 s16, s16, 17\n"
        "s_max_i32 s14, s14, s16\n"
        "s_mul_i32 s18, s18, 24\n"
        "s_sub_u32 s18, s18, 34\n"
        "s_max_i32 s14, s14, s18\n"
        "s_mul_i32 s20, s20, 24\n"
        "s_sub_u32 s20, s20, 51\n"
        "s_max_i32 s14, s14, s20\n"
        "s_mul_i32 s22, s22, 24\n"
        "s_sub_u32 s22, s22, 68\n"
        "s_max_i32 s14, s14, s22\n"
        "s_mul_i32 s24, s24, 24\n"
        "s_sub_u32 s24, s24, 85\n"
        "s_max_i32 s14, s14, s24\n"
        "s_mul_i32 s26, s26, 24\n"
        "s_sub_u32 s26, s26, 102\n"
        "s_max_i32 s14, s14, s26\n"
        "s_sub_u32 s14, s14, s10\n"
        "s_mov_b32 s42, s14\n"
        "s_mov_b32 s0, s10\n"
        "s_mov_b32 s1, 4000\n"
        "s_add_u32 s44, s10, s42\n"
        "s_sub_u32 s44, s44, 0x1000000\n"
        "s_lshr_b32 s36, s45, 8\n"
        "s_lshl_b32 s35, s36, 1\n"
        "s_mul_i32 s34, s36, 3\n"
        "s_mov_b32 s6, s46\n"
        "s_mov_b32 s5, 1350\n"
        "s_mov_b32 s7, 3072\n"
        "v_mbcnt_lo_u32_b32 v149, -1, 0\n"
        "v_mbcnt_hi_u32_b32 v149, -1, v149\n"
        "v_lshlrev_b32 v150, 5, v149\n"
        "v_and_b32 v149, 3, v149\n"
        "v_and_b32 v150, 0x780, v150\n"
        "v_lshl_add_u32 v149, v149, 2, v150\n"
        "v_add_u32 v149, s7, v149\n"
        "v_add_u32 v9, v9, v24\n"
        "v_add_u32 v10, v10, v24\n"
        "v_add_u32 v11, v11, v24\n"
        "v_mbcnt_lo_u32_b32 v20, -1, 0\n"
        "v_mbcnt_hi_u32_b32 v20, -1, v20\n"
        "v_and_b32 v20, 16, v20\n"
        "v_cmp_eq_u32 s[70:71], 0, v20\n"
        "s_mov_b32 s68, 0\n"
        "s_mov_b32 s69, 1310720\n"
        "s_cmp_eq_u32 s67, 1\n"
        "s_cbranch_scc1 RU1_loop\n"
        "s_cmp_eq_u32 s67, 2\n"
        "s_cbranch_scc1 RU2_loop\n"
        "s_cmp_eq_u32 s67, 3\n"
        "s_cbranch_scc1 RU3_loop\n"
        ".p2align 6\n"
        "s_nop 0\n"
        "RU0_loop:\n"
        "v_mov_b32 v72, 0\n"
        "v_mov_b32 v73, 0\n"
        "v_mov_b32 v74, 0\n"
        "v_mov_b32 v75, 0\n"
        "v_mov_b32 v76, 0\n"
        "v_mov_b32 v77, 0\n"
        "v_mov_b32 v78, 0\n"
        "v_mov_b32 v79, 0\n"
        "v_mov_b32 v88, 0\n"
        "v_mov_b32 v89, 0\n"
        "v_mov_b32 v90, 0\n"
        "v_mov_b32 v91, 0\n"
        "v_mov_b32 v92, 0\n"
        "v_mov_b32 v93, 0\n"
        "v_mov_b32 v94, 0\n"
        "v_mov_b32 v95, 0\n"
        "v_mov_b32 v96, 0\n"
        "v_mov_b32 v97, 0\n"
        "v_mov_b32 v98, 0\n"
        "v_mov_b32 v99, 0\n"
        "v_mov_b32 v100, 0\n"
        "v_mov_b32 v101, 0\n"
        "v_mov_b32 v102, 0\n"
        "v_mov_b32 v103, 0\n"
        "v_mov_b32 v104, 0\n"
        "v_mov_b32 v105, 0\n"
        "v_mov_b32 v106, 0\n"
        "v_mov_b32 v107, 0\n"
        "s_lshl_b32 s72, s64, 14\n"
        "s_add_u32 s72, s72, s49\n"
        "s_and_b32 s65, s64, 3\n"
        "s_lshl_b32 s65, s65, 15\n"
        "s_add_u32 s65, s65, 2293760\n"
        "s_add_u32 s48, s44, s46\n"
        "s_sub_u32 s48, s48, s42\n"
        "s_sub_u32 s48, s48, s0\n"
        "s_max_i32 s48, s48, 0\n"
        "s_min_i32 s48, s48, 0x10000\n"
        "s_mul_i32 s40, s48, s1\n"
        "s_lshr_b32 s40, s40, 16\n"
        "s_sub_u32 s41, s48, s40\n"
        "s_mul_i32 s40, s41, s1\n"
        "s_lshr_b32 s40, s40, 16\n"
        "s_sub_u32 s48, s48, s40\n"
        "s_add_u32 s48, s48, s0\n"
        "s_sub_u32 s48, s48, 620\n"
        "s_memtime s[50:51]\n"
        "s_waitcnt lgkmcnt(0)\n"
        "s_sub_u32 s41, s50, s0\n"
        "s_sub_u32 s50, s50, s48\n"
        "s_cmp_gt_u32 s41, 12000\n"
        "s_cselect_b32 s50, 0, s50\n"
        "s_add_u32 s45, s48, 544\n"
        "s_cmp_gt_i32 s50, 150\n"
        "s_cselect_b32 s48, s45, s48\n"
        "s_cbranch_scc1 RU0_twk\n"
        "s_cmp_gt_i32 s50, -60\n"
        "s_cbranch_scc1 RU0_twy\n"
        "RU0_twt:\n"
        "s_sleep 1\n"
        "s_memtime s[50:51]\n"
        "s_waitcnt lgkmcnt(0)\n"
        "s_sub_u32 s50, s50, s48\n"
        "s_cmp_lt_i32 s50, 0\n"
        "s_cbranch_scc1 RU0_twt\n"
        "RU0_twy:\n"
        "s_memtime s[10:11]\n"
        "s_memrealtime s[14:15]\n"
        "s_nop 2\n"
        "s_memrealtime s[16:17]\n"
        "s_nop 2\n"
        "s_memrealtime s[18:19]\n"
        "s_nop 2\n"
        "s_memrealtime s[20:21]\n"
        "s_nop 2\n"
        "s_memrealtime s[22:23]\n"
        "s_nop 2\n"
        "s_memrealtime s[24:25]\n"
        "s_nop 1\n"
        "s_memtime s[12:13]\n"
        "s_memrealtime s[26:27]\n"
        "s_waitcnt lgkmcnt(0)\n"
        "s_mul_i32 s14, s14, 24\n"
        "s_mul_i32 s16, s16, 24\n"
        "s_sub_u32 s16, s16, 17\n"
        "s_max_i32 s14, s14, s16\n"
        "s_mul_i32 s18, s18, 24\n"
        "s_sub_u32 s18, s18, 34\n"
        "s_max_i32 s14, s14, s18\n"
        "s_mul_i32 s20, s20, 24\n"
        "s_sub_u32 s20, s20, 51\n"
        "s_max_i32 s14, s14, s20\n"
        "s_mul_i32 s22, s22, 24\n"
        "s_sub_u32 s22, s22, 68\n"
        "s_max_i32 s14, s14, s22\n"
        "s_mul_i32 s24, s24, 24\n"
        "s_sub_u32 s24, s24, 85\n"
        "s_max_i32 s14, s14, s24\n"
        "s_mul_i32 s26, s26, 24\n"
        "s_sub_u32 s26, s26, 102\n"
        "s_max_i32 s14, s14, s26\n"
        "s_sub_u32 s14, s14, s10\n"
        "s_sub_u32 s41, s10, s0\n"
        "s_mov_b32 s0, s10\n"
        "s_mul_i32 s41, s41, s1\n"
        "s_lshr_b32 s41, s41, 16\n"
        "s_add_u32 s42, s42, s41\n"
        "s_sub_u32 s40, s12, s10\n"
        "s_sub_u32 s41, s14, s42\n"
        "s_sub_u32 s40, s40, 88\n"
        "s_cmp_gt_u32 s40, 32\n"
        "s_cselect_b32 s41, 0, s41\n"
        "s_add_u32 s42, s42, s41\n"
        "s_min_i32 s41, s41, 120\n"
        "s_max_i32 s41, s41, -120\n"
        "s_mul_i32 s41, s41, 7\n"
        "s_add_u32 s1, s1, s41\n"
        "s_max_i32 s1, s1, 0\n"
        "s_add_u32 s48, s44, s46\n"
        "s_sub_u32 s48, s48, s42\n"
        "s_sub_u32 s48, s48, s0\n"
        "s_max_i32 s48, s48, 0\n"
        "s_min_i32 s48, s48, 0x10000\n"
        "s_mul_i32 s40, s48, s1\n"
        "s_lshr_b32 s40, s40, 16\n"
        "s_sub_u32 s41, s48, s40\n"
        "s_mul_i32 s40, s41, s1\n"
        "s_lshr_b32 s40, s40, 16\n"
        "s_sub_u32 s48, s48, s40\n"
        "s_add_u32 s48, s48, s0\n"
        "s_sub_u32 s48, s48, 76\n"
        "RU0_twk:\n"
        "RU0_twsa:\n"
        "s_memtime s[50:51]\n"
        "s_getpc_b64 s[40:41]\n"
        "RU0_twsb:\n"
        "s_waitcnt lgkmcnt(0)\n"
        "s_sub_u32 s50, s48, s50\n"
        "s_cmp_gt_i32 s50, 96\n"
        "s_cbranch_scc1 RU0_twsa\n"
        "s_max_i32 s50, s50, 0\n"
        "s_and_b32 s50, s50, -4\n"
        "s_sub_u32 s50, RU0_twse-RU0_twsb, s50\n"
        "s_add_u32 s40, s40, s50\n"
        "s_addc_u32 s41, s41, 0\n"
        "s_setpc_b64 s[40:41]\n"
        "s_nop 0\n"
        "s_nop 0\n"
        "s_nop 0\n"
        "s_nop 0\n"
        "s_nop 0\n"
        "s_nop 0\n"
        "s_nop 0\n"
        "s_nop 0\n"
        "s_nop 0\n"
        "s_nop 0\n"
        "s_nop 0\n"
        "s_nop 0\n"
        "s_nop 0\n"
        "s_nop 0\n"
        "s_nop 0\n"
        "s_nop 0\n"
        "s_nop 0\n"
        "s_nop 0\n"
        "s_nop 0\n"
        "s_nop 0\n"
        "s_nop 0\n"
        "s_nop 0\n"
        "s_nop 0\n"
        "s_nop 0\n"
        "RU0_twse:\n"
        "RU0_poll:\n"
        "buffer_load_dwordx4 v[20:23], v3, s[60:63], s65 offen sc1\n"
        "buffer_load_dwordx4 v[24:27], v4, s[60:63], s65 offen sc1\n"
        "s_waitcnt vmcnt(1)\n"
        "ds_write_b128 v1, v[20:23]\n"
        "v_max3_u32 v32, v20, v21, v22\n"
        "s_waitcnt vmcnt(0)\n"
        "ds_write_b128 v1, v[24:27] offset:1024\n"
        "v_max3_u32 v32, v32, v23, v24\n"
        "v_max3_u32 v32, v32, v25, v26\n"
        "v_max_u32 v32, v32, v27\n"
        "v_cmp_eq_u32 vcc, -1, v32\n"
        "v_smfmac_f32_16x16x64_bf16 v[72:75], v[20:23], a[128:135], v13\n"
        "v_smfmac_f32_16x16x64_bf16 v[76:79], v[20:23], a[136:143], v13\n"
        "v_smfmac_f32_16x16x64_bf16 v[72:75], v[24:27], a[144:151], v13\n"
        "v_smfmac_f32_16x16x64_bf16 v[76:79], v[24:27], a[152:159], v13\n"
        "s_cbranch_vccnz RU0_miss\n"
        "RU0_got:\n"
        "s_waitcnt lgkmcnt(0)\n"
        "s_barrier\n"
        "ds_read_b128 v[32:35], v0 offset:0\n"
        "ds_read_b128 v[36:39], v0 offset:1024\n"
        "ds_read_b128 v[40:43], v0 offset:2048\n"
        "ds_read_b128 v[44:47], v0 offset:3072\n"
        "ds_read_b128 v[48:51], v0 offset:4096\n"
        "ds_read_b128 v[52:55], v0 offset:5120\n"
        "ds_read_b128 v[56:59], v0 offset:6144\n"
        "ds_read_b128 v[60:63], v0 offset:7168\n"
        "s_waitcnt lgkmcnt(7)\n"
        "v_smfmac_f32_16x16x64_bf16 v[72:75], v[32:35], a[0:7], v13\n"
        "v_smfmac_f32_16x16x64_bf16 v[76:79], v[32:35], a[8:15], v13\n"
        "s_waitcnt lgkmcnt(6)\n"
        "v_smfmac_f32_16x16x64_bf16 v[72:75], v[36:39], a[16:23], v13\n"
        "v_smfmac_f32_16x16x64_bf16 v[76:79], v[36:39], a[24:31], v13\n"
        "s_waitcnt lgkmcnt(5)\n"
        "v_smfmac_f32_16x16x64_bf16 v[72:75], v[40:43], a[32:39], v13\n"
        "v_smfmac_f32_16x16x64_bf16 v[76:79], v[40:43], a[40:47], v13\n"
        "s_waitcnt lgkmcnt(4)\n"
        "v_smfmac_f32_16x16x64_bf16 v[72:75], v[44:47], a[48:55], v13\n"
        "v_smfmac_f32_16x16x64_bf16 v[76:79], v[44:47], a[56:63], v13\n"
        "s_waitcnt lgkmcnt(3)\n"
        "v_smfmac_f32_16x16x64_bf16 v[72:75], v[48:51], a[64:71], v13\n"
        "v_smfmac_f32_16x16x64_bf16 v[76:79], v[48:51], a[72:79], v13\n"
        "s_waitcnt lgkmcnt(2)\n"
        "v_smfmac_f32_16x16x64_bf16 v[72:75], v[52:55], a[80:87], v13\n"
        "v_smfmac_f32_16x16x64_bf16 v[76:79], v[52:55], a[88:95], v13\n"
        "s_waitcnt lgkmcnt(1)\n"
        "v_smfmac_f32_16x16x64_bf16 v[72:75], v[56:59], a[96:103], v13\n"
        "v_smfmac_f32_16x16x64_bf16 v[76:79], v[56:59], a[104:111], v13\n"
        "s_waitcnt lgkmcnt(0)\n"
        "v_smfmac_f32_16x16x64_bf16 v[72:75], v[60:63], a[112:119], v13\n"
        "v_smfmac_f32_16x16x64_bf16 v[76:79], v[60:63], a[120:127], v13\n"
        "s_nop 3\n"
        "v_pk_add_f32 v[72:73], v[72:73], v[74:75]\n"
        "v_cvt_pk_bf16_f32 v72, v72, v73\n"
        "ds_write_b32 v6, v72\n"
        "s_nop 0\n"
        "v_pk_add_f32 v[76:77], v[76:77], v[78:79]\n"
        "v_cvt_pk_bf16_f32 v76, v76, v77\n"
        "ds_write_b32 v14, v76\n"
        "ds_read_b64_tr_b16 v[32:33], v7 offset:0\n"
        "ds_read_b64_tr_b16 v[34:35], v8 offset:0\n"
        "s_waitcnt lgkmcnt(2)\n"
        "s_barrier\n"
        "ds_read_b64_tr_b16 v[36:37], v7 offset:512\n"
        "ds_read_b64_tr_b16 v[38:39], v8 offset:512\n"
        "s_waitcnt lgkmcnt(2)\n"
        "v_smfmac_f32_16x16x64_bf16 v[88:91], v[32:35], a[160:167], v12\n"
        "ds_read_b64_tr_b16 v[40:41], v7 offset:1024\n"
        "ds_read_b64_tr_b16 v[42:43], v8 offset:1024\n"
        "v_smfmac_f32_16x16x64_bf16 v[92:95], v[32:35], a[192:199], v12\n"
        "ds_read_b64_tr_b16 v[44:45], v7 offset:1536\n"
        "ds_read_b64_tr_b16 v[46:47], v8 offset:1536\n"
        "s_waitcnt lgkmcnt(4)\n"
        "v_smfmac_f32_16x16x64_bf16 v[88:91], v[36:39], a[168:175], v12\n"
        "v_smfmac_f32_16x16x64_bf16 v[92:95], v[36:39], a[200:207], v12\n"
        "s_waitcnt lgkmcnt(2)\n"
        "v_smfmac_f32_16x16x64_bf16 v[88:91], v[40:43], a[176:183], v12\n"
        "v_smfmac_f32_16x16x64_bf16 v[92:95], v[40:43], a[208:215], v12\n"
        "s_waitcnt lgkmcnt(0)\n"
        "v_smfmac_f32_16x16x64_bf16 v[88:91], v[44:47], a[184:191], v12\n"
        "v_smfmac_f32_16x16x64_bf16 v[92:95], v[44:47], a[216:223], v12\n"
        "v_smfmac_f32_16x16x64_bf16 v[96:99], v[32:35], a[224:231], v12\n"
        "v_smfmac_f32_16x16x64_bf16 v[100:103], v[32:35], v[192:199], v12\n"
        "v_cvt_pk_bf16_f32 v128, v88, v89\n"
        "v_cvt_pk_bf16_f32 v129, v90, v91\n"
        "v_smfmac_f32_16x16x64_bf16 v[96:99], v[36:39], a[232:239], v12\n"
        "v_cvt_pk_bf16_f32 v130, v92, v93\n"
        "v_cvt_pk_bf16_f32 v131, v94, v95\n"
        "v_smfmac_f32_16x16x64_bf16 v[100:103], v[36:39], v[200:207], v12\n"
        "v_permlane16_swap_b32 v128, v130\n"
        "v_permlane16_swap_b32 v129, v131\n"
        "buffer_store_dwordx4 v[128:131], v9, s[60:63], s68 offen sc1\n"
        "v_smfmac_f32_16x16x64_bf16 v[96:99], v[40:43], a[240:247], v12\n"
        "v_smfmac_f32_16x16x64_bf16 v[100:103], v[40:43], v[208:215], v12\n"
        "v_smfmac_f32_16x16x64_bf16 v[96:99], v[44:47], a[248:255], v12\n"
        "v_smfmac_f32_16x16x64_bf16 v[100:103], v[44:47], v[216:223], v12\n"
        "v_smfmac_f32_16x16x64_bf16 v[104:107], v[32:35], v[224:231], v12\n"
        "v_smfmac_f32_16x16x64_bf16 v[104:107], v[36:39], v[232:239], v12\n"
        "v_cvt_pk_bf16_f32 v132, v96, v97\n"
        "v_cvt_pk_bf16_f32 v133, v98, v99\n"
        "v_smfmac_f32_16x16x64_bf16 v[104:107], v[40:43], v[240:247], v12\n"
        "v_cvt_pk_bf16_f32 v134, v100, v101\n"
        "v_cvt_pk_bf16_f32 v135, v102, v103\n"
        "v_smfmac_f32_16x16x64_bf16 v[104:107], v[44:47], v[248:255], v12\n"
        "s_nop 0\n"
        "v_permlane16_swap_b32 v132, v134\n"
        "v_permlane16_swap_b32 v133, v135\n"
        "buffer_store_dwordx4 v[132:135], v10, s[60:63], s68 offen sc1\n"
        "s_nop 3\n"
        "v_cvt_pk_bf16_f32 v136, v104, v105\n"
        "v_cvt_pk_bf16_f32 v137, v106, v107\n"
        "v_cvt_pk_bf16_f32 v138, v104, v105\n"
        "v_cvt_pk_bf16_f32 v139, v106, v107\n"
        "s_nop 1\n"
        "v_permlane16_swap_b32 v136, v138\n"
        "v_permlane16_swap_b32 v137, v139\n"
        "s_mov_b64 exec, s[70:71]\n"
        "buffer_store_dwordx4 v[136:139], v11, s[60:63], s68 offen sc1\n"
        "s_mov_b64 exec, -1\n"
        "buffer_load_dword v144, v149, s[60:63], s72 offen sc1\n"
        "s_mov_b64 exec, s[70:71]\n"
        "s_waitcnt vmcnt(0)\n"
        "buffer_store_dwordx4 v[16:19], v11, s[60:63], s69 offen sc1\n"
        "s_mov_b64 exec, -1\n"
        "buffer_store_dwordx4 v[16:19], v9, s[60:63], s69 offen sc1\n"
        "buffer_store_dwordx4 v[16:19], v10, s[60:63], s69 offen sc1\n"
        "RU0_h0:\n"
        "s_nop 1\n"
        "v_max_u32_dpp v144, v144, v144 quad_perm:[1,0,3,2] row_mask:0xf bank_mask:0xf\n"
        "s_nop 1\n"
        "v_max_u32_dpp v144, v144, v144 quad_perm:[2,3,0,1] row_mask:0xf bank_mask:0xf\n"
        "s_nop 1\n"
        "v_max_u32_dpp v144, v144, v144 row_half_mirror row_mask:0xf bank_mask:0xf\n"
        "s_nop 1\n"
        "v_readlane_b32 s10, v144, 0\n"
        "v_readlane_b32 s11, v144, 8\n"
        "v_readlane_b32 s12, v144, 16\n"
        "v_readlane_b32 s13, v144, 24\n"
        "v_readlane_b32 s14, v144, 32\n"
        "v_readlane_b32 s15, v144, 40\n"
        "v_readlane_b32 s16, v144, 48\n"
        "v_readlane_b32 s17, v144, 56\n"
        "s_nop 1\n"
        "s_max_u32 s14, s14, s15\n"
        "s_max_u32 s16, s16, s17\n"
        "s_max_u32 s41, s14, s16\n"
        "s_cmp_eq_u32 s41, -1\n"
        "s_cselect_b32 s48, 0, s44\n"
        "s_sub_u32 s41, s41, s48\n"
        "s_min_u32 s14, s10, s11\n"
        "s_max_u32 s11, s10, s11\n"
        "s_min_u32 s10, s12, s13\n"
        "s_max_u32 s13, s12, s13\n"
        "s_min_u32 s12, s14, s10\n"
        "s_max_u32 s10, s14, s10\n"
        "s_min_u32 s14, s11, s13\n"
        "s_max_u32 s13, s11, s13\n"
        "s_min_u32 s11, s14, s10\n"
        "s_max_u32 s10, s14, s10\n"
        "s_add_u32 s12, s12, s34\n"
        "s_add_u32 s11, s11, s35\n"
        "s_add_u32 s10, s10, s36\n"
        "s_max_u32 s12, s12, s11\n"
        "s_max_u32 s10, s10, s13\n"
        "s_max_u32 s40, s12, s10\n"
        "s_cmp_lg_u32 s13, -1\n"
        "s_cselect_b32 s44, s40, s44\n"
        "s_cbranch_scc1 RU0_h1\n"
        "s_cmp_eq_u32 s66, 0\n"
        "s_cbranch_scc1 RU0_h1\n"
        "s_sub_u32 s66, s66, 1\n"
        "buffer_load_dword v144, v149, s[60:63], s72 offen sc1\n"
        "s_waitcnt vmcnt(0)\n"
        "s_branch RU0_h0\n"
        "RU0_h1:\n"
        "s_add_u32 s5, s5, 6\n"
        "s_min_u32 s5, s5, s41\n"
        "s_add_u32 s46, s5, s6\n"
        "RU0_h2:\n"
        "s_mov_b32 s69, s68\n"
        "s_add_u32 s68, s68, 655360\n"
        "s_cmp_eq_u32 s68, 1966080\n"
        "s_cselect_b32 s68, 0, s68\n"
        "s_add_u32 s64, s64, 1\n"
        "s_cmp_lt_u32 s64, 64\n"
        "s_cbranch_scc1 RU0_loop\n"
        "s_endpgm\n"
        ".p2align 6\n"
        "s_nop 0\n"
        "RU1_loop:\n"
        "v_mov_b32 v72, 0\n"
        "v_mov_b32 v73, 0\n"
        "v_mov_b32 v74, 0\n"
        "v_mov_b32 v75, 0\n"
        "v_mov_b32 v76, 0\n"
        "v_mov_b32 v77, 0\n"
        "v_mov_b32 v78, 0\n"
        "v_mov_b32 v79, 0\n"
        "v_mov_b32 v88, 0\n"
        "v_mov_b32 v89, 0\n"
        "v_mov_b32 v90, 0\n"
        "v_mov_b32 v91, 0\n"
        "v_mov_b32 v92, 0\n"
        "v_mov_b32 v93, 0\n"
        "v_mov_b32 v94, 0\n"
        "v_mov_b32 v95, 0\n"
        "v_mov_b32 v96, 0\n"
        "v_mov_b32 v97, 0\n"
        "v_mov_b32 v98, 0\n"
        "v_mov_b32 v99, 0\n"
        "v_mov_b32 v100, 0\n"
        "v_mov_b32 v101, 0\n"
        "v_mov_b32 v102, 0\n"
        "v_mov_b32 v103, 0\n"
        "v_mov_b32 v104, 0\n"
        "v_mov_b32 v105, 0\n"
        "v_mov_b32 v106, 0\n"
        "v_mov_b32 v107, 0\n"
        "s_lshl_b32 s72, s64, 14\n"
        "s_add_u32 s72, s72, s49\n"
        "s_and_b32 s65, s64, 3\n"
        "s_lshl_b32 s65, s65, 15\n"
        "s_add_u32 s65, s65, 2293760\n"
        "s_add_u32 s48, s44, s46\n"
        "s_sub_u32 s48, s48, s42\n"
        "s_sub_u32 s48, s48, s0\n"
        "s_max_i32 s48, s48, 0\n"
        "s_min_i32 s48, s48, 0x10000\n"
        "s_mul_i32 s40, s48, s1\n"
        "s_lshr_b32 s40, s40, 16\n"
        "s_sub_u32 s41, s48, s40\n"
        "s_mul_i32 s40, s41, s1\n"
        "s_lshr_b32 s40, s40, 16\n"
        "s_sub_u32 s48, s48, s40\n"
        "s_add_u32 s48, s48, s0\n"
        "s_sub_u32 s48, s48, 620\n"
        "s_memtime s[50:51]\n"
        "s_waitcnt lgkmcnt(0)\n"
        "s_sub_u32 s41, s50, s0\n"
        "s_sub_u32 s50, s50, s48\n"
        "s_cmp_gt_u32 s41, 12000\n"
        "s_cselect_b32 s50, 0, s50\n"
        "s_add_u32 s45, s48, 544\n"
        "s_cmp_gt_i32 s50, 150\n"
        "s_cselect_b32 s48, s45, s48\n"
        "s_cbranch_scc1 RU1_twk\n"
        "s_cmp_gt_i32 s50, -60\n"
        "s_cbranch_scc1 RU1_twy\n"
        "RU1_twt:\n"
        "s_sleep 1\n"
        "s_memtime s[50:51]\n"
        "s_waitcnt lgkmcnt(0)\n"
        "s_sub_u32 s50, s50, s48\n"
        "s_cmp_lt_i32 s50, 0\n"
        "s_cbranch_scc1 RU1_twt\n"
        "RU1_twy:\n"
        "s_memtime s[10:11]\n"
        "s_memrealtime s[14:15]\n"
        "s_nop 2\n"
        "s_memrealtime s[16:17]\n"
        "s_nop 2\n"
        "s_memrealtime s[18:19]\n"
        "s_nop 2\n"
        "s_memrealtime s[20:21]\n"
        "s_nop 2\n"
        "s_memrealtime s[22:23]\n"
        "s_nop 2\n"
        "s_memrealtime s[24:25]\n"
        "s_nop 1\n"
        "s_memtime s[12:13]\n"
        "s_memrealtime s[26:27]\n"
        "s_waitcnt lgkmcnt(0)\n"
        "s_mul_i32 s14, s14, 24\n"
        "s_mul_i32 s16, s16, 24\n"
        "s_sub_u32 s16, s16, 17\n"
        "s_max_i32 s14, s14, s16\n"
        "s_mul_i32 s18, s18, 24\n"
        "s_sub_u32 s18, s18, 34\n"
        "s_max_i32 s14, s14, s18\n"
        "s_mul_i32 s20, s20, 24\n"
        "s_sub_u32 s20, s20, 51\n"
        "s_max_i32 s14, s14, s20\n"
        "s_mul_i32 s22, s22, 24\n"
        "s_sub_u32 s22, s22, 68\n"
        "s_max_i32 s14, s14, s22\n"
        "s_mul_i32 s24, s24, 24\n"
        "s_sub_u32 s24, s24, 85\n"
        "s_max_i32 s14, s14, s24\n"
        "s_mul_i32 s26, s26, 24\n"
        "s_sub_u32 s26, s26, 102\n"
        "s_max_i32 s14, s14, s26\n"
        "s_sub_u32 s14, s14, s10\n"
        "s_sub_u32 s41, s10, s0\n"
        "s_mov_b32 s0, s10\n"
        "s_mul_i32 s41, s41, s1\n"
        "s_lshr_b32 s41, s41, 16\n"
        "s_add_u32 s42, s42, s41\n"
        "s_sub_u32 s40, s12, s10\n"
        "s_sub_u32 s41, s14, s42\n"
        "s_sub_u32 s40, s40, 88\n"
        "s_cmp_gt_u32 s40, 32\n"
        "s_cselect_b32 s41, 0, s41\n"
        "s_add_u32 s42, s42, s41\n"
        "s_min_i32 s41, s41, 120\n"
        "s_max_i32 s41, s41, -120\n"
        "s_mul_i32 s41, s41, 7\n"
        "s_add_u32 s1, s1, s41\n"
        "s_max_i32 s1, s1, 0\n"
        "s_add_u32 s48, s44, s46\n"
        "s_sub_u32 s48, s48, s42\n"
        "s_sub_u32 s48, s48, s0\n"
        "s_max_i32 s48, s48, 0\n"
        "s_min_i32 s48, s48, 0x10000\n"
        "s_mul_i32 s40, s48, s1\n"
        "s_lshr_b32 s40, s40, 16\n"
        "s_sub_u32 s41, s48, s40\n"
        "s_mul_i32 s40, s41, s1\n"
        "s_lshr_b32 s40, s40, 16\n"
        "s_sub_u32 s48, s48, s40\n"
        "s_add_u32 s48, s48, s0\n"
        "s_sub_u32 s48, s48, 76\n"
        "RU1_twk:\n"
        "RU1_twsa:\n"
        "s_memtime s[50:51]\n"
        "s_getpc_b64 s[40:41]\n"
        "RU1_twsb:\n"
        "s_waitcnt lgkmcnt(0)\n"
        "s_sub_u32 s50, s48, s50\n"
        "s_cmp_gt_i32 s50, 96\n"
        "s_cbranch_scc1 RU1_twsa\n"
        "s_max_i32 s50, s50, 0\n"
        "s_and_b32 s50, s50, -4\n"
        "s_sub_u32 s50, RU1_twse-RU1_twsb, s50\n"
        "s_add_u32 s40, s40, s50\n"
        "s_addc_u32 s41, s41, 0\n"
        "s_setpc_b64 s[40:41]\n"
        "s_nop 0\n"
        "s_nop 0\n"
        "s_nop 0\n"
        "s_nop 0\n"
        "s_nop 0\n"
        "s_nop 0\n"
        "s_nop 0\n"
        "s_nop 0\n"
        "s_nop 0\n"
        "s_nop 0\n"
        "s_nop 0\n"
        "s_nop 0\n"
        "s_nop 0\n"
        "s_nop 0\n"
        "s_nop 0\n"
        "s_nop 0\n"
        "s_nop 0\n"
        "s_nop 0\n"
        "s_nop 0\n"
        "s_nop 0\n"
        "s_nop 0\n"
        "s_nop 0\n"
        "s_nop 0\n"
        "s_nop 0\n"
        "RU1_twse:\n"
        "RU1_poll:\n"
        "buffer_load_dwordx4 v[20:23], v3, s[60:63], s65 offen sc1\n"
        "buffer_load_dwordx4 v[24:27], v4, s[60:63], s65 offen sc1\n"
        "s_waitcnt vmcnt(1)\n"
        "ds_write_b128 v1, v[20:23]\n"
        "v_max3_u32 v32, v20, v21, v22\n"
        "s_waitcnt vmcnt(0)\n"
        "ds_write_b128 v1, v[24:27] offset:1024\n"
        "v_max3_u32 v32, v32, v23, v24\n"
        "v_max3_u32 v32, v32, v25, v26\n"
        "v_max_u32 v32, v32, v27\n"
        "v_cmp_eq_u32 vcc, -1, v32\n"
        "v_smfmac_f32_16x16x64_bf16 v[72:75], v[20:23], a[96:103], v13\n"
        "v_smfmac_f32_16x16x64_bf16 v[76:79], v[20:23], a[104:111], v13\n"
        "v_smfmac_f32_16x16x64_bf16 v[72:75], v[24:27], a[112:119], v13\n"
        "v_smfmac_f32_16x16x64_bf16 v[76:79], v[24:27], a[120:127], v13\n"
        "s_cbranch_vccnz RU1_miss\n"
        "RU1_got:\n"
        "s_waitcnt lgkmcnt(0)\n"
        "s_barrier\n"
        "ds_read_b128 v[32:35], v0 offset:0\n"
        "ds_read_b128 v[36:39], v0 offset:1024\n"
        "ds_read_b128 v[40:43], v0 offset:2048\n"
        "ds_read_b128 v[44:47], v0 offset:3072\n"
        "ds_read_b128 v[48:51], v0 offset:4096\n"
        "ds_read_b128 v[52:55], v0 offset:5120\n"
        "ds_read_b128 v[64:67], v0 offset:8192\n"
        "ds_read_b128 v[68:71], v0 offset:9216\n"
        "s_waitcnt lgkmcnt(7)\n"
        "v_smfmac_f32_16x16x64_bf16 v[72:75], v[32:35], a[0:7], v13\n"
        "v_smfmac_f32_16x16x64_bf16 v[76:79], v[32:35], a[8:15], v13\n"
        "s_waitcnt lgkmcnt(6)\n"
        "v_smfmac_f32_16x16x64_bf16 v[72:75], v[36:39], a[16:23], v13\n"
        "v_smfmac_f32_16x16x64_bf16 v[76:79], v[36:39], a[24:31], v13\n"
        "s_waitcnt lgkmcnt(5)\n"
        "v_smfmac_f32_16x16x64_bf16 v[72:75], v[40:43], a[32:39], v13\n"
        "v_smfmac_f32_16x16x64_bf16 v[76:79], v[40:43], a[40:47], v13\n"
        "s_waitcnt lgkmcnt(4)\n"
        "v_smfmac_f32_16x16x64_bf16 v[72:75], v[44:47], a[48:55], v13\n"
        "v_smfmac_f32_16x16x64_bf16 v[76:79], v[44:47], a[56:63], v13\n"
        "s_waitcnt lgkmcnt(3)\n"
        "v_smfmac_f32_16x16x64_bf16 v[72:75], v[48:51], a[64:71], v13\n"
        "v_smfmac_f32_16x16x64_bf16 v[76:79], v[48:51], a[72:79], v13\n"
        "s_waitcnt lgkmcnt(2)\n"
        "v_smfmac_f32_16x16x64_bf16 v[72:75], v[52:55], a[80:87], v13\n"
        "v_smfmac_f32_16x16x64_bf16 v[76:79], v[52:55], a[88:95], v13\n"
        "s_waitcnt lgkmcnt(1)\n"
        "v_smfmac_f32_16x16x64_bf16 v[72:75], v[64:67], a[128:135], v13\n"
        "v_smfmac_f32_16x16x64_bf16 v[76:79], v[64:67], a[136:143], v13\n"
        "s_waitcnt lgkmcnt(0)\n"
        "v_smfmac_f32_16x16x64_bf16 v[72:75], v[68:71], a[144:151], v13\n"
        "v_smfmac_f32_16x16x64_bf16 v[76:79], v[68:71], a[152:159], v13\n"
        "s_nop 3\n"
        "v_pk_add_f32 v[72:73], v[72:73], v[74:75]\n"
        "v_cvt_pk_bf16_f32 v72, v72, v73\n"
        "ds_write_b32 v6, v72\n"
        "s_nop 0\n"
        "v_pk_add_f32 v[76:77], v[76:77], v[78:79]\n"
        "v_cvt_pk_bf16_f32 v76, v76, v77\n"
        "ds_write_b32 v14, v76\n"
        "ds_read_b64_tr_b16 v[36:37], v7 offset:512\n"
        "ds_read_b64_tr_b16 v[38:39], v8 offset:512\n"
        "s_waitcnt lgkmcnt(2)\n"
        "s_barrier\n"
        "ds_read_b64_tr_b16 v[40:41], v7 offset:1024\n"
        "ds_read_b64_tr_b16 v[42:43], v8 offset:1024\n"
        "s_waitcnt lgkmcnt(2)\n"
        "v_smfmac_f32_16x16x64_bf16 v[88:91], v[36:39], a[168:175], v12\n"
        "ds_read_b64_tr_b16 v[44:45], v7 offset:1536\n"
        "ds_read_b64_tr_b16 v[46:47], v8 offset:1536\n"
        "v_smfmac_f32_16x16x64_bf16 v[92:95], v[36:39], a[200:207], v12\n"
        "ds_read_b64_tr_b16 v[32:33], v7 offset:0\n"
        "ds_read_b64_tr_b16 v[34:35], v8 offset:0\n"
        "s_waitcnt lgkmcnt(4)\n"
        "v_smfmac_f32_16x16x64_bf16 v[88:91], v[40:43], a[176:183], v12\n"
        "v_smfmac_f32_16x16x64_bf16 v[92:95], v[40:43], a[208:215], v12\n"
        "s_waitcnt lgkmcnt(2)\n"
        "v_smfmac_f32_16x16x64_bf16 v[88:91], v[44:47], a[184:191], v12\n"
        "v_smfmac_f32_16x16x64_bf16 v[92:95], v[44:47], a[216:223], v12\n"
        "s_waitcnt lgkmcnt(0)\n"
        "v_smfmac_f32_16x16x64_bf16 v[88:91], v[32:35], a[160:167], v12\n"
        "v_smfmac_f32_16x16x64_bf16 v[92:95], v[32:35], a[192:199], v12\n"
        "v_smfmac_f32_16x16x64_bf16 v[96:99], v[36:39], a[232:239], v12\n"
        "v_smfmac_f32_16x16x64_bf16 v[100:103], v[36:39], v[200:207], v12\n"
        "v_cvt_pk_bf16_f32 v128, v88, v89\n"
        "v_cvt_pk_bf16_f32 v129, v90, v91\n"
        "v_smfmac_f32_16x16x64_bf16 v[96:99], v[40:43], a[240:247], v12\n"
        "v_cvt_pk_bf16_f32 v130, v92, v93\n"
        "v_cvt_pk_bf16_f32 v131, v94, v95\n"
        "v_smfmac_f32_16x16x64_bf16 v[100:103], v[40:43], v[208:215], v12\n"
        "v_permlane16_swap_b32 v128, v130\n"
        "v_permlane16_swap_b32 v129, v131\n"
        "buffer_store_dwordx4 v[128:131], v9, s[60:63], s68 offen sc1\n"
        "v_smfmac_f32_16x16x64_bf16 v[96:99], v[44:47], a[248:255], v12\n"
        "v_smfmac_f32_16x16x64_bf16 v[100:103], v[44:47], v[216:223], v12\n"
        "v_smfmac_f32_16x16x64_bf16 v[96:99], v[32:35], a[224:231], v12\n"
        "v_smfmac_f32_16x16x64_bf16 v[100:103], v[32:35], v[192:199], v12\n"
        "v_smfmac_f32_16x16x64_bf16 v[104:107], v[36:39], v[232:239], v12\n"
        "v_smfmac_f32_16x16x64_bf16 v[104:107], v[40:43], v[240:247], v12\n"
        "v_cvt_pk_bf16_f32 v132, v96, v97\n"
        "v_cvt_pk_bf16_f32 v133, v98, v99\n"
        "v_smfmac_f32_16x16x64_bf16 v[104:107], v[44:47], v[248:255], v12\n"
        "v_cvt_pk_bf16_f32 v134, v100, v101\n"
        "v_cvt_pk_bf16_f32 v135, v102, v103\n"
        "v_smfmac_f32_16x16x64_bf16 v[104:107], v[32:35], v[224:231], v12\n"
        "s_nop 0\n"
        "v_permlane16_swap_b32 v132, v134\n"
        "v_permlane16_swap_b32 v133, v135\n"
        "buffer_store_dwordx4 v[132:135], v10, s[60:63], s68 offen sc1\n"
        "s_nop 3\n"
        "v_cvt_pk_bf16_f32 v136, v104, v105\n"
        "v_cvt_pk_bf16_f32 v137, v106, v107\n"
        "v_cvt_pk_bf16_f32 v138, v104, v105\n"
        "v_cvt_pk_bf16_f32 v139, v106, v107\n"
        "s_nop 1\n"
        "v_permlane16_swap_b32 v136, v138\n"
        "v_permlane16_swap_b32 v137, v139\n"
        "s_mov_b64 exec, s[70:71]\n"
        "buffer_store_dwordx4 v[136:139], v11, s[60:63], s68 offen sc1\n"
        "s_mov_b64 exec, -1\n"
        "buffer_load_dword v144, v149, s[60:63], s72 offen sc1\n"
        "s_mov_b64 exec, s[70:71]\n"
        "s_waitcnt vmcnt(0)\n"
        "buffer_store_dwordx4 v[16:19], v11, s[60:63], s69 offen sc1\n"
        "s_mov_b64 exec, -1\n"
        "buffer_store_dwordx4 v[16:19], v9, s[60:63], s69 offen sc1\n"
        "buffer_store_dwordx4 v[16:19], v10, s[60:63], s69 offen sc1\n"
        "RU1_h0:\n"
        "s_nop 1\n"
        "v_max_u32_dpp v144, v144, v144 quad_perm:[1,0,3,2] row_mask:0xf bank_mask:0xf\n"
        "s_nop 1\n"
        "v_max_u32_dpp v144, v144, v144 quad_perm:[2,3,0,1] row_mask:0xf bank_mask:0xf\n"
        "s_nop 1\n"
        "v_max_u32_dpp v144, v144, v144 row_half_mirror row_mask:0xf bank_mask:0xf\n"
        "s_nop 1\n"
        "v_readlane_b32 s10, v144, 0\n"
        "v_readlane_b32 s11, v144, 8\n"
        "v_readlane_b32 s12, v144, 16\n"
        "v_readlane_b32 s13, v144, 24\n"
        "v_readlane_b32 s14, v144, 32\n"
        "v_readlane_b32 s15, v144, 40\n"
        "v_readlane_b32 s16, v144, 48\n"
        "v_readlane_b32 s17, v144, 56\n"
        "s_nop 1\n"
        "s_max_u32 s14, s14, s15\n"
        "s_max_u32 s16, s16, s17\n"
        "s_max_u32 s41, s14, s16\n"
        "s_cmp_eq_u32 s41, -1\n"
        "s_cselect_b32 s48, 0, s44\n"
        "s_sub_u32 s41, s41, s48\n"
        "s_min_u32 s14, s10, s11\n"
        "s_max_u32 s11, s10, s11\n"
        "s_min_u32 s10, s12, s13\n"
        "s_max_u32 s13, s12, s13\n"
        "s_min_u32 s12, s14, s10\n"
        "s_max_u32 s10, s14, s10\n"
        "s_min_u32 s14, s11, s13\n"
        "s_max_u32 s13, s11, s13\n"
        "s_min_u32 s11, s14, s10\n"
        "s_max_u32 s10, s14, s10\n"
        "s_add_u32 s12, s12, s34\n"
        "s_add_u32 s11, s11, s35\n"
        "s_add_u32 s10, s10, s36\n"
        "s_max_u32 s12, s12, s11\n"
        "s_max_u32 s10, s10, s13\n"
        "s_max_u32 s40, s12, s10\n"
        "s_cmp_lg_u32 s13, -1\n"
        "s_cselect_b32 s44, s40, s44\n"
        "s_cbranch_scc1 RU1_h1\n"
        "s_cmp_eq_u32 s66, 0\n"
        "s_cbranch_scc1 RU1_h1\n"
        "s_sub_u32 s66, s66, 1\n"
        "buffer_load_dword v144, v149, s[60:63], s72 offen sc1\n"
        "s_waitcnt vmcnt(0)\n"
        "s_branch RU1_h0\n"
        "RU1_h1:\n"
        "s_add_u32 s5, s5, 6\n"
        "s_min_u32 s5, s5, s41\n"
        "s_add_u32 s46, s5, s6\n"
        "RU1_h2:\n"
        "s_mov_b32 s69, s68\n"
        "s_add_u32 s68, s68, 655360\n"
        "s_cmp_eq_u32 s68, 1966080\n"
        "s_cselect_b32 s68, 0, s68\n"
        "s_add_u32 s64, s64, 1\n"
        "s_cmp_lt_u32 s64, 64\n"
        "s_cbranch_scc1 RU1_loop\n"
        "s_endpgm\n"
        ".p2align 6\n"
        "s_nop 0\n"
        "RU2_loop:\n"
        "v_mov_b32 v72, 0\n"
        "v_mov_b32 v73, 0\n"
        "v_mov_b32 v74, 0\n"
        "v_mov_b32 v75, 0\n"
        "v_mov_b32 v76, 0\n"
        "v_mov_b32 v77, 0\n"
        "v_mov_b32 v78, 0\n"
        "v_mov_b32 v79, 0\n"
        "v_mov_b32 v88, 0\n"
        "v_mov_b32 v89, 0\n"
        "v_mov_b32 v90, 0\n"
        "v_mov_b32 v91, 0\n"
        "v_mov_b32 v92, 0\n"
        "v_mov_b32 v93, 0\n"
        "v_mov_b32 v94, 0\n"
        "v_mov_b32 v95, 0\n"
        "v_mov_b32 v96, 0\n"
        "v_mov_b32 v97, 0\n"
        "v_mov_b32 v98, 0\n"
        "v_mov_b32 v99, 0\n"
        "v_mov_b32 v100, 0\n"
        "v_mov_b32 v101, 0\n"
        "v_mov_b32 v102, 0\n"
        "v_mov_b32 v103, 0\n"
        "v_mov_b32 v104, 0\n"
        "v_mov_b32 v105, 0\n"
        "v_mov_b32 v106, 0\n"
        "v_mov_b32 v107, 0\n"
        "s_lshl_b32 s72, s64, 14\n"
        "s_add_u32 s72, s72, s49\n"
        "s_and_b32 s65, s64, 3\n"
        "s_lshl_b32 s65, s65, 15\n"
        "s_add_u32 s65, s65, 2293760\n"
        "s_add_u32 s48, s44, s46\n"
        "s_sub_u32 s48, s48, s42\n"
        "s_sub_u32 s48, s48, s0\n"
        "s_max_i32 s48, s48, 0\n"
        "s_min_i32 s48, s48, 0x10000\n"
        "s_mul_i32 s40, s48, s1\n"
        "s_lshr_b32 s40, s40, 16\n"
        "s_sub_u32 s41, s48, s40\n"
        "s_mul_i32 s40, s41, s1\n"
        "s_lshr_b32 s40, s40, 16\n"
        "s_sub_u32 s48, s48, s40\n"
        "s_add_u32 s48, s48, s0\n"
        "s_sub_u32 s48, s48, 620\n"
        "s_memtime s[50:51]\n"
        "s_waitcnt lgkmcnt(0)\n"
        "s_sub_u32 s41, s50, s0\n"
        "s_sub_u32 s50, s50, s48\n"
        "s_cmp_gt_u32 s41, 12000\n"
        "s_cselect_b32 s50, 0, s50\n"
        "s_add_u32 s45, s48, 544\n"
        "s_cmp_gt_i32 s50, 150\n"
        "s_cselect_b32 s48, s45, s48\n"
        "s_cbranch_scc1 RU2_twk\n"
        "s_cmp_gt_i32 s50, -60\n"
        "s_cbranch_scc1 RU2_twy\n"
        "RU2_twt:\n"
        "s_sleep 1\n"
        "s_memtime s[50:51]\n"
        "s_waitcnt lgkmcnt(0)\n"
        "s_sub_u32 s50, s50, s48\n"
        "s_cmp_lt_i32 s50, 0\n"
        "s_cbranch_scc1 RU2_twt\n"
        "RU2_twy:\n"
        "s_memtime s[10:11]\n"
        "s_memrealtime s[14:15]\n"
        "s_nop 2\n"
        "s_memrealtime s[16:17]\n"
        "s_nop 2\n"
        "s_memrealtime s[18:19]\n"
        "s_nop 2\n"
        "s_memrealtime s[20:21]\n"
        "s_nop 2\n"
        "s_memrealtime s[22:23]\n"
        "s_nop 2\n"
        "s_memrealtime s[24:25]\n"
        "s_nop 1\n"
        "s_memtime s[12:13]\n"
        "s_memrealtime s[26:27]\n"
        "s_waitcnt lgkmcnt(0)\n"
        "s_mul_i32 s14, s14, 24\n"
        "s_mul_i32 s16, s16, 24\n"
        "s_sub_u32 s16, s16, 17\n"
        "s_max_i32 s14, s14, s16\n"
        "s_mul_i32 s18, s18, 24\n"
        "s_sub_u32 s18, s18, 34\n"
        "s_max_i32 s14, s14, s18\n"
        "s_mul_i32 s20, s20, 24\n"
        "s_sub_u32 s20, s20, 51\n"
        "s_max_i32 s14, s14, s20\n"
        "s_mul_i32 s22, s22, 24\n"
        "s_sub_u32 s22, s22, 68\n"
        "s_max_i32 s14, s14, s22\n"
        "s_mul_i32 s24, s24, 24\n"
        "s_sub_u32 s24, s24, 85\n"
        "s_max_i32 s14, s14, s24\n"
        "s_mul_i32 s26, s26, 24\n"
        "s_sub_u32 s26, s26, 102\n"
        "s_max_i32 s14, s14, s26\n"
        "s_sub_u32 s14, s14, s10\n"
        "s_sub_u32 s41, s10, s0\n"
        "s_mov_b32 s0, s10\n"
        "s_mul_i32 s41, s41, s1\n"
        "s_lshr_b32 s41, s41, 16\n"
        "s_add_u32 s42, s42, s41\n"
        "s_sub_u32 s40, s12, s10\n"
        "s_sub_u32 s41, s14, s42\n"
        "s_sub_u32 s40, s40, 88\n"
        "s_cmp_gt_u32 s40, 32\n"
        "s_cselect_b32 s41, 0, s41\n"
        "s_add_u32 s42, s42, s41\n"
        "s_min_i32 s41, s41, 120\n"
        "s_max_i32 s41, s41, -120\n"
        "s_mul_i32 s41, s41, 7\n"
        "s_add_u32 s1, s1, s41\n"
        "s_max_i32 s1, s1, 0\n"
        "s_add_u32 s48, s44, s46\n"
        "s_sub_u32 s48, s48, s42\n"
        "s_sub_u32 s48, s48, s0\n"
        "s_max_i32 s48, s48, 0\n"
        "s_min_i32 s48, s48, 0x10000\n"
        "s_mul_i32 s40, s48, s1\n"
        "s_lshr_b32 s40, s40, 16\n"
        "s_sub_u32 s41, s48, s40\n"
        "s_mul_i32 s40, s41, s1\n"
        "s_lshr_b32 s40, s40, 16\n"
        "s_sub_u32 s48, s48, s40\n"
        "s_add_u32 s48, s48, s0\n"
        "s_sub_u32 s48, s48, 76\n"
        "RU2_twk:\n"
        "RU2_twsa:\n"
        "s_memtime s[50:51]\n"
        "s_getpc_b64 s[40:41]\n"
        "RU2_twsb:\n"
        "s_waitcnt lgkmcnt(0)\n"
        "s_sub_u32 s50, s48, s50\n"
        "s_cmp_gt_i32 s50, 96\n"
        "s_cbranch_scc1 RU2_twsa\n"
        "s_max_i32 s50, s50, 0\n"
        "s_and_b32 s50, s50, -4\n"
        "s_sub_u32 s50, RU2_twse-RU2_twsb, s50\n"
        "s_add_u32 s40, s40, s50\n"
        "s_addc_u32 s41, s41, 0\n"
        "s_setpc_b64 s[40:41]\n"
        "s_nop 0\n"
        "s_nop 0\n"
        "s_nop 0\n"
        "s_nop 0\n"
        "s_nop 0\n"
        "s_nop 0\n"
        "s_nop 0\n"
        "s_nop 0\n"
        "s_nop 0\n"
        "s_nop 0\n"
        "s_nop 0\n"
        "s_nop 0\n"
        "s_nop 0\n"
        "s_nop 0\n"
        "s_nop 0\n"
        "s_nop 0\n"
        "s_nop 0\n"
        "s_nop 0\n"
        "s_nop 0\n"
        "s_nop 0\n"
        "s_nop 0\n"
        "s_nop 0\n"
        "s_nop 0\n"
        "s_nop 0\n"
        "RU2_twse:\n"
        "RU2_poll:\n"
        "buffer_load_dwordx4 v[20:23], v3, s[60:63], s65 offen sc1\n"
        "buffer_load_dwordx4 v[24:27], v4, s[60:63], s65 offen sc1\n"
        "buffer_load_dwordx4 v[28:31], v5, s[60:63], s65 offen sc1\n"
        "s_waitcnt vmcnt(2)\n"
        "ds_write_b128 v1, v[20:23]\n"
        "v_max3_u32 v32, v20, v21, v22\n"
        "s_waitcnt vmcnt(1)\n"
        "ds_write_b128 v1, v[24:27] offset:1024\n"
        "v_max3_u32 v32, v32, v23, v24\n"
        "v_max3_u32 v32, v32, v25, v26\n"
        "s_waitcnt vmcnt(0)\n"
        "ds_write_b128 v1, v[28:31] offset:2048\n"
        "v_max3_u32 v32, v32, v27, v28\n"
        "v_max3_u32 v32, v32, v29, v30\n"
        "v_max_u32 v32, v32, v31\n"
        "v_cmp_eq_u32 vcc, -1, v32\n"
        "v_smfmac_f32_16x16x64_bf16 v[72:75], v[20:23], a[48:55], v13\n"
        "v_smfmac_f32_16x16x64_bf16 v[76:79], v[20:23], a[56:63], v13\n"
        "v_smfmac_f32_16x16x64_bf16 v[72:75], v[24:27], a[64:71], v13\n"
        "v_smfmac_f32_16x16x64_bf16 v[76:79], v[24:27], a[72:79], v13\n"
        "s_cbranch_vccnz RU2_miss\n"
        "RU2_got:\n"
        "s_waitcnt lgkmcnt(0)\n"
        "s_barrier\n"
        "ds_read_b128 v[32:35], v0 offset:0\n"
        "ds_read_b128 v[36:39], v0 offset:1024\n"
        "ds_read_b128 v[40:43], v0 offset:2048\n"
        "v_smfmac_f32_16x16x64_bf16 v[72:75], v[28:31], a[80:87], v13\n"
        "ds_read_b128 v[56:59], v0 offset:6144\n"
        "ds_read_b128 v[60:63], v0 offset:7168\n"
        "v_smfmac_f32_16x16x64_bf16 v[76:79], v[28:31], a[88:95], v13\n"
        "ds_read_b128 v[64:67], v0 offset:8192\n"
        "ds_read_b128 v[68:71], v0 offset:9216\n"
        "s_waitcnt lgkmcnt(6)\n"
        "v_smfmac_f32_16x16x64_bf16 v[72:75], v[32:35], a[0:7], v13\n"
        "v_smfmac_f32_16x16x64_bf16 v[76:79], v[32:35], a[8:15], v13\n"
        "s_waitcnt lgkmcnt(5)\n"
        "v_smfmac_f32_16x16x64_bf16 v[72:75], v[36:39], a[16:23], v13\n"
        "v_smfmac_f32_16x16x64_bf16 v[76:79], v[36:39], a[24:31], v13\n"
        "s_waitcnt lgkmcnt(4)\n"
        "v_smfmac_f32_16x16x64_bf16 v[72:75], v[40:43], a[32:39], v13\n"
        "v_smfmac_f32_16x16x64_bf16 v[76:79], v[40:43], a[40:47], v13\n"
        "s_waitcnt lgkmcnt(3)\n"
        "v_smfmac_f32_16x16x64_bf16 v[72:75], v[56:59], a[96:103], v13\n"
        "v_smfmac_f32_16x16x64_bf16 v[76:79], v[56:59], a[104:111], v13\n"
        "s_waitcnt lgkmcnt(2)\n"
        "v_smfmac_f32_16x16x64_bf16 v[72:75], v[60:63], a[112:119], v13\n"
        "v_smfmac_f32_16x16x64_bf16 v[76:79], v[60:63], a[120:127], v13\n"
        "s_waitcnt lgkmcnt(1)\n"
        "v_smfmac_f32_16x16x64_bf16 v[72:75], v[64:67], a[128:135], v13\n"
        "v_smfmac_f32_16x16x64_bf16 v[76:79], v[64:67], a[136:143], v13\n"
        "s_waitcnt lgkmcnt(0)\n"
        "v_smfmac_f32_16x16x64_bf16 v[72:75], v[68:71], a[144:151], v13\n"
        "v_smfmac_f32_16x16x64_bf16 v[76:79], v[68:71], a[152:159], v13\n"
        "s_nop 3\n"
        "v_pk_add_f32 v[72:73], v[72:73], v[74:75]\n"
        "v_cvt_pk_bf16_f32 v72, v72, v73\n"
        "ds_write_b32 v6, v72\n"
        "s_nop 0\n"
        "v_pk_add_f32 v[76:77], v[76:77], v[78:79]\n"
        "v_cvt_pk_bf16_f32 v76, v76, v77\n"
        "ds_write_b32 v14, v76\n"
        "ds_read_b64_tr_b16 v[40:41], v7 offset:1024\n"
        "ds_read_b64_tr_b16 v[42:43], v8 offset:1024\n"
        "s_waitcnt lgkmcnt(2)\n"
        "s_barrier\n"
        "ds_read_b64_tr_b16 v[44:45], v7 offset:1536\n"
        "ds_read_b64_tr_b16 v[46:47], v8 offset:1536\n"
        "s_waitcnt lgkmcnt(2)\n"
        "v_smfmac_f32_16x16x64_bf16 v[88:91], v[40:43], a[176:183], v12\n"
        "ds_read_b64_tr_b16 v[32:33], v7 offset:0\n"
        "ds_read_b64_tr_b16 v[34:35], v8 offset:0\n"
        "v_smfmac_f32_16x16x64_bf16 v[92:95], v[40:43], a[208:215], v12\n"
        "ds_read_b64_tr_b16 v[36:37], v7 offset:512\n"
        "ds_read_b64_tr_b16 v[38:39], v8 offset:512\n"
        "s_waitcnt lgkmcnt(4)\n"
        "v_smfmac_f32_16x16x64_bf16 v[88:91], v[44:47], a[184:191], v12\n"
        "v_smfmac_f32_16x16x64_bf16 v[92:95], v[44:47], a[216:223], v12\n"
        "s_waitcnt lgkmcnt(2)\n"
        "v_smfmac_f32_16x16x64_bf16 v[88:91], v[32:35], a[160:167], v12\n"
        "v_smfmac_f32_16x16x64_bf16 v[92:95], v[32:35], a[192:199], v12\n"
        "s_waitcnt lgkmcnt(0)\n"
        "v_smfmac_f32_16x16x64_bf16 v[88:91], v[36:39], a[168:175], v12\n"
        "v_smfmac_f32_16x16x64_bf16 v[92:95], v[36:39], a[200:207], v12\n"
        "v_smfmac_f32_16x16x64_bf16 v[96:99], v[40:43], a[240:247], v12\n"
        "v_smfmac_f32_16x16x64_bf16 v[100:103], v[40:43], v[208:215], v12\n"
        "v_cvt_pk_bf16_f32 v128, v88, v89\n"
        "v_cvt_pk_bf16_f32 v129, v90, v91\n"
        "v_smfmac_f32_16x16x64_bf16 v[96:99], v[44:47], a[248:255], v12\n"
        "v_cvt_pk_bf16_f32 v130, v92, v93\n"
        "v_cvt_pk_bf16_f32 v131, v94, v95\n"
        "v_smfmac_f32_16x16x64_bf16 v[100:103], v[44:47], v[216:223], v12\n"
        "v_permlane16_swap_b32 v128, v130\n"
        "v_permlane16_swap_b32 v129, v131\n"
        "buffer_store_dwordx4 v[128:131], v9, s[60:63], s68 offen sc1\n"
        "v_smfmac_f32_16x16x64_bf16 v[96:99], v[32:35], a[224:231], v12\n"
        "v_smfmac_f32_16x16x64_bf16 v[100:103], v[32:35], v[192:199], v12\n"
        "v_smfmac_f32_16x16x64_bf16 v[96:99], v[36:39], a[232:239], v12\n"
        "v_smfmac_f32_16x16x64_bf16 v[100:103], v[36:39], v[200:207], v12\n"
        "v_smfmac_f32_16x16x64_bf16 v[104:107], v[40:43], v[240:247], v12\n"
        "v_smfmac_f32_16x16x64_bf16 v[104:107], v[44:47], v[248:255], v12\n"
        "v_cvt_pk_bf16_f32 v132, v96, v97\n"
        "v_cvt_pk_bf16_f32 v133, v98, v99\n"
        "v_smfmac_f32_16x16x64_bf16 v[104:107], v[32:35], v[224:231], v12\n"
        "v_cvt_pk_bf16_f32 v134, v100, v101\n"
        "v_cvt_pk_bf16_f32 v135, v102, v103\n"
        "v_smfmac_f32_16x16x64_bf16 v[104:107], v[36:39], v[232:239], v12\n"
        "s_nop 0\n"
        "v_permlane16_swap_b32 v132, v134\n"
        "v_permlane16_swap_b32 v133, v135\n"
        "buffer_store_dwordx4 v[132:135], v10, s[60:63], s68 offen sc1\n"
        "s_nop 3\n"
        "v_cvt_pk_bf16_f32 v136, v104, v105\n"
        "v_cvt_pk_bf16_f32 v137, v106, v107\n"
        "v_cvt_pk_bf16_f32 v138, v104, v105\n"
        "v_cvt_pk_bf16_f32 v139, v106, v107\n"
        "s_nop 1\n"
        "v_permlane16_swap_b32 v136, v138\n"
        "v_permlane16_swap_b32 v137, v139\n"
        "s_mov_b64 exec, s[70:71]\n"
        "buffer_store_dwordx4 v[136:139], v11, s[60:63], s68 offen sc1\n"
        "s_mov_b64 exec, -1\n"
        "buffer_load_dword v144, v149, s[60:63], s72 offen sc1\n"
        "s_mov_b64 exec, s[70:71]\n"
        "s_waitcnt vmcnt(0)\n"
        "buffer_store_dwordx4 v[16:19], v11, s[60:63], s69 offen sc1\n"
        "s_mov_b64 exec, -1\n"
        "buffer_store_dwordx4 v[16:19], v9, s[60:63], s69 offen sc1\n"
        "buffer_store_dwordx4 v[16:19], v10, s[60:63], s69 offen sc1\n"
        "RU2_h0:\n"
        "s_nop 1\n"
        "v_max_u32_dpp v144, v144, v144 quad_perm:[1,0,3,2] row_mask:0xf bank_mask:0xf\n"
        "s_nop 1\n"
        "v_max_u32_dpp v144, v144, v144 quad_perm:[2,3,0,1] row_mask:0xf bank_mask:0xf\n"
        "s_nop 1\n"
        "v_max_u32_dpp v144, v144, v144 row_half_mirror row_mask:0xf bank_mask:0xf\n"
        "s_nop 1\n"
        "v_readlane_b32 s10, v144, 0\n"
        "v_readlane_b32 s11, v144, 8\n"
        "v_readlane_b32 s12, v144, 16\n"
        "v_readlane_b32 s13, v144, 24\n"
        "v_readlane_b32 s14, v144, 32\n"
        "v_readlane_b32 s15, v144, 40\n"
        "v_readlane_b32 s16, v144, 48\n"
        "v_readlane_b32 s17, v144, 56\n"
        "s_nop 1\n"
        "s_max_u32 s14, s14, s15\n"
        "s_max_u32 s16, s16, s17\n"
        "s_max_u32 s41, s14, s16\n"
        "s_cmp_eq_u32 s41, -1\n"
        "s_cselect_b32 s48, 0, s44\n"
        "s_sub_u32 s41, s41, s48\n"
        "s_min_u32 s14, s10, s11\n"
        "s_max_u32 s11, s10, s11\n"
        "s_min_u32 s10, s12, s13\n"
        "s_max_u32 s13, s12, s13\n"
        "s_min_u32 s12, s14, s10\n"
        "s_max_u32 s10, s14, s10\n"
        "s_min_u32 s14, s11, s13\n"
        "s_max_u32 s13, s11, s13\n"
        "s_min_u32 s11, s14, s10\n"
        "s_max_u32 s10, s14, s10\n"
        "s_add_u32 s12, s12, s34\n"
        "s_add_u32 s11, s11, s35\n"
        "s_add_u32 s10, s10, s36\n"
        "s_max_u32 s12, s12, s11\n"
        "s_max_u32 s10, s10, s13\n"
        "s_max_u32 s40, s12, s10\n"
        "s_cmp_lg_u32 s13, -1\n"
        "s_cselect_b32 s44, s40, s44\n"
        "s_cbranch_scc1 RU2_h1\n"
        "s_cmp_eq_u32 s66, 0\n"
        "s_cbranch_scc1 RU2_h1\n"
        "s_sub_u32 s66, s66, 1\n"
        "buffer_load_dword v144, v149, s[60:63], s72 offen sc1\n"
        "s_waitcnt vmcnt(0)\n"
        "s_branch RU2_h0\n"
        "RU2_h1:\n"
        "s_add_u32 s5, s5, 6\n"
        "s_min_u32 s5, s5, s41\n"
        "s_add_u32 s46, s5, s6\n"
        "RU2_h2:\n"
        "s_mov_b32 s69, s68\n"
        "s_add_u32 s68, s68, 655360\n"
        "s_cmp_eq_u32 s68, 1966080\n"
        "s_cselect_b32 s68, 0, s68\n"
        "s_add_u32 s64, s64, 1\n"
        "s_cmp_lt_u32 s64, 64\n"
        "s_cbranch_scc1 RU2_loop\n"
        "s_endpgm\n"
        ".p2align 6\n"
        "s_nop 0\n"
        "RU3_loop:\n"
        "v_mov_b32 v72, 0\n"
        "v_mov_b32 v73, 0\n"
        "v_mov_b32 v74, 0\n"
        "v_mov_b32 v75, 0\n"
        "v_mov_b32 v76, 0\n"
        "v_mov_b32 v77, 0\n"
        "v_mov_b32 v78, 0\n"
        "v_mov_b32 v79, 0\n"
        "v_mov_b32 v88, 0\n"
        "v_mov_b32 v89, 0\n"
        "v_mov_b32 v90, 0\n"
        "v_mov_b32 v91, 0\n"
        "v_mov_b32 v92, 0\n"
        "v_mov_b32 v93, 0\n"
        "v_mov_b32 v94, 0\n"
        "v_mov_b32 v95, 0\n"
        "v_mov_b32 v96, 0\n"
        "v_mov_b32 v97, 0\n"
        "v_mov_b32 v98, 0\n"
        "v_mov_b32 v99, 0\n"
        "v_mov_b32 v100, 0\n"
        "v_mov_b32 v101, 0\n"
        "v_mov_b32 v102, 0\n"
        "v_mov_b32 v103, 0\n"
        "v_mov_b32 v104, 0\n"
        "v_mov_b32 v105, 0\n"
        "v_mov_b32 v106, 0\n"
        "v_mov_b32 v107, 0\n"
        "s_lshl_b32 s72, s64, 14\n"
        "s_add_u32 s72, s72, s49\n"
        "s_and_b32 s65, s64, 3\n"
        "s_lshl_b32 s65, s65, 15\n"
        "s_add_u32 s65, s65, 2293760\n"
        "s_add_u32 s48, s44, s46\n"
        "s_sub_u32 s48, s48, s42\n"
        "s_sub_u32 s48, s48, s0\n"
        "s_max_i32 s48, s48, 0\n"
        "s_min_i32 s48, s48, 0x10000\n"
        "s_mul_i32 s40, s48, s1\n"
        "s_lshr_b32 s40, s40, 16\n"
        "s_sub_u32 s41, s48, s40\n"
        "s_mul_i32 s40, s41, s1\n"
        "s_lshr_b32 s40, s40, 16\n"
        "s_sub_u32 s48, s48, s40\n"
        "s_add_u32 s48, s48, s0\n"
        "s_sub_u32 s48, s48, 620\n"
        "s_memtime s[50:51]\n"
        "s_waitcnt lgkmcnt(0)\n"
        "s_sub_u32 s41, s50, s0\n"
        "s_sub_u32 s50, s50, s48\n"
        "s_cmp_gt_u32 s41, 12000\n"
        "s_cselect_b32 s50, 0, s50\n"
        "s_add_u32 s45, s48, 544\n"
        "s_cmp_gt_i32 s50, 150\n"
        "s_cselect_b32 s48, s45, s48\n"
        "s_cbranch_scc1 RU3_twk\n"
        "s_cmp_gt_i32 s50, -60\n"
        "s_cbranch_scc1 RU3_twy\n"
        "RU3_twt:\n"
        "s_sleep 1\n"
        "s_memtime s[50:51]\n"
        "s_waitcnt lgkmcnt(0)\n"
        "s_sub_u32 s50, s50, s48\n"
        "s_cmp_lt_i32 s50, 0\n"
        "s_cbranch_scc1 RU3_twt\n"
        "RU3_twy:\n"
        "s_memtime s[10:11]\n"
        "s_memrealtime s[14:15]\n"
        "s_nop 2\n"
        "s_memrealtime s[16:17]\n"
        "s_nop 2\n"
        "s_memrealtime s[18:19]\n"
        "s_nop 2\n"
        "s_memrealtime s[20:21]\n"
        "s_nop 2\n"
        "s_memrealtime s[22:23]\n"
        "s_nop 2\n"
        "s_memrealtime s[24:25]\n"
        "s_nop 1\n"
        "s_memtime s[12:13]\n"
        "s_memrealtime s[26:27]\n"
        "s_waitcnt lgkmcnt(0)\n"
        "s_mul_i32 s14, s14, 24\n"
        "s_mul_i32 s16, s16, 24\n"
        "s_sub_u32 s16, s16, 17\n"
        "s_max_i32 s14, s14, s16\n"
        "s_mul_i32 s18, s18, 24\n"
        "s_sub_u32 s18, s18, 34\n"
        "s_max_i32 s14, s14, s18\n"
        "s_mul_i32 s20, s20, 24\n"
        "s_sub_u32 s20, s20, 51\n"
        "s_max_i32 s14, s14, s20\n"
        "s_mul_i32 s22, s22, 24\n"
        "s_sub_u32 s22, s22, 68\n"
        "s_max_i32 s14, s14, s22\n"
        "s_mul_i32 s24, s24, 24\n"
        "s_sub_u32 s24, s24, 85\n"
        "s_max_i32 s14, s14, s24\n"
        "s_mul_i32 s26, s26, 24\n"
        "s_sub_u32 s26, s26, 102\n"
        "s_max_i32 s14, s14, s26\n"
        "s_sub_u32 s14, s14, s10\n"
        "s_sub_u32 s41, s10, s0\n"
        "s_mov_b32 s0, s10\n"
        "s_mul_i32 s41, s41, s1\n"
        "s_lshr_b32 s41, s41, 16\n"
        "s_add_u32 s42, s42, s41\n"
        "s_sub_u32 s40, s12, s10\n"
        "s_sub_u32 s41, s14, s42\n"
        "s_sub_u32 s40, s40, 88\n"
        "s_cmp_gt_u32 s40, 32\n"
        "s_cselect_b32 s41, 0, s41\n"
        "s_add_u32 s42, s42, s41\n"
        "s_min_i32 s41, s41, 120\n"
        "s_max_i32 s41, s41, -120\n"
        "s_mul_i32 s41, s41, 7\n"
        "s_add_u32 s1, s1, s41\n"
        "s_max_i32 s1, s1, 0\n"
        "s_add_u32 s48, s44, s46\n"
        "s_sub_u32 s48, s48, s42\n"
        "s_sub_u32 s48, s48, s0\n"
        "s_max_i32 s48, s48, 0\n"
        "s_min_i32 s48, s48, 0x10000\n"
        "s_mul_i32 s40, s48, s1\n"
        "s_lshr_b32 s40, s40, 16\n"
        "s_sub_u32 s41, s48, s40\n"
        "s_mul_i32 s40, s41, s1\n"
        "s_lshr_b32 s40, s40, 16\n"
        "s_sub_u32 s48, s48, s40\n"
        "s_add_u32 s48, s48, s0\n"
        "s_sub_u32 s48, s48, 76\n"
        "RU3_twk:\n"
        "RU3_twsa:\n"
        "s_memtime s[50:51]\n"
        "s_getpc_b64 s[40:41]\n"
        "RU3_twsb:\n"
        "s_waitcnt lgkmcnt(0)\n"
        "s_sub_u32 s50, s48, s50\n"
        "s_cmp_gt_i32 s50, 96\n"
        "s_cbranch_scc1 RU3_twsa\n"
        "s_max_i32 s50, s50, 0\n"
        "s_and_b32 s50, s50, -4\n"
        "s_sub_u32 s50, RU3_twse-RU3_twsb, s50\n"
        "s_add_u32 s40, s40, s50\n"
        "s_addc_u32 s41, s41, 0\n"
        "s_setpc_b64 s[40:41]\n"
        "s_nop 0\n"
        "s_nop 0\n"
        "s_nop 0\n"
        "s_nop 0\n"
        "s_nop 0\n"
        "s_nop 0\n"
        "s_nop 0\n"
        "s_nop 0\n"
        "s_nop 0\n"
        "s_nop 0\n"
        "s_nop 0\n"
        "s_nop 0\n"
        "s_nop 0\n"
        "s_nop 0\n"
        "s_nop 0\n"
        "s_nop 0\n"
        "s_nop 0\n"
        "s_nop 0\n"
        "s_nop 0\n"
        "s_nop 0\n"
        "s_nop 0\n"
        "s_nop 0\n"
        "s_nop 0\n"
        "s_nop 0\n"
        "RU3_twse:\n"
        "RU3_poll:\n"
        "buffer_load_dwordx4 v[20:23], v3, s[60:63], s65 offen sc1\n"
        "buffer_load_dwordx4 v[24:27], v4, s[60:63], s65 offen sc1\n"
        "buffer_load_dwordx4 v[28:31], v5, s[60:63], s65 offen sc1\n"
        "s_waitcnt vmcnt(2)\n"
        "ds_write_b128 v1, v[20:23]\n"
        "v_max3_u32 v32, v20, v21, v22\n"
        "s_waitcnt vmcnt(1)\n"
        "ds_write_b128 v1, v[24:27] offset:1024\n"
        "v_max3_u32 v32, v32, v23, v24\n"
        "v_max3_u32 v32, v32, v25, v26\n"
        "s_waitcnt vmcnt(0)\n"
        "ds_write_b128 v1, v[28:31] offset:2048\n"
        "v_max3_u32 v32, v32, v27, v28\n"
        "v_max3_u32 v32, v32, v29, v30\n"
        "v_max_u32 v32, v32, v31\n"
        "v_cmp_eq_u32 vcc, -1, v32\n"
        "v_smfmac_f32_16x16x64_bf16 v[72:75], v[20:23], a[0:7], v13\n"
        "v_smfmac_f32_16x16x64_bf16 v[76:79], v[20:23], a[8:15], v13\n"
        "v_smfmac_f32_16x16x64_bf16 v[72:75], v[24:27], a[16:23], v13\n"
        "v_smfmac_f32_16x16x64_bf16 v[76:79], v[24:27], a[24:31], v13\n"
        "s_cbranch_vccnz RU3_miss\n"
        "RU3_got:\n"
        "s_waitcnt lgkmcnt(0)\n"
        "s_barrier\n"
        "ds_read_b128 v[44:47], v0 offset:3072\n"
        "ds_read_b128 v[48:51], v0 offset:4096\n"
        "ds_read_b128 v[52:55], v0 offset:5120\n"
        "v_smfmac_f32_16x16x64_bf16 v[72:75], v[28:31], a[32:39], v13\n"
        "ds_read_b128 v[56:59], v0 offset:6144\n"
        "ds_read_b128 v[60:63], v0 offset:7168\n"
        "v_smfmac_f32_16x16x64_bf16 v[76:79], v[28:31], a[40:47], v13\n"
        "ds_read_b128 v[64:67], v0 offset:8192\n"
        "ds_read_b128 v[68:71], v0 offset:9216\n"
        "s_waitcnt lgkmcnt(6)\n"
        "v_smfmac_f32_16x16x64_bf16 v[72:75], v[44:47], a[48:55], v13\n"
        "v_smfmac_f32_16x16x64_bf16 v[76:79], v[44:47], a[56:63], v13\n"
        "s_waitcnt lgkmcnt(5)\n"
        "v_smfmac_f32_16x16x64_bf16 v[72:75], v[48:51], a[64:71], v13\n"
        "v_smfmac_f32_16x16x64_bf16 v[76:79], v[48:51], a[72:79], v13\n"
        "s_waitcnt lgkmcnt(4)\n"
        "v_smfmac_f32_16x16x64_bf16 v[72:75], v[52:55], a[80:87], v13\n"
        "v_smfmac_f32_16x16x64_bf16 v[76:79], v[52:55], a[88:95], v13\n"
        "s_waitcnt lgkmcnt(3)\n"
        "v_smfmac_f32_16x16x64_bf16 v[72:75], v[56:59], a[96:103], v13\n"
        "v_smfmac_f32_16x16x64_bf16 v[76:79], v[56:59], a[104:111], v13\n"
        "s_waitcnt lgkmcnt(2)\n"
        "v_smfmac_f32_16x16x64_bf16 v[72:75], v[60:63], a[112:119], v13\n"
        "v_smfmac_f32_16x16x64_bf16 v[76:79], v[60:63], a[120:127], v13\n"
        "s_waitcnt lgkmcnt(1)\n"
        "v_smfmac_f32_16x16x64_bf16 v[72:75], v[64:67], a[128:135], v13\n"
        "v_smfmac_f32_16x16x64_bf16 v[76:79], v[64:67], a[136:143], v13\n"
        "s_waitcnt lgkmcnt(0)\n"
        "v_smfmac_f32_16x16x64_bf16 v[72:75], v[68:71], a[144:151], v13\n"
        "v_smfmac_f32_16x16x64_bf16 v[76:79], v[68:71], a[152:159], v13\n"
        "s_nop 3\n"
        "v_pk_add_f32 v[72:73], v[72:73], v[74:75]\n"
        "v_cvt_pk_bf16_f32 v72, v72, v73\n"
        "ds_write_b32 v6, v72\n"
        "s_nop 0\n"
        "v_pk_add_f32 v[76:77], v[76:77], v[78:79]\n"
        "v_cvt_pk_bf16_f32 v76, v76, v77\n"
        "ds_write_b32 v14, v76\n"
        "ds_read_b64_tr_b16 v[44:45], v7 offset:1536\n"
        "ds_read_b64_tr_b16 v[46:47], v8 offset:1536\n"
        "s_waitcnt lgkmcnt(2)\n"
        "s_barrier\n"
        "ds_read_b64_tr_b16 v[32:33], v7 offset:0\n"
        "ds_read_b64_tr_b16 v[34:35], v8 offset:0\n"
        "s_waitcnt lgkmcnt(2)\n"
        "v_smfmac_f32_16x16x64_bf16 v[88:91], v[44:47], a[184:191], v12\n"
        "ds_read_b64_tr_b16 v[36:37], v7 offset:512\n"
        "ds_read_b64_tr_b16 v[38:39], v8 offset:512\n"
        "v_smfmac_f32_16x16x64_bf16 v[92:95], v[44:47], a[216:223], v12\n"
        "ds_read_b64_tr_b16 v[40:41], v7 offset:1024\n"
        "ds_read_b64_tr_b16 v[42:43], v8 offset:1024\n"
        "s_waitcnt lgkmcnt(4)\n"
        "v_smfmac_f32_16x16x64_bf16 v[88:91], v[32:35], a[160:167], v12\n"
        "v_smfmac_f32_16x16x64_bf16 v[92:95], v[32:35], a[192:199], v12\n"
        "s_waitcnt lgkmcnt(2)\n"
        "v_smfmac_f32_16x16x64_bf16 v[88:91], v[36:39], a[168:175], v12\n"
        "v_smfmac_f32_16x16x64_bf16 v[92:95], v[36:39], a[200:207], v12\n"
        "s_waitcnt lgkmcnt(0)\n"
        "v_smfmac_f32_16x16x64_bf16 v[88:91], v[40:43], a[176:183], v12\n"
        "v_smfmac_f32_16x16x64_bf16 v[92:95], v[40:43], a[208:215], v12\n"
        "v_smfmac_f32_16x16x64_bf16 v[96:99], v[44:47], a[248:255], v12\n"
        "v_smfmac_f32_16x16x64_bf16 v[100:103], v[44:47], v[216:223], v12\n"
        "v_cvt_pk_bf16_f32 v128, v88, v89\n"
        "v_cvt_pk_bf16_f32 v129, v90, v91\n"
        "v_smfmac_f32_16x16x64_bf16 v[96:99], v[32:35], a[224:231], v12\n"
        "v_cvt_pk_bf16_f32 v130, v92, v93\n"
        "v_cvt_pk_bf16_f32 v131, v94, v95\n"
        "v_smfmac_f32_16x16x64_bf16 v[100:103], v[32:35], v[192:199], v12\n"
        "v_permlane16_swap_b32 v128, v130\n"
        "v_permlane16_swap_b32 v129, v131\n"
        "buffer_store_dwordx4 v[128:131], v9, s[60:63], s68 offen sc1\n"
        "v_smfmac_f32_16x16x64_bf16 v[96:99], v[36:39], a[232:239], v12\n"
        "v_smfmac_f32_16x16x64_bf16 v[100:103], v[36:39], v[200:207], v12\n"
        "v_smfmac_f32_16x16x64_bf16 v[96:99], v[40:43], a[240:247], v12\n"
        "v_smfmac_f32_16x16x64_bf16 v[100:103], v[40:43], v[208:215], v12\n"
        "v_smfmac_f32_16x16x64_bf16 v[104:107], v[44:47], v[248:255], v12\n"
        "v_smfmac_f32_16x16x64_bf16 v[104:107], v[32:35], v[224:231], v12\n"
        "v_cvt_pk_bf16_f32 v132, v96, v97\n"
        "v_cvt_pk_bf16_f32 v133, v98, v99\n"
        "v_smfmac_f32_16x16x64_bf16 v[104:107], v[36:39], v[232:239], v12\n"
        "v_cvt_pk_bf16_f32 v134, v100, v101\n"
        "v_cvt_pk_bf16_f32 v135, v102, v103\n"
        "v_smfmac_f32_16x16x64_bf16 v[104:107], v[40:43], v[240:247], v12\n"
        "s_nop 0\n"
        "v_permlane16_swap_b32 v132, v134\n"
        "v_permlane16_swap_b32 v133, v135\n"
        "buffer_store_dwordx4 v[132:135], v10, s[60:63], s68 offen sc1\n"
        "s_nop 3\n"
        "v_cvt_pk_bf16_f32 v136, v104, v105\n"
        "v_cvt_pk_bf16_f32 v137, v106, v107\n"
        "v_cvt_pk_bf16_f32 v138, v104, v105\n"
        "v_cvt_pk_bf16_f32 v139, v106, v107\n"
        "s_nop 1\n"
        "v_permlane16_swap_b32 v136, v138\n"
        "v_permlane16_swap_b32 v137, v139\n"
        "s_mov_b64 exec, s[70:71]\n"
        "buffer_store_dwordx4 v[136:139], v11, s[60:63], s68 offen sc1\n"
        "s_mov_b64 exec, -1\n"
        "buffer_load_dword v144, v149, s[60:63], s72 offen sc1\n"
        "s_mov_b64 exec, s[70:71]\n"
        "s_waitcnt vmcnt(0)\n"
        "buffer_store_dwordx4 v[16:19], v11, s[60:63], s69 offen sc1\n"
        "s_mov_b64 exec, -1\n"
        "buffer_store_dwordx4 v[16:19], v9, s[60:63], s69 offen sc1\n"
        "buffer_store_dwordx4 v[16:19], v10, s[60:63], s69 offen sc1\n"
        "RU3_h0:\n"
        "s_nop 1\n"
        "v_max_u32_dpp v144, v144, v144 quad_perm:[1,0,3,2] row_mask:0xf bank_mask:0xf\n"
        "s_nop 1\n"
        "v_max_u32_dpp v144, v144, v144 quad_perm:[2,3,0,1] row_mask:0xf bank_mask:0xf\n"
        "s_nop 1\n"
        "v_max_u32_dpp v144, v144, v144 row_half_mirror row_mask:0xf bank_mask:0xf\n"
        "s_nop 1\n"
        "v_readlane_b32 s10, v144, 0\n"
        "v_readlane_b32 s11, v144, 8\n"
        "v_readlane_b32 s12, v144, 16\n"
        "v_readlane_b32 s13, v144, 24\n"
        "v_readlane_b32 s14, v144, 32\n"
        "v_readlane_b32 s15, v144, 40\n"
        "v_readlane_b32 s16, v144, 48\n"
        "v_readlane_b32 s17, v144, 56\n"
        "s_nop 1\n"
        "s_max_u32 s14, s14, s15\n"
        "s_max_u32 s16, s16, s17\n"
        "s_max_u32 s41, s14, s16\n"
        "s_cmp_eq_u32 s41, -1\n"
        "s_cselect_b32 s48, 0, s44\n"
        "s_sub_u32 s41, s41, s48\n"
        "s_min_u32 s14, s10, s11\n"
        "s_max_u32 s11, s10, s11\n"
        "s_min_u32 s10, s12, s13\n"
        "s_max_u32 s13, s12, s13\n"
        "s_min_u32 s12, s14, s10\n"
        "s_max_u32 s10, s14, s10\n"
        "s_min_u32 s14, s11, s13\n"
        "s_max_u32 s13, s11, s13\n"
        "s_min_u32 s11, s14, s10\n"
        "s_max_u32 s10, s14, s10\n"
        "s_add_u32 s12, s12, s34\n"
        "s_add_u32 s11, s11, s35\n"
        "s_add_u32 s10, s10, s36\n"
        "s_max_u32 s12, s12, s11\n"
        "s_max_u32 s10, s10, s13\n"
        "s_max_u32 s40, s12, s10\n"
        "s_cmp_lg_u32 s13, -1\n"
        "s_cselect_b32 s44, s40, s44\n"
        "s_cbranch_scc1 RU3_h1\n"
        "s_cmp_eq_u32 s66, 0\n"
        "s_cbranch_scc1 RU3_h1\n"
        "s_sub_u32 s66, s66, 1\n"
        "buffer_load_dword v144, v149, s[60:63], s72 offen sc1\n"
        "s_waitcnt vmcnt(0)\n"
        "s_branch RU3_h0\n"
        "RU3_h1:\n"
        "s_add_u32 s5, s5, 6\n"
        "s_min_u32 s5, s5, s41\n"
        "s_add_u32 s46, s5, s6\n"
        "RU3_h2:\n"
        "s_mov_b32 s69, s68\n"
        "s_add_u32 s68, s68, 655360\n"
        "s_cmp_eq_u32 s68, 1966080\n"
        "s_cselect_b32 s68, 0, s68\n"
        "s_add_u32 s64, s64, 1\n"
        "s_cmp_lt_u32 s64, 64\n"
        "s_cbranch_scc1 RU3_loop\n"
        "s_endpgm\n"
        "s_endpgm\n"
        "RU0_miss:\n"
        "s_waitcnt lgkmcnt(0)\n"
        "s_cmp_eq_u32 s66, 0\n"
        "s_cbranch_scc1 RU0_got\n"
        "s_sub_u32 s66, s66, 1\n"
        "s_nop 7\n"
        "v_mov_b32 v72, 0\n"
        "v_mov_b32 v73, 0\n"
        "v_mov_b32 v74, 0\n"
        "v_mov_b32 v75, 0\n"
        "v_mov_b32 v76, 0\n"
        "v_mov_b32 v77, 0\n"
        "v_mov_b32 v78, 0\n"
        "v_mov_b32 v79, 0\n"
        "s_branch RU0_poll\n"
        "RU1_miss:\n"
        "s_waitcnt lgkmcnt(0)\n"
        "s_cmp_eq_u32 s66, 0\n"
        "s_cbranch_scc1 RU1_got\n"
        "s_sub_u32 s66, s66, 1\n"
        "s_nop 7\n"
        "v_mov_b32 v72, 0\n"
        "v_mov_b32 v73, 0\n"
        "v_mov_b32 v74, 0\n"
        "v_mov_b32 v75, 0\n"
        "v_mov_b32 v76, 0\n"
        "v_mov_b32 v77, 0\n"
        "v_mov_b32 v78, 0\n"
        "v_mov_b32 v79, 0\n"
        "s_branch RU1_poll\n"
        "RU2_miss:\n"
        "s_waitcnt lgkmcnt(0)\n"
        "s_cmp_eq_u32 s66, 0\n"
        "s_cbranch_scc1 RU2_got\n"
        "s_sub_u32 s66, s66, 1\n"
        "s_nop 7\n"
        "v_mov_b32 v72, 0\n"
        "v_mov_b32 v73, 0\n"
        "v_mov_b32 v74, 0\n"
        "v_mov_b32 v75, 0\n"
        "v_mov_b32 v76, 0\n"
        "v_mov_b32 v77, 0\n"
        "v_mov_b32 v78, 0\n"
        "v_mov_b32 v79, 0\n"
        "s_branch RU2_poll\n"
        "RU3_miss:\n"
        "s_waitcnt lgkmcnt(0)\n"
        "s_cmp_eq_u32 s66, 0\n"
        "s_cbranch_scc1 RU3_got\n"
        "s_sub_u32 s66, s66, 1\n"
        "s_nop 7\n"
        "v_mov_b32 v72, 0\n"
        "v_mov_b32 v73, 0\n"
        "v_mov_b32 v74, 0\n"
        "v_mov_b32 v75, 0\n"
        "v_mov_b32 v76, 0\n"
        "v_mov_b32 v77, 0\n"
        "v_mov_b32 v78, 0\n"
        "v_mov_b32 v79, 0\n"
        "s_branch RU3_poll\n"
        "RU_fin:\n"
        "s_endpgm\n"
        ROLE_OPS(p.wv, p.wo) : "v32", "v33", "v34", "v35", "v36", "v37", "v38", "v39", "v40", "v41", "v42", "v43", "v44", "v45", "v46", "v47", "v48", "v49", "v50", "v51", "v52", "v53", "v54", "v55", "v56", "v57", "v58", "v59", "v60", "v61", "v62", "v63", "v64", "v65", "v66", "v67", "v68", "v69", "v70", "v71", "v72", "v73", "v74", "v75", "v76", "v77", "v78", "v79", "v80", "v81", "v82", "v83", "v84", "v85", "v86", "v87", "v88", "v89", "v90", "v91", "v92", "v93", "v94", "v95", "v96", "v97", "v98", "v99", "v100", "v101", "v102", "v103", "v104", "v105", "v106", "v107", "v108", "v109", "v110", "v111", "v112", "v113", "v114", "v115", "v116", "v117", "v118", "v119", "v120", "v121", "v122", "v123", "v124", "v125", "v126", "v127", "v128", "v129", "v130", "v131", "v132", "v133", "v134", "v135", "v136", "v137", "v138", "v139", "v140", "v141", "v142", "v143", "v144", "v145", "v146", "v147", "v148", "v149", "v150", "v151", "v152", "v153", "v154", "v155", "v156", "v157", "v158", "v159", "v160", "v161", "v162", "v163", "v164", "v165", "v166", "v167", "v168", "v169", "v170", "v171", "v172", "v173", "v174", "v175", "v176", "v177", "v178", "v179", "v180", "v181", "v182", "v183", "v184", "v185", "v186", "v187", "v188", "v189", "v190", "v192", "v193", "v194", "v195", "v196", "v197", "v198", "v199", "v200", "v201", "v202", "v203", "v204", "v205", "v206", "v207", "v208", "v209", "v210", "v211", "v212", "v213", "v214", "v215", "v216", "v217", "v218", "v219", "v220", "v221", "v222", "v223", "v224", "v225", "v226", "v227", "v228", "v229", "v230", "v231", "v232", "v233", "v234", "v235", "v236", "v237", "v238", "v239", "v240", "v241", "v242", "v243", "v244", "v245", "v246", "v247", "v248", "v249", "v250", "v251", "v252", "v253", "v254", "v255", "s38", "s39", "s40", "s41", "s42", "s43", "s44", "s45", "s46", "s47", "s48", "s49", "s50", "s51", "s52", "s53", "s54", "s55", "s56", "s57", "s58", "s59", "s60", "s61", "s62", "s63", "s64", "s65", "s66", "s67", "s68", "s69", "s70", "s71", "s72", "s73", "s74", "s75", "s76", "s77", "s78", "s79", "s80", "s81", "s82", "s83", "s84", "s85", "s86", "s87", "s88", "s89", "s90", "s91", "s92", "s93", "s94", "s95", "s96", "s97", "s98", "s99", "vcc", "scc", "memory", "m0", "a0", "a1", "a2", "a3", "a4", "a5", "a6", "a7", "a8", "a9", "a10", "a11", "a12", "a13", "a14", "a15", "a16", "a17", "a18", "a19", "a20", "a21", "a22", "a23", "a24", "a25", "a26", "a27", "a28", "a29", "a30", "a31", "a32", "a33", "a34", "a35", "a36", "a37", "a38", "a39", "a40", "a41", "a42", "a43", "a44", "a45", "a46", "a47", "a48", "a49", "a50", "a51", "a52", "a53", "a54", "a55", "a56", "a57", "a58", "a59", "a60", "a61", "a62", "a63", "a64", "a65", "a66", "a67", "a68", "a69", "a70", "a71", "a72", "a73", "a74", "a75", "a76", "a77", "a78", "a79", "a80", "a81", "a82", "a83", "a84", "a85", "a86", "a87", "a88", "a89", "a90", "a91", "a92", "a93", "a94", "a95", "a96", "a97", "a98", "a99", "a100", "a101", "a102", "a103", "a104", "a105", "a106", "a107", "a108", "a109", "a110", "a111", "a112", "a113", "a114", "a115", "a116", "a117", "a118", "a119", "a120", "a121", "a122", "a123", "a124", "a125", "a126", "a127", "a128", "a129", "a130", "a131", "a132", "a133", "a134", "a135", "a136", "a137", "a138", "a139", "a140", "a141", "a142", "a143", "a144", "a145", "a146", "a147", "a148", "a149", "a150", "a151", "a152", "a153", "a154", "a155", "a156", "a157", "a158", "a159", "a160", "a161", "a162", "a163", "a164", "a165", "a166", "a167", "a168", "a169", "a170", "a171", "a172", "a173", "a174", "a175", "a176", "a177", "a178", "a179", "a180", "a181", "a182", "a183", "a184", "a185", "a186", "a187", "a188", "a189", "a190", "a191", "a192", "a193", "a194", "a195", "a196", "a197", "a198", "a199", "a200", "a201", "a202", "a203", "a204", "a205", "a206", "a207", "a208", "a209", "a210", "a211", "a212", "a213", "a214", "a215", "a216", "a217", "a218", "a219", "a220", "a221", "a222", "a223", "a224", "a225", "a226", "a227", "a228", "a229", "a230", "a231", "a232", "a233", "a234", "a235", "a236", "a237", "a238", "a239", "a240", "a241", "a242", "a243", "a244", "a245", "a246", "a247", "a248", "a249", "a250", "a251", "a252", "a253", "a254", "a255");
    __builtin_unreachable();
}

DI u32 calibrate(rsrc_t rs, int wave, int lane, int reps) {
    const u32 cal = wave == 1 ? CAL0 : CAL1;
    u32 best[2] = {~0u, ~0u};
    for (int rep = 0; rep < reps; ++rep)
#pragma unroll
        for (int q = 0; q < 2; ++q) {
            DRAIN();
            u32 t0 = (u32)__builtin_amdgcn_s_memtime();
            SB();
            if (lane == 0) stc((u4)(0u), rs, cal + q * 8192 + rep * 128, 0);
            DRAIN();
            SB();
            u32 t1 = (u32)__builtin_amdgcn_s_memtime();
            best[q] = min(best[q], t1 - t0);
        }
    return best[1] < best[0] ? 1 : 0;
}
DI void role_cal(rsrc_t rs, int wave) {
    const int lane = lane_id();
    bool dead = false;
    const u32 ask = hello_ask(rs, wave, lane);
    for (int i = 0; i < 0; ++i) __builtin_amdgcn_s_sleep(16);
    const u32 near = wave == 1 || wave == 2 ? calibrate(rs, wave, lane, 0) : 0;
    hello_wait(rs, wave, lane, ask, dead);
    if ((wave == 1 || wave == 2) && lane == 0) stc1(near, rs, F_PAR + (wave - 1) * 4, 0);
}

DI void role_r2(const Params& p, rsrc_t rs, int c, int wave) {
    const int lane = lane_id(), tid = wave * 64 + lane;
    bool dead = false;
    const u32 ask = hello_ask(rs, wave, lane);
    if (tid < 96) {
        int j = tid & 15, q = tid >> 4, slot = q >> 1;
        u4 d = (u4)(SENT);
        if (slot == 0) { d = *(const u4*)(p.mem0 + (1 ? 2 * (j >> 2) + (j & 1) : j & 7) * 640 + 16 * c + 8 * (1 ? (j >> 1) & 1 : j >> 3)); d = (u4){nos(d.x), nos(d.y), nos(d.z), nos(d.w)}; }
        stc(d, rs, amap(c * 256 + j * 16) + (q & 1) * 8192 + slot * M_STR, M_OFF);
    }
    if (0) { stc((u4)(SENT), rs, c * 2048 + (tid & 127) * 16 + (tid >> 7) * 81920, PT); stc((u4)(SENT), rs, c * 2048 + (tid & 127) * 16 + (tid >> 7) * 81920, PT + 2 * 81920); }
    for (int sl = c; sl < 64; sl += 40) {
        stc((u4)(SENT), rs, tid * 16, HN + sl * 16384); stc((u4)(SENT), rs, tid * 16 + 8192, HN + sl * 16384);
        if (tid < 128) stc((u4)(SENT), rs, (tid >> 6) * 8192 + 4096 + (tid & 63) * 16, HN + sl * 16384);
    }
    warm_slow(p, c, wave);
    DRAIN(); BAR();
    const u32 near = !0 && c == 0 && (wave == 1 || wave == 2) ? calibrate(rs, wave, lane, 10) : 0;
    hello_wait(rs, wave, lane, ask, dead);
    if (tid == 0) stc1(0, rs, F_I2 + c * 4, 0);
    if (!0 && c == 0 && (wave == 1 || wave == 2) && lane == 0) stc1(near, rs, F_PAR + (wave - 1) * 4, 0);
    const u32 par1 = get_par(rs, lane, dead, 1), par0 = get_par(rs, lane, dead, 0);
    const u32 g = lane >> 4, col = lane & 15, tok = 16 * (wave & 1) + col;
    const u32 hz = 1 ? (col >> 1) & 1 : col >> 3, hr = 1 ? 2 * (col >> 2) + (col & 1) : col & 7;
    u32x32 ad = {};
#pragma unroll
    for (int mq = 0; mq < 2; ++mq) {
        ad[mq] = c * 16384 + (8 * wave + 4 * mq + g) * 256 + col * 16 + par0;
        ad[2 + mq] = col < 8 ? amap((8 * wave + 4 * mq + g) * 640 + col * 16) + par1 : 0xF0000000u;
    }
#pragma unroll
    for (int i = 0; i < 4; ++i) ad[4 + i] = amap(ppos(8 * wave + 4 * (i >> 1) + g, 8 + (i & 1) * 16 + col)) + par1;
    if (1) ad[2] = amap(ppos(8 * wave + 4 * hz + g, hr)) + par1;
    ad[8] = (8 * wave + (g >> 1)) * 1024 + (g & 1) * 256 + col * 16;
    ad[9] = (8 * wave + (g >> 1)) * 2560 + (g & 1) * 512 + col * 16;
    ad[10] = (8 * wave + g) * 2560 + 2048 + col * 16;
    ad[11] = amap(c * 256 + (1 ? 4 * (lane >> 4) + 2 * ((lane >> 2) & 1) + ((lane >> 3) & 1) : ((lane >> 2) & 1) * 8 + (lane >> 3)) * 16) + par1;
    if (1) ad[11] += (lane & 3) * 4;
    ad[12] = amap(c * 256 + col * 16) + par1;
    ad[13] = (wave * 8 + g) * 64 + col * 4;
    ad[14] = (1 ? ((lane >> 3) & 1) * 4 + (lane >> 4) : 1 ? ((lane >> 3) & 1) * 4 + ((lane >> 4) & 1) * 2 + (lane >> 5) : ((lane >> 3) & 3) * 2 + (lane >> 5)) * 64 + (lane & 7) * 8;
    ad[15] = (lane & (1 ? 2 : 8)) ? 0xEEEE : 0x4444;
    ad[16] = (lane & 31) * 16;
    ad[17] = ((g & 1) * 32 + tok) * 16 + (g >> 1) * 8;
    ad[18] = tok * 4;
    ad[19] = wave < 2 ? tok * 1280 + 32 * c + 8 * g : 0xF0000000u;
    ad[20] = 2048 + (wave * 128 + lane) * 16;
    ad[21] = 2048 + ((wave & 1) * 64 + lane) * 16;
    ad[22] = wave < 2 ? c * 2048 + wave * 1024 + lane * 16 : 0xF0000000u;
    { const u32 hd = 1 ? 8 * wave + (lane & 7) : lane & 31; ad[23] = 3072 + (hd >> WSH) * 128 + (hd & ((1 << WSH) - 1)) * 4 + par1; }
    wait_flags4(rs, F_B, lane, dead);
    wait_flags(rs, F_I1, 32, lane, dead);
    wait_flags(rs, F_IU, 32, lane, dead);
    wait_flags(rs, F_I2, 40, lane, dead);
    const int h = c;
    asm volatile(
        "s_mov_b32 s60, %[r0]\n"
        "s_mov_b32 s61, %[r1]\n"
        "s_mov_b32 s62, 0x0f000000\n"
        "s_mov_b32 s63, 0x00020000\n"
        "s_mov_b32 s67, %[wave]\n"
        "s_mov_b32 s58, %[h]\n"
        "s_mov_b32 s59, %[ts]\n"
        "s_mov_b32 s66, 0x100000\n"
        "s_mov_b32 s64, 0\n"
        "s_lshl_b32 s71, s67, 10\n"
        "s_getpc_b64 s[92:93]\n"
        "CPR2:\n"
        "s_add_u32 s92, s92, R2_loop-CPR2\n"
        "s_addc_u32 s93, s93, 0\n"
        "s_mov_b32 s94, R2_fin-R2_loop\n"
        "s_mov_b32 s95, 0x00020000\n"
        "v_mbcnt_lo_u32_b32 v255, -1, 0\n"
        "v_mbcnt_hi_u32_b32 v255, -1, v255\n"
        "v_lshlrev_b32 v255, 7, v255\n"
        "s_lshl_b32 s65, s67, 13\n"
        "v_add_u32 v255, s65, v255\n"
        "buffer_load_dword v255, v255, s[92:95], 0 offen\n"
        "s_mov_b32 s72, %[a0]\n"
        "s_mov_b32 s73, %[a1]\n"
        "s_mov_b32 s74, 2621440\n"
        "s_mov_b32 s75, 0x00020000\n"
        "v_mov_b32 v24, -1\n"
        "v_mov_b32 v25, -1\n"
        "v_mov_b32 v26, -1\n"
        "v_mov_b32 v27, -1\n"
        "v_readfirstlane_b32 s56, v23\n"
        "s_mov_b32 s38, %[t9]\n"
        "s_nop 3\n"
        "s_sub_u32 s56, s56, 3072\n"
        "s_lshl_b32 s70, s58, 15\n"
        "s_add_u32 s70, s70, 55574528\n"
        "s_lshl_b32 s68, s58, 10\n"
        "s_add_u32 s68, s68, 52428800\n"
        "s_lshr_b32 s69, s58, 2\n"
        "s_lshl_b32 s69, s69, 2\n"
        "s_add_u32 s69, s69, 4292608\n"
        "s_mov_b32 s76, 0\n"
        "s_mul_i32 s76, s76, 1310720\n"
        "s_add_u32 s76, s76, s70\n"
        "s_add_u32 s77, s76, 4096\n"
        "buffer_load_dwordx4 a[0:3], v8, s[60:63], s76 offen offset:0 sc1\n"
        "buffer_load_dwordx4 a[4:7], v8, s[60:63], s76 offen offset:2048 sc1\n"
        "buffer_load_dwordx4 a[8:11], v8, s[60:63], s76 offen offset:512 sc1\n"
        "buffer_load_dwordx4 a[12:15], v8, s[60:63], s76 offen offset:2560 sc1\n"
        "buffer_load_dwordx4 a[16:19], v8, s[60:63], s77 offen offset:0 sc1\n"
        "buffer_load_dwordx4 a[20:23], v8, s[60:63], s77 offen offset:2048 sc1\n"
        "buffer_load_dwordx4 a[24:27], v8, s[60:63], s77 offen offset:512 sc1\n"
        "buffer_load_dwordx4 a[28:31], v8, s[60:63], s77 offen offset:2560 sc1\n"
        "s_mov_b32 s46, %[t2]\n"
        "s_sub_u32 s47, %[t3], s46\n"
        "s_mov_b32 s39, %[t6]\n"
        "s_mov_b32 s55, 2621440\n"
        "s_memtime s[0:1]\n"
        "s_memrealtime s[4:5]\n"
        "s_nop 2\n"
        "s_memrealtime s[6:7]\n"
        "s_nop 2\n"
        "s_memrealtime s[8:9]\n"
        "s_nop 2\n"
        "s_memrealtime s[10:11]\n"
        "s_nop 2\n"
        "s_memrealtime s[12:13]\n"
        "s_nop 2\n"
        "s_memrealtime s[14:15]\n"
        "s_nop 1\n"
        "s_memtime s[2:3]\n"
        "s_memrealtime s[16:17]\n"
        "s_waitcnt lgkmcnt(0)\n"
        "s_mul_i32 s4, s4, 24\n"
        "s_mul_i32 s6, s6, 24\n"
        "s_sub_u32 s6, s6, 17\n"
        "s_max_i32 s4, s4, s6\n"
        "s_mul_i32 s8, s8, 24\n"
        "s_sub_u32 s8, s8, 34\n"
        "s_max_i32 s4, s4, s8\n"
        "s_mul_i32 s10, s10, 24\n"
        "s_sub_u32 s10, s10, 51\n"
        "s_max_i32 s4, s4, s10\n"
        "s_mul_i32 s12, s12, 24\n"
        "s_sub_u32 s12, s12, 68\n"
        "s_max_i32 s4, s4, s12\n"
        "s_mul_i32 s14, s14, 24\n"
        "s_sub_u32 s14, s14, 85\n"
        "s_max_i32 s4, s4, s14\n"
        "s_mul_i32 s16, s16, 24\n"
        "s_sub_u32 s16, s16, 102\n"
        "s_max_i32 s4, s4, s16\n"
        "s_sub_u32 s4, s4, s0\n"
        "s_mov_b32 s42, s4\n"
        "s_mov_b32 s34, s0\n"
        "s_mov_b32 s35, 4000\n"
        "s_add_u32 s44, s0, s42\n"
        "s_sub_u32 s44, s44, 0x1000000\n"
        "s_mov_b32 s36, s39\n"
        "s_mov_b32 s32, 5100\n"
        "s_mov_b32 s33, s44\n"
        "s_cmp_ge_u32 s58, 20\n"
        "s_cselect_b32 s65, 4, 0\n"
        "s_sub_u32 s45, s58, s65\n"
        "s_lshr_b32 s37, s45, 2\n"
        "s_lshl_b32 s37, s37, 7\n"
        "s_and_b32 s65, s45, 3\n"
        "s_lshl_b32 s65, s65, 2\n"
        "s_add_u32 s37, s37, s65\n"
        "s_add_u32 s37, s37, s56\n"
        "s_add_u32 s37, s37, 4096\n"
        "s_cmp_ge_u32 s58, 36\n"
        "s_cselect_b32 s37, 4026531840, s37\n"
        "s_sub_u32 s65, s58, 16\n"
        "s_cmp_lt_u32 s65, 4\n"
        "s_cselect_b32 s37, 4026531840, s37\n"
        "s_cmp_eq_u32 s67, 0\n"
        "s_cselect_b32 s37, s37, 4026531840\n"
        "s_mov_b32 s88, 4026531840\n"
        "v_mov_b32 v36, 0\n"
        "s_mov_b32 s78, 4227584\n"
        "s_mov_b32 s79, 4305152\n"
        "v_mov_b32 v28, -1\n"
        "v_mov_b32 v32, 0\n"
        "s_mov_b32 s65, 1998848\n"
        "buffer_load_dword v37, off, s[60:63], s65 sc1\n"
        "s_mov_b32 s65, 38793216\n"
        "buffer_load_dword v37, off, s[60:63], s65 offset:4092 sc1\n"
        "buffer_load_dword v192, v23, s[60:63], s55 offen sc1\n"
        "s_waitcnt vmcnt(0)\n"
        "R2_ih0:\n"
        "s_nop 1\n"
        "v_max_u32_dpp v192, v192, v192 row_ror:8 row_mask:0xf bank_mask:0xf\n"
        "s_nop 1\n"
        "v_max_u32_dpp v192, v192, v192 row_ror:4 row_mask:0xf bank_mask:0xf\n"
        "s_nop 1\n"
        "v_max_u32_dpp v192, v192, v192 row_ror:2 row_mask:0xf bank_mask:0xf\n"
        "s_nop 1\n"
        "v_max_u32_dpp v192, v192, v192 row_ror:1 row_mask:0xf bank_mask:0xf\n"
        "s_nop 1\n"
        "v_readlane_b32 s40, v192, 0\n"
        "v_readlane_b32 s45, v192, 16\n"
        "s_nop 1\n"
        "s_max_u32 s40, s40, s45\n"
        "s_cmp_lg_u32 s40, -1\n"
        "s_cselect_b32 s44, s40, s44\n"
        "s_cbranch_scc1 R2_ih1\n"
        "s_cmp_eq_u32 s66, 0\n"
        "s_cbranch_scc1 R2_ih1\n"
        "s_sub_u32 s66, s66, 1\n"
        "buffer_load_dword v192, v23, s[60:63], s55 offen sc1\n"
        "s_waitcnt vmcnt(0)\n"
        "s_branch R2_ih0\n"
        "R2_ih1:\n"
        "R2_ih2:\n"
        "s_sub_u32 s45, s44, s33\n"
        "s_mov_b32 s33, s44\n"
        "s_add_u32 s32, s32, 6\n"
        "s_min_u32 s32, s32, s45\n"
        "s_sub_u32 s39, s32, s36\n"
        "s_add_u32 s55, s55, 16384\n"
        ".p2align 6\n"
        "s_nop 0\n"
        "s_nop 0\n"
        "s_nop 0\n"
        "s_nop 0\n"
        "s_nop 0\n"
        "s_nop 0\n"
        "s_nop 0\n"
        "R2_loop:\n"
        "s_mov_b32 s80, 0\n"
        "s_and_b32 s81, s64, 3\n"
        "s_mul_i32 s81, s81, 49152\n"
        "s_add_u32 s81, s81, 2097152\n"
        "s_add_u32 s91, s64, 1\n"
        "s_and_b32 s91, s91, 3\n"
        "s_lshl_b32 s91, s91, 15\n"
        "s_add_u32 s91, s91, 2293760\n"
        "s_add_u32 s83, s64, 3\n"
        "s_and_b32 s83, s83, 3\n"
        "s_lshl_b32 s83, s83, 15\n"
        "s_add_u32 s83, s83, 2293760\n"
        "s_add_u32 s48, s44, s46\n"
        "s_sub_u32 s48, s48, s42\n"
        "s_sub_u32 s48, s48, s34\n"
        "s_max_i32 s48, s48, 0\n"
        "s_min_i32 s48, s48, 0x10000\n"
        "s_mul_i32 s40, s48, s35\n"
        "s_lshr_b32 s40, s40, 16\n"
        "s_sub_u32 s41, s48, s40\n"
        "s_mul_i32 s40, s41, s35\n"
        "s_lshr_b32 s40, s40, 16\n"
        "s_sub_u32 s48, s48, s40\n"
        "s_add_u32 s48, s48, s34\n"
        "s_sub_u32 s48, s48, 76\n"
        "R2_0tu:\n"
        "s_memtime s[50:51]\n"
        "s_waitcnt lgkmcnt(0)\n"
        "s_sub_u32 s50, s50, s48\n"
        "s_cmp_lt_i32 s50, 0\n"
        "s_cbranch_scc1 R2_0tu\n"
        "buffer_load_dwordx4 v[40:43], v0, s[60:63], s80 offen sc1\n"
        "buffer_load_dwordx4 v[44:47], v1, s[60:63], s80 offen sc1\n"
        "s_add_u32 s48, s48, s47\n"
        "R2_0tpa:\n"
        "s_memtime s[50:51]\n"
        "s_getpc_b64 s[40:41]\n"
        "R2_0tpb:\n"
        "s_waitcnt lgkmcnt(0)\n"
        "s_sub_u32 s50, s48, s50\n"
        "s_cmp_gt_i32 s50, 96\n"
        "s_cbranch_scc1 R2_0tpa\n"
        "s_max_i32 s50, s50, 0\n"
        "s_and_b32 s50, s50, -4\n"
        "s_sub_u32 s50, R2_0tpe-R2_0tpb, s50\n"
        "s_add_u32 s40, s40, s50\n"
        "s_addc_u32 s41, s41, 0\n"
        "s_setpc_b64 s[40:41]\n"
        "s_nop 0\n"
        "s_nop 0\n"
        "s_nop 0\n"
        "s_nop 0\n"
        "s_nop 0\n"
        "s_nop 0\n"
        "s_nop 0\n"
        "s_nop 0\n"
        "s_nop 0\n"
        "s_nop 0\n"
        "s_nop 0\n"
        "s_nop 0\n"
        "s_nop 0\n"
        "s_nop 0\n"
        "s_nop 0\n"
        "s_nop 0\n"
        "s_nop 0\n"
        "s_nop 0\n"
        "s_nop 0\n"
        "s_nop 0\n"
        "s_nop 0\n"
        "s_nop 0\n"
        "s_nop 0\n"
        "s_nop 0\n"
        "R2_0tpe:\n"
        "buffer_load_dwordx4 v[64:67], v2, s[60:63], s81 offen sc1\n"
        "buffer_load_dwordx4 v[72:75], v4, s[60:63], s81 offen sc1\n"
        "buffer_load_dwordx4 v[76:79], v5, s[60:63], s81 offen sc1\n"
        "buffer_load_dwordx4 v[80:83], v6, s[60:63], s81 offen sc1\n"
        "buffer_load_dwordx4 v[84:87], v7, s[60:63], s81 offen sc1\n"
        "s_waitcnt lgkmcnt(0)\n"
        "s_barrier\n"
        "ds_read_b128 v[100:103], v21 offset:0\n"
        "ds_read_b128 v[104:107], v21 offset:2048\n"
        "ds_read_b128 v[108:111], v21 offset:4096\n"
        "ds_read_b128 v[112:115], v21 offset:6144\n"
        "s_waitcnt vmcnt(17)\n"
        "v_cmp_eq_u32 vcc, -1, v36\n"
        "s_cbranch_vccz R2_0g1\n"
        "R2_0g0:\n"
        "buffer_load_dword v36, v18, s[60:63], s89 offen sc1\n"
        "s_waitcnt vmcnt(0)\n"
        "v_cmp_eq_u32 vcc, -1, v36\n"
        "s_cbranch_vccz R2_0g1\n"
        "s_cmp_eq_u32 s66, 0\n"
        "s_cbranch_scc1 R2_0g1\n"
        "s_sub_u32 s66, s66, 1\n"
        "s_branch R2_0g0\n"
        "R2_0g1:\n"
        "v_sub_f32 v37, 1.0, v36\n"
        "v_lshlrev_b32 v164, 16, v34\n"
        "v_and_b32 v165, 0xffff0000, v34\n"
        "v_lshlrev_b32 v166, 16, v35\n"
        "v_and_b32 v167, 0xffff0000, v35\n"
        "v_cvt_pk_bf16_f32 v37, v37, v37\n"
        "v_lshlrev_b32 v170, 16, v38\n"
        "v_and_b32 v171, 0xffff0000, v38\n"
        "v_lshlrev_b32 v172, 16, v39\n"
        "v_and_b32 v173, 0xffff0000, v39\n"
        "v_and_b32 v37, 0xffff0000, v37\n"
        "v_mul_f32 v170, v36, v170\n"
        "v_mul_f32 v171, v36, v171\n"
        "v_mul_f32 v172, v36, v172\n"
        "v_mul_f32 v173, v36, v173\n"
        "v_mul_f32 v164, v37, v164\n"
        "v_mul_f32 v165, v37, v165\n"
        "v_mul_f32 v166, v37, v166\n"
        "v_mul_f32 v167, v37, v167\n"
        "v_cvt_pk_bf16_f32 v174, v170, v171\n"
        "v_cvt_pk_bf16_f32 v175, v172, v173\n"
        "v_cvt_pk_bf16_f32 v168, v164, v165\n"
        "v_cvt_pk_bf16_f32 v169, v166, v167\n"
        "v_lshlrev_b32 v170, 16, v174\n"
        "v_and_b32 v171, 0xffff0000, v174\n"
        "v_lshlrev_b32 v172, 16, v175\n"
        "v_and_b32 v173, 0xffff0000, v175\n"
        "v_lshlrev_b32 v164, 16, v168\n"
        "v_and_b32 v165, 0xffff0000, v168\n"
        "v_lshlrev_b32 v166, 16, v169\n"
        "v_and_b32 v167, 0xffff0000, v169\n"
        "v_pk_add_f32 v[170:171], v[170:171], v[164:165]\n"
        "v_pk_add_f32 v[172:173], v[172:173], v[166:167]\n"
        "v_cvt_pk_bf16_f32 v168, v170, v171\n"
        "v_cvt_pk_bf16_f32 v169, v172, v173\n"
        "buffer_store_dwordx2 v[168:169], v19, s[72:75], s88 offen\n"
        "s_waitcnt lgkmcnt(2)\n"
        "v_pk_add_f32 v[100:101], v[100:101], v[104:105]\n"
        "v_pk_add_f32 v[102:103], v[102:103], v[106:107]\n"
        "s_waitcnt lgkmcnt(1)\n"
        "v_pk_add_f32 v[100:101], v[100:101], v[108:109]\n"
        "v_pk_add_f32 v[102:103], v[102:103], v[110:111]\n"
        "s_waitcnt lgkmcnt(0)\n"
        "v_pk_add_f32 v[100:101], v[100:101], v[112:113]\n"
        "v_pk_add_f32 v[102:103], v[102:103], v[114:115]\n"
        "v_cvt_pk_bf16_f32 v38, v100, v101\n"
        "v_cvt_pk_bf16_f32 v39, v102, v103\n"
        "R2_0p:\n"
        "v_mov_b32 v88, 0\n"
        "v_mov_b32 v89, 0\n"
        "v_mov_b32 v90, 0\n"
        "v_mov_b32 v91, 0\n"
        "s_waitcnt vmcnt(6)\n"
        "v_max3_u32 v93, v40, v41, v42\n"
        "v_max3_u32 v93, v93, v43, v44\n"
        "v_max3_u32 v93, v93, v45, v46\n"
        "v_max_u32 v93, v93, v47\n"
        "s_waitcnt vmcnt(5)\n"
        "v_mfma_f32_16x16x32_bf16 v[68:71], v[64:67], v[40:43], 0\n"
        "v_mfma_f32_16x16x32_bf16 v[96:99], v[64:67], v[44:47], 0\n"
        "v_max3_u32 v92, v93, v64, v65\n"
        "v_max3_u32 v92, v92, v66, v67\n"
        "s_waitcnt vmcnt(4)\n"
        "v_smfmac_f32_16x16x64_bf16 v[88:91], v[72:75], a[0:7], v15\n"
        "v_max3_u32 v92, v92, v72, v73\n"
        "v_max3_u32 v92, v92, v74, v75\n"
        "s_waitcnt vmcnt(3)\n"
        "v_smfmac_f32_16x16x64_bf16 v[88:91], v[76:79], a[8:15], v15\n"
        "v_max3_u32 v92, v92, v76, v77\n"
        "v_max3_u32 v92, v92, v78, v79\n"
        "s_waitcnt vmcnt(2)\n"
        "v_smfmac_f32_16x16x64_bf16 v[88:91], v[80:83], a[16:23], v15\n"
        "v_max3_u32 v92, v92, v80, v81\n"
        "v_max3_u32 v92, v92, v82, v83\n"
        "v_pk_add_f32 v[68:69], v[68:69], v[98:99]\n"
        "s_waitcnt vmcnt(1)\n"
        "v_smfmac_f32_16x16x64_bf16 v[88:91], v[84:87], a[24:31], v15\n"
        "v_max3_u32 v92, v92, v84, v85\n"
        "v_max3_u32 v92, v92, v86, v87\n"
        "v_cmp_eq_u32 vcc, -1, v92\n"
        "s_cbranch_vccnz R2_0miss\n"
        "R2_0q:\n"
        "s_nop 3\n"
        "v_pk_add_f32 v[88:89], v[88:89], v[90:91]\n"
        "v_pk_add_f32 v[88:89], v[88:89], v[68:69]\n"
        "ds_write2_b32 v13, v88, v89 offset1:64\n"
        "s_cmp_eq_u32 s67, 0\n"
        "s_waitcnt lgkmcnt(0)\n"
        "s_barrier\n"
        "s_cbranch_scc0 R2_0v\n"
        "ds_read_b64 v[92:93], v14\n"
        "ds_read_b64 v[94:95], v14 offset:512\n"
        "ds_read_b64 v[96:97], v14 offset:1024\n"
        "ds_read_b64 v[98:99], v14 offset:1536\n"
        "s_waitcnt lgkmcnt(2)\n"
        "v_pk_add_f32 v[92:93], v[92:93], v[94:95]\n"
        "s_waitcnt lgkmcnt(0)\n"
        "v_pk_add_f32 v[96:97], v[96:97], v[98:99]\n"
        "v_pk_add_f32 v[92:93], v[92:93], v[96:97]\n"
        "v_cvt_pk_bf16_f32 v92, v92, v93\n"
        "v_min_u32 v92, -2, v92\n"
        "buffer_store_dword v92, v11, s[60:63], s91 offen sc1\n"
        "s_memtime s[50:51]\n"
        "s_mov_b64 exec, 0xffff\n"
        "buffer_store_dwordx4 v[24:27], v12, s[60:63], s83 offen sc1\n"
        "s_mov_b64 exec, -1\n"
        "s_branch R2_0w\n"
        "R2_0v:\n"
        "s_sleep 2\n"
        "R2_0w:\n"
        "s_sub_u32 s82, s64, 1\n"
        "s_mul_i32 s84, s82, 81920\n"
        "s_add_u32 s84, s84, 38797312\n"
        "s_cmp_eq_u32 s64, 0\n"
        "s_cselect_b32 s84, 4026531840, s84\n"
        "s_add_u32 s65, s55, s37\n"
        "s_waitcnt lgkmcnt(0)\n"
        "s_sub_u32 s41, s50, s34\n"
        "s_mul_i32 s41, s41, s35\n"
        "s_ashr_i32 s41, s41, 16\n"
        "s_add_u32 s50, s50, s42\n"
        "s_add_u32 s50, s50, s41\n"
        "s_min_u32 s50, s50, -2\n"
        "v_mov_b32 v33, s50\n"
        "s_mov_b64 exec, 1\n"
        "buffer_store_dword v33, off, s[60:63], s65 sc1\n"
        "s_mov_b64 exec, -1\n"
        "s_add_u32 s85, s84, 5120\n"
        "s_add_u32 s86, s84, 10240\n"
        "s_add_u32 s87, s84, 15360\n"
        "buffer_load_dwordx4 v[100:103], v9, s[60:63], s84 offen offset:0 sc1\n"
        "buffer_load_dwordx4 v[104:107], v9, s[60:63], s84 offen offset:256 sc1\n"
        "buffer_load_dwordx4 v[108:111], v9, s[60:63], s85 offen offset:0 sc1\n"
        "buffer_load_dwordx4 v[112:115], v9, s[60:63], s85 offen offset:256 sc1\n"
        "buffer_load_dwordx4 v[116:119], v9, s[60:63], s84 offen offset:1280 sc1\n"
        "buffer_load_dwordx4 v[120:123], v9, s[60:63], s85 offen offset:1280 sc1\n"
        "buffer_load_dwordx4 v[124:127], v9, s[60:63], s86 offen offset:0 sc1\n"
        "buffer_load_dwordx4 v[128:131], v9, s[60:63], s86 offen offset:256 sc1\n"
        "buffer_load_dwordx4 v[132:135], v9, s[60:63], s87 offen offset:0 sc1\n"
        "buffer_load_dwordx4 v[136:139], v9, s[60:63], s87 offen offset:256 sc1\n"
        "buffer_load_dwordx4 v[140:143], v9, s[60:63], s86 offen offset:1280 sc1\n"
        "buffer_load_dwordx4 v[144:147], v9, s[60:63], s87 offen offset:1280 sc1\n"
        "buffer_load_dwordx4 v[148:151], v10, s[60:63], s84 offen offset:0 sc1\n"
        "buffer_load_dwordx4 v[152:155], v10, s[60:63], s84 offen offset:256 sc1\n"
        "buffer_load_dwordx4 v[156:159], v10, s[60:63], s86 offen offset:0 sc1\n"
        "buffer_load_dwordx4 v[160:163], v10, s[60:63], s86 offen offset:256 sc1\n"
        "s_memtime s[0:1]\n"
        "s_memrealtime s[4:5]\n"
        "s_nop 2\n"
        "s_memrealtime s[6:7]\n"
        "s_nop 2\n"
        "s_memrealtime s[8:9]\n"
        "s_nop 2\n"
        "s_memrealtime s[10:11]\n"
        "s_nop 2\n"
        "s_memrealtime s[12:13]\n"
        "s_nop 2\n"
        "s_memrealtime s[14:15]\n"
        "s_nop 1\n"
        "s_memtime s[2:3]\n"
        "s_memrealtime s[16:17]\n"
        "s_waitcnt lgkmcnt(0)\n"
        "s_mul_i32 s4, s4, 24\n"
        "s_mul_i32 s6, s6, 24\n"
        "s_sub_u32 s6, s6, 17\n"
        "s_max_i32 s4, s4, s6\n"
        "s_mul_i32 s8, s8, 24\n"
        "s_sub_u32 s8, s8, 34\n"
        "s_max_i32 s4, s4, s8\n"
        "s_mul_i32 s10, s10, 24\n"
        "s_sub_u32 s10, s10, 51\n"
        "s_max_i32 s4, s4, s10\n"
        "s_mul_i32 s12, s12, 24\n"
        "s_sub_u32 s12, s12, 68\n"
        "s_max_i32 s4, s4, s12\n"
        "s_mul_i32 s14, s14, 24\n"
        "s_sub_u32 s14, s14, 85\n"
        "s_max_i32 s4, s4, s14\n"
        "s_mul_i32 s16, s16, 24\n"
        "s_sub_u32 s16, s16, 102\n"
        "s_max_i32 s4, s4, s16\n"
        "s_sub_u32 s4, s4, s0\n"
        "s_sub_u32 s41, s0, s34\n"
        "s_mov_b32 s34, s0\n"
        "s_mul_i32 s41, s41, s35\n"
        "s_lshr_b32 s41, s41, 16\n"
        "s_add_u32 s42, s42, s41\n"
        "s_sub_u32 s40, s2, s0\n"
        "s_sub_u32 s41, s4, s42\n"
        "s_sub_u32 s40, s40, 88\n"
        "s_cmp_gt_u32 s40, 32\n"
        "s_cselect_b32 s41, 0, s41\n"
        "s_add_u32 s42, s42, s41\n"
        "s_min_i32 s41, s41, 120\n"
        "s_max_i32 s41, s41, -120\n"
        "s_mul_i32 s41, s41, 7\n"
        "s_add_u32 s35, s35, s41\n"
        "s_max_i32 s35, s35, 0\n"
        "v_cmp_eq_u32 vcc, -1, v32\n"
        "s_cbranch_vccz R2_0ff1\n"
        "R2_0ff0:\n"
        "buffer_load_dword v32, off, s[60:63], s79 sc1\n"
        "s_waitcnt vmcnt(0)\n"
        "v_cmp_eq_u32 vcc, -1, v32\n"
        "s_cbranch_vccz R2_0ff1\n"
        "s_sleep 1\n"
        "s_cmp_eq_u32 s66, 0\n"
        "s_cbranch_scc1 R2_0ff1\n"
        "s_sub_u32 s66, s66, 1\n"
        "s_branch R2_0ff0\n"
        "R2_0ff1:\n"
        "s_sub_u32 s82, s64, 2\n"
        "s_mul_i32 s88, s82, 40960\n"
        "s_lshl_b32 s89, s82, 7\n"
        "s_add_u32 s89, s89, 4296704\n"
        "s_cmp_lt_u32 s64, 2\n"
        "s_cselect_b32 s88, 4026531840, s88\n"
        "s_cselect_b32 s89, 4026531840, s89\n"
        "s_add_u32 s90, s88, s68\n"
        "buffer_load_dwordx2 v[34:35], v17, s[60:63], s90 offen sc1\n"
        "buffer_load_dword v36, v18, s[60:63], s89 offen sc1\n"
        "v_max3_u32 v33, v28, v29, v30\n"
        "v_max_u32 v33, v33, v31\n"
        "v_cmp_eq_u32 vcc, -1, v33\n"
        "s_cbranch_vccz R2_0fb1\n"
        "R2_0fb0:\n"
        "buffer_load_dwordx4 v[28:31], v16, s[60:63], s78 offen sc1\n"
        "s_waitcnt vmcnt(0)\n"
        "v_max3_u32 v33, v28, v29, v30\n"
        "v_max_u32 v33, v33, v31\n"
        "v_cmp_eq_u32 vcc, -1, v33\n"
        "s_cbranch_vccz R2_0fb1\n"
        "s_sleep 1\n"
        "s_cmp_eq_u32 s66, 0\n"
        "s_cbranch_scc1 R2_0fb1\n"
        "s_sub_u32 s66, s66, 1\n"
        "s_branch R2_0fb0\n"
        "R2_0fb1:\n"
        "s_add_u32 s76, s64, 1\n"
        "s_mul_i32 s76, s76, 1310720\n"
        "s_add_u32 s76, s76, s70\n"
        "s_add_u32 s77, s76, 4096\n"
        "buffer_load_dwordx4 a[32:35], v8, s[60:63], s76 offen offset:0 sc1\n"
        "buffer_load_dwordx4 a[36:39], v8, s[60:63], s76 offen offset:2048 sc1\n"
        "buffer_load_dwordx4 a[40:43], v8, s[60:63], s76 offen offset:512 sc1\n"
        "buffer_load_dwordx4 a[44:47], v8, s[60:63], s76 offen offset:2560 sc1\n"
        "buffer_load_dwordx4 a[48:51], v8, s[60:63], s77 offen offset:0 sc1\n"
        "buffer_load_dwordx4 a[52:55], v8, s[60:63], s77 offen offset:2048 sc1\n"
        "buffer_load_dwordx4 a[56:59], v8, s[60:63], s77 offen offset:512 sc1\n"
        "buffer_load_dwordx4 a[60:63], v8, s[60:63], s77 offen offset:2560 sc1\n"
        "s_add_u32 s78, s64, 2\n"
        "s_min_u32 s78, s78, 63\n"
        "s_lshl_b32 s78, s78, 9\n"
        "s_add_u32 s78, s78, 4227072\n"
        "s_sub_u32 s79, s64, 1\n"
        "s_lshl_b32 s79, s79, 6\n"
        "s_add_u32 s79, s79, s69\n"
        "s_cmp_lt_u32 s64, 1\n"
        "s_cselect_b32 s79, 4305152, s79\n"
        "buffer_load_dwordx4 v[28:31], v16, s[60:63], s78 offen sc1\n"
        "buffer_load_dword v32, off, s[60:63], s79 sc1\n"
        "s_add_u32 s48, s44, s39\n"
        "s_sub_u32 s48, s48, s42\n"
        "s_sub_u32 s48, s48, s34\n"
        "s_max_i32 s48, s48, 0\n"
        "s_min_i32 s48, s48, 0x10000\n"
        "s_mul_i32 s40, s48, s35\n"
        "s_lshr_b32 s40, s40, 16\n"
        "s_sub_u32 s41, s48, s40\n"
        "s_mul_i32 s40, s41, s35\n"
        "s_lshr_b32 s40, s40, 16\n"
        "s_sub_u32 s48, s48, s40\n"
        "s_add_u32 s48, s48, s34\n"
        "s_sub_u32 s48, s48, 110\n"
        "s_branch R2_0thz\n"
        "R2_0th:\n"
        "s_sleep 1\n"
        "R2_0thz:\n"
        "s_memtime s[50:51]\n"
        "s_waitcnt lgkmcnt(0)\n"
        "s_sub_u32 s50, s50, s48\n"
        "s_cmp_lt_i32 s50, 0\n"
        "s_cbranch_scc1 R2_0th\n"
        "s_add_u32 s54, s55, s56\n"
        "s_buffer_load_dwordx4 s[0:3], s[60:63], s54 offset:0\n"
        "s_buffer_load_dwordx4 s[4:7], s[60:63], s54 offset:128\n"
        "s_waitcnt vmcnt(27)\n"
        "v_mfma_f32_16x16x32_bf16 v[180:183], a[64:67], v[100:103], 0\n"
        "s_waitcnt vmcnt(26)\n"
        "v_mfma_f32_16x16x32_bf16 v[184:187], a[64:67], v[104:107], 0\n"
        "s_waitcnt vmcnt(25)\n"
        "v_mfma_f32_16x16x32_bf16 v[180:183], a[68:71], v[108:111], v[180:183]\n"
        "s_waitcnt vmcnt(24)\n"
        "v_mfma_f32_16x16x32_bf16 v[184:187], a[68:71], v[112:115], v[184:187]\n"
        "s_waitcnt vmcnt(23)\n"
        "v_mfma_f32_16x16x32_bf16 v[184:187], a[72:75], v[116:119], v[184:187]\n"
        "s_waitcnt vmcnt(22)\n"
        "v_mfma_f32_16x16x32_bf16 v[184:187], a[76:79], v[120:123], v[184:187]\n"
        "s_waitcnt vmcnt(21)\n"
        "v_mfma_f32_16x16x32_bf16 v[180:183], a[80:83], v[124:127], v[180:183]\n"
        "s_waitcnt vmcnt(20)\n"
        "v_mfma_f32_16x16x32_bf16 v[184:187], a[80:83], v[128:131], v[184:187]\n"
        "s_waitcnt vmcnt(19)\n"
        "v_mfma_f32_16x16x32_bf16 v[180:183], a[84:87], v[132:135], v[180:183]\n"
        "s_waitcnt vmcnt(18)\n"
        "v_mfma_f32_16x16x32_bf16 v[184:187], a[84:87], v[136:139], v[184:187]\n"
        "s_waitcnt vmcnt(17)\n"
        "v_mfma_f32_16x16x32_bf16 v[184:187], a[88:91], v[140:143], v[184:187]\n"
        "s_waitcnt vmcnt(16)\n"
        "v_mfma_f32_16x16x32_bf16 v[184:187], a[92:95], v[144:147], v[184:187]\n"
        "s_waitcnt vmcnt(15)\n"
        "v_mfma_f32_16x16x32_bf16 v[180:183], v[56:59], v[148:151], v[180:183]\n"
        "s_waitcnt vmcnt(14)\n"
        "v_mfma_f32_16x16x32_bf16 v[184:187], v[56:59], v[152:155], v[184:187]\n"
        "s_waitcnt vmcnt(13)\n"
        "v_mfma_f32_16x16x32_bf16 v[180:183], v[60:63], v[156:159], v[180:183]\n"
        "s_waitcnt vmcnt(12)\n"
        "v_mfma_f32_16x16x32_bf16 v[184:187], v[60:63], v[160:163], v[184:187]\n"
        "s_nop 6\n"
        "ds_write_b128 v20, v[180:183]\n"
        "ds_write_b128 v20, v[184:187] offset:1024\n"
        "s_mov_b32 s45, 2\n"
        "R2_0k0:\n"
        "s_waitcnt lgkmcnt(0)\n"
        "s_max_u32 s0, s0, s1\n"
        "s_max_u32 s2, s2, s3\n"
        "s_max_u32 s4, s4, s5\n"
        "s_max_u32 s6, s6, s7\n"
        "s_max_u32 s0, s0, s2\n"
        "s_max_u32 s0, s0, s4\n"
        "s_max_u32 s0, s0, s6\n"
        "s_cmp_lg_u32 s0, -1\n"
        "s_cselect_b32 s44, s0, s44\n"
        "s_cbranch_scc1 R2_0h2\n"
        "s_sub_u32 s45, s45, 1\n"
        "s_cbranch_scc1 R2_0k1\n"
        "s_add_u32 s54, s54, 1024\n"
        "s_buffer_load_dwordx4 s[0:3], s[60:63], s54 offset:0\n"
        "s_buffer_load_dwordx4 s[4:7], s[60:63], s54 offset:128\n"
        "s_branch R2_0k0\n"
        "R2_0k1:\n"
        "buffer_load_dword v192, v23, s[60:63], s55 offen sc1\n"
        "s_waitcnt vmcnt(0)\n"
        "R2_0h0:\n"
        "s_nop 1\n"
        "v_max_u32_dpp v192, v192, v192 row_ror:8 row_mask:0xf bank_mask:0xf\n"
        "s_nop 1\n"
        "v_max_u32_dpp v192, v192, v192 row_ror:4 row_mask:0xf bank_mask:0xf\n"
        "s_nop 1\n"
        "v_max_u32_dpp v192, v192, v192 row_ror:2 row_mask:0xf bank_mask:0xf\n"
        "s_nop 1\n"
        "v_max_u32_dpp v192, v192, v192 row_ror:1 row_mask:0xf bank_mask:0xf\n"
        "s_nop 1\n"
        "v_readlane_b32 s40, v192, 0\n"
        "v_readlane_b32 s45, v192, 16\n"
        "s_nop 1\n"
        "s_max_u32 s40, s40, s45\n"
        "s_cmp_lg_u32 s40, -1\n"
        "s_cselect_b32 s44, s40, s44\n"
        "s_cbranch_scc1 R2_0h1\n"
        "s_cmp_eq_u32 s66, 0\n"
        "s_cbranch_scc1 R2_0h1\n"
        "s_sub_u32 s66, s66, 1\n"
        "buffer_load_dword v192, v23, s[60:63], s55 offen sc1\n"
        "s_waitcnt vmcnt(0)\n"
        "s_branch R2_0h0\n"
        "R2_0h1:\n"
        "R2_0h2:\n"
        "s_sub_u32 s45, s44, s33\n"
        "s_mov_b32 s33, s44\n"
        "s_add_u32 s32, s32, 6\n"
        "s_min_u32 s32, s32, s45\n"
        "s_sub_u32 s39, s32, s36\n"
        "s_add_u32 s55, s55, 16384\n"
        "s_add_u32 s64, s64, 1\n"
        "s_mov_b32 s80, 655360\n"
        "s_and_b32 s81, s64, 3\n"
        "s_mul_i32 s81, s81, 49152\n"
        "s_add_u32 s81, s81, 2097152\n"
        "s_add_u32 s91, s64, 1\n"
        "s_and_b32 s91, s91, 3\n"
        "s_lshl_b32 s91, s91, 15\n"
        "s_add_u32 s91, s91, 2293760\n"
        "s_add_u32 s83, s64, 3\n"
        "s_and_b32 s83, s83, 3\n"
        "s_lshl_b32 s83, s83, 15\n"
        "s_add_u32 s83, s83, 2293760\n"
        "s_add_u32 s48, s44, s46\n"
        "s_sub_u32 s48, s48, s42\n"
        "s_sub_u32 s48, s48, s34\n"
        "s_max_i32 s48, s48, 0\n"
        "s_min_i32 s48, s48, 0x10000\n"
        "s_mul_i32 s40, s48, s35\n"
        "s_lshr_b32 s40, s40, 16\n"
        "s_sub_u32 s41, s48, s40\n"
        "s_mul_i32 s40, s41, s35\n"
        "s_lshr_b32 s40, s40, 16\n"
        "s_sub_u32 s48, s48, s40\n"
        "s_add_u32 s48, s48, s34\n"
        "s_sub_u32 s48, s48, 76\n"
        "R2_1tu:\n"
        "s_memtime s[50:51]\n"
        "s_waitcnt lgkmcnt(0)\n"
        "s_sub_u32 s50, s50, s48\n"
        "s_cmp_lt_i32 s50, 0\n"
        "s_cbranch_scc1 R2_1tu\n"
        "buffer_load_dwordx4 v[48:51], v0, s[60:63], s80 offen sc1\n"
        "buffer_load_dwordx4 v[52:55], v1, s[60:63], s80 offen sc1\n"
        "s_add_u32 s48, s48, s47\n"
        "R2_1tpa:\n"
        "s_memtime s[50:51]\n"
        "s_getpc_b64 s[40:41]\n"
        "R2_1tpb:\n"
        "s_waitcnt lgkmcnt(0)\n"
        "s_sub_u32 s50, s48, s50\n"
        "s_cmp_gt_i32 s50, 96\n"
        "s_cbranch_scc1 R2_1tpa\n"
        "s_max_i32 s50, s50, 0\n"
        "s_and_b32 s50, s50, -4\n"
        "s_sub_u32 s50, R2_1tpe-R2_1tpb, s50\n"
        "s_add_u32 s40, s40, s50\n"
        "s_addc_u32 s41, s41, 0\n"
        "s_setpc_b64 s[40:41]\n"
        "s_nop 0\n"
        "s_nop 0\n"
        "s_nop 0\n"
        "s_nop 0\n"
        "s_nop 0\n"
        "s_nop 0\n"
        "s_nop 0\n"
        "s_nop 0\n"
        "s_nop 0\n"
        "s_nop 0\n"
        "s_nop 0\n"
        "s_nop 0\n"
        "s_nop 0\n"
        "s_nop 0\n"
        "s_nop 0\n"
        "s_nop 0\n"
        "s_nop 0\n"
        "s_nop 0\n"
        "s_nop 0\n"
        "s_nop 0\n"
        "s_nop 0\n"
        "s_nop 0\n"
        "s_nop 0\n"
        "s_nop 0\n"
        "R2_1tpe:\n"
        "buffer_load_dwordx4 v[64:67], v2, s[60:63], s81 offen sc1\n"
        "buffer_load_dwordx4 v[72:75], v4, s[60:63], s81 offen sc1\n"
        "buffer_load_dwordx4 v[76:79], v5, s[60:63], s81 offen sc1\n"
        "buffer_load_dwordx4 v[80:83], v6, s[60:63], s81 offen sc1\n"
        "buffer_load_dwordx4 v[84:87], v7, s[60:63], s81 offen sc1\n"
        "s_waitcnt lgkmcnt(0)\n"
        "s_barrier\n"
        "ds_read_b128 v[100:103], v21 offset:0\n"
        "ds_read_b128 v[104:107], v21 offset:2048\n"
        "ds_read_b128 v[108:111], v21 offset:4096\n"
        "ds_read_b128 v[112:115], v21 offset:6144\n"
        "s_waitcnt vmcnt(17)\n"
        "v_cmp_eq_u32 vcc, -1, v36\n"
        "s_cbranch_vccz R2_1g1\n"
        "R2_1g0:\n"
        "buffer_load_dword v36, v18, s[60:63], s89 offen sc1\n"
        "s_waitcnt vmcnt(0)\n"
        "v_cmp_eq_u32 vcc, -1, v36\n"
        "s_cbranch_vccz R2_1g1\n"
        "s_cmp_eq_u32 s66, 0\n"
        "s_cbranch_scc1 R2_1g1\n"
        "s_sub_u32 s66, s66, 1\n"
        "s_branch R2_1g0\n"
        "R2_1g1:\n"
        "v_sub_f32 v37, 1.0, v36\n"
        "v_lshlrev_b32 v164, 16, v34\n"
        "v_and_b32 v165, 0xffff0000, v34\n"
        "v_lshlrev_b32 v166, 16, v35\n"
        "v_and_b32 v167, 0xffff0000, v35\n"
        "v_cvt_pk_bf16_f32 v37, v37, v37\n"
        "v_lshlrev_b32 v170, 16, v38\n"
        "v_and_b32 v171, 0xffff0000, v38\n"
        "v_lshlrev_b32 v172, 16, v39\n"
        "v_and_b32 v173, 0xffff0000, v39\n"
        "v_and_b32 v37, 0xffff0000, v37\n"
        "v_mul_f32 v170, v36, v170\n"
        "v_mul_f32 v171, v36, v171\n"
        "v_mul_f32 v172, v36, v172\n"
        "v_mul_f32 v173, v36, v173\n"
        "v_mul_f32 v164, v37, v164\n"
        "v_mul_f32 v165, v37, v165\n"
        "v_mul_f32 v166, v37, v166\n"
        "v_mul_f32 v167, v37, v167\n"
        "v_cvt_pk_bf16_f32 v174, v170, v171\n"
        "v_cvt_pk_bf16_f32 v175, v172, v173\n"
        "v_cvt_pk_bf16_f32 v168, v164, v165\n"
        "v_cvt_pk_bf16_f32 v169, v166, v167\n"
        "v_lshlrev_b32 v170, 16, v174\n"
        "v_and_b32 v171, 0xffff0000, v174\n"
        "v_lshlrev_b32 v172, 16, v175\n"
        "v_and_b32 v173, 0xffff0000, v175\n"
        "v_lshlrev_b32 v164, 16, v168\n"
        "v_and_b32 v165, 0xffff0000, v168\n"
        "v_lshlrev_b32 v166, 16, v169\n"
        "v_and_b32 v167, 0xffff0000, v169\n"
        "v_pk_add_f32 v[170:171], v[170:171], v[164:165]\n"
        "v_pk_add_f32 v[172:173], v[172:173], v[166:167]\n"
        "v_cvt_pk_bf16_f32 v168, v170, v171\n"
        "v_cvt_pk_bf16_f32 v169, v172, v173\n"
        "buffer_store_dwordx2 v[168:169], v19, s[72:75], s88 offen\n"
        "s_waitcnt lgkmcnt(2)\n"
        "v_pk_add_f32 v[100:101], v[100:101], v[104:105]\n"
        "v_pk_add_f32 v[102:103], v[102:103], v[106:107]\n"
        "s_waitcnt lgkmcnt(1)\n"
        "v_pk_add_f32 v[100:101], v[100:101], v[108:109]\n"
        "v_pk_add_f32 v[102:103], v[102:103], v[110:111]\n"
        "s_waitcnt lgkmcnt(0)\n"
        "v_pk_add_f32 v[100:101], v[100:101], v[112:113]\n"
        "v_pk_add_f32 v[102:103], v[102:103], v[114:115]\n"
        "v_cvt_pk_bf16_f32 v38, v100, v101\n"
        "v_cvt_pk_bf16_f32 v39, v102, v103\n"
        "R2_1p:\n"
        "v_mov_b32 v88, 0\n"
        "v_mov_b32 v89, 0\n"
        "v_mov_b32 v90, 0\n"
        "v_mov_b32 v91, 0\n"
        "s_waitcnt vmcnt(6)\n"
        "v_max3_u32 v93, v48, v49, v50\n"
        "v_max3_u32 v93, v93, v51, v52\n"
        "v_max3_u32 v93, v93, v53, v54\n"
        "v_max_u32 v93, v93, v55\n"
        "s_waitcnt vmcnt(5)\n"
        "v_mfma_f32_16x16x32_bf16 v[68:71], v[64:67], v[48:51], 0\n"
        "v_mfma_f32_16x16x32_bf16 v[96:99], v[64:67], v[52:55], 0\n"
        "v_max3_u32 v92, v93, v64, v65\n"
        "v_max3_u32 v92, v92, v66, v67\n"
        "s_waitcnt vmcnt(4)\n"
        "v_smfmac_f32_16x16x64_bf16 v[88:91], v[72:75], a[32:39], v15\n"
        "v_max3_u32 v92, v92, v72, v73\n"
        "v_max3_u32 v92, v92, v74, v75\n"
        "s_waitcnt vmcnt(3)\n"
        "v_smfmac_f32_16x16x64_bf16 v[88:91], v[76:79], a[40:47], v15\n"
        "v_max3_u32 v92, v92, v76, v77\n"
        "v_max3_u32 v92, v92, v78, v79\n"
        "s_waitcnt vmcnt(2)\n"
        "v_smfmac_f32_16x16x64_bf16 v[88:91], v[80:83], a[48:55], v15\n"
        "v_max3_u32 v92, v92, v80, v81\n"
        "v_max3_u32 v92, v92, v82, v83\n"
        "v_pk_add_f32 v[68:69], v[68:69], v[98:99]\n"
        "s_waitcnt vmcnt(1)\n"
        "v_smfmac_f32_16x16x64_bf16 v[88:91], v[84:87], a[56:63], v15\n"
        "v_max3_u32 v92, v92, v84, v85\n"
        "v_max3_u32 v92, v92, v86, v87\n"
        "v_cmp_eq_u32 vcc, -1, v92\n"
        "s_cbranch_vccnz R2_1miss\n"
        "R2_1q:\n"
        "s_nop 3\n"
        "v_pk_add_f32 v[88:89], v[88:89], v[90:91]\n"
        "v_pk_add_f32 v[88:89], v[88:89], v[68:69]\n"
        "ds_write2_b32 v13, v88, v89 offset1:64\n"
        "s_cmp_eq_u32 s67, 0\n"
        "s_waitcnt lgkmcnt(0)\n"
        "s_barrier\n"
        "s_cbranch_scc0 R2_1v\n"
        "ds_read_b64 v[92:93], v14\n"
        "ds_read_b64 v[94:95], v14 offset:512\n"
        "ds_read_b64 v[96:97], v14 offset:1024\n"
        "ds_read_b64 v[98:99], v14 offset:1536\n"
        "s_waitcnt lgkmcnt(2)\n"
        "v_pk_add_f32 v[92:93], v[92:93], v[94:95]\n"
        "s_waitcnt lgkmcnt(0)\n"
        "v_pk_add_f32 v[96:97], v[96:97], v[98:99]\n"
        "v_pk_add_f32 v[92:93], v[92:93], v[96:97]\n"
        "v_cvt_pk_bf16_f32 v92, v92, v93\n"
        "v_min_u32 v92, -2, v92\n"
        "buffer_store_dword v92, v11, s[60:63], s91 offen sc1\n"
        "s_memtime s[50:51]\n"
        "s_mov_b64 exec, 0xffff\n"
        "buffer_store_dwordx4 v[24:27], v12, s[60:63], s83 offen sc1\n"
        "s_mov_b64 exec, -1\n"
        "s_branch R2_1w\n"
        "R2_1v:\n"
        "s_sleep 2\n"
        "R2_1w:\n"
        "s_sub_u32 s82, s64, 1\n"
        "s_mul_i32 s84, s82, 81920\n"
        "s_add_u32 s84, s84, 38797312\n"
        "s_cmp_eq_u32 s64, 0\n"
        "s_cselect_b32 s84, 4026531840, s84\n"
        "s_add_u32 s65, s55, s37\n"
        "s_waitcnt lgkmcnt(0)\n"
        "s_sub_u32 s41, s50, s34\n"
        "s_mul_i32 s41, s41, s35\n"
        "s_ashr_i32 s41, s41, 16\n"
        "s_add_u32 s50, s50, s42\n"
        "s_add_u32 s50, s50, s41\n"
        "s_min_u32 s50, s50, -2\n"
        "v_mov_b32 v33, s50\n"
        "s_mov_b64 exec, 1\n"
        "buffer_store_dword v33, off, s[60:63], s65 sc1\n"
        "s_mov_b64 exec, -1\n"
        "s_add_u32 s85, s84, 5120\n"
        "s_add_u32 s86, s84, 10240\n"
        "s_add_u32 s87, s84, 15360\n"
        "buffer_load_dwordx4 v[100:103], v9, s[60:63], s84 offen offset:0 sc1\n"
        "buffer_load_dwordx4 v[104:107], v9, s[60:63], s84 offen offset:256 sc1\n"
        "buffer_load_dwordx4 v[108:111], v9, s[60:63], s85 offen offset:0 sc1\n"
        "buffer_load_dwordx4 v[112:115], v9, s[60:63], s85 offen offset:256 sc1\n"
        "buffer_load_dwordx4 v[116:119], v9, s[60:63], s84 offen offset:1280 sc1\n"
        "buffer_load_dwordx4 v[120:123], v9, s[60:63], s85 offen offset:1280 sc1\n"
        "buffer_load_dwordx4 v[124:127], v9, s[60:63], s86 offen offset:0 sc1\n"
        "buffer_load_dwordx4 v[128:131], v9, s[60:63], s86 offen offset:256 sc1\n"
        "buffer_load_dwordx4 v[132:135], v9, s[60:63], s87 offen offset:0 sc1\n"
        "buffer_load_dwordx4 v[136:139], v9, s[60:63], s87 offen offset:256 sc1\n"
        "buffer_load_dwordx4 v[140:143], v9, s[60:63], s86 offen offset:1280 sc1\n"
        "buffer_load_dwordx4 v[144:147], v9, s[60:63], s87 offen offset:1280 sc1\n"
        "buffer_load_dwordx4 v[148:151], v10, s[60:63], s84 offen offset:0 sc1\n"
        "buffer_load_dwordx4 v[152:155], v10, s[60:63], s84 offen offset:256 sc1\n"
        "buffer_load_dwordx4 v[156:159], v10, s[60:63], s86 offen offset:0 sc1\n"
        "buffer_load_dwordx4 v[160:163], v10, s[60:63], s86 offen offset:256 sc1\n"
        "s_memtime s[0:1]\n"
        "s_memrealtime s[4:5]\n"
        "s_nop 2\n"
        "s_memrealtime s[6:7]\n"
        "s_nop 2\n"
        "s_memrealtime s[8:9]\n"
        "s_nop 2\n"
        "s_memrealtime s[10:11]\n"
        "s_nop 2\n"
        "s_memrealtime s[12:13]\n"
        "s_nop 2\n"
        "s_memrealtime s[14:15]\n"
        "s_nop 1\n"
        "s_memtime s[2:3]\n"
        "s_memrealtime s[16:17]\n"
        "s_waitcnt lgkmcnt(0)\n"
        "s_mul_i32 s4, s4, 24\n"
        "s_mul_i32 s6, s6, 24\n"
        "s_sub_u32 s6, s6, 17\n"
        "s_max_i32 s4, s4, s6\n"
        "s_mul_i32 s8, s8, 24\n"
        "s_sub_u32 s8, s8, 34\n"
        "s_max_i32 s4, s4, s8\n"
        "s_mul_i32 s10, s10, 24\n"
        "s_sub_u32 s10, s10, 51\n"
        "s_max_i32 s4, s4, s10\n"
        "s_mul_i32 s12, s12, 24\n"
        "s_sub_u32 s12, s12, 68\n"
        "s_max_i32 s4, s4, s12\n"
        "s_mul_i32 s14, s14, 24\n"
        "s_sub_u32 s14, s14, 85\n"
        "s_max_i32 s4, s4, s14\n"
        "s_mul_i32 s16, s16, 24\n"
        "s_sub_u32 s16, s16, 102\n"
        "s_max_i32 s4, s4, s16\n"
        "s_sub_u32 s4, s4, s0\n"
        "s_sub_u32 s41, s0, s34\n"
        "s_mov_b32 s34, s0\n"
        "s_mul_i32 s41, s41, s35\n"
        "s_lshr_b32 s41, s41, 16\n"
        "s_add_u32 s42, s42, s41\n"
        "s_sub_u32 s40, s2, s0\n"
        "s_sub_u32 s41, s4, s42\n"
        "s_sub_u32 s40, s40, 88\n"
        "s_cmp_gt_u32 s40, 32\n"
        "s_cselect_b32 s41, 0, s41\n"
        "s_add_u32 s42, s42, s41\n"
        "s_min_i32 s41, s41, 120\n"
        "s_max_i32 s41, s41, -120\n"
        "s_mul_i32 s41, s41, 7\n"
        "s_add_u32 s35, s35, s41\n"
        "s_max_i32 s35, s35, 0\n"
        "v_cmp_eq_u32 vcc, -1, v32\n"
        "s_cbranch_vccz R2_1ff1\n"
        "R2_1ff0:\n"
        "buffer_load_dword v32, off, s[60:63], s79 sc1\n"
        "s_waitcnt vmcnt(0)\n"
        "v_cmp_eq_u32 vcc, -1, v32\n"
        "s_cbranch_vccz R2_1ff1\n"
        "s_sleep 1\n"
        "s_cmp_eq_u32 s66, 0\n"
        "s_cbranch_scc1 R2_1ff1\n"
        "s_sub_u32 s66, s66, 1\n"
        "s_branch R2_1ff0\n"
        "R2_1ff1:\n"
        "s_sub_u32 s82, s64, 2\n"
        "s_mul_i32 s88, s82, 40960\n"
        "s_lshl_b32 s89, s82, 7\n"
        "s_add_u32 s89, s89, 4296704\n"
        "s_cmp_lt_u32 s64, 2\n"
        "s_cselect_b32 s88, 4026531840, s88\n"
        "s_cselect_b32 s89, 4026531840, s89\n"
        "s_add_u32 s90, s88, s68\n"
        "buffer_load_dwordx2 v[34:35], v17, s[60:63], s90 offen sc1\n"
        "buffer_load_dword v36, v18, s[60:63], s89 offen sc1\n"
        "v_max3_u32 v33, v28, v29, v30\n"
        "v_max_u32 v33, v33, v31\n"
        "v_cmp_eq_u32 vcc, -1, v33\n"
        "s_cbranch_vccz R2_1fb1\n"
        "R2_1fb0:\n"
        "buffer_load_dwordx4 v[28:31], v16, s[60:63], s78 offen sc1\n"
        "s_waitcnt vmcnt(0)\n"
        "v_max3_u32 v33, v28, v29, v30\n"
        "v_max_u32 v33, v33, v31\n"
        "v_cmp_eq_u32 vcc, -1, v33\n"
        "s_cbranch_vccz R2_1fb1\n"
        "s_sleep 1\n"
        "s_cmp_eq_u32 s66, 0\n"
        "s_cbranch_scc1 R2_1fb1\n"
        "s_sub_u32 s66, s66, 1\n"
        "s_branch R2_1fb0\n"
        "R2_1fb1:\n"
        "s_add_u32 s76, s64, 1\n"
        "s_mul_i32 s76, s76, 1310720\n"
        "s_add_u32 s76, s76, s70\n"
        "s_add_u32 s77, s76, 4096\n"
        "buffer_load_dwordx4 a[64:67], v8, s[60:63], s76 offen offset:0 sc1\n"
        "buffer_load_dwordx4 a[68:71], v8, s[60:63], s76 offen offset:2048 sc1\n"
        "buffer_load_dwordx4 a[72:75], v8, s[60:63], s76 offen offset:512 sc1\n"
        "buffer_load_dwordx4 a[76:79], v8, s[60:63], s76 offen offset:2560 sc1\n"
        "buffer_load_dwordx4 a[80:83], v8, s[60:63], s77 offen offset:0 sc1\n"
        "buffer_load_dwordx4 a[84:87], v8, s[60:63], s77 offen offset:2048 sc1\n"
        "buffer_load_dwordx4 a[88:91], v8, s[60:63], s77 offen offset:512 sc1\n"
        "buffer_load_dwordx4 a[92:95], v8, s[60:63], s77 offen offset:2560 sc1\n"
        "s_add_u32 s78, s64, 2\n"
        "s_min_u32 s78, s78, 63\n"
        "s_lshl_b32 s78, s78, 9\n"
        "s_add_u32 s78, s78, 4227072\n"
        "s_sub_u32 s79, s64, 1\n"
        "s_lshl_b32 s79, s79, 6\n"
        "s_add_u32 s79, s79, s69\n"
        "s_cmp_lt_u32 s64, 1\n"
        "s_cselect_b32 s79, 4305152, s79\n"
        "buffer_load_dwordx4 v[28:31], v16, s[60:63], s78 offen sc1\n"
        "buffer_load_dword v32, off, s[60:63], s79 sc1\n"
        "s_add_u32 s48, s44, s39\n"
        "s_sub_u32 s48, s48, s42\n"
        "s_sub_u32 s48, s48, s34\n"
        "s_max_i32 s48, s48, 0\n"
        "s_min_i32 s48, s48, 0x10000\n"
        "s_mul_i32 s40, s48, s35\n"
        "s_lshr_b32 s40, s40, 16\n"
        "s_sub_u32 s41, s48, s40\n"
        "s_mul_i32 s40, s41, s35\n"
        "s_lshr_b32 s40, s40, 16\n"
        "s_sub_u32 s48, s48, s40\n"
        "s_add_u32 s48, s48, s34\n"
        "s_sub_u32 s48, s48, 110\n"
        "s_branch R2_1thz\n"
        "R2_1th:\n"
        "s_sleep 1\n"
        "R2_1thz:\n"
        "s_memtime s[50:51]\n"
        "s_waitcnt lgkmcnt(0)\n"
        "s_sub_u32 s50, s50, s48\n"
        "s_cmp_lt_i32 s50, 0\n"
        "s_cbranch_scc1 R2_1th\n"
        "s_add_u32 s54, s55, s56\n"
        "s_buffer_load_dwordx4 s[0:3], s[60:63], s54 offset:0\n"
        "s_buffer_load_dwordx4 s[4:7], s[60:63], s54 offset:128\n"
        "s_waitcnt vmcnt(27)\n"
        "v_mfma_f32_16x16x32_bf16 v[180:183], a[0:3], v[100:103], 0\n"
        "s_waitcnt vmcnt(26)\n"
        "v_mfma_f32_16x16x32_bf16 v[184:187], a[0:3], v[104:107], 0\n"
        "s_waitcnt vmcnt(25)\n"
        "v_mfma_f32_16x16x32_bf16 v[180:183], a[4:7], v[108:111], v[180:183]\n"
        "s_waitcnt vmcnt(24)\n"
        "v_mfma_f32_16x16x32_bf16 v[184:187], a[4:7], v[112:115], v[184:187]\n"
        "s_waitcnt vmcnt(23)\n"
        "v_mfma_f32_16x16x32_bf16 v[184:187], a[8:11], v[116:119], v[184:187]\n"
        "s_waitcnt vmcnt(22)\n"
        "v_mfma_f32_16x16x32_bf16 v[184:187], a[12:15], v[120:123], v[184:187]\n"
        "s_waitcnt vmcnt(21)\n"
        "v_mfma_f32_16x16x32_bf16 v[180:183], a[16:19], v[124:127], v[180:183]\n"
        "s_waitcnt vmcnt(20)\n"
        "v_mfma_f32_16x16x32_bf16 v[184:187], a[16:19], v[128:131], v[184:187]\n"
        "s_waitcnt vmcnt(19)\n"
        "v_mfma_f32_16x16x32_bf16 v[180:183], a[20:23], v[132:135], v[180:183]\n"
        "s_waitcnt vmcnt(18)\n"
        "v_mfma_f32_16x16x32_bf16 v[184:187], a[20:23], v[136:139], v[184:187]\n"
        "s_waitcnt vmcnt(17)\n"
        "v_mfma_f32_16x16x32_bf16 v[184:187], a[24:27], v[140:143], v[184:187]\n"
        "s_waitcnt vmcnt(16)\n"
        "v_mfma_f32_16x16x32_bf16 v[184:187], a[28:31], v[144:147], v[184:187]\n"
        "s_waitcnt vmcnt(15)\n"
        "v_mfma_f32_16x16x32_bf16 v[180:183], v[40:43], v[148:151], v[180:183]\n"
        "s_waitcnt vmcnt(14)\n"
        "v_mfma_f32_16x16x32_bf16 v[184:187], v[40:43], v[152:155], v[184:187]\n"
        "s_waitcnt vmcnt(13)\n"
        "v_mfma_f32_16x16x32_bf16 v[180:183], v[44:47], v[156:159], v[180:183]\n"
        "s_waitcnt vmcnt(12)\n"
        "v_mfma_f32_16x16x32_bf16 v[184:187], v[44:47], v[160:163], v[184:187]\n"
        "s_nop 6\n"
        "ds_write_b128 v20, v[180:183]\n"
        "ds_write_b128 v20, v[184:187] offset:1024\n"
        "s_mov_b32 s45, 2\n"
        "R2_1k0:\n"
        "s_waitcnt lgkmcnt(0)\n"
        "s_max_u32 s0, s0, s1\n"
        "s_max_u32 s2, s2, s3\n"
        "s_max_u32 s4, s4, s5\n"
        "s_max_u32 s6, s6, s7\n"
        "s_max_u32 s0, s0, s2\n"
        "s_max_u32 s0, s0, s4\n"
        "s_max_u32 s0, s0, s6\n"
        "s_cmp_lg_u32 s0, -1\n"
        "s_cselect_b32 s44, s0, s44\n"
        "s_cbranch_scc1 R2_1h2\n"
        "s_sub_u32 s45, s45, 1\n"
        "s_cbranch_scc1 R2_1k1\n"
        "s_add_u32 s54, s54, 1024\n"
        "s_buffer_load_dwordx4 s[0:3], s[60:63], s54 offset:0\n"
        "s_buffer_load_dwordx4 s[4:7], s[60:63], s54 offset:128\n"
        "s_branch R2_1k0\n"
        "R2_1k1:\n"
        "buffer_load_dword v192, v23, s[60:63], s55 offen sc1\n"
        "s_waitcnt vmcnt(0)\n"
        "R2_1h0:\n"
        "s_nop 1\n"
        "v_max_u32_dpp v192, v192, v192 row_ror:8 row_mask:0xf bank_mask:0xf\n"
        "s_nop 1\n"
        "v_max_u32_dpp v192, v192, v192 row_ror:4 row_mask:0xf bank_mask:0xf\n"
        "s_nop 1\n"
        "v_max_u32_dpp v192, v192, v192 row_ror:2 row_mask:0xf bank_mask:0xf\n"
        "s_nop 1\n"
        "v_max_u32_dpp v192, v192, v192 row_ror:1 row_mask:0xf bank_mask:0xf\n"
        "s_nop 1\n"
        "v_readlane_b32 s40, v192, 0\n"
        "v_readlane_b32 s45, v192, 16\n"
        "s_nop 1\n"
        "s_max_u32 s40, s40, s45\n"
        "s_cmp_lg_u32 s40, -1\n"
        "s_cselect_b32 s44, s40, s44\n"
        "s_cbranch_scc1 R2_1h1\n"
        "s_cmp_eq_u32 s66, 0\n"
        "s_cbranch_scc1 R2_1h1\n"
        "s_sub_u32 s66, s66, 1\n"
        "buffer_load_dword v192, v23, s[60:63], s55 offen sc1\n"
        "s_waitcnt vmcnt(0)\n"
        "s_branch R2_1h0\n"
        "R2_1h1:\n"
        "R2_1h2:\n"
        "s_sub_u32 s45, s44, s33\n"
        "s_mov_b32 s33, s44\n"
        "s_add_u32 s32, s32, 6\n"
        "s_min_u32 s32, s32, s45\n"
        "s_sub_u32 s39, s32, s36\n"
        "s_add_u32 s55, s55, 16384\n"
        "s_add_u32 s64, s64, 1\n"
        "s_mov_b32 s80, 1310720\n"
        "s_and_b32 s81, s64, 3\n"
        "s_mul_i32 s81, s81, 49152\n"
        "s_add_u32 s81, s81, 2097152\n"
        "s_add_u32 s91, s64, 1\n"
        "s_and_b32 s91, s91, 3\n"
        "s_lshl_b32 s91, s91, 15\n"
        "s_add_u32 s91, s91, 2293760\n"
        "s_add_u32 s83, s64, 3\n"
        "s_and_b32 s83, s83, 3\n"
        "s_lshl_b32 s83, s83, 15\n"
        "s_add_u32 s83, s83, 2293760\n"
        "s_add_u32 s48, s44, s46\n"
        "s_sub_u32 s48, s48, s42\n"
        "s_sub_u32 s48, s48, s34\n"
        "s_max_i32 s48, s48, 0\n"
        "s_min_i32 s48, s48, 0x10000\n"
        "s_mul_i32 s40, s48, s35\n"
        "s_lshr_b32 s40, s40, 16\n"
        "s_sub_u32 s41, s48, s40\n"
        "s_mul_i32 s40, s41, s35\n"
        "s_lshr_b32 s40, s40, 16\n"
        "s_sub_u32 s48, s48, s40\n"
        "s_add_u32 s48, s48, s34\n"
        "s_sub_u32 s48, s48, 76\n"
        "R2_2tu:\n"
        "s_memtime s[50:51]\n"
        "s_waitcnt lgkmcnt(0)\n"
        "s_sub_u32 s50, s50, s48\n"
        "s_cmp_lt_i32 s50, 0\n"
        "s_cbranch_scc1 R2_2tu\n"
        "buffer_load_dwordx4 v[56:59], v0, s[60:63], s80 offen sc1\n"
        "buffer_load_dwordx4 v[60:63], v1, s[60:63], s80 offen sc1\n"
        "s_add_u32 s48, s48, s47\n"
        "R2_2tpa:\n"
        "s_memtime s[50:51]\n"
        "s_getpc_b64 s[40:41]\n"
        "R2_2tpb:\n"
        "s_waitcnt lgkmcnt(0)\n"
        "s_sub_u32 s50, s48, s50\n"
        "s_cmp_gt_i32 s50, 96\n"
        "s_cbranch_scc1 R2_2tpa\n"
        "s_max_i32 s50, s50, 0\n"
        "s_and_b32 s50, s50, -4\n"
        "s_sub_u32 s50, R2_2tpe-R2_2tpb, s50\n"
        "s_add_u32 s40, s40, s50\n"
        "s_addc_u32 s41, s41, 0\n"
        "s_setpc_b64 s[40:41]\n"
        "s_nop 0\n"
        "s_nop 0\n"
        "s_nop 0\n"
        "s_nop 0\n"
        "s_nop 0\n"
        "s_nop 0\n"
        "s_nop 0\n"
        "s_nop 0\n"
        "s_nop 0\n"
        "s_nop 0\n"
        "s_nop 0\n"
        "s_nop 0\n"
        "s_nop 0\n"
        "s_nop 0\n"
        "s_nop 0\n"
        "s_nop 0\n"
        "s_nop 0\n"
        "s_nop 0\n"
        "s_nop 0\n"
        "s_nop 0\n"
        "s_nop 0\n"
        "s_nop 0\n"
        "s_nop 0\n"
        "s_nop 0\n"
        "R2_2tpe:\n"
        "buffer_load_dwordx4 v[64:67], v2, s[60:63], s81 offen sc1\n"
        "buffer_load_dwordx4 v[72:75], v4, s[60:63], s81 offen sc1\n"
        "buffer_load_dwordx4 v[76:79], v5, s[60:63], s81 offen sc1\n"
        "buffer_load_dwordx4 v[80:83], v6, s[60:63], s81 offen sc1\n"
        "buffer_load_dwordx4 v[84:87], v7, s[60:63], s81 offen sc1\n"
        "s_waitcnt lgkmcnt(0)\n"
        "s_barrier\n"
        "ds_read_b128 v[100:103], v21 offset:0\n"
        "ds_read_b128 v[104:107], v21 offset:2048\n"
        "ds_read_b128 v[108:111], v21 offset:4096\n"
        "ds_read_b128 v[112:115], v21 offset:6144\n"
        "s_waitcnt vmcnt(17)\n"
        "v_cmp_eq_u32 vcc, -1, v36\n"
        "s_cbranch_vccz R2_2g1\n"
        "R2_2g0:\n"
        "buffer_load_dword v36, v18, s[60:63], s89 offen sc1\n"
        "s_waitcnt vmcnt(0)\n"
        "v_cmp_eq_u32 vcc, -1, v36\n"
        "s_cbranch_vccz R2_2g1\n"
        "s_cmp_eq_u32 s66, 0\n"
        "s_cbranch_scc1 R2_2g1\n"
        "s_sub_u32 s66, s66, 1\n"
        "s_branch R2_2g0\n"
        "R2_2g1:\n"
        "v_sub_f32 v37, 1.0, v36\n"
        "v_lshlrev_b32 v164, 16, v34\n"
        "v_and_b32 v165, 0xffff0000, v34\n"
        "v_lshlrev_b32 v166, 16, v35\n"
        "v_and_b32 v167, 0xffff0000, v35\n"
        "v_cvt_pk_bf16_f32 v37, v37, v37\n"
        "v_lshlrev_b32 v170, 16, v38\n"
        "v_and_b32 v171, 0xffff0000, v38\n"
        "v_lshlrev_b32 v172, 16, v39\n"
        "v_and_b32 v173, 0xffff0000, v39\n"
        "v_and_b32 v37, 0xffff0000, v37\n"
        "v_mul_f32 v170, v36, v170\n"
        "v_mul_f32 v171, v36, v171\n"
        "v_mul_f32 v172, v36, v172\n"
        "v_mul_f32 v173, v36, v173\n"
        "v_mul_f32 v164, v37, v164\n"
        "v_mul_f32 v165, v37, v165\n"
        "v_mul_f32 v166, v37, v166\n"
        "v_mul_f32 v167, v37, v167\n"
        "v_cvt_pk_bf16_f32 v174, v170, v171\n"
        "v_cvt_pk_bf16_f32 v175, v172, v173\n"
        "v_cvt_pk_bf16_f32 v168, v164, v165\n"
        "v_cvt_pk_bf16_f32 v169, v166, v167\n"
        "v_lshlrev_b32 v170, 16, v174\n"
        "v_and_b32 v171, 0xffff0000, v174\n"
        "v_lshlrev_b32 v172, 16, v175\n"
        "v_and_b32 v173, 0xffff0000, v175\n"
        "v_lshlrev_b32 v164, 16, v168\n"
        "v_and_b32 v165, 0xffff0000, v168\n"
        "v_lshlrev_b32 v166, 16, v169\n"
        "v_and_b32 v167, 0xffff0000, v169\n"
        "v_pk_add_f32 v[170:171], v[170:171], v[164:165]\n"
        "v_pk_add_f32 v[172:173], v[172:173], v[166:167]\n"
        "v_cvt_pk_bf16_f32 v168, v170, v171\n"
        "v_cvt_pk_bf16_f32 v169, v172, v173\n"
        "buffer_store_dwordx2 v[168:169], v19, s[72:75], s88 offen\n"
        "s_waitcnt lgkmcnt(2)\n"
        "v_pk_add_f32 v[100:101], v[100:101], v[104:105]\n"
        "v_pk_add_f32 v[102:103], v[102:103], v[106:107]\n"
        "s_waitcnt lgkmcnt(1)\n"
        "v_pk_add_f32 v[100:101], v[100:101], v[108:109]\n"
        "v_pk_add_f32 v[102:103], v[102:103], v[110:111]\n"
        "s_waitcnt lgkmcnt(0)\n"
        "v_pk_add_f32 v[100:101], v[100:101], v[112:113]\n"
        "v_pk_add_f32 v[102:103], v[102:103], v[114:115]\n"
        "v_cvt_pk_bf16_f32 v38, v100, v101\n"
        "v_cvt_pk_bf16_f32 v39, v102, v103\n"
        "R2_2p:\n"
        "v_mov_b32 v88, 0\n"
        "v_mov_b32 v89, 0\n"
        "v_mov_b32 v90, 0\n"
        "v_mov_b32 v91, 0\n"
        "s_waitcnt vmcnt(6)\n"
        "v_max3_u32 v93, v56, v57, v58\n"
        "v_max3_u32 v93, v93, v59, v60\n"
        "v_max3_u32 v93, v93, v61, v62\n"
        "v_max_u32 v93, v93, v63\n"
        "s_waitcnt vmcnt(5)\n"
        "v_mfma_f32_16x16x32_bf16 v[68:71], v[64:67], v[56:59], 0\n"
        "v_mfma_f32_16x16x32_bf16 v[96:99], v[64:67], v[60:63], 0\n"
        "v_max3_u32 v92, v93, v64, v65\n"
        "v_max3_u32 v92, v92, v66, v67\n"
        "s_waitcnt vmcnt(4)\n"
        "v_smfmac_f32_16x16x64_bf16 v[88:91], v[72:75], a[64:71], v15\n"
        "v_max3_u32 v92, v92, v72, v73\n"
        "v_max3_u32 v92, v92, v74, v75\n"
        "s_waitcnt vmcnt(3)\n"
        "v_smfmac_f32_16x16x64_bf16 v[88:91], v[76:79], a[72:79], v15\n"
        "v_max3_u32 v92, v92, v76, v77\n"
        "v_max3_u32 v92, v92, v78, v79\n"
        "s_waitcnt vmcnt(2)\n"
        "v_smfmac_f32_16x16x64_bf16 v[88:91], v[80:83], a[80:87], v15\n"
        "v_max3_u32 v92, v92, v80, v81\n"
        "v_max3_u32 v92, v92, v82, v83\n"
        "v_pk_add_f32 v[68:69], v[68:69], v[98:99]\n"
        "s_waitcnt vmcnt(1)\n"
        "v_smfmac_f32_16x16x64_bf16 v[88:91], v[84:87], a[88:95], v15\n"
        "v_max3_u32 v92, v92, v84, v85\n"
        "v_max3_u32 v92, v92, v86, v87\n"
        "v_cmp_eq_u32 vcc, -1, v92\n"
        "s_cbranch_vccnz R2_2miss\n"
        "R2_2q:\n"
        "s_nop 3\n"
        "v_pk_add_f32 v[88:89], v[88:89], v[90:91]\n"
        "v_pk_add_f32 v[88:89], v[88:89], v[68:69]\n"
        "ds_write2_b32 v13, v88, v89 offset1:64\n"
        "s_cmp_eq_u32 s67, 0\n"
        "s_waitcnt lgkmcnt(0)\n"
        "s_barrier\n"
        "s_cbranch_scc0 R2_2v\n"
        "ds_read_b64 v[92:93], v14\n"
        "ds_read_b64 v[94:95], v14 offset:512\n"
        "ds_read_b64 v[96:97], v14 offset:1024\n"
        "ds_read_b64 v[98:99], v14 offset:1536\n"
        "s_waitcnt lgkmcnt(2)\n"
        "v_pk_add_f32 v[92:93], v[92:93], v[94:95]\n"
        "s_waitcnt lgkmcnt(0)\n"
        "v_pk_add_f32 v[96:97], v[96:97], v[98:99]\n"
        "v_pk_add_f32 v[92:93], v[92:93], v[96:97]\n"
        "v_cvt_pk_bf16_f32 v92, v92, v93\n"
        "v_min_u32 v92, -2, v92\n"
        "buffer_store_dword v92, v11, s[60:63], s91 offen sc1\n"
        "s_memtime s[50:51]\n"
        "s_mov_b64 exec, 0xffff\n"
        "buffer_store_dwordx4 v[24:27], v12, s[60:63], s83 offen sc1\n"
        "s_mov_b64 exec, -1\n"
        "s_branch R2_2w\n"
        "R2_2v:\n"
        "s_sleep 2\n"
        "R2_2w:\n"
        "s_sub_u32 s82, s64, 1\n"
        "s_mul_i32 s84, s82, 81920\n"
        "s_add_u32 s84, s84, 38797312\n"
        "s_cmp_eq_u32 s64, 0\n"
        "s_cselect_b32 s84, 4026531840, s84\n"
        "s_add_u32 s65, s55, s37\n"
        "s_waitcnt lgkmcnt(0)\n"
        "s_sub_u32 s41, s50, s34\n"
        "s_mul_i32 s41, s41, s35\n"
        "s_ashr_i32 s41, s41, 16\n"
        "s_add_u32 s50, s50, s42\n"
        "s_add_u32 s50, s50, s41\n"
        "s_min_u32 s50, s50, -2\n"
        "v_mov_b32 v33, s50\n"
        "s_mov_b64 exec, 1\n"
        "buffer_store_dword v33, off, s[60:63], s65 sc1\n"
        "s_mov_b64 exec, -1\n"
        "s_add_u32 s85, s84, 5120\n"
        "s_add_u32 s86, s84, 10240\n"
        "s_add_u32 s87, s84, 15360\n"
        "buffer_load_dwordx4 v[100:103], v9, s[60:63], s84 offen offset:0 sc1\n"
        "buffer_load_dwordx4 v[104:107], v9, s[60:63], s84 offen offset:256 sc1\n"
        "buffer_load_dwordx4 v[108:111], v9, s[60:63], s85 offen offset:0 sc1\n"
        "buffer_load_dwordx4 v[112:115], v9, s[60:63], s85 offen offset:256 sc1\n"
        "buffer_load_dwordx4 v[116:119], v9, s[60:63], s84 offen offset:1280 sc1\n"
        "buffer_load_dwordx4 v[120:123], v9, s[60:63], s85 offen offset:1280 sc1\n"
        "buffer_load_dwordx4 v[124:127], v9, s[60:63], s86 offen offset:0 sc1\n"
        "buffer_load_dwordx4 v[128:131], v9, s[60:63], s86 offen offset:256 sc1\n"
        "buffer_load_dwordx4 v[132:135], v9, s[60:63], s87 offen offset:0 sc1\n"
        "buffer_load_dwordx4 v[136:139], v9, s[60:63], s87 offen offset:256 sc1\n"
        "buffer_load_dwordx4 v[140:143], v9, s[60:63], s86 offen offset:1280 sc1\n"
        "buffer_load_dwordx4 v[144:147], v9, s[60:63], s87 offen offset:1280 sc1\n"
        "buffer_load_dwordx4 v[148:151], v10, s[60:63], s84 offen offset:0 sc1\n"
        "buffer_load_dwordx4 v[152:155], v10, s[60:63], s84 offen offset:256 sc1\n"
        "buffer_load_dwordx4 v[156:159], v10, s[60:63], s86 offen offset:0 sc1\n"
        "buffer_load_dwordx4 v[160:163], v10, s[60:63], s86 offen offset:256 sc1\n"
        "s_memtime s[0:1]\n"
        "s_memrealtime s[4:5]\n"
        "s_nop 2\n"
        "s_memrealtime s[6:7]\n"
        "s_nop 2\n"
        "s_memrealtime s[8:9]\n"
        "s_nop 2\n"
        "s_memrealtime s[10:11]\n"
        "s_nop 2\n"
        "s_memrealtime s[12:13]\n"
        "s_nop 2\n"
        "s_memrealtime s[14:15]\n"
        "s_nop 1\n"
        "s_memtime s[2:3]\n"
        "s_memrealtime s[16:17]\n"
        "s_waitcnt lgkmcnt(0)\n"
        "s_mul_i32 s4, s4, 24\n"
        "s_mul_i32 s6, s6, 24\n"
        "s_sub_u32 s6, s6, 17\n"
        "s_max_i32 s4, s4, s6\n"
        "s_mul_i32 s8, s8, 24\n"
        "s_sub_u32 s8, s8, 34\n"
        "s_max_i32 s4, s4, s8\n"
        "s_mul_i32 s10, s10, 24\n"
        "s_sub_u32 s10, s10, 51\n"
        "s_max_i32 s4, s4, s10\n"
        "s_mul_i32 s12, s12, 24\n"
        "s_sub_u32 s12, s12, 68\n"
        "s_max_i32 s4, s4, s12\n"
        "s_mul_i32 s14, s14, 24\n"
        "s_sub_u32 s14, s14, 85\n"
        "s_max_i32 s4, s4, s14\n"
        "s_mul_i32 s16, s16, 24\n"
        "s_sub_u32 s16, s16, 102\n"
        "s_max_i32 s4, s4, s16\n"
        "s_sub_u32 s4, s4, s0\n"
        "s_sub_u32 s41, s0, s34\n"
        "s_mov_b32 s34, s0\n"
        "s_mul_i32 s41, s41, s35\n"
        "s_lshr_b32 s41, s41, 16\n"
        "s_add_u32 s42, s42, s41\n"
        "s_sub_u32 s40, s2, s0\n"
        "s_sub_u32 s41, s4, s42\n"
        "s_sub_u32 s40, s40, 88\n"
        "s_cmp_gt_u32 s40, 32\n"
        "s_cselect_b32 s41, 0, s41\n"
        "s_add_u32 s42, s42, s41\n"
        "s_min_i32 s41, s41, 120\n"
        "s_max_i32 s41, s41, -120\n"
        "s_mul_i32 s41, s41, 7\n"
        "s_add_u32 s35, s35, s41\n"
        "s_max_i32 s35, s35, 0\n"
        "v_cmp_eq_u32 vcc, -1, v32\n"
        "s_cbranch_vccz R2_2ff1\n"
        "R2_2ff0:\n"
        "buffer_load_dword v32, off, s[60:63], s79 sc1\n"
        "s_waitcnt vmcnt(0)\n"
        "v_cmp_eq_u32 vcc, -1, v32\n"
        "s_cbranch_vccz R2_2ff1\n"
        "s_sleep 1\n"
        "s_cmp_eq_u32 s66, 0\n"
        "s_cbranch_scc1 R2_2ff1\n"
        "s_sub_u32 s66, s66, 1\n"
        "s_branch R2_2ff0\n"
        "R2_2ff1:\n"
        "s_sub_u32 s82, s64, 2\n"
        "s_mul_i32 s88, s82, 40960\n"
        "s_lshl_b32 s89, s82, 7\n"
        "s_add_u32 s89, s89, 4296704\n"
        "s_cmp_lt_u32 s64, 2\n"
        "s_cselect_b32 s88, 4026531840, s88\n"
        "s_cselect_b32 s89, 4026531840, s89\n"
        "s_add_u32 s90, s88, s68\n"
        "buffer_load_dwordx2 v[34:35], v17, s[60:63], s90 offen sc1\n"
        "buffer_load_dword v36, v18, s[60:63], s89 offen sc1\n"
        "v_max3_u32 v33, v28, v29, v30\n"
        "v_max_u32 v33, v33, v31\n"
        "v_cmp_eq_u32 vcc, -1, v33\n"
        "s_cbranch_vccz R2_2fb1\n"
        "R2_2fb0:\n"
        "buffer_load_dwordx4 v[28:31], v16, s[60:63], s78 offen sc1\n"
        "s_waitcnt vmcnt(0)\n"
        "v_max3_u32 v33, v28, v29, v30\n"
        "v_max_u32 v33, v33, v31\n"
        "v_cmp_eq_u32 vcc, -1, v33\n"
        "s_cbranch_vccz R2_2fb1\n"
        "s_sleep 1\n"
        "s_cmp_eq_u32 s66, 0\n"
        "s_cbranch_scc1 R2_2fb1\n"
        "s_sub_u32 s66, s66, 1\n"
        "s_branch R2_2fb0\n"
        "R2_2fb1:\n"
        "s_add_u32 s76, s64, 1\n"
        "s_mul_i32 s76, s76, 1310720\n"
        "s_add_u32 s76, s76, s70\n"
        "s_add_u32 s77, s76, 4096\n"
        "buffer_load_dwordx4 a[0:3], v8, s[60:63], s76 offen offset:0 sc1\n"
        "buffer_load_dwordx4 a[4:7], v8, s[60:63], s76 offen offset:2048 sc1\n"
        "buffer_load_dwordx4 a[8:11], v8, s[60:63], s76 offen offset:512 sc1\n"
        "buffer_load_dwordx4 a[12:15], v8, s[60:63], s76 offen offset:2560 sc1\n"
        "buffer_load_dwordx4 a[16:19], v8, s[60:63], s77 offen offset:0 sc1\n"
        "buffer_load_dwordx4 a[20:23], v8, s[60:63], s77 offen offset:2048 sc1\n"
        "buffer_load_dwordx4 a[24:27], v8, s[60:63], s77 offen offset:512 sc1\n"
        "buffer_load_dwordx4 a[28:31], v8, s[60:63], s77 offen offset:2560 sc1\n"
        "s_add_u32 s78, s64, 2\n"
        "s_min_u32 s78, s78, 63\n"
        "s_lshl_b32 s78, s78, 9\n"
        "s_add_u32 s78, s78, 4227072\n"
        "s_sub_u32 s79, s64, 1\n"
        "s_lshl_b32 s79, s79, 6\n"
        "s_add_u32 s79, s79, s69\n"
        "s_cmp_lt_u32 s64, 1\n"
        "s_cselect_b32 s79, 4305152, s79\n"
        "buffer_load_dwordx4 v[28:31], v16, s[60:63], s78 offen sc1\n"
        "buffer_load_dword v32, off, s[60:63], s79 sc1\n"
        "s_add_u32 s48, s44, s39\n"
        "s_sub_u32 s48, s48, s42\n"
        "s_sub_u32 s48, s48, s34\n"
        "s_max_i32 s48, s48, 0\n"
        "s_min_i32 s48, s48, 0x10000\n"
        "s_mul_i32 s40, s48, s35\n"
        "s_lshr_b32 s40, s40, 16\n"
        "s_sub_u32 s41, s48, s40\n"
        "s_mul_i32 s40, s41, s35\n"
        "s_lshr_b32 s40, s40, 16\n"
        "s_sub_u32 s48, s48, s40\n"
        "s_add_u32 s48, s48, s34\n"
        "s_sub_u32 s48, s48, 110\n"
        "s_branch R2_2thz\n"
        "R2_2th:\n"
        "s_sleep 1\n"
        "R2_2thz:\n"
        "s_memtime s[50:51]\n"
        "s_waitcnt lgkmcnt(0)\n"
        "s_sub_u32 s50, s50, s48\n"
        "s_cmp_lt_i32 s50, 0\n"
        "s_cbranch_scc1 R2_2th\n"
        "s_add_u32 s54, s55, s56\n"
        "s_buffer_load_dwordx4 s[0:3], s[60:63], s54 offset:0\n"
        "s_buffer_load_dwordx4 s[4:7], s[60:63], s54 offset:128\n"
        "buffer_load_dword v193, v23, s[60:63], s55 offen sc1\n"
        "s_waitcnt vmcnt(28)\n"
        "v_mfma_f32_16x16x32_bf16 v[180:183], a[32:35], v[100:103], 0\n"
        "s_waitcnt vmcnt(27)\n"
        "v_mfma_f32_16x16x32_bf16 v[184:187], a[32:35], v[104:107], 0\n"
        "s_waitcnt vmcnt(26)\n"
        "v_mfma_f32_16x16x32_bf16 v[180:183], a[36:39], v[108:111], v[180:183]\n"
        "s_waitcnt vmcnt(25)\n"
        "v_mfma_f32_16x16x32_bf16 v[184:187], a[36:39], v[112:115], v[184:187]\n"
        "s_waitcnt vmcnt(24)\n"
        "v_mfma_f32_16x16x32_bf16 v[184:187], a[40:43], v[116:119], v[184:187]\n"
        "s_waitcnt vmcnt(23)\n"
        "v_mfma_f32_16x16x32_bf16 v[184:187], a[44:47], v[120:123], v[184:187]\n"
        "s_waitcnt vmcnt(22)\n"
        "v_mfma_f32_16x16x32_bf16 v[180:183], a[48:51], v[124:127], v[180:183]\n"
        "s_waitcnt vmcnt(21)\n"
        "v_mfma_f32_16x16x32_bf16 v[184:187], a[48:51], v[128:131], v[184:187]\n"
        "s_waitcnt vmcnt(20)\n"
        "v_mfma_f32_16x16x32_bf16 v[180:183], a[52:55], v[132:135], v[180:183]\n"
        "s_waitcnt vmcnt(19)\n"
        "v_mfma_f32_16x16x32_bf16 v[184:187], a[52:55], v[136:139], v[184:187]\n"
        "s_waitcnt vmcnt(18)\n"
        "v_mfma_f32_16x16x32_bf16 v[184:187], a[56:59], v[140:143], v[184:187]\n"
        "s_waitcnt vmcnt(17)\n"
        "v_mfma_f32_16x16x32_bf16 v[184:187], a[60:63], v[144:147], v[184:187]\n"
        "s_waitcnt vmcnt(16)\n"
        "v_mfma_f32_16x16x32_bf16 v[180:183], v[48:51], v[148:151], v[180:183]\n"
        "s_waitcnt vmcnt(15)\n"
        "v_mfma_f32_16x16x32_bf16 v[184:187], v[48:51], v[152:155], v[184:187]\n"
        "s_waitcnt vmcnt(14)\n"
        "v_mfma_f32_16x16x32_bf16 v[180:183], v[52:55], v[156:159], v[180:183]\n"
        "s_waitcnt vmcnt(13)\n"
        "v_mfma_f32_16x16x32_bf16 v[184:187], v[52:55], v[160:163], v[184:187]\n"
        "s_nop 6\n"
        "ds_write_b128 v20, v[180:183]\n"
        "ds_write_b128 v20, v[184:187] offset:1024\n"
        "s_mov_b32 s45, 2\n"
        "R2_2k0:\n"
        "s_waitcnt lgkmcnt(0)\n"
        "s_max_u32 s0, s0, s1\n"
        "s_max_u32 s2, s2, s3\n"
        "s_max_u32 s4, s4, s5\n"
        "s_max_u32 s6, s6, s7\n"
        "s_max_u32 s0, s0, s2\n"
        "s_max_u32 s0, s0, s4\n"
        "s_max_u32 s0, s0, s6\n"
        "s_cmp_lg_u32 s0, -1\n"
        "s_cselect_b32 s44, s0, s44\n"
        "s_cbranch_scc1 R2_2h2\n"
        "s_sub_u32 s45, s45, 1\n"
        "s_cbranch_scc1 R2_2k1\n"
        "s_add_u32 s54, s54, 1024\n"
        "s_buffer_load_dwordx4 s[0:3], s[60:63], s54 offset:0\n"
        "s_buffer_load_dwordx4 s[4:7], s[60:63], s54 offset:128\n"
        "s_branch R2_2k0\n"
        "R2_2k1:\n"
        "buffer_load_dword v192, v23, s[60:63], s55 offen sc1\n"
        "s_waitcnt vmcnt(0)\n"
        "R2_2h0:\n"
        "s_nop 1\n"
        "v_max_u32_dpp v192, v192, v192 row_ror:8 row_mask:0xf bank_mask:0xf\n"
        "s_nop 1\n"
        "v_max_u32_dpp v192, v192, v192 row_ror:4 row_mask:0xf bank_mask:0xf\n"
        "s_nop 1\n"
        "v_max_u32_dpp v192, v192, v192 row_ror:2 row_mask:0xf bank_mask:0xf\n"
        "s_nop 1\n"
        "v_max_u32_dpp v192, v192, v192 row_ror:1 row_mask:0xf bank_mask:0xf\n"
        "s_nop 1\n"
        "v_readlane_b32 s40, v192, 0\n"
        "v_readlane_b32 s45, v192, 16\n"
        "s_nop 1\n"
        "s_max_u32 s40, s40, s45\n"
        "s_cmp_lg_u32 s40, -1\n"
        "s_cselect_b32 s44, s40, s44\n"
        "s_cbranch_scc1 R2_2h1\n"
        "s_cmp_eq_u32 s66, 0\n"
        "s_cbranch_scc1 R2_2h1\n"
        "s_sub_u32 s66, s66, 1\n"
        "buffer_load_dword v192, v23, s[60:63], s55 offen sc1\n"
        "s_waitcnt vmcnt(0)\n"
        "s_branch R2_2h0\n"
        "R2_2h1:\n"
        "R2_2h2:\n"
        "s_sub_u32 s45, s44, s33\n"
        "s_mov_b32 s33, s44\n"
        "s_add_u32 s32, s32, 6\n"
        "s_min_u32 s32, s32, s45\n"
        "s_sub_u32 s39, s32, s36\n"
        "s_add_u32 s55, s55, 16384\n"
        "s_add_u32 s64, s64, 1\n"
        "s_cmp_lt_u32 s64, 63\n"
        "s_cbranch_scc1 R2_loop\n"
        "s_add_u32 s79, s69, 3904\n"
        "s_sub_u32 s54, s55, 16384\n"
        "s_mov_b32 s84, 43876352\n"
        "s_mov_b32 s80, 0\n"
        "buffer_load_dword v30, off, s[60:63], s79 offset:0 sc1\n"
        "buffer_load_dword v31, off, s[60:63], s79 offset:64 sc1\n"
        "buffer_load_dword v32, off, s[60:63], s79 offset:128 sc1\n"
        "s_add_u32 s85, s84, 5120\n"
        "s_add_u32 s86, s84, 10240\n"
        "s_add_u32 s87, s84, 15360\n"
        "s_waitcnt vmcnt(3)\n"
        "R2_ch0:\n"
        "s_nop 1\n"
        "v_max_u32_dpp v193, v193, v193 row_ror:8 row_mask:0xf bank_mask:0xf\n"
        "s_nop 1\n"
        "v_max_u32_dpp v193, v193, v193 row_ror:4 row_mask:0xf bank_mask:0xf\n"
        "s_nop 1\n"
        "v_max_u32_dpp v193, v193, v193 row_ror:2 row_mask:0xf bank_mask:0xf\n"
        "s_nop 1\n"
        "v_max_u32_dpp v193, v193, v193 row_ror:1 row_mask:0xf bank_mask:0xf\n"
        "s_nop 1\n"
        "v_readlane_b32 s40, v193, 0\n"
        "v_readlane_b32 s45, v193, 16\n"
        "s_nop 1\n"
        "s_max_u32 s40, s40, s45\n"
        "s_cmp_lg_u32 s40, -1\n"
        "s_cselect_b32 s44, s40, s44\n"
        "s_cbranch_scc1 R2_ch1\n"
        "s_cmp_eq_u32 s66, 0\n"
        "s_cbranch_scc1 R2_ch1\n"
        "s_sub_u32 s66, s66, 1\n"
        "buffer_load_dword v193, v23, s[60:63], s54 offen sc1\n"
        "s_waitcnt vmcnt(0)\n"
        "s_branch R2_ch0\n"
        "R2_ch1:\n"
        "R2_ch2:\n"
        "buffer_load_dwordx4 a[100:103], v9, s[60:63], s84 offen offset:0 sc1\n"
        "buffer_load_dwordx4 a[104:107], v9, s[60:63], s84 offen offset:256 sc1\n"
        "buffer_load_dwordx4 a[108:111], v9, s[60:63], s85 offen offset:0 sc1\n"
        "buffer_load_dwordx4 a[112:115], v9, s[60:63], s85 offen offset:256 sc1\n"
        "buffer_load_dwordx4 a[116:119], v9, s[60:63], s84 offen offset:1280 sc1\n"
        "buffer_load_dwordx4 a[120:123], v9, s[60:63], s85 offen offset:1280 sc1\n"
        "buffer_load_dwordx4 a[124:127], v9, s[60:63], s86 offen offset:0 sc1\n"
        "buffer_load_dwordx4 a[128:131], v9, s[60:63], s86 offen offset:256 sc1\n"
        "buffer_load_dwordx4 a[132:135], v9, s[60:63], s87 offen offset:0 sc1\n"
        "buffer_load_dwordx4 a[136:139], v9, s[60:63], s87 offen offset:256 sc1\n"
        "buffer_load_dwordx4 a[140:143], v9, s[60:63], s86 offen offset:1280 sc1\n"
        "buffer_load_dwordx4 a[144:147], v9, s[60:63], s87 offen offset:1280 sc1\n"
        "buffer_load_dwordx4 a[148:151], v10, s[60:63], s84 offen offset:0 sc1\n"
        "buffer_load_dwordx4 a[152:155], v10, s[60:63], s84 offen offset:256 sc1\n"
        "buffer_load_dwordx4 a[156:159], v10, s[60:63], s86 offen offset:0 sc1\n"
        "buffer_load_dwordx4 a[160:163], v10, s[60:63], s86 offen offset:256 sc1\n"
        "s_waitcnt lgkmcnt(0)\n"
        "s_barrier\n"
        "ds_read_b128 v[64:67], v21 offset:0\n"
        "ds_read_b128 v[68:71], v21 offset:2048\n"
        "ds_read_b128 v[72:75], v21 offset:4096\n"
        "ds_read_b128 v[76:79], v21 offset:6144\n"
        "s_waitcnt vmcnt(63)\n"
        "v_cmp_eq_u32 vcc, -1, v36\n"
        "s_cbranch_vccz R2_bg1\n"
        "R2_bg0:\n"
        "buffer_load_dword v36, v18, s[60:63], s89 offen sc1\n"
        "s_waitcnt vmcnt(0)\n"
        "v_cmp_eq_u32 vcc, -1, v36\n"
        "s_cbranch_vccz R2_bg1\n"
        "s_cmp_eq_u32 s66, 0\n"
        "s_cbranch_scc1 R2_bg1\n"
        "s_sub_u32 s66, s66, 1\n"
        "s_branch R2_bg0\n"
        "R2_bg1:\n"
        "v_sub_f32 v37, 1.0, v36\n"
        "v_lshlrev_b32 v164, 16, v34\n"
        "v_and_b32 v165, 0xffff0000, v34\n"
        "v_lshlrev_b32 v166, 16, v35\n"
        "v_and_b32 v167, 0xffff0000, v35\n"
        "v_cvt_pk_bf16_f32 v37, v37, v37\n"
        "v_lshlrev_b32 v170, 16, v38\n"
        "v_and_b32 v171, 0xffff0000, v38\n"
        "v_lshlrev_b32 v172, 16, v39\n"
        "v_and_b32 v173, 0xffff0000, v39\n"
        "v_and_b32 v37, 0xffff0000, v37\n"
        "v_mul_f32 v170, v36, v170\n"
        "v_mul_f32 v171, v36, v171\n"
        "v_mul_f32 v172, v36, v172\n"
        "v_mul_f32 v173, v36, v173\n"
        "v_mul_f32 v164, v37, v164\n"
        "v_mul_f32 v165, v37, v165\n"
        "v_mul_f32 v166, v37, v166\n"
        "v_mul_f32 v167, v37, v167\n"
        "v_cvt_pk_bf16_f32 v174, v170, v171\n"
        "v_cvt_pk_bf16_f32 v175, v172, v173\n"
        "v_cvt_pk_bf16_f32 v168, v164, v165\n"
        "v_cvt_pk_bf16_f32 v169, v166, v167\n"
        "v_lshlrev_b32 v170, 16, v174\n"
        "v_and_b32 v171, 0xffff0000, v174\n"
        "v_lshlrev_b32 v172, 16, v175\n"
        "v_and_b32 v173, 0xffff0000, v175\n"
        "v_lshlrev_b32 v164, 16, v168\n"
        "v_and_b32 v165, 0xffff0000, v168\n"
        "v_lshlrev_b32 v166, 16, v169\n"
        "v_and_b32 v167, 0xffff0000, v169\n"
        "v_pk_add_f32 v[170:171], v[170:171], v[164:165]\n"
        "v_pk_add_f32 v[172:173], v[172:173], v[166:167]\n"
        "v_cvt_pk_bf16_f32 v168, v170, v171\n"
        "v_cvt_pk_bf16_f32 v169, v172, v173\n"
        "buffer_store_dwordx2 v[168:169], v19, s[72:75], s88 offen\n"
        "s_waitcnt lgkmcnt(2)\n"
        "v_pk_add_f32 v[64:65], v[64:65], v[68:69]\n"
        "v_pk_add_f32 v[66:67], v[66:67], v[70:71]\n"
        "s_waitcnt lgkmcnt(1)\n"
        "v_pk_add_f32 v[64:65], v[64:65], v[72:73]\n"
        "v_pk_add_f32 v[66:67], v[66:67], v[74:75]\n"
        "s_waitcnt lgkmcnt(0)\n"
        "v_pk_add_f32 v[64:65], v[64:65], v[76:77]\n"
        "v_pk_add_f32 v[66:67], v[66:67], v[78:79]\n"
        "v_cvt_pk_bf16_f32 v38, v64, v65\n"
        "v_cvt_pk_bf16_f32 v39, v66, v67\n"
        "s_add_u32 s48, s44, s46\n"
        "s_sub_u32 s48, s48, s42\n"
        "s_sub_u32 s48, s48, s34\n"
        "s_max_i32 s48, s48, 0\n"
        "s_min_i32 s48, s48, 0x10000\n"
        "s_mul_i32 s40, s48, s35\n"
        "s_lshr_b32 s40, s40, 16\n"
        "s_sub_u32 s41, s48, s40\n"
        "s_mul_i32 s40, s41, s35\n"
        "s_lshr_b32 s40, s40, 16\n"
        "s_sub_u32 s48, s48, s40\n"
        "s_add_u32 s48, s48, s34\n"
        "s_sub_u32 s48, s48, 40\n"
        "R2_tu:\n"
        "s_memtime s[50:51]\n"
        "s_waitcnt lgkmcnt(0)\n"
        "s_sub_u32 s50, s50, s48\n"
        "s_cmp_lt_i32 s50, 0\n"
        "s_cbranch_scc1 R2_tu\n"
        "buffer_load_dwordx4 v[40:43], v0, s[60:63], s80 offen sc1\n"
        "buffer_load_dwordx4 v[44:47], v1, s[60:63], s80 offen sc1\n"
        "s_waitcnt vmcnt(19)\n"
        "v_max3_u32 v33, v30, v31, v32\n"
        "v_cmp_eq_u32 vcc, -1, v33\n"
        "s_cbranch_vccz R2_f1\n"
        "R2_f0:\n"
        "buffer_load_dword v30, off, s[60:63], s79 offset:0 sc1\n"
        "buffer_load_dword v31, off, s[60:63], s79 offset:64 sc1\n"
        "buffer_load_dword v32, off, s[60:63], s79 offset:128 sc1\n"
        "s_waitcnt vmcnt(0)\n"
        "v_max3_u32 v33, v30, v31, v32\n"
        "v_cmp_eq_u32 vcc, -1, v33\n"
        "s_cbranch_vccz R2_f1\n"
        "s_sleep 1\n"
        "s_cmp_eq_u32 s66, 0\n"
        "s_cbranch_scc1 R2_f1\n"
        "s_sub_u32 s66, s66, 1\n"
        "s_branch R2_f0\n"
        "R2_f1:\n"
        "s_add_u32 s90, s68, 2498560\n"
        "s_mov_b32 s89, 4304512\n"
        "buffer_load_dwordx2 v[34:35], v17, s[60:63], s90 offen sc1\n"
        "buffer_load_dword v36, v18, s[60:63], s89 offen sc1\n"
        "s_add_u32 s90, s68, 2539520\n"
        "s_mov_b32 s89, 4304640\n"
        "buffer_load_dwordx2 v[188:189], v17, s[60:63], s90 offen sc1\n"
        "buffer_load_dword v190, v18, s[60:63], s89 offen sc1\n"
        "s_add_u32 s90, s68, 2580480\n"
        "s_mov_b32 s89, 4304768\n"
        "buffer_load_dwordx2 v[194:195], v17, s[60:63], s90 offen sc1\n"
        "buffer_load_dword v196, v18, s[60:63], s89 offen sc1\n"
        "s_barrier\n"
        "s_mov_b32 s84, 43958272\n"
        "s_add_u32 s85, s84, 5120\n"
        "s_add_u32 s86, s84, 10240\n"
        "s_add_u32 s87, s84, 15360\n"
        "s_add_u32 s48, s44, s38\n"
        "s_sub_u32 s48, s48, s42\n"
        "s_sub_u32 s48, s48, s34\n"
        "s_max_i32 s48, s48, 0\n"
        "s_min_i32 s48, s48, 0x10000\n"
        "s_mul_i32 s40, s48, s35\n"
        "s_lshr_b32 s40, s40, 16\n"
        "s_sub_u32 s41, s48, s40\n"
        "s_mul_i32 s40, s41, s35\n"
        "s_lshr_b32 s40, s40, 16\n"
        "s_sub_u32 s48, s48, s40\n"
        "s_add_u32 s48, s48, s34\n"
        "s_sub_u32 s48, s48, 40\n"
        "R2_tl:\n"
        "s_memtime s[50:51]\n"
        "s_waitcnt lgkmcnt(0)\n"
        "s_sub_u32 s50, s50, s48\n"
        "s_cmp_lt_i32 s50, 0\n"
        "s_cbranch_scc1 R2_tl\n"
        "buffer_load_dwordx4 v[100:103], v9, s[60:63], s84 offen offset:0 sc1\n"
        "buffer_load_dwordx4 v[104:107], v9, s[60:63], s84 offen offset:256 sc1\n"
        "buffer_load_dwordx4 v[108:111], v9, s[60:63], s85 offen offset:0 sc1\n"
        "buffer_load_dwordx4 v[112:115], v9, s[60:63], s85 offen offset:256 sc1\n"
        "buffer_load_dwordx4 v[116:119], v9, s[60:63], s84 offen offset:1280 sc1\n"
        "buffer_load_dwordx4 v[120:123], v9, s[60:63], s85 offen offset:1280 sc1\n"
        "buffer_load_dwordx4 v[124:127], v9, s[60:63], s86 offen offset:0 sc1\n"
        "buffer_load_dwordx4 v[128:131], v9, s[60:63], s86 offen offset:256 sc1\n"
        "buffer_load_dwordx4 v[132:135], v9, s[60:63], s87 offen offset:0 sc1\n"
        "buffer_load_dwordx4 v[136:139], v9, s[60:63], s87 offen offset:256 sc1\n"
        "buffer_load_dwordx4 v[140:143], v9, s[60:63], s86 offen offset:1280 sc1\n"
        "buffer_load_dwordx4 v[144:147], v9, s[60:63], s87 offen offset:1280 sc1\n"
        "buffer_load_dwordx4 v[148:151], v10, s[60:63], s84 offen offset:0 sc1\n"
        "buffer_load_dwordx4 v[152:155], v10, s[60:63], s84 offen offset:256 sc1\n"
        "buffer_load_dwordx4 v[156:159], v10, s[60:63], s86 offen offset:0 sc1\n"
        "buffer_load_dwordx4 v[160:163], v10, s[60:63], s86 offen offset:256 sc1\n"
        "s_waitcnt vmcnt(40)\n"
        "v_mfma_f32_16x16x32_bf16 v[180:183], a[64:67], a[100:103], 0\n"
        "s_waitcnt vmcnt(39)\n"
        "v_mfma_f32_16x16x32_bf16 v[184:187], a[64:67], a[104:107], 0\n"
        "s_waitcnt vmcnt(38)\n"
        "v_mfma_f32_16x16x32_bf16 v[180:183], a[68:71], a[108:111], v[180:183]\n"
        "s_waitcnt vmcnt(37)\n"
        "v_mfma_f32_16x16x32_bf16 v[184:187], a[68:71], a[112:115], v[184:187]\n"
        "s_waitcnt vmcnt(36)\n"
        "v_mfma_f32_16x16x32_bf16 v[184:187], a[72:75], a[116:119], v[184:187]\n"
        "s_waitcnt vmcnt(35)\n"
        "v_mfma_f32_16x16x32_bf16 v[184:187], a[76:79], a[120:123], v[184:187]\n"
        "s_waitcnt vmcnt(34)\n"
        "v_mfma_f32_16x16x32_bf16 v[180:183], a[80:83], a[124:127], v[180:183]\n"
        "s_waitcnt vmcnt(33)\n"
        "v_mfma_f32_16x16x32_bf16 v[184:187], a[80:83], a[128:131], v[184:187]\n"
        "s_waitcnt vmcnt(32)\n"
        "v_mfma_f32_16x16x32_bf16 v[180:183], a[84:87], a[132:135], v[180:183]\n"
        "s_waitcnt vmcnt(31)\n"
        "v_mfma_f32_16x16x32_bf16 v[184:187], a[84:87], a[136:139], v[184:187]\n"
        "s_waitcnt vmcnt(30)\n"
        "v_mfma_f32_16x16x32_bf16 v[184:187], a[88:91], a[140:143], v[184:187]\n"
        "s_waitcnt vmcnt(29)\n"
        "v_mfma_f32_16x16x32_bf16 v[184:187], a[92:95], a[144:147], v[184:187]\n"
        "s_waitcnt vmcnt(28)\n"
        "v_mfma_f32_16x16x32_bf16 v[180:183], v[56:59], a[148:151], v[180:183]\n"
        "s_waitcnt vmcnt(27)\n"
        "v_mfma_f32_16x16x32_bf16 v[184:187], v[56:59], a[152:155], v[184:187]\n"
        "s_waitcnt vmcnt(26)\n"
        "v_mfma_f32_16x16x32_bf16 v[180:183], v[60:63], a[156:159], v[180:183]\n"
        "s_waitcnt vmcnt(25)\n"
        "v_mfma_f32_16x16x32_bf16 v[184:187], v[60:63], a[160:163], v[184:187]\n"
        "s_nop 6\n"
        "ds_write_b128 v20, v[180:183]\n"
        "ds_write_b128 v20, v[184:187] offset:1024\n"
        "s_mov_b32 s88, 2498560\n"
        "s_mov_b32 s89, 4304512\n"
        "s_waitcnt lgkmcnt(0)\n"
        "s_barrier\n"
        "ds_read_b128 v[64:67], v21 offset:0\n"
        "ds_read_b128 v[68:71], v21 offset:2048\n"
        "ds_read_b128 v[72:75], v21 offset:4096\n"
        "ds_read_b128 v[76:79], v21 offset:6144\n"
        "s_waitcnt vmcnt(16)\n"
        "v_cmp_eq_u32 vcc, -1, v36\n"
        "s_cbranch_vccz R2_ag1\n"
        "R2_ag0:\n"
        "buffer_load_dword v36, v18, s[60:63], s89 offen sc1\n"
        "s_waitcnt vmcnt(0)\n"
        "v_cmp_eq_u32 vcc, -1, v36\n"
        "s_cbranch_vccz R2_ag1\n"
        "s_cmp_eq_u32 s66, 0\n"
        "s_cbranch_scc1 R2_ag1\n"
        "s_sub_u32 s66, s66, 1\n"
        "s_branch R2_ag0\n"
        "R2_ag1:\n"
        "v_sub_f32 v37, 1.0, v36\n"
        "v_lshlrev_b32 v164, 16, v34\n"
        "v_and_b32 v165, 0xffff0000, v34\n"
        "v_lshlrev_b32 v166, 16, v35\n"
        "v_and_b32 v167, 0xffff0000, v35\n"
        "v_cvt_pk_bf16_f32 v37, v37, v37\n"
        "v_lshlrev_b32 v170, 16, v38\n"
        "v_and_b32 v171, 0xffff0000, v38\n"
        "v_lshlrev_b32 v172, 16, v39\n"
        "v_and_b32 v173, 0xffff0000, v39\n"
        "v_and_b32 v37, 0xffff0000, v37\n"
        "v_mul_f32 v170, v36, v170\n"
        "v_mul_f32 v171, v36, v171\n"
        "v_mul_f32 v172, v36, v172\n"
        "v_mul_f32 v173, v36, v173\n"
        "v_mul_f32 v164, v37, v164\n"
        "v_mul_f32 v165, v37, v165\n"
        "v_mul_f32 v166, v37, v166\n"
        "v_mul_f32 v167, v37, v167\n"
        "v_cvt_pk_bf16_f32 v174, v170, v171\n"
        "v_cvt_pk_bf16_f32 v175, v172, v173\n"
        "v_cvt_pk_bf16_f32 v168, v164, v165\n"
        "v_cvt_pk_bf16_f32 v169, v166, v167\n"
        "v_lshlrev_b32 v170, 16, v174\n"
        "v_and_b32 v171, 0xffff0000, v174\n"
        "v_lshlrev_b32 v172, 16, v175\n"
        "v_and_b32 v173, 0xffff0000, v175\n"
        "v_lshlrev_b32 v164, 16, v168\n"
        "v_and_b32 v165, 0xffff0000, v168\n"
        "v_lshlrev_b32 v166, 16, v169\n"
        "v_and_b32 v167, 0xffff0000, v169\n"
        "v_pk_add_f32 v[170:171], v[170:171], v[164:165]\n"
        "v_pk_add_f32 v[172:173], v[172:173], v[166:167]\n"
        "v_cvt_pk_bf16_f32 v168, v170, v171\n"
        "v_cvt_pk_bf16_f32 v169, v172, v173\n"
        "buffer_store_dwordx2 v[168:169], v19, s[72:75], s88 offen\n"
        "s_waitcnt lgkmcnt(2)\n"
        "v_pk_add_f32 v[64:65], v[64:65], v[68:69]\n"
        "v_pk_add_f32 v[66:67], v[66:67], v[70:71]\n"
        "s_waitcnt lgkmcnt(1)\n"
        "v_pk_add_f32 v[64:65], v[64:65], v[72:73]\n"
        "v_pk_add_f32 v[66:67], v[66:67], v[74:75]\n"
        "s_waitcnt lgkmcnt(0)\n"
        "v_pk_add_f32 v[64:65], v[64:65], v[76:77]\n"
        "v_pk_add_f32 v[66:67], v[66:67], v[78:79]\n"
        "v_cvt_pk_bf16_f32 v38, v64, v65\n"
        "v_cvt_pk_bf16_f32 v39, v66, v67\n"
        "s_mov_b32 s88, 2539520\n"
        "s_mov_b32 s89, 4304640\n"
        "s_waitcnt vmcnt(63)\n"
        "v_cmp_eq_u32 vcc, -1, v190\n"
        "s_cbranch_vccz R2_ug1\n"
        "R2_ug0:\n"
        "buffer_load_dword v190, v18, s[60:63], s89 offen sc1\n"
        "s_waitcnt vmcnt(0)\n"
        "v_cmp_eq_u32 vcc, -1, v190\n"
        "s_cbranch_vccz R2_ug1\n"
        "s_cmp_eq_u32 s66, 0\n"
        "s_cbranch_scc1 R2_ug1\n"
        "s_sub_u32 s66, s66, 1\n"
        "s_branch R2_ug0\n"
        "R2_ug1:\n"
        "v_sub_f32 v37, 1.0, v190\n"
        "v_lshlrev_b32 v164, 16, v188\n"
        "v_and_b32 v165, 0xffff0000, v188\n"
        "v_lshlrev_b32 v166, 16, v189\n"
        "v_and_b32 v167, 0xffff0000, v189\n"
        "v_cvt_pk_bf16_f32 v37, v37, v37\n"
        "v_lshlrev_b32 v170, 16, v38\n"
        "v_and_b32 v171, 0xffff0000, v38\n"
        "v_lshlrev_b32 v172, 16, v39\n"
        "v_and_b32 v173, 0xffff0000, v39\n"
        "v_and_b32 v37, 0xffff0000, v37\n"
        "v_mul_f32 v170, v190, v170\n"
        "v_mul_f32 v171, v190, v171\n"
        "v_mul_f32 v172, v190, v172\n"
        "v_mul_f32 v173, v190, v173\n"
        "v_mul_f32 v164, v37, v164\n"
        "v_mul_f32 v165, v37, v165\n"
        "v_mul_f32 v166, v37, v166\n"
        "v_mul_f32 v167, v37, v167\n"
        "v_cvt_pk_bf16_f32 v174, v170, v171\n"
        "v_cvt_pk_bf16_f32 v175, v172, v173\n"
        "v_cvt_pk_bf16_f32 v168, v164, v165\n"
        "v_cvt_pk_bf16_f32 v169, v166, v167\n"
        "v_lshlrev_b32 v170, 16, v174\n"
        "v_and_b32 v171, 0xffff0000, v174\n"
        "v_lshlrev_b32 v172, 16, v175\n"
        "v_and_b32 v173, 0xffff0000, v175\n"
        "v_lshlrev_b32 v164, 16, v168\n"
        "v_and_b32 v165, 0xffff0000, v168\n"
        "v_lshlrev_b32 v166, 16, v169\n"
        "v_and_b32 v167, 0xffff0000, v169\n"
        "v_pk_add_f32 v[170:171], v[170:171], v[164:165]\n"
        "v_pk_add_f32 v[172:173], v[172:173], v[166:167]\n"
        "v_cvt_pk_bf16_f32 v168, v170, v171\n"
        "v_cvt_pk_bf16_f32 v169, v172, v173\n"
        "buffer_store_dwordx2 v[168:169], v19, s[72:75], s88 offen\n"
        "R2_x0:\n"
        "v_max3_u32 v93, v40, v41, v42\n"
        "v_max3_u32 v93, v93, v43, v44\n"
        "v_max3_u32 v93, v93, v45, v46\n"
        "v_max_u32 v93, v93, v47\n"
        "v_cmp_eq_u32 vcc, -1, v93\n"
        "s_cbranch_vccz R2_x1\n"
        "buffer_load_dwordx4 v[40:43], v0, s[60:63], s80 offen sc1\n"
        "buffer_load_dwordx4 v[44:47], v1, s[60:63], s80 offen sc1\n"
        "s_waitcnt vmcnt(0)\n"
        "s_cmp_eq_u32 s66, 0\n"
        "s_cbranch_scc1 R2_x1\n"
        "s_sub_u32 s66, s66, 1\n"
        "s_branch R2_x0\n"
        "R2_x1:\n"
        "s_barrier\n"
        "R2_y0:\n"
        "s_waitcnt vmcnt(17)\n"
        "v_mfma_f32_16x16x32_bf16 v[180:183], a[0:3], v[100:103], 0\n"
        "v_max3_u32 v92, v100, v100, v101\n"
        "v_max3_u32 v92, v92, v102, v103\n"
        "s_waitcnt vmcnt(16)\n"
        "v_mfma_f32_16x16x32_bf16 v[184:187], a[0:3], v[104:107], 0\n"
        "v_max3_u32 v92, v92, v104, v105\n"
        "v_max3_u32 v92, v92, v106, v107\n"
        "s_waitcnt vmcnt(15)\n"
        "v_mfma_f32_16x16x32_bf16 v[180:183], a[4:7], v[108:111], v[180:183]\n"
        "v_max3_u32 v92, v92, v108, v109\n"
        "v_max3_u32 v92, v92, v110, v111\n"
        "s_waitcnt vmcnt(14)\n"
        "v_mfma_f32_16x16x32_bf16 v[184:187], a[4:7], v[112:115], v[184:187]\n"
        "v_max3_u32 v92, v92, v112, v113\n"
        "v_max3_u32 v92, v92, v114, v115\n"
        "s_waitcnt vmcnt(13)\n"
        "v_mfma_f32_16x16x32_bf16 v[184:187], a[8:11], v[116:119], v[184:187]\n"
        "v_max3_u32 v92, v92, v116, v117\n"
        "v_max3_u32 v92, v92, v118, v119\n"
        "s_waitcnt vmcnt(12)\n"
        "v_mfma_f32_16x16x32_bf16 v[184:187], a[12:15], v[120:123], v[184:187]\n"
        "v_max3_u32 v92, v92, v120, v121\n"
        "v_max3_u32 v92, v92, v122, v123\n"
        "s_waitcnt vmcnt(11)\n"
        "v_mfma_f32_16x16x32_bf16 v[180:183], a[16:19], v[124:127], v[180:183]\n"
        "v_max3_u32 v92, v92, v124, v125\n"
        "v_max3_u32 v92, v92, v126, v127\n"
        "s_waitcnt vmcnt(10)\n"
        "v_mfma_f32_16x16x32_bf16 v[184:187], a[16:19], v[128:131], v[184:187]\n"
        "v_max3_u32 v92, v92, v128, v129\n"
        "v_max3_u32 v92, v92, v130, v131\n"
        "s_waitcnt vmcnt(9)\n"
        "v_mfma_f32_16x16x32_bf16 v[180:183], a[20:23], v[132:135], v[180:183]\n"
        "v_max3_u32 v92, v92, v132, v133\n"
        "v_max3_u32 v92, v92, v134, v135\n"
        "s_waitcnt vmcnt(8)\n"
        "v_mfma_f32_16x16x32_bf16 v[184:187], a[20:23], v[136:139], v[184:187]\n"
        "v_max3_u32 v92, v92, v136, v137\n"
        "v_max3_u32 v92, v92, v138, v139\n"
        "s_waitcnt vmcnt(7)\n"
        "v_mfma_f32_16x16x32_bf16 v[184:187], a[24:27], v[140:143], v[184:187]\n"
        "v_max3_u32 v92, v92, v140, v141\n"
        "v_max3_u32 v92, v92, v142, v143\n"
        "s_waitcnt vmcnt(6)\n"
        "v_mfma_f32_16x16x32_bf16 v[184:187], a[28:31], v[144:147], v[184:187]\n"
        "v_max3_u32 v92, v92, v144, v145\n"
        "v_max3_u32 v92, v92, v146, v147\n"
        "s_waitcnt vmcnt(5)\n"
        "v_mfma_f32_16x16x32_bf16 v[180:183], v[40:43], v[148:151], v[180:183]\n"
        "v_max3_u32 v92, v92, v148, v149\n"
        "v_max3_u32 v92, v92, v150, v151\n"
        "s_waitcnt vmcnt(4)\n"
        "v_mfma_f32_16x16x32_bf16 v[184:187], v[40:43], v[152:155], v[184:187]\n"
        "v_max3_u32 v92, v92, v152, v153\n"
        "v_max3_u32 v92, v92, v154, v155\n"
        "s_waitcnt vmcnt(3)\n"
        "v_mfma_f32_16x16x32_bf16 v[180:183], v[44:47], v[156:159], v[180:183]\n"
        "v_max3_u32 v92, v92, v156, v157\n"
        "v_max3_u32 v92, v92, v158, v159\n"
        "s_waitcnt vmcnt(2)\n"
        "v_mfma_f32_16x16x32_bf16 v[184:187], v[44:47], v[160:163], v[184:187]\n"
        "v_max3_u32 v92, v92, v160, v161\n"
        "v_max3_u32 v92, v92, v162, v163\n"
        "v_cmp_eq_u32 vcc, -1, v92\n"
        "s_cbranch_vccz R2_y1\n"
        "buffer_load_dwordx4 v[100:103], v9, s[60:63], s84 offen offset:0 sc1\n"
        "buffer_load_dwordx4 v[104:107], v9, s[60:63], s84 offen offset:256 sc1\n"
        "buffer_load_dwordx4 v[108:111], v9, s[60:63], s85 offen offset:0 sc1\n"
        "buffer_load_dwordx4 v[112:115], v9, s[60:63], s85 offen offset:256 sc1\n"
        "buffer_load_dwordx4 v[116:119], v9, s[60:63], s84 offen offset:1280 sc1\n"
        "buffer_load_dwordx4 v[120:123], v9, s[60:63], s85 offen offset:1280 sc1\n"
        "buffer_load_dwordx4 v[124:127], v9, s[60:63], s86 offen offset:0 sc1\n"
        "buffer_load_dwordx4 v[128:131], v9, s[60:63], s86 offen offset:256 sc1\n"
        "buffer_load_dwordx4 v[132:135], v9, s[60:63], s87 offen offset:0 sc1\n"
        "buffer_load_dwordx4 v[136:139], v9, s[60:63], s87 offen offset:256 sc1\n"
        "buffer_load_dwordx4 v[140:143], v9, s[60:63], s86 offen offset:1280 sc1\n"
        "buffer_load_dwordx4 v[144:147], v9, s[60:63], s87 offen offset:1280 sc1\n"
        "buffer_load_dwordx4 v[148:151], v10, s[60:63], s84 offen offset:0 sc1\n"
        "buffer_load_dwordx4 v[152:155], v10, s[60:63], s84 offen offset:256 sc1\n"
        "buffer_load_dwordx4 v[156:159], v10, s[60:63], s86 offen offset:0 sc1\n"
        "buffer_load_dwordx4 v[160:163], v10, s[60:63], s86 offen offset:256 sc1\n"
        "s_waitcnt vmcnt(0)\n"
        "s_cmp_eq_u32 s66, 0\n"
        "s_cbranch_scc1 R2_y1\n"
        "s_sub_u32 s66, s66, 1\n"
        "s_branch R2_y0\n"
        "R2_y1:\n"
        "s_nop 3\n"
        "ds_write_b128 v20, v[180:183]\n"
        "ds_write_b128 v20, v[184:187] offset:1024\n"
        "s_mov_b32 s88, 2580480\n"
        "s_mov_b32 s89, 4304768\n"
        "s_waitcnt lgkmcnt(0)\n"
        "s_barrier\n"
        "ds_read_b128 v[64:67], v21 offset:0\n"
        "ds_read_b128 v[68:71], v21 offset:2048\n"
        "ds_read_b128 v[72:75], v21 offset:4096\n"
        "ds_read_b128 v[76:79], v21 offset:6144\n"
        "s_waitcnt lgkmcnt(2)\n"
        "v_pk_add_f32 v[64:65], v[64:65], v[68:69]\n"
        "v_pk_add_f32 v[66:67], v[66:67], v[70:71]\n"
        "s_waitcnt lgkmcnt(1)\n"
        "v_pk_add_f32 v[64:65], v[64:65], v[72:73]\n"
        "v_pk_add_f32 v[66:67], v[66:67], v[74:75]\n"
        "s_waitcnt lgkmcnt(0)\n"
        "v_pk_add_f32 v[64:65], v[64:65], v[76:77]\n"
        "v_pk_add_f32 v[66:67], v[66:67], v[78:79]\n"
        "v_cvt_pk_bf16_f32 v38, v64, v65\n"
        "v_cvt_pk_bf16_f32 v39, v66, v67\n"
        "s_waitcnt vmcnt(63)\n"
        "v_cmp_eq_u32 vcc, -1, v196\n"
        "s_cbranch_vccz R2_tg1\n"
        "R2_tg0:\n"
        "buffer_load_dword v196, v18, s[60:63], s89 offen sc1\n"
        "s_waitcnt vmcnt(0)\n"
        "v_cmp_eq_u32 vcc, -1, v196\n"
        "s_cbranch_vccz R2_tg1\n"
        "s_cmp_eq_u32 s66, 0\n"
        "s_cbranch_scc1 R2_tg1\n"
        "s_sub_u32 s66, s66, 1\n"
        "s_branch R2_tg0\n"
        "R2_tg1:\n"
        "v_sub_f32 v37, 1.0, v196\n"
        "v_lshlrev_b32 v164, 16, v194\n"
        "v_and_b32 v165, 0xffff0000, v194\n"
        "v_lshlrev_b32 v166, 16, v195\n"
        "v_and_b32 v167, 0xffff0000, v195\n"
        "v_cvt_pk_bf16_f32 v37, v37, v37\n"
        "v_lshlrev_b32 v170, 16, v38\n"
        "v_and_b32 v171, 0xffff0000, v38\n"
        "v_lshlrev_b32 v172, 16, v39\n"
        "v_and_b32 v173, 0xffff0000, v39\n"
        "v_and_b32 v37, 0xffff0000, v37\n"
        "v_mul_f32 v170, v196, v170\n"
        "v_mul_f32 v171, v196, v171\n"
        "v_mul_f32 v172, v196, v172\n"
        "v_mul_f32 v173, v196, v173\n"
        "v_mul_f32 v164, v37, v164\n"
        "v_mul_f32 v165, v37, v165\n"
        "v_mul_f32 v166, v37, v166\n"
        "v_mul_f32 v167, v37, v167\n"
        "v_cvt_pk_bf16_f32 v174, v170, v171\n"
        "v_cvt_pk_bf16_f32 v175, v172, v173\n"
        "v_cvt_pk_bf16_f32 v168, v164, v165\n"
        "v_cvt_pk_bf16_f32 v169, v166, v167\n"
        "v_lshlrev_b32 v170, 16, v174\n"
        "v_and_b32 v171, 0xffff0000, v174\n"
        "v_lshlrev_b32 v172, 16, v175\n"
        "v_and_b32 v173, 0xffff0000, v175\n"
        "v_lshlrev_b32 v164, 16, v168\n"
        "v_and_b32 v165, 0xffff0000, v168\n"
        "v_lshlrev_b32 v166, 16, v169\n"
        "v_and_b32 v167, 0xffff0000, v169\n"
        "v_pk_add_f32 v[170:171], v[170:171], v[164:165]\n"
        "v_pk_add_f32 v[172:173], v[172:173], v[166:167]\n"
        "v_cvt_pk_bf16_f32 v168, v170, v171\n"
        "v_cvt_pk_bf16_f32 v169, v172, v173\n"
        "buffer_store_dwordx2 v[168:169], v19, s[72:75], s88 offen\n"
        "s_endpgm\n"
        "R2_0miss:\n"
        "v_cmp_eq_u32 vcc, -1, v93\n"
        "s_cmp_eq_u32 s66, 0\n"
        "s_cbranch_scc1 R2_0q\n"
        "s_sub_u32 s66, s66, 1\n"
        "s_cbranch_vccz R2_0r\n"
        "buffer_load_dwordx4 v[40:43], v0, s[60:63], s80 offen sc1\n"
        "buffer_load_dwordx4 v[44:47], v1, s[60:63], s80 offen sc1\n"
        "R2_0r:\n"
        "buffer_load_dwordx4 v[64:67], v2, s[60:63], s81 offen sc1\n"
        "buffer_load_dwordx4 v[72:75], v4, s[60:63], s81 offen sc1\n"
        "buffer_load_dwordx4 v[76:79], v5, s[60:63], s81 offen sc1\n"
        "buffer_load_dwordx4 v[80:83], v6, s[60:63], s81 offen sc1\n"
        "buffer_load_dwordx4 v[84:87], v7, s[60:63], s81 offen sc1\n"
        "s_mov_b32 s65, 4026531840\n"
        "buffer_store_dword v20, off, s[72:75], s65\n"
        "s_branch R2_0p\n"
        "R2_1miss:\n"
        "v_cmp_eq_u32 vcc, -1, v93\n"
        "s_cmp_eq_u32 s66, 0\n"
        "s_cbranch_scc1 R2_1q\n"
        "s_sub_u32 s66, s66, 1\n"
        "s_cbranch_vccz R2_1r\n"
        "buffer_load_dwordx4 v[48:51], v0, s[60:63], s80 offen sc1\n"
        "buffer_load_dwordx4 v[52:55], v1, s[60:63], s80 offen sc1\n"
        "R2_1r:\n"
        "buffer_load_dwordx4 v[64:67], v2, s[60:63], s81 offen sc1\n"
        "buffer_load_dwordx4 v[72:75], v4, s[60:63], s81 offen sc1\n"
        "buffer_load_dwordx4 v[76:79], v5, s[60:63], s81 offen sc1\n"
        "buffer_load_dwordx4 v[80:83], v6, s[60:63], s81 offen sc1\n"
        "buffer_load_dwordx4 v[84:87], v7, s[60:63], s81 offen sc1\n"
        "s_mov_b32 s65, 4026531840\n"
        "buffer_store_dword v20, off, s[72:75], s65\n"
        "s_branch R2_1p\n"
        "R2_2miss:\n"
        "v_cmp_eq_u32 vcc, -1, v93\n"
        "s_cmp_eq_u32 s66, 0\n"
        "s_cbranch_scc1 R2_2q\n"
        "s_sub_u32 s66, s66, 1\n"
        "s_cbranch_vccz R2_2r\n"
        "buffer_load_dwordx4 v[56:59], v0, s[60:63], s80 offen sc1\n"
        "buffer_load_dwordx4 v[60:63], v1, s[60:63], s80 offen sc1\n"
        "R2_2r:\n"
        "buffer_load_dwordx4 v[64:67], v2, s[60:63], s81 offen sc1\n"
        "buffer_load_dwordx4 v[72:75], v4, s[60:63], s81 offen sc1\n"
        "buffer_load_dwordx4 v[76:79], v5, s[60:63], s81 offen sc1\n"
        "buffer_load_dwordx4 v[80:83], v6, s[60:63], s81 offen sc1\n"
        "buffer_load_dwordx4 v[84:87], v7, s[60:63], s81 offen sc1\n"
        "s_mov_b32 s65, 4026531840\n"
        "buffer_store_dword v20, off, s[72:75], s65\n"
        "s_branch R2_2p\n"
        "R2_fin:\n"
        "s_endpgm\n"
        ROLE_OPS(p.out, p.out) : "v32", "v33", "v34", "v35", "v36", "v37", "v38", "v39", "v40", "v41", "v42", "v43", "v44", "v45", "v46", "v47", "v48", "v49", "v50", "v51", "v52", "v53", "v54", "v55", "v56", "v57", "v58", "v59", "v60", "v61", "v62", "v63", "v64", "v65", "v66", "v67", "v68", "v69", "v70", "v71", "v72", "v73", "v74", "v75", "v76", "v77", "v78", "v79", "v80", "v81", "v82", "v83", "v84", "v85", "v86", "v87", "v88", "v89", "v90", "v91", "v92", "v93", "v94", "v95", "v96", "v97", "v98", "v99", "v100", "v101", "v102", "v103", "v104", "v105", "v106", "v107", "v108", "v109", "v110", "v111", "v112", "v113", "v114", "v115", "v116", "v117", "v118", "v119", "v120", "v121", "v122", "v123", "v124", "v125", "v126", "v127", "v128", "v129", "v130", "v131", "v132", "v133", "v134", "v135", "v136", "v137", "v138", "v139", "v140", "v141", "v142", "v143", "v144", "v145", "v146", "v147", "v148", "v149", "v150", "v151", "v152", "v153", "v154", "v155", "v156", "v157", "v158", "v159", "v160", "v161", "v162", "v163", "v164", "v165", "v166", "v167", "v168", "v169", "v170", "v171", "v172", "v173", "v174", "v175", "v176", "v177", "v178", "v179", "v180", "v181", "v182", "v183", "v184", "v185", "v186", "v187", "v188", "v189", "v190", "v192", "v193", "v194", "v195", "v196", "v197", "v198", "v199", "v200", "v201", "v202", "v203", "v204", "v205", "v206", "v207", "v208", "v209", "v210", "v211", "v212", "v213", "v214", "v215", "v216", "v217", "v218", "v219", "v220", "v221", "v222", "v223", "v224", "v225", "v226", "v227", "v228", "v229", "v230", "v231", "v232", "v233", "v234", "v235", "v236", "v237", "v238", "v239", "v240", "v241", "v242", "v243", "v244", "v245", "v246", "v247", "v248", "v249", "v250", "v251", "v252", "v253", "v254", "v255", "s38", "s39", "s40", "s41", "s42", "s43", "s44", "s45", "s46", "s47", "s48", "s49", "s50", "s51", "s52", "s53", "s54", "s55", "s56", "s57", "s58", "s59", "s60", "s61", "s62", "s63", "s64", "s65", "s66", "s67", "s68", "s69", "s70", "s71", "s72", "s73", "s74", "s75", "s76", "s77", "s78", "s79", "s80", "s81", "s82", "s83", "s84", "s85", "s86", "s87", "s88", "s89", "s90", "s91", "s92", "s93", "s94", "s95", "s96", "s97", "s98", "s99", "vcc", "scc", "memory", "m0", "a0", "a1", "a2", "a3", "a4", "a5", "a6", "a7", "a8", "a9", "a10", "a11", "a12", "a13", "a14", "a15", "a16", "a17", "a18", "a19", "a20", "a21", "a22", "a23", "a24", "a25", "a26", "a27", "a28", "a29", "a30", "a31", "a32", "a33", "a34", "a35", "a36", "a37", "a38", "a39", "a40", "a41", "a42", "a43", "a44", "a45", "a46", "a47", "a48", "a49", "a50", "a51", "a52", "a53", "a54", "a55", "a56", "a57", "a58", "a59", "a60", "a61", "a62", "a63", "a64", "a65", "a66", "a67", "a68", "a69", "a70", "a71", "a72", "a73", "a74", "a75", "a76", "a77", "a78", "a79", "a80", "a81", "a82", "a83", "a84", "a85", "a86", "a87", "a88", "a89", "a90", "a91", "a92", "a93", "a94", "a95", "a96", "a97", "a98", "a99", "a100", "a101", "a102", "a103", "a104", "a105", "a106", "a107", "a108", "a109", "a110", "a111", "a112", "a113", "a114", "a115", "a116", "a117", "a118", "a119", "a120", "a121", "a122", "a123", "a124", "a125", "a126", "a127", "a128", "a129", "a130", "a131", "a132", "a133", "a134", "a135", "a136", "a137", "a138", "a139", "a140", "a141", "a142", "a143", "a144", "a145", "a146", "a147", "a148", "a149", "a150", "a151", "a152", "a153", "a154", "a155", "a156", "a157", "a158", "a159", "a160", "a161", "a162", "a163", "a164", "a165", "a166", "a167", "a168", "a169", "a170", "a171", "a172", "a173", "a174", "a175", "a176", "a177", "a178", "a179", "a180", "a181", "a182", "a183", "a184", "a185", "a186", "a187", "a188", "a189", "a190", "a191", "a192", "a193", "a194", "a195", "a196", "a197", "a198", "a199", "a200", "a201", "a202", "a203", "a204", "a205", "a206", "a207", "a208", "a209", "a210", "a211", "a212", "a213", "a214", "a215", "a216", "a217", "a218", "a219", "a220", "a221", "a222", "a223", "a224", "a225", "a226", "a227", "a228", "a229", "a230", "a231", "a232", "a233", "a234", "a235", "a236", "a237", "a238", "a239", "a240", "a241", "a242", "a243", "a244", "a245", "a246", "a247", "a248", "a249", "a250", "a251", "a252", "a253", "a254", "a255");
    __builtin_unreachable();
}

DI void role_h(const Params& p, rsrc_t rs, int h, int wave) {
    const int lane = lane_id();
    bool dead = false;
    const u32 ask = hello_ask(rs, wave, lane);
    warm_helper(p, wave);
    hello_wait(rs, wave, lane, ask, dead);
    const u32 par1 = get_par(rs, lane, dead, 1);
    u32x32 ad = {};
    ad[8] = lane * 16;
    if (0) {
        const u32 g = lane >> 4, col = lane & 15;
        ad[8] = (8 * wave + (g >> 1)) * 1024 + (g & 1) * 256 + col * 16;
        ad[9] = (8 * wave + (g >> 1)) * 2560 + (g & 1) * 512 + col * 16;
        ad[11] = h * 4096 + wave * 1024 + lane * 16;
        ad[20] = wave * 4096 + lane * 16;
        ad[21] = wave * 1024 + lane * 16;
    }
    ad[16] = (lane & 31) * 16;
    ad[23] = 3072 + ((lane & 31) >> WSH) * 128 + (lane & ((1 << WSH) - 1)) * 4 + par1;
    wait_flags(rs, F_I2, 40, lane, dead);
    asm volatile(
        "s_mov_b32 s60, %[r0]\n"
        "s_mov_b32 s61, %[r1]\n"
        "s_mov_b32 s62, 0x0f000000\n"
        "s_mov_b32 s63, 0x00020000\n"
        "s_mov_b32 s67, %[wave]\n"
        "s_mov_b32 s58, %[h]\n"
        "s_mov_b32 s59, %[ts]\n"
        "s_mov_b32 s66, 0x100000\n"
        "s_mov_b32 s64, 0\n"
        "s_lshl_b32 s71, s67, 10\n"
        "s_lshl_b32 s70, s58, 16\n"
        "s_lshl_b32 s65, s67, 14\n"
        "s_add_u32 s70, s70, s65\n"
        "s_add_u32 s70, s70, 58195968\n"
        "s_mov_b32 s78, 4228096\n"
        "s_mov_b32 s55, 2621440\n"
        "s_mov_b32 s39, %[t7]\n"
        "s_mov_b32 s38, %[t8]\n"
        "s_lshl_b32 s71, s58, 2\n"
        "s_add_u32 s71, s71, s67\n"
        "s_mul_i32 s71, s71, 2048\n"
        "s_cmp_ge_u32 s71, 81920\n"
        "s_cselect_b32 s65, 81920, 0\n"
        "s_sub_u32 s71, s71, s65\n"
        "s_add_u32 s71, s71, 38715392\n"
        "s_memtime s[50:51]\n"
        "s_memrealtime s[40:41]\n"
        "s_waitcnt lgkmcnt(0)\n"
        "s_mul_i32 s40, s40, 24\n"
        "s_sub_u32 s40, s40, s50\n"
        "s_mov_b32 s42, s40\n"
        "s_add_u32 s44, s50, s42\n"
        "s_sub_u32 s44, s44, 0x1000000\n"
        "s_mov_b32 s36, s39\n"
        "s_mov_b32 s32, 5100\n"
        "s_mov_b32 s33, s44\n"
        "RH_loop:\n"
        "s_add_u32 s48, s44, s39\n"
        "s_sub_u32 s42, s42, 16\n"
        "RH_twc:\n"
        "s_sleep 1\n"
        "s_memtime s[50:51]\n"
        "s_memrealtime s[40:41]\n"
        "s_waitcnt lgkmcnt(0)\n"
        "s_mul_i32 s40, s40, 24\n"
        "s_sub_u32 s40, s40, s50\n"
        "s_sub_u32 s41, s40, s42\n"
        "s_cmp_gt_i32 s41, 0\n"
        "s_cselect_b32 s42, s40, s42\n"
        "s_add_u32 s50, s50, s42\n"
        "s_sub_u32 s50, s48, s50\n"
        "s_cmp_gt_i32 s50, 140\n"
        "s_cbranch_scc1 RH_twc\n"
        "s_sub_u32 s48, s48, s42\n"
        "RH_tw:\n"
        "s_memtime s[50:51]\n"
        "s_waitcnt lgkmcnt(0)\n"
        "s_sub_u32 s50, s50, s48\n"
        "s_cmp_lt_i32 s50, 0\n"
        "s_cbranch_scc1 RH_tw\n"
        "RH_s0:\n"
        "buffer_load_dword v176, v23, s[60:63], s55 offen sc1\n"
        "s_waitcnt vmcnt(0)\n"
        "v_cmp_eq_u32 vcc, -1, v176\n"
        "s_cbranch_vccz RH_s1\n"
        "s_sleep 4\n"
        "s_cmp_eq_u32 s66, 0\n"
        "s_cbranch_scc1 RH_s1\n"
        "s_sub_u32 s66, s66, 1\n"
        "s_branch RH_s0\n"
        "RH_s1:\n"
        "RH_h0:\n"
        "s_nop 1\n"
        "v_max_u32_dpp v176, v176, v176 row_ror:8 row_mask:0xf bank_mask:0xf\n"
        "s_nop 1\n"
        "v_max_u32_dpp v176, v176, v176 row_ror:4 row_mask:0xf bank_mask:0xf\n"
        "s_nop 1\n"
        "v_max_u32_dpp v176, v176, v176 row_ror:2 row_mask:0xf bank_mask:0xf\n"
        "s_nop 1\n"
        "v_max_u32_dpp v176, v176, v176 row_ror:1 row_mask:0xf bank_mask:0xf\n"
        "s_nop 1\n"
        "v_readlane_b32 s40, v176, 0\n"
        "v_readlane_b32 s45, v176, 16\n"
        "s_nop 1\n"
        "s_max_u32 s40, s40, s45\n"
        "s_cmp_lg_u32 s40, -1\n"
        "s_cselect_b32 s44, s40, s44\n"
        "s_cbranch_scc1 RH_h1\n"
        "s_cmp_eq_u32 s66, 0\n"
        "s_cbranch_scc1 RH_h1\n"
        "s_sub_u32 s66, s66, 1\n"
        "buffer_load_dword v176, v23, s[60:63], s55 offen sc1\n"
        "s_waitcnt vmcnt(0)\n"
        "s_branch RH_h0\n"
        "RH_h1:\n"
        "RH_h2:\n"
        "s_sub_u32 s45, s44, s33\n"
        "s_mov_b32 s33, s44\n"
        "s_add_u32 s32, s32, 6\n"
        "s_min_u32 s32, s32, s45\n"
        "s_sub_u32 s39, s32, s36\n"
        "s_add_u32 s48, s44, s38\n"
        "s_sub_u32 s42, s42, 16\n"
        "RH_tgc:\n"
        "s_sleep 1\n"
        "s_memtime s[50:51]\n"
        "s_memrealtime s[40:41]\n"
        "s_waitcnt lgkmcnt(0)\n"
        "s_mul_i32 s40, s40, 24\n"
        "s_sub_u32 s40, s40, s50\n"
        "s_sub_u32 s41, s40, s42\n"
        "s_cmp_gt_i32 s41, 0\n"
        "s_cselect_b32 s42, s40, s42\n"
        "s_add_u32 s50, s50, s42\n"
        "s_sub_u32 s50, s48, s50\n"
        "s_cmp_gt_i32 s50, 140\n"
        "s_cbranch_scc1 RH_tgc\n"
        "s_sub_u32 s48, s48, s42\n"
        "RH_tg:\n"
        "s_memtime s[50:51]\n"
        "s_waitcnt lgkmcnt(0)\n"
        "s_sub_u32 s50, s50, s48\n"
        "s_cmp_lt_i32 s50, 0\n"
        "s_cbranch_scc1 RH_tg\n"
        "buffer_load_dwordx4 v[28:31], v16, s[60:63], s78 offen sc1\n"
        "buffer_load_dwordx4 v[96:99], v8, s[60:63], s71 offen offset:0 sc1\n"
        "buffer_load_dwordx4 v[100:103], v8, s[60:63], s71 offen offset:1024 sc1\n"
        "s_add_u32 s48, s48, 1300\n"
        "s_waitcnt vmcnt(2)\n"
        "RH_f0:\n"
        "v_max3_u32 v33, v28, v29, v30\n"
        "v_max_u32 v33, v33, v31\n"
        "v_cmp_eq_u32 vcc, -1, v33\n"
        "s_cbranch_vccz RH_f1\n"
        "s_memtime s[50:51]\n"
        "s_waitcnt lgkmcnt(0)\n"
        "s_sub_u32 s50, s50, s48\n"
        "s_cmp_lt_i32 s50, 0\n"
        "s_cbranch_scc0 RH_sk\n"
        "buffer_load_dwordx4 v[28:31], v16, s[60:63], s78 offen sc1\n"
        "s_waitcnt vmcnt(0)\n"
        "s_branch RH_f0\n"
        "RH_f1:\n"
        "s_mov_b32 s76, s70\n"
        "buffer_load_dwordx4 v[32:35], v8, s[60:63], s76 offen offset:0 sc1\n"
        "buffer_load_dwordx4 v[36:39], v8, s[60:63], s76 offen offset:1024 sc1\n"
        "buffer_load_dwordx4 v[40:43], v8, s[60:63], s76 offen offset:2048 sc1\n"
        "buffer_load_dwordx4 v[44:47], v8, s[60:63], s76 offen offset:3072 sc1\n"
        "s_add_u32 s76, s76, 4096\n"
        "buffer_load_dwordx4 v[48:51], v8, s[60:63], s76 offen offset:0 sc1\n"
        "buffer_load_dwordx4 v[52:55], v8, s[60:63], s76 offen offset:1024 sc1\n"
        "buffer_load_dwordx4 v[56:59], v8, s[60:63], s76 offen offset:2048 sc1\n"
        "buffer_load_dwordx4 v[60:63], v8, s[60:63], s76 offen offset:3072 sc1\n"
        "s_add_u32 s76, s76, 4096\n"
        "buffer_load_dwordx4 v[64:67], v8, s[60:63], s76 offen offset:0 sc1\n"
        "buffer_load_dwordx4 v[68:71], v8, s[60:63], s76 offen offset:1024 sc1\n"
        "buffer_load_dwordx4 v[72:75], v8, s[60:63], s76 offen offset:2048 sc1\n"
        "buffer_load_dwordx4 v[76:79], v8, s[60:63], s76 offen offset:3072 sc1\n"
        "s_add_u32 s76, s76, 4096\n"
        "buffer_load_dwordx4 v[80:83], v8, s[60:63], s76 offen offset:0 sc1\n"
        "buffer_load_dwordx4 v[84:87], v8, s[60:63], s76 offen offset:1024 sc1\n"
        "buffer_load_dwordx4 v[88:91], v8, s[60:63], s76 offen offset:2048 sc1\n"
        "buffer_load_dwordx4 v[92:95], v8, s[60:63], s76 offen offset:3072 sc1\n"
        "RH_sk:\n"
        "s_add_u32 s70, s70, 1310720\n"
        "s_add_u32 s78, s78, 512\n"
        "s_add_u32 s55, s55, 16384\n"
        "s_add_u32 s71, s71, 81920\n"
        "s_add_u32 s64, s64, 1\n"
        "s_cmp_lt_u32 s64, 62\n"
        "s_cbranch_scc1 RH_loop\n"
        "s_waitcnt vmcnt(0)\n"
        "s_endpgm\n"
        ROLE_OPS(p.out, p.out) : "v32", "v33", "v34", "v35", "v36", "v37", "v38", "v39", "v40", "v41", "v42", "v43", "v44", "v45", "v46", "v47", "v48", "v49", "v50", "v51", "v52", "v53", "v54", "v55", "v56", "v57", "v58", "v59", "v60", "v61", "v62", "v63", "v64", "v65", "v66", "v67", "v68", "v69", "v70", "v71", "v72", "v73", "v74", "v75", "v76", "v77", "v78", "v79", "v80", "v81", "v82", "v83", "v84", "v85", "v86", "v87", "v88", "v89", "v90", "v91", "v92", "v93", "v94", "v95", "v96", "v97", "v98", "v99", "v100", "v101", "v102", "v103", "v104", "v105", "v106", "v107", "v108", "v109", "v110", "v111", "v112", "v113", "v114", "v115", "v116", "v117", "v118", "v119", "v120", "v121", "v122", "v123", "v124", "v125", "v126", "v127", "v128", "v129", "v130", "v131", "v132", "v133", "v134", "v135", "v136", "v137", "v138", "v139", "v140", "v141", "v142", "v143", "v144", "v145", "v146", "v147", "v148", "v149", "v150", "v151", "v152", "v153", "v154", "v155", "v156", "v157", "v158", "v159", "v160", "v161", "v162", "v163", "v164", "v165", "v166", "v167", "v168", "v169", "v170", "v171", "v172", "v173", "v174", "v175", "v176", "v177", "v178", "v179", "v180", "v181", "v182", "v183", "v184", "v185", "v186", "v187", "v188", "v189", "v190", "v192", "v193", "v194", "v195", "v196", "v197", "v198", "v199", "v200", "v201", "v202", "v203", "v204", "v205", "v206", "v207", "v208", "v209", "v210", "v211", "v212", "v213", "v214", "v215", "v216", "v217", "v218", "v219", "v220", "v221", "v222", "v223", "v224", "v225", "v226", "v227", "v228", "v229", "v230", "v231", "v232", "v233", "v234", "v235", "v236", "v237", "v238", "v239", "v240", "v241", "v242", "v243", "v244", "v245", "v246", "v247", "v248", "v249", "v250", "v251", "v252", "v253", "v254", "v255", "s38", "s39", "s40", "s41", "s42", "s43", "s44", "s45", "s46", "s47", "s48", "s49", "s50", "s51", "s52", "s53", "s54", "s55", "s56", "s57", "s58", "s59", "s60", "s61", "s62", "s63", "s64", "s65", "s66", "s67", "s68", "s69", "s70", "s71", "s72", "s73", "s74", "s75", "s76", "s77", "s78", "s79", "s80", "s81", "s82", "s83", "s84", "s85", "s86", "s87", "s88", "s89", "s90", "s91", "s92", "s93", "s94", "s95", "s96", "s97", "s98", "s99", "vcc", "scc", "memory", "m0", "a0", "a1", "a2", "a3", "a4", "a5", "a6", "a7", "a8", "a9", "a10", "a11", "a12", "a13", "a14", "a15", "a16", "a17", "a18", "a19", "a20", "a21", "a22", "a23", "a24", "a25", "a26", "a27", "a28", "a29", "a30", "a31", "a32", "a33", "a34", "a35", "a36", "a37", "a38", "a39", "a40", "a41", "a42", "a43", "a44", "a45", "a46", "a47", "a48", "a49", "a50", "a51", "a52", "a53", "a54", "a55", "a56", "a57", "a58", "a59", "a60", "a61", "a62", "a63", "a64", "a65", "a66", "a67", "a68", "a69", "a70", "a71", "a72", "a73", "a74", "a75", "a76", "a77", "a78", "a79", "a80", "a81", "a82", "a83", "a84", "a85", "a86", "a87", "a88", "a89", "a90", "a91", "a92", "a93", "a94", "a95", "a96", "a97", "a98", "a99", "a100", "a101", "a102", "a103", "a104", "a105", "a106", "a107", "a108", "a109", "a110", "a111", "a112", "a113", "a114", "a115", "a116", "a117", "a118", "a119", "a120", "a121", "a122", "a123", "a124", "a125", "a126", "a127", "a128", "a129", "a130", "a131", "a132", "a133", "a134", "a135", "a136", "a137", "a138", "a139", "a140", "a141", "a142", "a143", "a144", "a145", "a146", "a147", "a148", "a149", "a150", "a151", "a152", "a153", "a154", "a155", "a156", "a157", "a158", "a159", "a160", "a161", "a162", "a163", "a164", "a165", "a166", "a167", "a168", "a169", "a170", "a171", "a172", "a173", "a174", "a175", "a176", "a177", "a178", "a179", "a180", "a181", "a182", "a183", "a184", "a185", "a186", "a187", "a188", "a189", "a190", "a191", "a192", "a193", "a194", "a195", "a196", "a197", "a198", "a199", "a200", "a201", "a202", "a203", "a204", "a205", "a206", "a207", "a208", "a209", "a210", "a211", "a212", "a213", "a214", "a215", "a216", "a217", "a218", "a219", "a220", "a221", "a222", "a223", "a224", "a225", "a226", "a227", "a228", "a229", "a230", "a231", "a232", "a233", "a234", "a235", "a236", "a237", "a238", "a239", "a240", "a241", "a242", "a243", "a244", "a245", "a246", "a247", "a248", "a249", "a250", "a251", "a252", "a253", "a254", "a255");
    __builtin_unreachable();
}

DI void xaddr(u32x32& ad, int tid, int lane) {
    const u32 rd = (lane & 31) * 1280 + (((lane >> 5) ^ (lane & 15)) << 4);
#pragma unroll
    for (int u = 0; u < 8; ++u) ad[u] = rd ^ (32 * u);
#pragma unroll
    for (int i = 0; i < 10; ++i) { u32 n = i * 256 + tid, t = n / 80, ch = n % 80; ad[8 + i] = t * 1280 + ((ch & ~15u) | ((ch ^ t) & 15u)) * 16; }
    ad[21] = lane == 0 ? 0u : 0xF0000000u;
    ad[22] = 139264 + (lane & 31) * 256 + (((lane >> 5) ^ (lane & 15)) << 4);
    ad[30] = (tid >> 4) * 1280 + (((tid ^ (tid >> 4)) & 15) << 4);
}
DI void dense_stage_addr(u32x32& ad, int tid, int wave, int lane) {
    const u32 r = tid >> 4, ch = ((tid & 15) ^ (4 * (r & 3))) << 4;
    ad[24] = r * 8192 + ch; ad[25] = r * 1280 + ch;
    const u32 rho = (lane >> 2) & 3, c4 = lane & 3;
    const u32 cx = (((4 * wave + 2 * ((lane >> 4) & 1) + (c4 >> 1)) ^ (4 * rho)) << 4) + (c4 & 1) * 8;
    ad[26] = (8 * (lane >> 5) + rho) * 256 + cx; ad[27] = (4 * (lane >> 5) + rho) * 256 + cx;
}

DI void role_wqk(const Params& p, rsrc_t rs, int h, int wave) {
    const int lane = lane_id(), tid = wave * 64 + lane;
    u32x32 ad = {}; xaddr(ad, tid, lane); dense_stage_addr(ad, tid, wave, lane);
    ad[18] = h * 8192 + wave * 2048 + lane * 16;
    asm volatile(
        "s_mov_b32 s60, %[r0]\n"
        "s_mov_b32 s61, %[r1]\n"
        "s_mov_b32 s62, 0x0f000000\n"
        "s_mov_b32 s63, 0x00020000\n"
        "s_mov_b32 s67, %[wave]\n"
        "s_mov_b32 s58, %[h]\n"
        "s_mov_b32 s59, %[ts]\n"
        "s_mov_b32 s66, 0x100000\n"
        "s_mov_b32 s64, 0\n"
        "s_lshl_b32 s71, s67, 10\n"
        "s_mov_b32 s72, %[x0]\n"
        "s_mov_b32 s73, %[x1]\n"
        "s_mov_b32 s74, 2621440\n"
        "s_mov_b32 s75, 0x00020000\n"
        "s_mov_b32 s70, 0\n"
        "s_mov_b32 s68, 4026531840\n"
        "s_mov_b32 s69, 4026531840\n"
        "s_mov_b32 s76, 4026531840\n"
        "s_mov_b32 s70, 81920\n"
        "v_mov_b32 v184, v22\n"
        "v_mov_b32 v185, v30\n"
        "s_mov_b32 s80, %[a0]\n"
        "s_mov_b32 s81, %[a1]\n"
        "s_mov_b32 s82, 5242880\n"
        "s_mov_b32 s83, 0x00020000\n"
        "s_mov_b32 s84, %[b0]\n"
        "s_mov_b32 s85, %[b1]\n"
        "s_mov_b32 s86, s82\n"
        "s_mov_b32 s87, 0x00020000\n"
        "s_lshl_b32 s78, s58, 8\n"
        "s_mul_i32 s79, s58, 163840\n"
        "s_add_u32 s88, %[hs], 4333568\n"
        "s_add_u32 s89, %[hs], 4325376\n"
        "s_add_u32 s90, %[hs], 4305920\n"
        "buffer_load_dword v188, off, s[60:63], s88 sc1\n"
        "s_add_u32 m0, s71, 40960\n"
        "s_add_u32 s65, s78, 0\n"
        "buffer_load_dwordx4 v24, s[80:83], s65 offen lds\n"
        "s_add_u32 m0, s71, 45056\n"
        "s_add_u32 s65, s78, 131072\n"
        "buffer_load_dwordx4 v24, s[80:83], s65 offen lds\n"
        "s_add_u32 m0, s71, 49152\n"
        "s_add_u32 s65, s78, 262144\n"
        "buffer_load_dwordx4 v24, s[80:83], s65 offen lds\n"
        "s_add_u32 m0, s71, 53248\n"
        "s_add_u32 s65, s78, 393216\n"
        "buffer_load_dwordx4 v24, s[80:83], s65 offen lds\n"
        "s_add_u32 m0, s71, 57344\n"
        "s_add_u32 s65, s78, 0\n"
        "buffer_load_dwordx4 v24, s[84:87], s65 offen lds\n"
        "s_add_u32 m0, s71, 61440\n"
        "s_add_u32 s65, s78, 131072\n"
        "buffer_load_dwordx4 v24, s[84:87], s65 offen lds\n"
        "s_add_u32 m0, s71, 65536\n"
        "s_add_u32 s65, s78, 262144\n"
        "buffer_load_dwordx4 v24, s[84:87], s65 offen lds\n"
        "s_add_u32 m0, s71, 69632\n"
        "s_add_u32 s65, s78, 393216\n"
        "buffer_load_dwordx4 v24, s[84:87], s65 offen lds\n"
        "s_add_u32 m0, s71, 73728\n"
        "s_add_u32 s65, s78, 524288\n"
        "buffer_load_dwordx4 v24, s[80:83], s65 offen lds\n"
        "s_add_u32 m0, s71, 77824\n"
        "s_add_u32 s65, s78, 655360\n"
        "buffer_load_dwordx4 v24, s[80:83], s65 offen lds\n"
        "s_add_u32 m0, s71, 81920\n"
        "s_add_u32 s65, s78, 786432\n"
        "buffer_load_dwordx4 v24, s[80:83], s65 offen lds\n"
        "s_add_u32 m0, s71, 86016\n"
        "s_add_u32 s65, s78, 917504\n"
        "buffer_load_dwordx4 v24, s[80:83], s65 offen lds\n"
        "s_add_u32 m0, s71, 90112\n"
        "s_add_u32 s65, s78, 524288\n"
        "buffer_load_dwordx4 v24, s[84:87], s65 offen lds\n"
        "s_add_u32 m0, s71, 94208\n"
        "s_add_u32 s65, s78, 655360\n"
        "buffer_load_dwordx4 v24, s[84:87], s65 offen lds\n"
        "s_add_u32 m0, s71, 98304\n"
        "s_add_u32 s65, s78, 786432\n"
        "buffer_load_dwordx4 v24, s[84:87], s65 offen lds\n"
        "s_add_u32 m0, s71, 102400\n"
        "s_add_u32 s65, s78, 917504\n"
        "buffer_load_dwordx4 v24, s[84:87], s65 offen lds\n"
        "s_add_u32 m0, s71, 0\n"
        "s_mov_b32 s65, 0\n"
        "buffer_load_dwordx4 v8, s[72:75], s65 offen lds\n"
        "s_add_u32 m0, s71, 4096\n"
        "s_mov_b32 s65, 0\n"
        "buffer_load_dwordx4 v9, s[72:75], s65 offen lds\n"
        "s_add_u32 m0, s71, 8192\n"
        "s_mov_b32 s65, 0\n"
        "buffer_load_dwordx4 v10, s[72:75], s65 offen lds\n"
        "s_add_u32 m0, s71, 12288\n"
        "s_mov_b32 s65, 0\n"
        "buffer_load_dwordx4 v11, s[72:75], s65 offen lds\n"
        "s_add_u32 m0, s71, 16384\n"
        "s_mov_b32 s65, 0\n"
        "buffer_load_dwordx4 v12, s[72:75], s65 offen lds\n"
        "s_add_u32 m0, s71, 20480\n"
        "s_mov_b32 s65, 0\n"
        "buffer_load_dwordx4 v13, s[72:75], s65 offen lds\n"
        "s_add_u32 m0, s71, 24576\n"
        "s_mov_b32 s65, 0\n"
        "buffer_load_dwordx4 v14, s[72:75], s65 offen lds\n"
        "s_add_u32 m0, s71, 28672\n"
        "s_mov_b32 s65, 0\n"
        "buffer_load_dwordx4 v15, s[72:75], s65 offen lds\n"
        "s_add_u32 m0, s71, 32768\n"
        "s_mov_b32 s65, 0\n"
        "buffer_load_dwordx4 v16, s[72:75], s65 offen lds\n"
        "s_add_u32 m0, s71, 36864\n"
        "s_mov_b32 s65, 0\n"
        "buffer_load_dwordx4 v17, s[72:75], s65 offen lds\n"
        "s_add_u32 m0, s71, 139264\n"
        "s_mov_b32 s65, 40960\n"
        "buffer_load_dwordx4 v185, s[72:75], s65 offen lds\n"
        "s_add_u32 m0, s71, 143360\n"
        "s_mov_b32 s65, 61440\n"
        "buffer_load_dwordx4 v185, s[72:75], s65 offen lds\n"
        "s_add_u32 m0, s71, 106496\n"
        "s_add_u32 s65, s78, 1048576\n"
        "buffer_load_dwordx4 v24, s[80:83], s65 offen lds\n"
        "s_add_u32 m0, s71, 110592\n"
        "s_add_u32 s65, s78, 1179648\n"
        "buffer_load_dwordx4 v24, s[80:83], s65 offen lds\n"
        "s_add_u32 m0, s71, 114688\n"
        "s_add_u32 s65, s78, 1310720\n"
        "buffer_load_dwordx4 v24, s[80:83], s65 offen lds\n"
        "s_add_u32 m0, s71, 118784\n"
        "s_add_u32 s65, s78, 1441792\n"
        "buffer_load_dwordx4 v24, s[80:83], s65 offen lds\n"
        "s_add_u32 m0, s71, 122880\n"
        "s_add_u32 s65, s78, 1048576\n"
        "buffer_load_dwordx4 v24, s[84:87], s65 offen lds\n"
        "s_add_u32 m0, s71, 126976\n"
        "s_add_u32 s65, s78, 1179648\n"
        "buffer_load_dwordx4 v24, s[84:87], s65 offen lds\n"
        "s_add_u32 m0, s71, 131072\n"
        "s_add_u32 s65, s78, 1310720\n"
        "buffer_load_dwordx4 v24, s[84:87], s65 offen lds\n"
        "s_add_u32 m0, s71, 135168\n"
        "s_add_u32 s65, s78, 1441792\n"
        "buffer_load_dwordx4 v24, s[84:87], s65 offen lds\n"
        "s_add_u32 m0, s71, 147456\n"
        "s_mov_b32 s65, 41216\n"
        "buffer_load_dwordx4 v185, s[72:75], s65 offen lds\n"
        "s_add_u32 m0, s71, 151552\n"
        "s_mov_b32 s65, 61696\n"
        "buffer_load_dwordx4 v185, s[72:75], s65 offen lds\n"
        "v_add_u32 v28, 40960, v26\n"
        "v_add_u32 v29, 40960, v27\n"
        "s_waitcnt vmcnt(10)\n"
        "s_barrier\n"
        "ds_read_b64_tr_b16 a[0:1], v28 offset:0\n"
        "ds_read_b64_tr_b16 a[2:3], v28 offset:1024\n"
        "ds_read_b64_tr_b16 a[4:5], v28 offset:4096\n"
        "ds_read_b64_tr_b16 a[6:7], v28 offset:5120\n"
        "ds_read_b64_tr_b16 a[8:9], v28 offset:8192\n"
        "ds_read_b64_tr_b16 a[10:11], v28 offset:9216\n"
        "ds_read_b64_tr_b16 a[12:13], v28 offset:12288\n"
        "ds_read_b64_tr_b16 a[14:15], v28 offset:13312\n"
        "ds_read_b64_tr_b16 a[16:17], v28 offset:16384\n"
        "ds_read_b64_tr_b16 a[18:19], v28 offset:17408\n"
        "ds_read_b64_tr_b16 a[20:21], v28 offset:20480\n"
        "ds_read_b64_tr_b16 a[22:23], v28 offset:21504\n"
        "ds_read_b64_tr_b16 a[24:25], v28 offset:24576\n"
        "ds_read_b64_tr_b16 a[26:27], v28 offset:25600\n"
        "ds_read_b64_tr_b16 a[28:29], v28 offset:28672\n"
        "ds_read_b64_tr_b16 a[30:31], v28 offset:29696\n"
        "s_waitcnt lgkmcnt(0)\n"
        "s_barrier\n"
        "s_add_u32 m0, s71, 40960\n"
        "s_add_u32 s65, s78, 1572864\n"
        "buffer_load_dwordx4 v24, s[80:83], s65 offen lds\n"
        "v_add_u32 v188, 1, v188\n"
        "s_mov_b64 exec, 1\n"
        "buffer_store_dword v188, off, s[60:63], s89 sc1\n"
        "s_mov_b64 exec, -1\n"
        "ds_read_b128 v[32:35], v0 offset:0\n"
        "ds_read_b128 v[36:39], v1 offset:0\n"
        "ds_read_b128 v[40:43], v2 offset:0\n"
        "ds_read_b128 v[44:47], v3 offset:0\n"
        "v_xor_b32 v186, 0, v184\n"
        "ds_read_b128 v[144:147], v186 offset:0\n"
        "v_xor_b32 v186, 32, v184\n"
        "ds_read_b128 v[148:151], v186 offset:0\n"
        "v_xor_b32 v186, 64, v184\n"
        "ds_read_b128 v[152:155], v186 offset:0\n"
        "v_xor_b32 v186, 96, v184\n"
        "ds_read_b128 v[156:159], v186 offset:0\n"
        "s_add_u32 m0, s71, 45056\n"
        "s_add_u32 s65, s78, 1703936\n"
        "buffer_load_dwordx4 v24, s[80:83], s65 offen lds\n"
        "s_add_u32 m0, s71, 49152\n"
        "s_add_u32 s65, s78, 1835008\n"
        "buffer_load_dwordx4 v24, s[80:83], s65 offen lds\n"
        "s_waitcnt lgkmcnt(7)\n"
        "v_mfma_f32_32x32x16_bf16 v[64:79], a[0:3], v[32:35], 0\n"
        "s_add_u32 m0, s71, 53248\n"
        "s_add_u32 s65, s78, 1966080\n"
        "buffer_load_dwordx4 v24, s[80:83], s65 offen lds\n"
        "v_mfma_f32_32x32x16_bf16 v[80:95], a[16:19], v[32:35], 0\n"
        "s_waitcnt lgkmcnt(6)\n"
        "v_mfma_f32_32x32x16_bf16 v[64:79], a[4:7], v[36:39], v[64:79]\n"
        "v_mfma_f32_32x32x16_bf16 v[80:95], a[20:23], v[36:39], v[80:95]\n"
        "s_add_u32 m0, s71, 57344\n"
        "s_add_u32 s65, s78, 1572864\n"
        "buffer_load_dwordx4 v24, s[84:87], s65 offen lds\n"
        "s_waitcnt lgkmcnt(5)\n"
        "v_mfma_f32_32x32x16_bf16 v[64:79], a[8:11], v[40:43], v[64:79]\n"
        "v_mfma_f32_32x32x16_bf16 v[80:95], a[24:27], v[40:43], v[80:95]\n"
        "s_waitcnt lgkmcnt(4)\n"
        "v_mfma_f32_32x32x16_bf16 v[64:79], a[12:15], v[44:47], v[64:79]\n"
        "s_add_u32 m0, s71, 61440\n"
        "s_add_u32 s65, s78, 1703936\n"
        "buffer_load_dwordx4 v24, s[84:87], s65 offen lds\n"
        "v_mfma_f32_32x32x16_bf16 v[80:95], a[28:31], v[44:47], v[80:95]\n"
        "s_waitcnt lgkmcnt(3)\n"
        "v_mfma_f32_32x32x16_bf16 v[96:111], a[0:3], v[144:147], 0\n"
        "v_mfma_f32_32x32x16_bf16 v[112:127], a[16:19], v[144:147], 0\n"
        "s_add_u32 m0, s71, 65536\n"
        "s_add_u32 s65, s78, 1835008\n"
        "buffer_load_dwordx4 v24, s[84:87], s65 offen lds\n"
        "s_waitcnt lgkmcnt(2)\n"
        "v_mfma_f32_32x32x16_bf16 v[96:111], a[4:7], v[148:151], v[96:111]\n"
        "v_mfma_f32_32x32x16_bf16 v[112:127], a[20:23], v[148:151], v[112:127]\n"
        "s_waitcnt lgkmcnt(1)\n"
        "v_mfma_f32_32x32x16_bf16 v[96:111], a[8:11], v[152:155], v[96:111]\n"
        "s_add_u32 m0, s71, 69632\n"
        "s_add_u32 s65, s78, 1966080\n"
        "buffer_load_dwordx4 v24, s[84:87], s65 offen lds\n"
        "v_mfma_f32_32x32x16_bf16 v[112:127], a[24:27], v[152:155], v[112:127]\n"
        "s_waitcnt lgkmcnt(0)\n"
        "v_mfma_f32_32x32x16_bf16 v[96:111], a[12:15], v[156:159], v[96:111]\n"
        "v_mfma_f32_32x32x16_bf16 v[112:127], a[28:31], v[156:159], v[112:127]\n"
        "v_add_u32 v28, 73728, v26\n"
        "v_add_u32 v29, 73728, v27\n"
        "s_waitcnt vmcnt(31)\n"
        "s_barrier\n"
        "ds_read_b64_tr_b16 a[32:33], v28 offset:0\n"
        "ds_read_b64_tr_b16 a[34:35], v28 offset:1024\n"
        "ds_read_b64_tr_b16 a[36:37], v28 offset:4096\n"
        "ds_read_b64_tr_b16 a[38:39], v28 offset:5120\n"
        "ds_read_b64_tr_b16 a[40:41], v28 offset:8192\n"
        "ds_read_b64_tr_b16 a[42:43], v28 offset:9216\n"
        "ds_read_b64_tr_b16 a[44:45], v28 offset:12288\n"
        "ds_read_b64_tr_b16 a[46:47], v28 offset:13312\n"
        "ds_read_b64_tr_b16 a[48:49], v28 offset:16384\n"
        "ds_read_b64_tr_b16 a[50:51], v28 offset:17408\n"
        "ds_read_b64_tr_b16 a[52:53], v28 offset:20480\n"
        "ds_read_b64_tr_b16 a[54:55], v28 offset:21504\n"
        "ds_read_b64_tr_b16 a[56:57], v28 offset:24576\n"
        "ds_read_b64_tr_b16 a[58:59], v28 offset:25600\n"
        "ds_read_b64_tr_b16 a[60:61], v28 offset:28672\n"
        "ds_read_b64_tr_b16 a[62:63], v28 offset:29696\n"
        "s_waitcnt lgkmcnt(0)\n"
        "s_barrier\n"
        "s_add_u32 m0, s71, 73728\n"
        "s_add_u32 s65, s78, 2097152\n"
        "buffer_load_dwordx4 v24, s[80:83], s65 offen lds\n"
        "ds_read_b128 v[48:51], v4 offset:0\n"
        "ds_read_b128 v[52:55], v5 offset:0\n"
        "ds_read_b128 v[56:59], v6 offset:0\n"
        "ds_read_b128 v[60:63], v7 offset:0\n"
        "v_xor_b32 v186, 128, v184\n"
        "ds_read_b128 v[144:147], v186 offset:0\n"
        "v_xor_b32 v186, 160, v184\n"
        "ds_read_b128 v[148:151], v186 offset:0\n"
        "v_xor_b32 v186, 192, v184\n"
        "ds_read_b128 v[152:155], v186 offset:0\n"
        "v_xor_b32 v186, 224, v184\n"
        "ds_read_b128 v[156:159], v186 offset:0\n"
        "s_add_u32 m0, s71, 77824\n"
        "s_add_u32 s65, s78, 2228224\n"
        "buffer_load_dwordx4 v24, s[80:83], s65 offen lds\n"
        "s_add_u32 m0, s71, 81920\n"
        "s_add_u32 s65, s78, 2359296\n"
        "buffer_load_dwordx4 v24, s[80:83], s65 offen lds\n"
        "s_waitcnt lgkmcnt(7)\n"
        "v_mfma_f32_32x32x16_bf16 v[64:79], a[32:35], v[48:51], v[64:79]\n"
        "s_add_u32 m0, s71, 86016\n"
        "s_add_u32 s65, s78, 2490368\n"
        "buffer_load_dwordx4 v24, s[80:83], s65 offen lds\n"
        "v_mfma_f32_32x32x16_bf16 v[80:95], a[48:51], v[48:51], v[80:95]\n"
        "s_waitcnt lgkmcnt(6)\n"
        "v_mfma_f32_32x32x16_bf16 v[64:79], a[36:39], v[52:55], v[64:79]\n"
        "s_add_u32 m0, s71, 90112\n"
        "s_add_u32 s65, s78, 2097152\n"
        "buffer_load_dwordx4 v24, s[84:87], s65 offen lds\n"
        "v_mfma_f32_32x32x16_bf16 v[80:95], a[52:55], v[52:55], v[80:95]\n"
        "s_waitcnt lgkmcnt(5)\n"
        "v_mfma_f32_32x32x16_bf16 v[64:79], a[40:43], v[56:59], v[64:79]\n"
        "s_add_u32 m0, s71, 94208\n"
        "s_add_u32 s65, s78, 2228224\n"
        "buffer_load_dwordx4 v24, s[84:87], s65 offen lds\n"
        "v_mfma_f32_32x32x16_bf16 v[80:95], a[56:59], v[56:59], v[80:95]\n"
        "s_waitcnt lgkmcnt(4)\n"
        "v_mfma_f32_32x32x16_bf16 v[64:79], a[44:47], v[60:63], v[64:79]\n"
        "s_add_u32 m0, s71, 98304\n"
        "s_add_u32 s65, s78, 2359296\n"
        "buffer_load_dwordx4 v24, s[84:87], s65 offen lds\n"
        "v_mfma_f32_32x32x16_bf16 v[80:95], a[60:63], v[60:63], v[80:95]\n"
        "s_waitcnt lgkmcnt(3)\n"
        "v_mfma_f32_32x32x16_bf16 v[96:111], a[32:35], v[144:147], v[96:111]\n"
        "s_add_u32 m0, s71, 102400\n"
        "s_add_u32 s65, s78, 2490368\n"
        "buffer_load_dwordx4 v24, s[84:87], s65 offen lds\n"
        "v_mfma_f32_32x32x16_bf16 v[112:127], a[48:51], v[144:147], v[112:127]\n"
        "s_waitcnt lgkmcnt(2)\n"
        "v_mfma_f32_32x32x16_bf16 v[96:111], a[36:39], v[148:151], v[96:111]\n"
        "s_add_u32 m0, s71, 155648\n"
        "s_mov_b32 s65, 41472\n"
        "buffer_load_dwordx4 v185, s[72:75], s65 offen lds\n"
        "v_mfma_f32_32x32x16_bf16 v[112:127], a[52:55], v[148:151], v[112:127]\n"
        "s_waitcnt lgkmcnt(1)\n"
        "v_mfma_f32_32x32x16_bf16 v[96:111], a[40:43], v[152:155], v[96:111]\n"
        "s_add_u32 m0, s71, 159744\n"
        "s_mov_b32 s65, 61952\n"
        "buffer_load_dwordx4 v185, s[72:75], s65 offen lds\n"
        "v_mfma_f32_32x32x16_bf16 v[112:127], a[56:59], v[152:155], v[112:127]\n"
        "s_waitcnt lgkmcnt(0)\n"
        "v_mfma_f32_32x32x16_bf16 v[96:111], a[44:47], v[156:159], v[96:111]\n"
        "v_mfma_f32_32x32x16_bf16 v[112:127], a[60:63], v[156:159], v[112:127]\n"
        "v_add_u32 v28, 106496, v26\n"
        "v_add_u32 v29, 106496, v27\n"
        "s_waitcnt vmcnt(19)\n"
        "s_barrier\n"
        "ds_read_b64_tr_b16 a[64:65], v28 offset:0\n"
        "ds_read_b64_tr_b16 a[66:67], v28 offset:1024\n"
        "ds_read_b64_tr_b16 a[68:69], v28 offset:4096\n"
        "ds_read_b64_tr_b16 a[70:71], v28 offset:5120\n"
        "ds_read_b64_tr_b16 a[72:73], v28 offset:8192\n"
        "ds_read_b64_tr_b16 a[74:75], v28 offset:9216\n"
        "ds_read_b64_tr_b16 a[76:77], v28 offset:12288\n"
        "ds_read_b64_tr_b16 a[78:79], v28 offset:13312\n"
        "ds_read_b64_tr_b16 a[80:81], v28 offset:16384\n"
        "ds_read_b64_tr_b16 a[82:83], v28 offset:17408\n"
        "ds_read_b64_tr_b16 a[84:85], v28 offset:20480\n"
        "ds_read_b64_tr_b16 a[86:87], v28 offset:21504\n"
        "ds_read_b64_tr_b16 a[88:89], v28 offset:24576\n"
        "ds_read_b64_tr_b16 a[90:91], v28 offset:25600\n"
        "ds_read_b64_tr_b16 a[92:93], v28 offset:28672\n"
        "ds_read_b64_tr_b16 a[94:95], v28 offset:29696\n"
        "s_waitcnt lgkmcnt(0)\n"
        "s_barrier\n"
        "s_add_u32 m0, s71, 106496\n"
        "s_add_u32 s65, s78, 2621440\n"
        "buffer_load_dwordx4 v24, s[80:83], s65 offen lds\n"
        "ds_read_b128 v[32:35], v0 offset:256\n"
        "ds_read_b128 v[36:39], v1 offset:256\n"
        "ds_read_b128 v[40:43], v2 offset:256\n"
        "ds_read_b128 v[44:47], v3 offset:256\n"
        "v_xor_b32 v186, 0, v184\n"
        "ds_read_b128 v[144:147], v186 offset:8192\n"
        "v_xor_b32 v186, 32, v184\n"
        "ds_read_b128 v[148:151], v186 offset:8192\n"
        "v_xor_b32 v186, 64, v184\n"
        "ds_read_b128 v[152:155], v186 offset:8192\n"
        "v_xor_b32 v186, 96, v184\n"
        "ds_read_b128 v[156:159], v186 offset:8192\n"
        "s_add_u32 m0, s71, 110592\n"
        "s_add_u32 s65, s78, 2752512\n"
        "buffer_load_dwordx4 v24, s[80:83], s65 offen lds\n"
        "s_add_u32 m0, s71, 114688\n"
        "s_add_u32 s65, s78, 2883584\n"
        "buffer_load_dwordx4 v24, s[80:83], s65 offen lds\n"
        "s_waitcnt lgkmcnt(7)\n"
        "v_mfma_f32_32x32x16_bf16 v[64:79], a[64:67], v[32:35], v[64:79]\n"
        "s_add_u32 m0, s71, 118784\n"
        "s_add_u32 s65, s78, 3014656\n"
        "buffer_load_dwordx4 v24, s[80:83], s65 offen lds\n"
        "v_mfma_f32_32x32x16_bf16 v[80:95], a[80:83], v[32:35], v[80:95]\n"
        "s_waitcnt lgkmcnt(6)\n"
        "v_mfma_f32_32x32x16_bf16 v[64:79], a[68:71], v[36:39], v[64:79]\n"
        "v_mfma_f32_32x32x16_bf16 v[80:95], a[84:87], v[36:39], v[80:95]\n"
        "s_add_u32 m0, s71, 122880\n"
        "s_add_u32 s65, s78, 2621440\n"
        "buffer_load_dwordx4 v24, s[84:87], s65 offen lds\n"
        "s_waitcnt lgkmcnt(5)\n"
        "v_mfma_f32_32x32x16_bf16 v[64:79], a[72:75], v[40:43], v[64:79]\n"
        "v_mfma_f32_32x32x16_bf16 v[80:95], a[88:91], v[40:43], v[80:95]\n"
        "s_waitcnt lgkmcnt(4)\n"
        "v_mfma_f32_32x32x16_bf16 v[64:79], a[76:79], v[44:47], v[64:79]\n"
        "s_add_u32 m0, s71, 126976\n"
        "s_add_u32 s65, s78, 2752512\n"
        "buffer_load_dwordx4 v24, s[84:87], s65 offen lds\n"
        "v_mfma_f32_32x32x16_bf16 v[80:95], a[92:95], v[44:47], v[80:95]\n"
        "s_waitcnt lgkmcnt(3)\n"
        "v_mfma_f32_32x32x16_bf16 v[96:111], a[64:67], v[144:147], v[96:111]\n"
        "v_mfma_f32_32x32x16_bf16 v[112:127], a[80:83], v[144:147], v[112:127]\n"
        "s_add_u32 m0, s71, 131072\n"
        "s_add_u32 s65, s78, 2883584\n"
        "buffer_load_dwordx4 v24, s[84:87], s65 offen lds\n"
        "s_waitcnt lgkmcnt(2)\n"
        "v_mfma_f32_32x32x16_bf16 v[96:111], a[68:71], v[148:151], v[96:111]\n"
        "v_mfma_f32_32x32x16_bf16 v[112:127], a[84:87], v[148:151], v[112:127]\n"
        "s_waitcnt lgkmcnt(1)\n"
        "v_mfma_f32_32x32x16_bf16 v[96:111], a[72:75], v[152:155], v[96:111]\n"
        "s_add_u32 m0, s71, 135168\n"
        "s_add_u32 s65, s78, 3014656\n"
        "buffer_load_dwordx4 v24, s[84:87], s65 offen lds\n"
        "v_mfma_f32_32x32x16_bf16 v[112:127], a[88:91], v[152:155], v[112:127]\n"
        "s_waitcnt lgkmcnt(0)\n"
        "v_mfma_f32_32x32x16_bf16 v[96:111], a[76:79], v[156:159], v[96:111]\n"
        "v_mfma_f32_32x32x16_bf16 v[112:127], a[92:95], v[156:159], v[112:127]\n"
        "v_add_u32 v28, 40960, v26\n"
        "v_add_u32 v29, 40960, v27\n"
        "s_waitcnt vmcnt(18)\n"
        "s_barrier\n"
        "ds_read_b64_tr_b16 a[96:97], v28 offset:0\n"
        "ds_read_b64_tr_b16 a[98:99], v28 offset:1024\n"
        "ds_read_b64_tr_b16 a[100:101], v28 offset:4096\n"
        "ds_read_b64_tr_b16 a[102:103], v28 offset:5120\n"
        "ds_read_b64_tr_b16 a[104:105], v28 offset:8192\n"
        "ds_read_b64_tr_b16 a[106:107], v28 offset:9216\n"
        "ds_read_b64_tr_b16 a[108:109], v28 offset:12288\n"
        "ds_read_b64_tr_b16 a[110:111], v28 offset:13312\n"
        "ds_read_b64_tr_b16 a[112:113], v28 offset:16384\n"
        "ds_read_b64_tr_b16 a[114:115], v28 offset:17408\n"
        "ds_read_b64_tr_b16 a[116:117], v28 offset:20480\n"
        "ds_read_b64_tr_b16 a[118:119], v28 offset:21504\n"
        "ds_read_b64_tr_b16 a[120:121], v28 offset:24576\n"
        "ds_read_b64_tr_b16 a[122:123], v28 offset:25600\n"
        "ds_read_b64_tr_b16 a[124:125], v28 offset:28672\n"
        "ds_read_b64_tr_b16 a[126:127], v28 offset:29696\n"
        "s_waitcnt lgkmcnt(0)\n"
        "s_barrier\n"
        "s_add_u32 m0, s71, 40960\n"
        "s_add_u32 s65, s78, 3145728\n"
        "buffer_load_dwordx4 v24, s[80:83], s65 offen lds\n"
        "ds_read_b128 v[48:51], v4 offset:256\n"
        "ds_read_b128 v[52:55], v5 offset:256\n"
        "ds_read_b128 v[56:59], v6 offset:256\n"
        "ds_read_b128 v[60:63], v7 offset:256\n"
        "v_xor_b32 v186, 128, v184\n"
        "ds_read_b128 v[144:147], v186 offset:8192\n"
        "v_xor_b32 v186, 160, v184\n"
        "ds_read_b128 v[148:151], v186 offset:8192\n"
        "v_xor_b32 v186, 192, v184\n"
        "ds_read_b128 v[152:155], v186 offset:8192\n"
        "v_xor_b32 v186, 224, v184\n"
        "ds_read_b128 v[156:159], v186 offset:8192\n"
        "s_add_u32 m0, s71, 45056\n"
        "s_add_u32 s65, s78, 3276800\n"
        "buffer_load_dwordx4 v24, s[80:83], s65 offen lds\n"
        "s_add_u32 m0, s71, 49152\n"
        "s_add_u32 s65, s78, 3407872\n"
        "buffer_load_dwordx4 v24, s[80:83], s65 offen lds\n"
        "s_waitcnt lgkmcnt(7)\n"
        "v_mfma_f32_32x32x16_bf16 v[64:79], a[96:99], v[48:51], v[64:79]\n"
        "s_add_u32 m0, s71, 53248\n"
        "s_add_u32 s65, s78, 3538944\n"
        "buffer_load_dwordx4 v24, s[80:83], s65 offen lds\n"
        "v_mfma_f32_32x32x16_bf16 v[80:95], a[112:115], v[48:51], v[80:95]\n"
        "s_waitcnt lgkmcnt(6)\n"
        "v_mfma_f32_32x32x16_bf16 v[64:79], a[100:103], v[52:55], v[64:79]\n"
        "s_add_u32 m0, s71, 57344\n"
        "s_add_u32 s65, s78, 3145728\n"
        "buffer_load_dwordx4 v24, s[84:87], s65 offen lds\n"
        "v_mfma_f32_32x32x16_bf16 v[80:95], a[116:119], v[52:55], v[80:95]\n"
        "s_waitcnt lgkmcnt(5)\n"
        "v_mfma_f32_32x32x16_bf16 v[64:79], a[104:107], v[56:59], v[64:79]\n"
        "s_add_u32 m0, s71, 61440\n"
        "s_add_u32 s65, s78, 3276800\n"
        "buffer_load_dwordx4 v24, s[84:87], s65 offen lds\n"
        "v_mfma_f32_32x32x16_bf16 v[80:95], a[120:123], v[56:59], v[80:95]\n"
        "s_waitcnt lgkmcnt(4)\n"
        "v_mfma_f32_32x32x16_bf16 v[64:79], a[108:111], v[60:63], v[64:79]\n"
        "s_add_u32 m0, s71, 65536\n"
        "s_add_u32 s65, s78, 3407872\n"
        "buffer_load_dwordx4 v24, s[84:87], s65 offen lds\n"
        "v_mfma_f32_32x32x16_bf16 v[80:95], a[124:127], v[60:63], v[80:95]\n"
        "s_waitcnt lgkmcnt(3)\n"
        "v_mfma_f32_32x32x16_bf16 v[96:111], a[96:99], v[144:147], v[96:111]\n"
        "s_add_u32 m0, s71, 69632\n"
        "s_add_u32 s65, s78, 3538944\n"
        "buffer_load_dwordx4 v24, s[84:87], s65 offen lds\n"
        "v_mfma_f32_32x32x16_bf16 v[112:127], a[112:115], v[144:147], v[112:127]\n"
        "s_waitcnt lgkmcnt(2)\n"
        "v_mfma_f32_32x32x16_bf16 v[96:111], a[100:103], v[148:151], v[96:111]\n"
        "s_add_u32 m0, s71, 139264\n"
        "s_mov_b32 s65, 41728\n"
        "buffer_load_dwordx4 v185, s[72:75], s65 offen lds\n"
        "v_mfma_f32_32x32x16_bf16 v[112:127], a[116:119], v[148:151], v[112:127]\n"
        "s_waitcnt lgkmcnt(1)\n"
        "v_mfma_f32_32x32x16_bf16 v[96:111], a[104:107], v[152:155], v[96:111]\n"
        "s_add_u32 m0, s71, 143360\n"
        "s_mov_b32 s65, 62208\n"
        "buffer_load_dwordx4 v185, s[72:75], s65 offen lds\n"
        "v_mfma_f32_32x32x16_bf16 v[112:127], a[120:123], v[152:155], v[112:127]\n"
        "s_waitcnt lgkmcnt(0)\n"
        "v_mfma_f32_32x32x16_bf16 v[96:111], a[108:111], v[156:159], v[96:111]\n"
        "v_mfma_f32_32x32x16_bf16 v[112:127], a[124:127], v[156:159], v[112:127]\n"
        "v_add_u32 v28, 73728, v26\n"
        "v_add_u32 v29, 73728, v27\n"
        "s_waitcnt vmcnt(18)\n"
        "s_barrier\n"
        "ds_read_b64_tr_b16 a[128:129], v28 offset:0\n"
        "ds_read_b64_tr_b16 a[130:131], v28 offset:1024\n"
        "ds_read_b64_tr_b16 a[132:133], v28 offset:4096\n"
        "ds_read_b64_tr_b16 a[134:135], v28 offset:5120\n"
        "ds_read_b64_tr_b16 a[136:137], v28 offset:8192\n"
        "ds_read_b64_tr_b16 a[138:139], v28 offset:9216\n"
        "ds_read_b64_tr_b16 a[140:141], v28 offset:12288\n"
        "ds_read_b64_tr_b16 a[142:143], v28 offset:13312\n"
        "ds_read_b64_tr_b16 a[144:145], v28 offset:16384\n"
        "ds_read_b64_tr_b16 a[146:147], v28 offset:17408\n"
        "ds_read_b64_tr_b16 a[148:149], v28 offset:20480\n"
        "ds_read_b64_tr_b16 a[150:151], v28 offset:21504\n"
        "ds_read_b64_tr_b16 a[152:153], v28 offset:24576\n"
        "ds_read_b64_tr_b16 a[154:155], v28 offset:25600\n"
        "ds_read_b64_tr_b16 a[156:157], v28 offset:28672\n"
        "ds_read_b64_tr_b16 a[158:159], v28 offset:29696\n"
        "s_waitcnt lgkmcnt(0)\n"
        "s_barrier\n"
        "s_add_u32 m0, s71, 73728\n"
        "s_add_u32 s65, s78, 3670016\n"
        "buffer_load_dwordx4 v24, s[80:83], s65 offen lds\n"
        "ds_read_b128 v[32:35], v0 offset:512\n"
        "ds_read_b128 v[36:39], v1 offset:512\n"
        "ds_read_b128 v[40:43], v2 offset:512\n"
        "ds_read_b128 v[44:47], v3 offset:512\n"
        "v_xor_b32 v186, 0, v184\n"
        "ds_read_b128 v[144:147], v186 offset:16384\n"
        "v_xor_b32 v186, 32, v184\n"
        "ds_read_b128 v[148:151], v186 offset:16384\n"
        "v_xor_b32 v186, 64, v184\n"
        "ds_read_b128 v[152:155], v186 offset:16384\n"
        "v_xor_b32 v186, 96, v184\n"
        "ds_read_b128 v[156:159], v186 offset:16384\n"
        "s_add_u32 m0, s71, 77824\n"
        "s_add_u32 s65, s78, 3801088\n"
        "buffer_load_dwordx4 v24, s[80:83], s65 offen lds\n"
        "s_add_u32 m0, s71, 81920\n"
        "s_add_u32 s65, s78, 3932160\n"
        "buffer_load_dwordx4 v24, s[80:83], s65 offen lds\n"
        "s_waitcnt lgkmcnt(7)\n"
        "v_mfma_f32_32x32x16_bf16 v[64:79], a[128:131], v[32:35], v[64:79]\n"
        "s_add_u32 m0, s71, 86016\n"
        "s_add_u32 s65, s78, 4063232\n"
        "buffer_load_dwordx4 v24, s[80:83], s65 offen lds\n"
        "v_mfma_f32_32x32x16_bf16 v[80:95], a[144:147], v[32:35], v[80:95]\n"
        "s_waitcnt lgkmcnt(6)\n"
        "v_mfma_f32_32x32x16_bf16 v[64:79], a[132:135], v[36:39], v[64:79]\n"
        "v_mfma_f32_32x32x16_bf16 v[80:95], a[148:151], v[36:39], v[80:95]\n"
        "s_add_u32 m0, s71, 90112\n"
        "s_add_u32 s65, s78, 3670016\n"
        "buffer_load_dwordx4 v24, s[84:87], s65 offen lds\n"
        "s_waitcnt lgkmcnt(5)\n"
        "v_mfma_f32_32x32x16_bf16 v[64:79], a[136:139], v[40:43], v[64:79]\n"
        "v_mfma_f32_32x32x16_bf16 v[80:95], a[152:155], v[40:43], v[80:95]\n"
        "s_waitcnt lgkmcnt(4)\n"
        "v_mfma_f32_32x32x16_bf16 v[64:79], a[140:143], v[44:47], v[64:79]\n"
        "s_add_u32 m0, s71, 94208\n"
        "s_add_u32 s65, s78, 3801088\n"
        "buffer_load_dwordx4 v24, s[84:87], s65 offen lds\n"
        "v_mfma_f32_32x32x16_bf16 v[80:95], a[156:159], v[44:47], v[80:95]\n"
        "s_waitcnt lgkmcnt(3)\n"
        "v_mfma_f32_32x32x16_bf16 v[96:111], a[128:131], v[144:147], v[96:111]\n"
        "v_mfma_f32_32x32x16_bf16 v[112:127], a[144:147], v[144:147], v[112:127]\n"
        "s_add_u32 m0, s71, 98304\n"
        "s_add_u32 s65, s78, 3932160\n"
        "buffer_load_dwordx4 v24, s[84:87], s65 offen lds\n"
        "s_waitcnt lgkmcnt(2)\n"
        "v_mfma_f32_32x32x16_bf16 v[96:111], a[132:135], v[148:151], v[96:111]\n"
        "v_mfma_f32_32x32x16_bf16 v[112:127], a[148:151], v[148:151], v[112:127]\n"
        "s_waitcnt lgkmcnt(1)\n"
        "v_mfma_f32_32x32x16_bf16 v[96:111], a[136:139], v[152:155], v[96:111]\n"
        "s_add_u32 m0, s71, 102400\n"
        "s_add_u32 s65, s78, 4063232\n"
        "buffer_load_dwordx4 v24, s[84:87], s65 offen lds\n"
        "v_mfma_f32_32x32x16_bf16 v[112:127], a[152:155], v[152:155], v[112:127]\n"
        "s_waitcnt lgkmcnt(0)\n"
        "v_mfma_f32_32x32x16_bf16 v[96:111], a[140:143], v[156:159], v[96:111]\n"
        "v_mfma_f32_32x32x16_bf16 v[112:127], a[156:159], v[156:159], v[112:127]\n"
        "v_add_u32 v28, 106496, v26\n"
        "v_add_u32 v29, 106496, v27\n"
        "s_waitcnt vmcnt(18)\n"
        "s_barrier\n"
        "ds_read_b64_tr_b16 a[160:161], v28 offset:0\n"
        "ds_read_b64_tr_b16 a[162:163], v28 offset:1024\n"
        "ds_read_b64_tr_b16 a[164:165], v28 offset:4096\n"
        "ds_read_b64_tr_b16 a[166:167], v28 offset:5120\n"
        "ds_read_b64_tr_b16 a[168:169], v28 offset:8192\n"
        "ds_read_b64_tr_b16 a[170:171], v28 offset:9216\n"
        "ds_read_b64_tr_b16 a[172:173], v28 offset:12288\n"
        "ds_read_b64_tr_b16 a[174:175], v28 offset:13312\n"
        "ds_read_b64_tr_b16 a[176:177], v28 offset:16384\n"
        "ds_read_b64_tr_b16 a[178:179], v28 offset:17408\n"
        "ds_read_b64_tr_b16 a[180:181], v28 offset:20480\n"
        "ds_read_b64_tr_b16 a[182:183], v28 offset:21504\n"
        "ds_read_b64_tr_b16 a[184:185], v28 offset:24576\n"
        "ds_read_b64_tr_b16 a[186:187], v28 offset:25600\n"
        "ds_read_b64_tr_b16 a[188:189], v28 offset:28672\n"
        "ds_read_b64_tr_b16 a[190:191], v28 offset:29696\n"
        "s_waitcnt lgkmcnt(0)\n"
        "s_barrier\n"
        "s_add_u32 m0, s71, 106496\n"
        "s_add_u32 s65, s78, 4194304\n"
        "buffer_load_dwordx4 v24, s[80:83], s65 offen lds\n"
        "ds_read_b128 v[48:51], v4 offset:512\n"
        "ds_read_b128 v[52:55], v5 offset:512\n"
        "ds_read_b128 v[56:59], v6 offset:512\n"
        "ds_read_b128 v[60:63], v7 offset:512\n"
        "v_xor_b32 v186, 128, v184\n"
        "ds_read_b128 v[144:147], v186 offset:16384\n"
        "v_xor_b32 v186, 160, v184\n"
        "ds_read_b128 v[148:151], v186 offset:16384\n"
        "v_xor_b32 v186, 192, v184\n"
        "ds_read_b128 v[152:155], v186 offset:16384\n"
        "v_xor_b32 v186, 224, v184\n"
        "ds_read_b128 v[156:159], v186 offset:16384\n"
        "s_add_u32 m0, s71, 110592\n"
        "s_add_u32 s65, s78, 4325376\n"
        "buffer_load_dwordx4 v24, s[80:83], s65 offen lds\n"
        "s_add_u32 m0, s71, 114688\n"
        "s_add_u32 s65, s78, 4456448\n"
        "buffer_load_dwordx4 v24, s[80:83], s65 offen lds\n"
        "s_waitcnt lgkmcnt(7)\n"
        "v_mfma_f32_32x32x16_bf16 v[64:79], a[160:163], v[48:51], v[64:79]\n"
        "s_add_u32 m0, s71, 118784\n"
        "s_add_u32 s65, s78, 4587520\n"
        "buffer_load_dwordx4 v24, s[80:83], s65 offen lds\n"
        "v_mfma_f32_32x32x16_bf16 v[80:95], a[176:179], v[48:51], v[80:95]\n"
        "s_waitcnt lgkmcnt(6)\n"
        "v_mfma_f32_32x32x16_bf16 v[64:79], a[164:167], v[52:55], v[64:79]\n"
        "s_add_u32 m0, s71, 122880\n"
        "s_add_u32 s65, s78, 4194304\n"
        "buffer_load_dwordx4 v24, s[84:87], s65 offen lds\n"
        "v_mfma_f32_32x32x16_bf16 v[80:95], a[180:183], v[52:55], v[80:95]\n"
        "s_waitcnt lgkmcnt(5)\n"
        "v_mfma_f32_32x32x16_bf16 v[64:79], a[168:171], v[56:59], v[64:79]\n"
        "s_add_u32 m0, s71, 126976\n"
        "s_add_u32 s65, s78, 4325376\n"
        "buffer_load_dwordx4 v24, s[84:87], s65 offen lds\n"
        "v_mfma_f32_32x32x16_bf16 v[80:95], a[184:187], v[56:59], v[80:95]\n"
        "s_waitcnt lgkmcnt(4)\n"
        "v_mfma_f32_32x32x16_bf16 v[64:79], a[172:175], v[60:63], v[64:79]\n"
        "s_add_u32 m0, s71, 131072\n"
        "s_add_u32 s65, s78, 4456448\n"
        "buffer_load_dwordx4 v24, s[84:87], s65 offen lds\n"
        "v_mfma_f32_32x32x16_bf16 v[80:95], a[188:191], v[60:63], v[80:95]\n"
        "s_waitcnt lgkmcnt(3)\n"
        "v_mfma_f32_32x32x16_bf16 v[96:111], a[160:163], v[144:147], v[96:111]\n"
        "s_add_u32 m0, s71, 135168\n"
        "s_add_u32 s65, s78, 4587520\n"
        "buffer_load_dwordx4 v24, s[84:87], s65 offen lds\n"
        "v_mfma_f32_32x32x16_bf16 v[112:127], a[176:179], v[144:147], v[112:127]\n"
        "s_waitcnt lgkmcnt(2)\n"
        "v_mfma_f32_32x32x16_bf16 v[96:111], a[164:167], v[148:151], v[96:111]\n"
        "s_add_u32 m0, s71, 147456\n"
        "s_mov_b32 s65, 41984\n"
        "buffer_load_dwordx4 v185, s[72:75], s65 offen lds\n"
        "v_mfma_f32_32x32x16_bf16 v[112:127], a[180:183], v[148:151], v[112:127]\n"
        "s_waitcnt lgkmcnt(1)\n"
        "v_mfma_f32_32x32x16_bf16 v[96:111], a[168:171], v[152:155], v[96:111]\n"
        "s_add_u32 m0, s71, 151552\n"
        "s_mov_b32 s65, 62464\n"
        "buffer_load_dwordx4 v185, s[72:75], s65 offen lds\n"
        "v_mfma_f32_32x32x16_bf16 v[112:127], a[184:187], v[152:155], v[112:127]\n"
        "s_waitcnt lgkmcnt(0)\n"
        "v_mfma_f32_32x32x16_bf16 v[96:111], a[172:175], v[156:159], v[96:111]\n"
        "v_mfma_f32_32x32x16_bf16 v[112:127], a[188:191], v[156:159], v[112:127]\n"
        "v_add_u32 v28, 40960, v26\n"
        "v_add_u32 v29, 40960, v27\n"
        "s_waitcnt vmcnt(18)\n"
        "s_barrier\n"
        "ds_read_b64_tr_b16 a[192:193], v28 offset:0\n"
        "ds_read_b64_tr_b16 a[194:195], v28 offset:1024\n"
        "ds_read_b64_tr_b16 a[196:197], v28 offset:4096\n"
        "ds_read_b64_tr_b16 a[198:199], v28 offset:5120\n"
        "ds_read_b64_tr_b16 a[200:201], v28 offset:8192\n"
        "ds_read_b64_tr_b16 a[202:203], v28 offset:9216\n"
        "ds_read_b64_tr_b16 a[204:205], v28 offset:12288\n"
        "ds_read_b64_tr_b16 a[206:207], v28 offset:13312\n"
        "ds_read_b64_tr_b16 a[208:209], v28 offset:16384\n"
        "ds_read_b64_tr_b16 a[210:211], v28 offset:17408\n"
        "ds_read_b64_tr_b16 a[212:213], v28 offset:20480\n"
        "ds_read_b64_tr_b16 a[214:215], v28 offset:21504\n"
        "ds_read_b64_tr_b16 a[216:217], v28 offset:24576\n"
        "ds_read_b64_tr_b16 a[218:219], v28 offset:25600\n"
        "ds_read_b64_tr_b16 a[220:221], v28 offset:28672\n"
        "ds_read_b64_tr_b16 a[222:223], v28 offset:29696\n"
        "s_waitcnt lgkmcnt(0)\n"
        "s_barrier\n"
        "s_add_u32 m0, s71, 40960\n"
        "s_add_u32 s65, s78, 4718592\n"
        "buffer_load_dwordx4 v24, s[80:83], s65 offen lds\n"
        "ds_read_b128 v[32:35], v0 offset:768\n"
        "ds_read_b128 v[36:39], v1 offset:768\n"
        "ds_read_b128 v[40:43], v2 offset:768\n"
        "ds_read_b128 v[44:47], v3 offset:768\n"
        "v_xor_b32 v186, 0, v184\n"
        "ds_read_b128 v[144:147], v186 offset:0\n"
        "v_xor_b32 v186, 32, v184\n"
        "ds_read_b128 v[148:151], v186 offset:0\n"
        "v_xor_b32 v186, 64, v184\n"
        "ds_read_b128 v[152:155], v186 offset:0\n"
        "v_xor_b32 v186, 96, v184\n"
        "ds_read_b128 v[156:159], v186 offset:0\n"
        "s_add_u32 m0, s71, 45056\n"
        "s_add_u32 s65, s78, 4849664\n"
        "buffer_load_dwordx4 v24, s[80:83], s65 offen lds\n"
        "s_add_u32 m0, s71, 49152\n"
        "s_add_u32 s65, s78, 4980736\n"
        "buffer_load_dwordx4 v24, s[80:83], s65 offen lds\n"
        "s_waitcnt lgkmcnt(7)\n"
        "v_mfma_f32_32x32x16_bf16 v[64:79], a[192:195], v[32:35], v[64:79]\n"
        "s_add_u32 m0, s71, 53248\n"
        "s_add_u32 s65, s78, 5111808\n"
        "buffer_load_dwordx4 v24, s[80:83], s65 offen lds\n"
        "v_mfma_f32_32x32x16_bf16 v[80:95], a[208:211], v[32:35], v[80:95]\n"
        "s_waitcnt lgkmcnt(6)\n"
        "v_mfma_f32_32x32x16_bf16 v[64:79], a[196:199], v[36:39], v[64:79]\n"
        "v_mfma_f32_32x32x16_bf16 v[80:95], a[212:215], v[36:39], v[80:95]\n"
        "s_add_u32 m0, s71, 57344\n"
        "s_add_u32 s65, s78, 4718592\n"
        "buffer_load_dwordx4 v24, s[84:87], s65 offen lds\n"
        "s_waitcnt lgkmcnt(5)\n"
        "v_mfma_f32_32x32x16_bf16 v[64:79], a[200:203], v[40:43], v[64:79]\n"
        "v_mfma_f32_32x32x16_bf16 v[80:95], a[216:219], v[40:43], v[80:95]\n"
        "s_waitcnt lgkmcnt(4)\n"
        "v_mfma_f32_32x32x16_bf16 v[64:79], a[204:207], v[44:47], v[64:79]\n"
        "s_add_u32 m0, s71, 61440\n"
        "s_add_u32 s65, s78, 4849664\n"
        "buffer_load_dwordx4 v24, s[84:87], s65 offen lds\n"
        "v_mfma_f32_32x32x16_bf16 v[80:95], a[220:223], v[44:47], v[80:95]\n"
        "s_waitcnt lgkmcnt(3)\n"
        "v_mfma_f32_32x32x16_bf16 v[96:111], a[192:195], v[144:147], v[96:111]\n"
        "v_mfma_f32_32x32x16_bf16 v[112:127], a[208:211], v[144:147], v[112:127]\n"
        "s_add_u32 m0, s71, 65536\n"
        "s_add_u32 s65, s78, 4980736\n"
        "buffer_load_dwordx4 v24, s[84:87], s65 offen lds\n"
        "s_waitcnt lgkmcnt(2)\n"
        "v_mfma_f32_32x32x16_bf16 v[96:111], a[196:199], v[148:151], v[96:111]\n"
        "v_mfma_f32_32x32x16_bf16 v[112:127], a[212:215], v[148:151], v[112:127]\n"
        "s_waitcnt lgkmcnt(1)\n"
        "v_mfma_f32_32x32x16_bf16 v[96:111], a[200:203], v[152:155], v[96:111]\n"
        "s_add_u32 m0, s71, 69632\n"
        "s_add_u32 s65, s78, 5111808\n"
        "buffer_load_dwordx4 v24, s[84:87], s65 offen lds\n"
        "v_mfma_f32_32x32x16_bf16 v[112:127], a[216:219], v[152:155], v[112:127]\n"
        "s_waitcnt lgkmcnt(0)\n"
        "v_mfma_f32_32x32x16_bf16 v[96:111], a[204:207], v[156:159], v[96:111]\n"
        "v_mfma_f32_32x32x16_bf16 v[112:127], a[220:223], v[156:159], v[112:127]\n"
        "v_add_u32 v28, 73728, v26\n"
        "v_add_u32 v29, 73728, v27\n"
        "s_waitcnt vmcnt(18)\n"
        "s_barrier\n"
        "ds_read_b64_tr_b16 a[224:225], v28 offset:0\n"
        "ds_read_b64_tr_b16 a[226:227], v28 offset:1024\n"
        "ds_read_b64_tr_b16 a[228:229], v28 offset:4096\n"
        "ds_read_b64_tr_b16 a[230:231], v28 offset:5120\n"
        "ds_read_b64_tr_b16 a[232:233], v28 offset:8192\n"
        "ds_read_b64_tr_b16 a[234:235], v28 offset:9216\n"
        "ds_read_b64_tr_b16 a[236:237], v28 offset:12288\n"
        "ds_read_b64_tr_b16 a[238:239], v28 offset:13312\n"
        "ds_read_b64_tr_b16 a[240:241], v28 offset:16384\n"
        "ds_read_b64_tr_b16 a[242:243], v28 offset:17408\n"
        "ds_read_b64_tr_b16 a[244:245], v28 offset:20480\n"
        "ds_read_b64_tr_b16 a[246:247], v28 offset:21504\n"
        "ds_read_b64_tr_b16 a[248:249], v28 offset:24576\n"
        "ds_read_b64_tr_b16 a[250:251], v28 offset:25600\n"
        "ds_read_b64_tr_b16 a[252:253], v28 offset:28672\n"
        "ds_read_b64_tr_b16 a[254:255], v28 offset:29696\n"
        "s_waitcnt lgkmcnt(0)\n"
        "s_barrier\n"
        "ds_read_b128 v[48:51], v4 offset:768\n"
        "ds_read_b128 v[52:55], v5 offset:768\n"
        "ds_read_b128 v[56:59], v6 offset:768\n"
        "ds_read_b128 v[60:63], v7 offset:768\n"
        "v_xor_b32 v186, 128, v184\n"
        "ds_read_b128 v[144:147], v186 offset:0\n"
        "v_xor_b32 v186, 160, v184\n"
        "ds_read_b128 v[148:151], v186 offset:0\n"
        "v_xor_b32 v186, 192, v184\n"
        "ds_read_b128 v[152:155], v186 offset:0\n"
        "v_xor_b32 v186, 224, v184\n"
        "ds_read_b128 v[156:159], v186 offset:0\n"
        "s_waitcnt lgkmcnt(7)\n"
        "v_mfma_f32_32x32x16_bf16 v[64:79], a[224:227], v[48:51], v[64:79]\n"
        "v_mfma_f32_32x32x16_bf16 v[80:95], a[240:243], v[48:51], v[80:95]\n"
        "s_waitcnt lgkmcnt(6)\n"
        "v_mfma_f32_32x32x16_bf16 v[64:79], a[228:231], v[52:55], v[64:79]\n"
        "v_mfma_f32_32x32x16_bf16 v[80:95], a[244:247], v[52:55], v[80:95]\n"
        "s_waitcnt lgkmcnt(5)\n"
        "v_mfma_f32_32x32x16_bf16 v[64:79], a[232:235], v[56:59], v[64:79]\n"
        "v_mfma_f32_32x32x16_bf16 v[80:95], a[248:251], v[56:59], v[80:95]\n"
        "s_waitcnt lgkmcnt(4)\n"
        "v_mfma_f32_32x32x16_bf16 v[64:79], a[236:239], v[60:63], v[64:79]\n"
        "v_mfma_f32_32x32x16_bf16 v[80:95], a[252:255], v[60:63], v[80:95]\n"
        "s_waitcnt lgkmcnt(3)\n"
        "v_mfma_f32_32x32x16_bf16 v[96:111], a[224:227], v[144:147], v[96:111]\n"
        "v_mfma_f32_32x32x16_bf16 v[112:127], a[240:243], v[144:147], v[112:127]\n"
        "s_waitcnt lgkmcnt(2)\n"
        "v_mfma_f32_32x32x16_bf16 v[96:111], a[228:231], v[148:151], v[96:111]\n"
        "v_mfma_f32_32x32x16_bf16 v[112:127], a[244:247], v[148:151], v[112:127]\n"
        "s_waitcnt lgkmcnt(1)\n"
        "v_mfma_f32_32x32x16_bf16 v[96:111], a[232:235], v[152:155], v[96:111]\n"
        "v_mfma_f32_32x32x16_bf16 v[112:127], a[248:251], v[152:155], v[112:127]\n"
        "s_waitcnt lgkmcnt(0)\n"
        "v_mfma_f32_32x32x16_bf16 v[96:111], a[236:239], v[156:159], v[96:111]\n"
        "v_mfma_f32_32x32x16_bf16 v[112:127], a[252:255], v[156:159], v[112:127]\n"
        "v_add_u32 v28, 106496, v26\n"
        "v_add_u32 v29, 106496, v27\n"
        "s_waitcnt vmcnt(8)\n"
        "s_barrier\n"
        "ds_read_b64_tr_b16 v[192:193], v28 offset:0\n"
        "ds_read_b64_tr_b16 v[194:195], v28 offset:1024\n"
        "ds_read_b64_tr_b16 v[196:197], v28 offset:4096\n"
        "ds_read_b64_tr_b16 v[198:199], v28 offset:5120\n"
        "ds_read_b64_tr_b16 v[200:201], v28 offset:8192\n"
        "ds_read_b64_tr_b16 v[202:203], v28 offset:9216\n"
        "ds_read_b64_tr_b16 v[204:205], v28 offset:12288\n"
        "ds_read_b64_tr_b16 v[206:207], v28 offset:13312\n"
        "ds_read_b64_tr_b16 v[208:209], v28 offset:16384\n"
        "ds_read_b64_tr_b16 v[210:211], v28 offset:17408\n"
        "ds_read_b64_tr_b16 v[212:213], v28 offset:20480\n"
        "ds_read_b64_tr_b16 v[214:215], v28 offset:21504\n"
        "ds_read_b64_tr_b16 v[216:217], v28 offset:24576\n"
        "ds_read_b64_tr_b16 v[218:219], v28 offset:25600\n"
        "ds_read_b64_tr_b16 v[220:221], v28 offset:28672\n"
        "ds_read_b64_tr_b16 v[222:223], v28 offset:29696\n"
        "s_waitcnt lgkmcnt(0)\n"
        "s_barrier\n"
        "buffer_load_dword v189, off, s[60:63], s88 sc1\n"
        "ds_read_b128 v[32:35], v0 offset:1024\n"
        "ds_read_b128 v[36:39], v1 offset:1024\n"
        "ds_read_b128 v[40:43], v2 offset:1024\n"
        "ds_read_b128 v[44:47], v3 offset:1024\n"
        "v_xor_b32 v186, 0, v184\n"
        "ds_read_b128 v[144:147], v186 offset:8192\n"
        "v_xor_b32 v186, 32, v184\n"
        "ds_read_b128 v[148:151], v186 offset:8192\n"
        "v_xor_b32 v186, 64, v184\n"
        "ds_read_b128 v[152:155], v186 offset:8192\n"
        "v_xor_b32 v186, 96, v184\n"
        "ds_read_b128 v[156:159], v186 offset:8192\n"
        "s_waitcnt lgkmcnt(7)\n"
        "v_mfma_f32_32x32x16_bf16 v[64:79], v[192:195], v[32:35], v[64:79]\n"
        "v_mfma_f32_32x32x16_bf16 v[80:95], v[208:211], v[32:35], v[80:95]\n"
        "s_waitcnt lgkmcnt(6)\n"
        "v_mfma_f32_32x32x16_bf16 v[64:79], v[196:199], v[36:39], v[64:79]\n"
        "v_mfma_f32_32x32x16_bf16 v[80:95], v[212:215], v[36:39], v[80:95]\n"
        "s_waitcnt lgkmcnt(5)\n"
        "v_mfma_f32_32x32x16_bf16 v[64:79], v[200:203], v[40:43], v[64:79]\n"
        "v_mfma_f32_32x32x16_bf16 v[80:95], v[216:219], v[40:43], v[80:95]\n"
        "s_waitcnt lgkmcnt(4)\n"
        "v_mfma_f32_32x32x16_bf16 v[64:79], v[204:207], v[44:47], v[64:79]\n"
        "v_mfma_f32_32x32x16_bf16 v[80:95], v[220:223], v[44:47], v[80:95]\n"
        "s_waitcnt lgkmcnt(3)\n"
        "v_mfma_f32_32x32x16_bf16 v[96:111], v[192:195], v[144:147], v[96:111]\n"
        "v_mfma_f32_32x32x16_bf16 v[112:127], v[208:211], v[144:147], v[112:127]\n"
        "s_waitcnt lgkmcnt(2)\n"
        "v_mfma_f32_32x32x16_bf16 v[96:111], v[196:199], v[148:151], v[96:111]\n"
        "v_mfma_f32_32x32x16_bf16 v[112:127], v[212:215], v[148:151], v[112:127]\n"
        "s_waitcnt lgkmcnt(1)\n"
        "v_mfma_f32_32x32x16_bf16 v[96:111], v[200:203], v[152:155], v[96:111]\n"
        "v_mfma_f32_32x32x16_bf16 v[112:127], v[216:219], v[152:155], v[112:127]\n"
        "s_waitcnt lgkmcnt(0)\n"
        "v_mfma_f32_32x32x16_bf16 v[96:111], v[204:207], v[156:159], v[96:111]\n"
        "v_mfma_f32_32x32x16_bf16 v[112:127], v[220:223], v[156:159], v[112:127]\n"
        "v_add_u32 v28, 40960, v26\n"
        "v_add_u32 v29, 40960, v27\n"
        "s_waitcnt vmcnt(1)\n"
        "s_barrier\n"
        "ds_read_b64_tr_b16 v[224:225], v28 offset:0\n"
        "ds_read_b64_tr_b16 v[226:227], v28 offset:1024\n"
        "ds_read_b64_tr_b16 v[228:229], v28 offset:4096\n"
        "ds_read_b64_tr_b16 v[230:231], v28 offset:5120\n"
        "ds_read_b64_tr_b16 v[232:233], v28 offset:8192\n"
        "ds_read_b64_tr_b16 v[234:235], v28 offset:9216\n"
        "ds_read_b64_tr_b16 v[236:237], v28 offset:12288\n"
        "ds_read_b64_tr_b16 v[238:239], v28 offset:13312\n"
        "ds_read_b64_tr_b16 v[240:241], v28 offset:16384\n"
        "ds_read_b64_tr_b16 v[242:243], v28 offset:17408\n"
        "ds_read_b64_tr_b16 v[244:245], v28 offset:20480\n"
        "ds_read_b64_tr_b16 v[246:247], v28 offset:21504\n"
        "ds_read_b64_tr_b16 v[248:249], v28 offset:24576\n"
        "ds_read_b64_tr_b16 v[250:251], v28 offset:25600\n"
        "ds_read_b64_tr_b16 v[252:253], v28 offset:28672\n"
        "ds_read_b64_tr_b16 v[254:255], v28 offset:29696\n"
        "s_waitcnt lgkmcnt(0)\n"
        "s_barrier\n"
        "ds_read_b128 v[48:51], v4 offset:1024\n"
        "ds_read_b128 v[52:55], v5 offset:1024\n"
        "ds_read_b128 v[56:59], v6 offset:1024\n"
        "ds_read_b128 v[60:63], v7 offset:1024\n"
        "v_xor_b32 v186, 128, v184\n"
        "ds_read_b128 v[144:147], v186 offset:8192\n"
        "v_xor_b32 v186, 160, v184\n"
        "ds_read_b128 v[148:151], v186 offset:8192\n"
        "v_xor_b32 v186, 192, v184\n"
        "ds_read_b128 v[152:155], v186 offset:8192\n"
        "v_xor_b32 v186, 224, v184\n"
        "ds_read_b128 v[156:159], v186 offset:8192\n"
        "s_waitcnt lgkmcnt(7)\n"
        "v_mfma_f32_32x32x16_bf16 v[64:79], v[224:227], v[48:51], v[64:79]\n"
        "v_mfma_f32_32x32x16_bf16 v[80:95], v[240:243], v[48:51], v[80:95]\n"
        "s_waitcnt lgkmcnt(6)\n"
        "v_mfma_f32_32x32x16_bf16 v[64:79], v[228:231], v[52:55], v[64:79]\n"
        "v_mfma_f32_32x32x16_bf16 v[80:95], v[244:247], v[52:55], v[80:95]\n"
        "s_waitcnt lgkmcnt(5)\n"
        "v_mfma_f32_32x32x16_bf16 v[64:79], v[232:235], v[56:59], v[64:79]\n"
        "v_mfma_f32_32x32x16_bf16 v[80:95], v[248:251], v[56:59], v[80:95]\n"
        "s_waitcnt lgkmcnt(4)\n"
        "v_mfma_f32_32x32x16_bf16 v[64:79], v[236:239], v[60:63], v[64:79]\n"
        "v_mfma_f32_32x32x16_bf16 v[80:95], v[252:255], v[60:63], v[80:95]\n"
        "s_mov_b32 s68, 5242880\n"
        "s_mov_b32 s76, 22020096\n"
        "s_waitcnt lgkmcnt(3)\n"
        "v_mfma_f32_32x32x16_bf16 v[96:111], v[224:227], v[144:147], v[96:111]\n"
        "v_mfma_f32_32x32x16_bf16 v[112:127], v[240:243], v[144:147], v[112:127]\n"
        "s_waitcnt lgkmcnt(2)\n"
        "v_mfma_f32_32x32x16_bf16 v[96:111], v[228:231], v[148:151], v[96:111]\n"
        "v_mfma_f32_32x32x16_bf16 v[112:127], v[244:247], v[148:151], v[112:127]\n"
        "s_waitcnt lgkmcnt(1)\n"
        "v_mfma_f32_32x32x16_bf16 v[96:111], v[232:235], v[152:155], v[96:111]\n"
        "v_mfma_f32_32x32x16_bf16 v[112:127], v[248:251], v[152:155], v[112:127]\n"
        "v_cvt_pk_bf16_f32 v128, v64, v65\n"
        "v_cvt_pk_bf16_f32 v129, v66, v67\n"
        "v_cvt_pk_bf16_f32 v130, v68, v69\n"
        "v_cvt_pk_bf16_f32 v131, v70, v71\n"
        "buffer_store_dwordx4 v[128:131], v18, s[60:63], s68 offen offset:0 sc1\n"
        "v_cvt_pk_bf16_f32 v132, v72, v73\n"
        "v_cvt_pk_bf16_f32 v133, v74, v75\n"
        "v_cvt_pk_bf16_f32 v134, v76, v77\n"
        "v_cvt_pk_bf16_f32 v135, v78, v79\n"
        "buffer_store_dwordx4 v[132:135], v18, s[60:63], s68 offen offset:1024 sc1\n"
        "s_waitcnt lgkmcnt(0)\n"
        "v_mfma_f32_32x32x16_bf16 v[96:111], v[236:239], v[156:159], v[96:111]\n"
        "v_mfma_f32_32x32x16_bf16 v[112:127], v[252:255], v[156:159], v[112:127]\n"
        "v_cvt_pk_bf16_f32 v136, v80, v81\n"
        "v_cvt_pk_bf16_f32 v137, v82, v83\n"
        "v_cvt_pk_bf16_f32 v138, v84, v85\n"
        "v_cvt_pk_bf16_f32 v139, v86, v87\n"
        "buffer_store_dwordx4 v[136:139], v18, s[60:63], s76 offen offset:0 sc1\n"
        "v_cvt_pk_bf16_f32 v140, v88, v89\n"
        "v_cvt_pk_bf16_f32 v141, v90, v91\n"
        "v_cvt_pk_bf16_f32 v142, v92, v93\n"
        "v_cvt_pk_bf16_f32 v143, v94, v95\n"
        "buffer_store_dwordx4 v[140:143], v18, s[60:63], s76 offen offset:1024 sc1\n"
        "s_mov_b32 s68, 5505024\n"
        "s_mov_b32 s76, 22282240\n"
        "s_nop 7\n"
        "s_nop 3\n"
        "v_cvt_pk_bf16_f32 v160, v96, v97\n"
        "v_cvt_pk_bf16_f32 v161, v98, v99\n"
        "v_cvt_pk_bf16_f32 v162, v100, v101\n"
        "v_cvt_pk_bf16_f32 v163, v102, v103\n"
        "buffer_store_dwordx4 v[160:163], v18, s[60:63], s68 offen offset:0 sc1\n"
        "v_cvt_pk_bf16_f32 v164, v104, v105\n"
        "v_cvt_pk_bf16_f32 v165, v106, v107\n"
        "v_cvt_pk_bf16_f32 v166, v108, v109\n"
        "v_cvt_pk_bf16_f32 v167, v110, v111\n"
        "buffer_store_dwordx4 v[164:167], v18, s[60:63], s68 offen offset:1024 sc1\n"
        "v_cvt_pk_bf16_f32 v168, v112, v113\n"
        "v_cvt_pk_bf16_f32 v169, v114, v115\n"
        "v_cvt_pk_bf16_f32 v170, v116, v117\n"
        "v_cvt_pk_bf16_f32 v171, v118, v119\n"
        "buffer_store_dwordx4 v[168:171], v18, s[60:63], s76 offen offset:0 sc1\n"
        "v_cvt_pk_bf16_f32 v172, v120, v121\n"
        "v_cvt_pk_bf16_f32 v173, v122, v123\n"
        "v_cvt_pk_bf16_f32 v174, v124, v125\n"
        "v_cvt_pk_bf16_f32 v175, v126, v127\n"
        "buffer_store_dwordx4 v[172:175], v18, s[60:63], s76 offen offset:1024 sc1\n"
        "s_waitcnt vmcnt(8)\n"
        "v_cmp_ne_u32 vcc, v188, v189\n"
        "s_cbranch_vccz HS966_qkb\n"
        "HS966_qka:\n"
        "buffer_load_dword v189, off, s[60:63], s88 sc1\n"
        "s_waitcnt vmcnt(0)\n"
        "v_cmp_ne_u32 vcc, v188, v189\n"
        "s_cbranch_vccz HS966_qkb\n"
        "s_sleep 1\n"
        "s_cmp_eq_u32 s66, 0\n"
        "s_cbranch_scc1 HS966_qkb\n"
        "s_sub_u32 s66, s66, 1\n"
        "s_branch HS966_qka\n"
        "HS966_qkb:\n"
        "s_mov_b64 exec, 1\n"
        "v_mov_b32 v189, 0\n"
        "buffer_store_dword v189, off, s[60:63], s90 sc1\n"
        "s_mov_b64 exec, -1\n"
        "s_lshl_b32 s77, s58, 2\n"
        "s_add_u32 s77, s77, s67\n"
        "s_lshl_b32 s77, s77, 2\n"
        "s_add_u32 s77, s77, 4194304\n"
        "s_add_u32 m0, s71, 40960\n"
        "s_nop 0\n"
        "buffer_load_dwordx4 v8, s[72:75], s70 offen lds\n"
        "s_add_u32 m0, s71, 45056\n"
        "s_nop 0\n"
        "buffer_load_dwordx4 v9, s[72:75], s70 offen lds\n"
        "s_add_u32 m0, s71, 49152\n"
        "s_nop 0\n"
        "buffer_load_dwordx4 v10, s[72:75], s70 offen lds\n"
        "s_add_u32 m0, s71, 53248\n"
        "s_nop 0\n"
        "buffer_load_dwordx4 v11, s[72:75], s70 offen lds\n"
        "s_add_u32 m0, s71, 57344\n"
        "s_nop 0\n"
        "buffer_load_dwordx4 v12, s[72:75], s70 offen lds\n"
        "s_add_u32 m0, s71, 61440\n"
        "s_nop 0\n"
        "buffer_load_dwordx4 v13, s[72:75], s70 offen lds\n"
        "s_add_u32 m0, s71, 65536\n"
        "s_nop 0\n"
        "buffer_load_dwordx4 v14, s[72:75], s70 offen lds\n"
        "s_add_u32 m0, s71, 69632\n"
        "s_nop 0\n"
        "buffer_load_dwordx4 v15, s[72:75], s70 offen lds\n"
        "s_add_u32 m0, s71, 73728\n"
        "s_nop 0\n"
        "buffer_load_dwordx4 v16, s[72:75], s70 offen lds\n"
        "s_add_u32 m0, s71, 77824\n"
        "s_nop 0\n"
        "buffer_load_dwordx4 v17, s[72:75], s70 offen lds\n"
        "s_waitcnt vmcnt(15)\n"
        "buffer_store_dword v20, v21, s[60:63], s77 offen sc1\n"
        "s_waitcnt vmcnt(11)\n"
        "buffer_store_dword v20, v21, s[60:63], s77 offen offset:512 sc1\n"
        "s_mov_b32 s64, 1\n"
        "s_waitcnt vmcnt(2)\n"
        "s_barrier\n"
        "ds_read_b128 v[32:35], v0 offset:40960\n"
        "ds_read_b128 v[36:39], v1 offset:40960\n"
        "ds_read_b128 v[40:43], v2 offset:40960\n"
        "ds_read_b128 v[44:47], v3 offset:40960\n"
        "ds_read_b128 v[48:51], v4 offset:40960\n"
        "ds_read_b128 v[52:55], v5 offset:40960\n"
        "s_mov_b32 s68, 4026531840\n"
        "s_mov_b32 s76, 4026531840\n"
        "s_mov_b32 s70, 122880\n"
        "s_mov_b32 s64, 2\n"
        "s_branch QK_p1\n"
        "QK_loop:\n"
        "s_waitcnt lgkmcnt(5)\n"
        "v_mfma_f32_32x32x16_bf16 v[64:79], a[0:3], v[32:35], 0\n"
        "ds_read_b128 v[56:59], v6 offset:0\n"
        "v_mfma_f32_32x32x16_bf16 v[80:95], a[16:19], v[32:35], 0\n"
        "s_waitcnt lgkmcnt(5)\n"
        "v_mfma_f32_32x32x16_bf16 v[64:79], a[4:7], v[36:39], v[64:79]\n"
        "ds_read_b128 v[60:63], v7 offset:0\n"
        "v_cvt_pk_bf16_f32 v128, v96, v97\n"
        "v_cvt_pk_bf16_f32 v129, v98, v99\n"
        "v_mfma_f32_32x32x16_bf16 v[80:95], a[20:23], v[36:39], v[80:95]\n"
        "v_cvt_pk_bf16_f32 v130, v100, v101\n"
        "v_cvt_pk_bf16_f32 v131, v102, v103\n"
        "s_waitcnt lgkmcnt(5)\n"
        "v_mfma_f32_32x32x16_bf16 v[64:79], a[8:11], v[40:43], v[64:79]\n"
        "ds_read_b128 v[32:35], v0 offset:256\n"
        "buffer_store_dwordx4 v[128:131], v18, s[60:63], s68 offen offset:0 sc1\n"
        "v_cvt_pk_bf16_f32 v132, v104, v105\n"
        "v_mfma_f32_32x32x16_bf16 v[80:95], a[24:27], v[40:43], v[80:95]\n"
        "v_cvt_pk_bf16_f32 v133, v106, v107\n"
        "v_cvt_pk_bf16_f32 v134, v108, v109\n"
        "s_waitcnt lgkmcnt(5)\n"
        "v_mfma_f32_32x32x16_bf16 v[64:79], a[12:15], v[44:47], v[64:79]\n"
        "ds_read_b128 v[36:39], v1 offset:256\n"
        "v_cvt_pk_bf16_f32 v135, v110, v111\n"
        "buffer_store_dwordx4 v[132:135], v18, s[60:63], s68 offen offset:1024 sc1\n"
        "v_mfma_f32_32x32x16_bf16 v[80:95], a[28:31], v[44:47], v[80:95]\n"
        "v_cvt_pk_bf16_f32 v136, v112, v113\n"
        "v_cvt_pk_bf16_f32 v137, v114, v115\n"
        "s_waitcnt lgkmcnt(5)\n"
        "v_mfma_f32_32x32x16_bf16 v[64:79], a[32:35], v[48:51], v[64:79]\n"
        "ds_read_b128 v[40:43], v2 offset:256\n"
        "v_cvt_pk_bf16_f32 v138, v116, v117\n"
        "v_cvt_pk_bf16_f32 v139, v118, v119\n"
        "v_mfma_f32_32x32x16_bf16 v[80:95], a[48:51], v[48:51], v[80:95]\n"
        "buffer_store_dwordx4 v[136:139], v18, s[60:63], s76 offen offset:0 sc1\n"
        "v_cvt_pk_bf16_f32 v140, v120, v121\n"
        "s_waitcnt lgkmcnt(5)\n"
        "v_mfma_f32_32x32x16_bf16 v[64:79], a[36:39], v[52:55], v[64:79]\n"
        "ds_read_b128 v[44:47], v3 offset:256\n"
        "v_cvt_pk_bf16_f32 v141, v122, v123\n"
        "v_cvt_pk_bf16_f32 v142, v124, v125\n"
        "v_mfma_f32_32x32x16_bf16 v[80:95], a[52:55], v[52:55], v[80:95]\n"
        "v_cvt_pk_bf16_f32 v143, v126, v127\n"
        "buffer_store_dwordx4 v[140:143], v18, s[60:63], s76 offen offset:1024 sc1\n"
        "s_waitcnt lgkmcnt(5)\n"
        "v_mfma_f32_32x32x16_bf16 v[64:79], a[40:43], v[56:59], v[64:79]\n"
        "ds_read_b128 v[48:51], v4 offset:256\n"
        "v_mfma_f32_32x32x16_bf16 v[80:95], a[56:59], v[56:59], v[80:95]\n"
        "s_waitcnt lgkmcnt(5)\n"
        "v_mfma_f32_32x32x16_bf16 v[64:79], a[44:47], v[60:63], v[64:79]\n"
        "ds_read_b128 v[52:55], v5 offset:256\n"
        "v_mfma_f32_32x32x16_bf16 v[80:95], a[60:63], v[60:63], v[80:95]\n"
        "s_waitcnt lgkmcnt(5)\n"
        "v_mfma_f32_32x32x16_bf16 v[64:79], a[64:67], v[32:35], v[64:79]\n"
        "ds_read_b128 v[56:59], v6 offset:256\n"
        "v_mfma_f32_32x32x16_bf16 v[80:95], a[80:83], v[32:35], v[80:95]\n"
        "s_waitcnt lgkmcnt(5)\n"
        "v_mfma_f32_32x32x16_bf16 v[64:79], a[68:71], v[36:39], v[64:79]\n"
        "ds_read_b128 v[60:63], v7 offset:256\n"
        "v_mfma_f32_32x32x16_bf16 v[80:95], a[84:87], v[36:39], v[80:95]\n"
        "s_waitcnt lgkmcnt(5)\n"
        "v_mfma_f32_32x32x16_bf16 v[64:79], a[72:75], v[40:43], v[64:79]\n"
        "ds_read_b128 v[32:35], v0 offset:512\n"
        "s_add_u32 m0, s71, 40960\n"
        "v_mfma_f32_32x32x16_bf16 v[80:95], a[88:91], v[40:43], v[80:95]\n"
        "s_nop 0\n"
        "buffer_load_dwordx4 v8, s[72:75], s70 offen lds\n"
        "s_waitcnt lgkmcnt(5)\n"
        "v_mfma_f32_32x32x16_bf16 v[64:79], a[76:79], v[44:47], v[64:79]\n"
        "ds_read_b128 v[36:39], v1 offset:512\n"
        "s_add_u32 m0, s71, 45056\n"
        "v_mfma_f32_32x32x16_bf16 v[80:95], a[92:95], v[44:47], v[80:95]\n"
        "s_nop 0\n"
        "buffer_load_dwordx4 v9, s[72:75], s70 offen lds\n"
        "s_waitcnt lgkmcnt(5)\n"
        "v_mfma_f32_32x32x16_bf16 v[64:79], a[96:99], v[48:51], v[64:79]\n"
        "ds_read_b128 v[40:43], v2 offset:512\n"
        "s_add_u32 m0, s71, 49152\n"
        "v_mfma_f32_32x32x16_bf16 v[80:95], a[112:115], v[48:51], v[80:95]\n"
        "s_nop 0\n"
        "buffer_load_dwordx4 v10, s[72:75], s70 offen lds\n"
        "s_waitcnt lgkmcnt(5)\n"
        "v_mfma_f32_32x32x16_bf16 v[64:79], a[100:103], v[52:55], v[64:79]\n"
        "ds_read_b128 v[44:47], v3 offset:512\n"
        "s_add_u32 m0, s71, 53248\n"
        "v_mfma_f32_32x32x16_bf16 v[80:95], a[116:119], v[52:55], v[80:95]\n"
        "s_nop 0\n"
        "buffer_load_dwordx4 v11, s[72:75], s70 offen lds\n"
        "s_waitcnt lgkmcnt(5)\n"
        "v_mfma_f32_32x32x16_bf16 v[64:79], a[104:107], v[56:59], v[64:79]\n"
        "ds_read_b128 v[48:51], v4 offset:512\n"
        "s_add_u32 m0, s71, 57344\n"
        "v_mfma_f32_32x32x16_bf16 v[80:95], a[120:123], v[56:59], v[80:95]\n"
        "s_nop 0\n"
        "buffer_load_dwordx4 v12, s[72:75], s70 offen lds\n"
        "s_waitcnt lgkmcnt(5)\n"
        "v_mfma_f32_32x32x16_bf16 v[64:79], a[108:111], v[60:63], v[64:79]\n"
        "ds_read_b128 v[52:55], v5 offset:512\n"
        "s_add_u32 m0, s71, 61440\n"
        "v_mfma_f32_32x32x16_bf16 v[80:95], a[124:127], v[60:63], v[80:95]\n"
        "s_nop 0\n"
        "buffer_load_dwordx4 v13, s[72:75], s70 offen lds\n"
        "s_waitcnt lgkmcnt(5)\n"
        "v_mfma_f32_32x32x16_bf16 v[64:79], a[128:131], v[32:35], v[64:79]\n"
        "ds_read_b128 v[56:59], v6 offset:512\n"
        "s_add_u32 m0, s71, 65536\n"
        "v_mfma_f32_32x32x16_bf16 v[80:95], a[144:147], v[32:35], v[80:95]\n"
        "s_nop 0\n"
        "buffer_load_dwordx4 v14, s[72:75], s70 offen lds\n"
        "s_waitcnt lgkmcnt(5)\n"
        "v_mfma_f32_32x32x16_bf16 v[64:79], a[132:135], v[36:39], v[64:79]\n"
        "ds_read_b128 v[60:63], v7 offset:512\n"
        "s_add_u32 m0, s71, 69632\n"
        "v_mfma_f32_32x32x16_bf16 v[80:95], a[148:151], v[36:39], v[80:95]\n"
        "s_nop 0\n"
        "buffer_load_dwordx4 v15, s[72:75], s70 offen lds\n"
        "s_waitcnt lgkmcnt(5)\n"
        "v_mfma_f32_32x32x16_bf16 v[64:79], a[136:139], v[40:43], v[64:79]\n"
        "ds_read_b128 v[32:35], v0 offset:768\n"
        "s_add_u32 m0, s71, 73728\n"
        "v_mfma_f32_32x32x16_bf16 v[80:95], a[152:155], v[40:43], v[80:95]\n"
        "s_nop 0\n"
        "buffer_load_dwordx4 v16, s[72:75], s70 offen lds\n"
        "s_waitcnt lgkmcnt(5)\n"
        "v_mfma_f32_32x32x16_bf16 v[64:79], a[140:143], v[44:47], v[64:79]\n"
        "ds_read_b128 v[36:39], v1 offset:768\n"
        "s_add_u32 m0, s71, 77824\n"
        "v_mfma_f32_32x32x16_bf16 v[80:95], a[156:159], v[44:47], v[80:95]\n"
        "s_nop 0\n"
        "buffer_load_dwordx4 v17, s[72:75], s70 offen lds\n"
        "s_waitcnt lgkmcnt(5)\n"
        "v_mfma_f32_32x32x16_bf16 v[64:79], a[160:163], v[48:51], v[64:79]\n"
        "ds_read_b128 v[40:43], v2 offset:768\n"
        "v_mfma_f32_32x32x16_bf16 v[80:95], a[176:179], v[48:51], v[80:95]\n"
        "s_waitcnt lgkmcnt(5)\n"
        "v_mfma_f32_32x32x16_bf16 v[64:79], a[164:167], v[52:55], v[64:79]\n"
        "ds_read_b128 v[44:47], v3 offset:768\n"
        "v_mfma_f32_32x32x16_bf16 v[80:95], a[180:183], v[52:55], v[80:95]\n"
        "s_waitcnt lgkmcnt(5)\n"
        "v_mfma_f32_32x32x16_bf16 v[64:79], a[168:171], v[56:59], v[64:79]\n"
        "ds_read_b128 v[48:51], v4 offset:768\n"
        "v_mfma_f32_32x32x16_bf16 v[80:95], a[184:187], v[56:59], v[80:95]\n"
        "s_waitcnt lgkmcnt(5)\n"
        "v_mfma_f32_32x32x16_bf16 v[64:79], a[172:175], v[60:63], v[64:79]\n"
        "ds_read_b128 v[52:55], v5 offset:768\n"
        "v_mfma_f32_32x32x16_bf16 v[80:95], a[188:191], v[60:63], v[80:95]\n"
        "s_waitcnt lgkmcnt(5)\n"
        "v_mfma_f32_32x32x16_bf16 v[64:79], a[192:195], v[32:35], v[64:79]\n"
        "ds_read_b128 v[56:59], v6 offset:768\n"
        "v_mfma_f32_32x32x16_bf16 v[80:95], a[208:211], v[32:35], v[80:95]\n"
        "s_waitcnt lgkmcnt(5)\n"
        "v_mfma_f32_32x32x16_bf16 v[64:79], a[196:199], v[36:39], v[64:79]\n"
        "ds_read_b128 v[60:63], v7 offset:768\n"
        "s_waitcnt vmcnt(10)\n"
        "v_mfma_f32_32x32x16_bf16 v[80:95], a[212:215], v[36:39], v[80:95]\n"
        "buffer_store_dword v20, v21, s[60:63], s69 offen sc1\n"
        "s_waitcnt lgkmcnt(5)\n"
        "v_mfma_f32_32x32x16_bf16 v[64:79], a[200:203], v[40:43], v[64:79]\n"
        "ds_read_b128 v[32:35], v0 offset:1024\n"
        "v_mfma_f32_32x32x16_bf16 v[80:95], a[216:219], v[40:43], v[80:95]\n"
        "s_waitcnt lgkmcnt(5)\n"
        "v_mfma_f32_32x32x16_bf16 v[64:79], a[204:207], v[44:47], v[64:79]\n"
        "ds_read_b128 v[36:39], v1 offset:1024\n"
        "v_mfma_f32_32x32x16_bf16 v[80:95], a[220:223], v[44:47], v[80:95]\n"
        "s_waitcnt lgkmcnt(5)\n"
        "v_mfma_f32_32x32x16_bf16 v[64:79], a[224:227], v[48:51], v[64:79]\n"
        "ds_read_b128 v[40:43], v2 offset:1024\n"
        "v_mfma_f32_32x32x16_bf16 v[80:95], a[240:243], v[48:51], v[80:95]\n"
        "s_waitcnt lgkmcnt(5)\n"
        "v_mfma_f32_32x32x16_bf16 v[64:79], a[228:231], v[52:55], v[64:79]\n"
        "ds_read_b128 v[44:47], v3 offset:1024\n"
        "v_mfma_f32_32x32x16_bf16 v[80:95], a[244:247], v[52:55], v[80:95]\n"
        "s_waitcnt lgkmcnt(5)\n"
        "v_mfma_f32_32x32x16_bf16 v[64:79], a[232:235], v[56:59], v[64:79]\n"
        "ds_read_b128 v[48:51], v4 offset:1024\n"
        "v_mfma_f32_32x32x16_bf16 v[80:95], a[248:251], v[56:59], v[80:95]\n"
        "s_waitcnt lgkmcnt(5)\n"
        "v_mfma_f32_32x32x16_bf16 v[64:79], a[236:239], v[60:63], v[64:79]\n"
        "ds_read_b128 v[52:55], v5 offset:1024\n"
        "v_mfma_f32_32x32x16_bf16 v[80:95], a[252:255], v[60:63], v[80:95]\n"
        "s_waitcnt lgkmcnt(5)\n"
        "v_mfma_f32_32x32x16_bf16 v[64:79], v[192:195], v[32:35], v[64:79]\n"
        "ds_read_b128 v[56:59], v6 offset:1024\n"
        "v_mfma_f32_32x32x16_bf16 v[80:95], v[208:211], v[32:35], v[80:95]\n"
        "s_waitcnt lgkmcnt(5)\n"
        "v_mfma_f32_32x32x16_bf16 v[64:79], v[196:199], v[36:39], v[64:79]\n"
        "ds_read_b128 v[60:63], v7 offset:1024\n"
        "v_mfma_f32_32x32x16_bf16 v[80:95], v[212:215], v[36:39], v[80:95]\n"
        "s_waitcnt lgkmcnt(5)\n"
        "v_mfma_f32_32x32x16_bf16 v[64:79], v[200:203], v[40:43], v[64:79]\n"
        "v_mfma_f32_32x32x16_bf16 v[80:95], v[216:219], v[40:43], v[80:95]\n"
        "s_waitcnt lgkmcnt(4)\n"
        "v_mfma_f32_32x32x16_bf16 v[64:79], v[204:207], v[44:47], v[64:79]\n"
        "v_mfma_f32_32x32x16_bf16 v[80:95], v[220:223], v[44:47], v[80:95]\n"
        "s_waitcnt vmcnt(1) lgkmcnt(0)\n"
        "s_barrier\n"
        "v_mfma_f32_32x32x16_bf16 v[64:79], v[224:227], v[48:51], v[64:79]\n"
        "ds_read_b128 v[32:35], v0 offset:40960\n"
        "ds_read_b128 v[36:39], v1 offset:40960\n"
        "v_mfma_f32_32x32x16_bf16 v[80:95], v[240:243], v[48:51], v[80:95]\n"
        "ds_read_b128 v[40:43], v2 offset:40960\n"
        "v_mfma_f32_32x32x16_bf16 v[64:79], v[228:231], v[52:55], v[64:79]\n"
        "ds_read_b128 v[44:47], v3 offset:40960\n"
        "v_mfma_f32_32x32x16_bf16 v[80:95], v[244:247], v[52:55], v[80:95]\n"
        "v_mfma_f32_32x32x16_bf16 v[64:79], v[232:235], v[56:59], v[64:79]\n"
        "ds_read_b128 v[48:51], v4 offset:40960\n"
        "v_mfma_f32_32x32x16_bf16 v[80:95], v[248:251], v[56:59], v[80:95]\n"
        "v_mfma_f32_32x32x16_bf16 v[64:79], v[236:239], v[60:63], v[64:79]\n"
        "ds_read_b128 v[52:55], v5 offset:40960\n"
        "v_mfma_f32_32x32x16_bf16 v[80:95], v[252:255], v[60:63], v[80:95]\n"
        "s_mul_i32 s68, s64, 262144\n"
        "s_add_u32 s68, s68, 5242880\n"
        "s_mul_i32 s69, s64, 512\n"
        "s_add_u32 s69, s69, s77\n"
        "s_add_u32 s70, s70, 40960\n"
        "s_add_u32 s64, s64, 1\n"
        "s_add_u32 s76, s68, 16777216\n"
        "s_cmp_lt_u32 s64, 64\n"
        "s_cbranch_scc0 QK_end\n"
        "QK_p1:\n"
        "s_waitcnt lgkmcnt(5)\n"
        "v_mfma_f32_32x32x16_bf16 v[96:111], a[0:3], v[32:35], 0\n"
        "ds_read_b128 v[56:59], v6 offset:40960\n"
        "v_mfma_f32_32x32x16_bf16 v[112:127], a[16:19], v[32:35], 0\n"
        "s_waitcnt lgkmcnt(5)\n"
        "v_mfma_f32_32x32x16_bf16 v[96:111], a[4:7], v[36:39], v[96:111]\n"
        "ds_read_b128 v[60:63], v7 offset:40960\n"
        "v_cvt_pk_bf16_f32 v128, v64, v65\n"
        "v_cvt_pk_bf16_f32 v129, v66, v67\n"
        "v_mfma_f32_32x32x16_bf16 v[112:127], a[20:23], v[36:39], v[112:127]\n"
        "v_cvt_pk_bf16_f32 v130, v68, v69\n"
        "v_cvt_pk_bf16_f32 v131, v70, v71\n"
        "s_waitcnt lgkmcnt(5)\n"
        "v_mfma_f32_32x32x16_bf16 v[96:111], a[8:11], v[40:43], v[96:111]\n"
        "ds_read_b128 v[32:35], v0 offset:41216\n"
        "buffer_store_dwordx4 v[128:131], v18, s[60:63], s68 offen offset:0 sc1\n"
        "v_cvt_pk_bf16_f32 v132, v72, v73\n"
        "v_mfma_f32_32x32x16_bf16 v[112:127], a[24:27], v[40:43], v[112:127]\n"
        "v_cvt_pk_bf16_f32 v133, v74, v75\n"
        "v_cvt_pk_bf16_f32 v134, v76, v77\n"
        "s_waitcnt lgkmcnt(5)\n"
        "v_mfma_f32_32x32x16_bf16 v[96:111], a[12:15], v[44:47], v[96:111]\n"
        "ds_read_b128 v[36:39], v1 offset:41216\n"
        "v_cvt_pk_bf16_f32 v135, v78, v79\n"
        "buffer_store_dwordx4 v[132:135], v18, s[60:63], s68 offen offset:1024 sc1\n"
        "v_mfma_f32_32x32x16_bf16 v[112:127], a[28:31], v[44:47], v[112:127]\n"
        "v_cvt_pk_bf16_f32 v136, v80, v81\n"
        "v_cvt_pk_bf16_f32 v137, v82, v83\n"
        "s_waitcnt lgkmcnt(5)\n"
        "v_mfma_f32_32x32x16_bf16 v[96:111], a[32:35], v[48:51], v[96:111]\n"
        "ds_read_b128 v[40:43], v2 offset:41216\n"
        "v_cvt_pk_bf16_f32 v138, v84, v85\n"
        "v_cvt_pk_bf16_f32 v139, v86, v87\n"
        "v_mfma_f32_32x32x16_bf16 v[112:127], a[48:51], v[48:51], v[112:127]\n"
        "buffer_store_dwordx4 v[136:139], v18, s[60:63], s76 offen offset:0 sc1\n"
        "v_cvt_pk_bf16_f32 v140, v88, v89\n"
        "s_waitcnt lgkmcnt(5)\n"
        "v_mfma_f32_32x32x16_bf16 v[96:111], a[36:39], v[52:55], v[96:111]\n"
        "ds_read_b128 v[44:47], v3 offset:41216\n"
        "v_cvt_pk_bf16_f32 v141, v90, v91\n"
        "v_cvt_pk_bf16_f32 v142, v92, v93\n"
        "v_mfma_f32_32x32x16_bf16 v[112:127], a[52:55], v[52:55], v[112:127]\n"
        "v_cvt_pk_bf16_f32 v143, v94, v95\n"
        "buffer_store_dwordx4 v[140:143], v18, s[60:63], s76 offen offset:1024 sc1\n"
        "s_waitcnt lgkmcnt(5)\n"
        "v_mfma_f32_32x32x16_bf16 v[96:111], a[40:43], v[56:59], v[96:111]\n"
        "ds_read_b128 v[48:51], v4 offset:41216\n"
        "v_mfma_f32_32x32x16_bf16 v[112:127], a[56:59], v[56:59], v[112:127]\n"
        "s_waitcnt lgkmcnt(5)\n"
        "v_mfma_f32_32x32x16_bf16 v[96:111], a[44:47], v[60:63], v[96:111]\n"
        "ds_read_b128 v[52:55], v5 offset:41216\n"
        "v_mfma_f32_32x32x16_bf16 v[112:127], a[60:63], v[60:63], v[112:127]\n"
        "s_waitcnt lgkmcnt(5)\n"
        "v_mfma_f32_32x32x16_bf16 v[96:111], a[64:67], v[32:35], v[96:111]\n"
        "ds_read_b128 v[56:59], v6 offset:41216\n"
        "v_mfma_f32_32x32x16_bf16 v[112:127], a[80:83], v[32:35], v[112:127]\n"
        "s_waitcnt lgkmcnt(5)\n"
        "v_mfma_f32_32x32x16_bf16 v[96:111], a[68:71], v[36:39], v[96:111]\n"
        "ds_read_b128 v[60:63], v7 offset:41216\n"
        "v_mfma_f32_32x32x16_bf16 v[112:127], a[84:87], v[36:39], v[112:127]\n"
        "s_waitcnt lgkmcnt(5)\n"
        "v_mfma_f32_32x32x16_bf16 v[96:111], a[72:75], v[40:43], v[96:111]\n"
        "ds_read_b128 v[32:35], v0 offset:41472\n"
        "s_add_u32 m0, s71, 0\n"
        "v_mfma_f32_32x32x16_bf16 v[112:127], a[88:91], v[40:43], v[112:127]\n"
        "s_nop 0\n"
        "buffer_load_dwordx4 v8, s[72:75], s70 offen lds\n"
        "s_waitcnt lgkmcnt(5)\n"
        "v_mfma_f32_32x32x16_bf16 v[96:111], a[76:79], v[44:47], v[96:111]\n"
        "ds_read_b128 v[36:39], v1 offset:41472\n"
        "s_add_u32 m0, s71, 4096\n"
        "v_mfma_f32_32x32x16_bf16 v[112:127], a[92:95], v[44:47], v[112:127]\n"
        "s_nop 0\n"
        "buffer_load_dwordx4 v9, s[72:75], s70 offen lds\n"
        "s_waitcnt lgkmcnt(5)\n"
        "v_mfma_f32_32x32x16_bf16 v[96:111], a[96:99], v[48:51], v[96:111]\n"
        "ds_read_b128 v[40:43], v2 offset:41472\n"
        "s_add_u32 m0, s71, 8192\n"
        "v_mfma_f32_32x32x16_bf16 v[112:127], a[112:115], v[48:51], v[112:127]\n"
        "s_nop 0\n"
        "buffer_load_dwordx4 v10, s[72:75], s70 offen lds\n"
        "s_waitcnt lgkmcnt(5)\n"
        "v_mfma_f32_32x32x16_bf16 v[96:111], a[100:103], v[52:55], v[96:111]\n"
        "ds_read_b128 v[44:47], v3 offset:41472\n"
        "s_add_u32 m0, s71, 12288\n"
        "v_mfma_f32_32x32x16_bf16 v[112:127], a[116:119], v[52:55], v[112:127]\n"
        "s_nop 0\n"
        "buffer_load_dwordx4 v11, s[72:75], s70 offen lds\n"
        "s_waitcnt lgkmcnt(5)\n"
        "v_mfma_f32_32x32x16_bf16 v[96:111], a[104:107], v[56:59], v[96:111]\n"
        "ds_read_b128 v[48:51], v4 offset:41472\n"
        "s_add_u32 m0, s71, 16384\n"
        "v_mfma_f32_32x32x16_bf16 v[112:127], a[120:123], v[56:59], v[112:127]\n"
        "s_nop 0\n"
        "buffer_load_dwordx4 v12, s[72:75], s70 offen lds\n"
        "s_waitcnt lgkmcnt(5)\n"
        "v_mfma_f32_32x32x16_bf16 v[96:111], a[108:111], v[60:63], v[96:111]\n"
        "ds_read_b128 v[52:55], v5 offset:41472\n"
        "s_add_u32 m0, s71, 20480\n"
        "v_mfma_f32_32x32x16_bf16 v[112:127], a[124:127], v[60:63], v[112:127]\n"
        "s_nop 0\n"
        "buffer_load_dwordx4 v13, s[72:75], s70 offen lds\n"
        "s_waitcnt lgkmcnt(5)\n"
        "v_mfma_f32_32x32x16_bf16 v[96:111], a[128:131], v[32:35], v[96:111]\n"
        "ds_read_b128 v[56:59], v6 offset:41472\n"
        "s_add_u32 m0, s71, 24576\n"
        "v_mfma_f32_32x32x16_bf16 v[112:127], a[144:147], v[32:35], v[112:127]\n"
        "s_nop 0\n"
        "buffer_load_dwordx4 v14, s[72:75], s70 offen lds\n"
        "s_waitcnt lgkmcnt(5)\n"
        "v_mfma_f32_32x32x16_bf16 v[96:111], a[132:135], v[36:39], v[96:111]\n"
        "ds_read_b128 v[60:63], v7 offset:41472\n"
        "s_add_u32 m0, s71, 28672\n"
        "v_mfma_f32_32x32x16_bf16 v[112:127], a[148:151], v[36:39], v[112:127]\n"
        "s_nop 0\n"
        "buffer_load_dwordx4 v15, s[72:75], s70 offen lds\n"
        "s_waitcnt lgkmcnt(5)\n"
        "v_mfma_f32_32x32x16_bf16 v[96:111], a[136:139], v[40:43], v[96:111]\n"
        "ds_read_b128 v[32:35], v0 offset:41728\n"
        "s_add_u32 m0, s71, 32768\n"
        "v_mfma_f32_32x32x16_bf16 v[112:127], a[152:155], v[40:43], v[112:127]\n"
        "s_nop 0\n"
        "buffer_load_dwordx4 v16, s[72:75], s70 offen lds\n"
        "s_waitcnt lgkmcnt(5)\n"
        "v_mfma_f32_32x32x16_bf16 v[96:111], a[140:143], v[44:47], v[96:111]\n"
        "ds_read_b128 v[36:39], v1 offset:41728\n"
        "s_add_u32 m0, s71, 36864\n"
        "v_mfma_f32_32x32x16_bf16 v[112:127], a[156:159], v[44:47], v[112:127]\n"
        "s_nop 0\n"
        "buffer_load_dwordx4 v17, s[72:75], s70 offen lds\n"
        "s_waitcnt lgkmcnt(5)\n"
        "v_mfma_f32_32x32x16_bf16 v[96:111], a[160:163], v[48:51], v[96:111]\n"
        "ds_read_b128 v[40:43], v2 offset:41728\n"
        "v_mfma_f32_32x32x16_bf16 v[112:127], a[176:179], v[48:51], v[112:127]\n"
        "s_waitcnt lgkmcnt(5)\n"
        "v_mfma_f32_32x32x16_bf16 v[96:111], a[164:167], v[52:55], v[96:111]\n"
        "ds_read_b128 v[44:47], v3 offset:41728\n"
        "v_mfma_f32_32x32x16_bf16 v[112:127], a[180:183], v[52:55], v[112:127]\n"
        "s_waitcnt lgkmcnt(5)\n"
        "v_mfma_f32_32x32x16_bf16 v[96:111], a[168:171], v[56:59], v[96:111]\n"
        "ds_read_b128 v[48:51], v4 offset:41728\n"
        "v_mfma_f32_32x32x16_bf16 v[112:127], a[184:187], v[56:59], v[112:127]\n"
        "s_waitcnt lgkmcnt(5)\n"
        "v_mfma_f32_32x32x16_bf16 v[96:111], a[172:175], v[60:63], v[96:111]\n"
        "ds_read_b128 v[52:55], v5 offset:41728\n"
        "v_mfma_f32_32x32x16_bf16 v[112:127], a[188:191], v[60:63], v[112:127]\n"
        "s_waitcnt lgkmcnt(5)\n"
        "v_mfma_f32_32x32x16_bf16 v[96:111], a[192:195], v[32:35], v[96:111]\n"
        "ds_read_b128 v[56:59], v6 offset:41728\n"
        "v_mfma_f32_32x32x16_bf16 v[112:127], a[208:211], v[32:35], v[112:127]\n"
        "s_waitcnt lgkmcnt(5)\n"
        "v_mfma_f32_32x32x16_bf16 v[96:111], a[196:199], v[36:39], v[96:111]\n"
        "ds_read_b128 v[60:63], v7 offset:41728\n"
        "s_waitcnt vmcnt(10)\n"
        "v_mfma_f32_32x32x16_bf16 v[112:127], a[212:215], v[36:39], v[112:127]\n"
        "buffer_store_dword v20, v21, s[60:63], s69 offen sc1\n"
        "s_waitcnt lgkmcnt(5)\n"
        "v_mfma_f32_32x32x16_bf16 v[96:111], a[200:203], v[40:43], v[96:111]\n"
        "ds_read_b128 v[32:35], v0 offset:41984\n"
        "v_mfma_f32_32x32x16_bf16 v[112:127], a[216:219], v[40:43], v[112:127]\n"
        "s_waitcnt lgkmcnt(5)\n"
        "v_mfma_f32_32x32x16_bf16 v[96:111], a[204:207], v[44:47], v[96:111]\n"
        "ds_read_b128 v[36:39], v1 offset:41984\n"
        "v_mfma_f32_32x32x16_bf16 v[112:127], a[220:223], v[44:47], v[112:127]\n"
        "s_waitcnt lgkmcnt(5)\n"
        "v_mfma_f32_32x32x16_bf16 v[96:111], a[224:227], v[48:51], v[96:111]\n"
        "ds_read_b128 v[40:43], v2 offset:41984\n"
        "v_mfma_f32_32x32x16_bf16 v[112:127], a[240:243], v[48:51], v[112:127]\n"
        "s_waitcnt lgkmcnt(5)\n"
        "v_mfma_f32_32x32x16_bf16 v[96:111], a[228:231], v[52:55], v[96:111]\n"
        "ds_read_b128 v[44:47], v3 offset:41984\n"
        "v_mfma_f32_32x32x16_bf16 v[112:127], a[244:247], v[52:55], v[112:127]\n"
        "s_waitcnt lgkmcnt(5)\n"
        "v_mfma_f32_32x32x16_bf16 v[96:111], a[232:235], v[56:59], v[96:111]\n"
        "ds_read_b128 v[48:51], v4 offset:41984\n"
        "v_mfma_f32_32x32x16_bf16 v[112:127], a[248:251], v[56:59], v[112:127]\n"
        "s_waitcnt lgkmcnt(5)\n"
        "v_mfma_f32_32x32x16_bf16 v[96:111], a[236:239], v[60:63], v[96:111]\n"
        "ds_read_b128 v[52:55], v5 offset:41984\n"
        "v_mfma_f32_32x32x16_bf16 v[112:127], a[252:255], v[60:63], v[112:127]\n"
        "s_waitcnt lgkmcnt(5)\n"
        "v_mfma_f32_32x32x16_bf16 v[96:111], v[192:195], v[32:35], v[96:111]\n"
        "ds_read_b128 v[56:59], v6 offset:41984\n"
        "v_mfma_f32_32x32x16_bf16 v[112:127], v[208:211], v[32:35], v[112:127]\n"
        "s_waitcnt lgkmcnt(5)\n"
        "v_mfma_f32_32x32x16_bf16 v[96:111], v[196:199], v[36:39], v[96:111]\n"
        "ds_read_b128 v[60:63], v7 offset:41984\n"
        "v_mfma_f32_32x32x16_bf16 v[112:127], v[212:215], v[36:39], v[112:127]\n"
        "s_waitcnt lgkmcnt(5)\n"
        "v_mfma_f32_32x32x16_bf16 v[96:111], v[200:203], v[40:43], v[96:111]\n"
        "v_mfma_f32_32x32x16_bf16 v[112:127], v[216:219], v[40:43], v[112:127]\n"
        "s_waitcnt lgkmcnt(4)\n"
        "v_mfma_f32_32x32x16_bf16 v[96:111], v[204:207], v[44:47], v[96:111]\n"
        "v_mfma_f32_32x32x16_bf16 v[112:127], v[220:223], v[44:47], v[112:127]\n"
        "s_waitcnt vmcnt(1) lgkmcnt(0)\n"
        "s_barrier\n"
        "v_mfma_f32_32x32x16_bf16 v[96:111], v[224:227], v[48:51], v[96:111]\n"
        "ds_read_b128 v[32:35], v0 offset:0\n"
        "ds_read_b128 v[36:39], v1 offset:0\n"
        "v_mfma_f32_32x32x16_bf16 v[112:127], v[240:243], v[48:51], v[112:127]\n"
        "ds_read_b128 v[40:43], v2 offset:0\n"
        "v_mfma_f32_32x32x16_bf16 v[96:111], v[228:231], v[52:55], v[96:111]\n"
        "ds_read_b128 v[44:47], v3 offset:0\n"
        "v_mfma_f32_32x32x16_bf16 v[112:127], v[244:247], v[52:55], v[112:127]\n"
        "v_mfma_f32_32x32x16_bf16 v[96:111], v[232:235], v[56:59], v[96:111]\n"
        "ds_read_b128 v[48:51], v4 offset:0\n"
        "v_mfma_f32_32x32x16_bf16 v[112:127], v[248:251], v[56:59], v[112:127]\n"
        "v_mfma_f32_32x32x16_bf16 v[96:111], v[236:239], v[60:63], v[96:111]\n"
        "ds_read_b128 v[52:55], v5 offset:0\n"
        "v_mfma_f32_32x32x16_bf16 v[112:127], v[252:255], v[60:63], v[112:127]\n"
        "s_mul_i32 s68, s64, 262144\n"
        "s_add_u32 s68, s68, 5242880\n"
        "s_mul_i32 s69, s64, 512\n"
        "s_add_u32 s69, s69, s77\n"
        "s_add_u32 s70, s70, 40960\n"
        "s_add_u32 s64, s64, 1\n"
        "s_add_u32 s76, s68, 16777216\n"
        "s_branch QK_loop\n"
        "QK_end:\n"
        "s_nop 7\n"
        "s_nop 7\n"
        "v_cvt_pk_bf16_f32 v128, v64, v65\n"
        "v_cvt_pk_bf16_f32 v129, v66, v67\n"
        "v_cvt_pk_bf16_f32 v130, v68, v69\n"
        "v_cvt_pk_bf16_f32 v131, v70, v71\n"
        "buffer_store_dwordx4 v[128:131], v18, s[60:63], s68 offen offset:0 sc1\n"
        "v_cvt_pk_bf16_f32 v132, v72, v73\n"
        "v_cvt_pk_bf16_f32 v133, v74, v75\n"
        "v_cvt_pk_bf16_f32 v134, v76, v77\n"
        "v_cvt_pk_bf16_f32 v135, v78, v79\n"
        "buffer_store_dwordx4 v[132:135], v18, s[60:63], s68 offen offset:1024 sc1\n"
        "v_cvt_pk_bf16_f32 v136, v80, v81\n"
        "v_cvt_pk_bf16_f32 v137, v82, v83\n"
        "v_cvt_pk_bf16_f32 v138, v84, v85\n"
        "v_cvt_pk_bf16_f32 v139, v86, v87\n"
        "buffer_store_dwordx4 v[136:139], v18, s[60:63], s76 offen offset:0 sc1\n"
        "v_cvt_pk_bf16_f32 v140, v88, v89\n"
        "v_cvt_pk_bf16_f32 v141, v90, v91\n"
        "v_cvt_pk_bf16_f32 v142, v92, v93\n"
        "v_cvt_pk_bf16_f32 v143, v94, v95\n"
        "buffer_store_dwordx4 v[140:143], v18, s[60:63], s76 offen offset:1024 sc1\n"
        "s_waitcnt vmcnt(0)\n"
        "buffer_store_dword v20, v21, s[60:63], s69 offen sc1\n"
        "s_endpgm\n"
        ROLE_OPS(p.wq, p.wk) : "v32", "v33", "v34", "v35", "v36", "v37", "v38", "v39", "v40", "v41", "v42", "v43", "v44", "v45", "v46", "v47", "v48", "v49", "v50", "v51", "v52", "v53", "v54", "v55", "v56", "v57", "v58", "v59", "v60", "v61", "v62", "v63", "v64", "v65", "v66", "v67", "v68", "v69", "v70", "v71", "v72", "v73", "v74", "v75", "v76", "v77", "v78", "v79", "v80", "v81", "v82", "v83", "v84", "v85", "v86", "v87", "v88", "v89", "v90", "v91", "v92", "v93", "v94", "v95", "v96", "v97", "v98", "v99", "v100", "v101", "v102", "v103", "v104", "v105", "v106", "v107", "v108", "v109", "v110", "v111", "v112", "v113", "v114", "v115", "v116", "v117", "v118", "v119", "v120", "v121", "v122", "v123", "v124", "v125", "v126", "v127", "v128", "v129", "v130", "v131", "v132", "v133", "v134", "v135", "v136", "v137", "v138", "v139", "v140", "v141", "v142", "v143", "v144", "v145", "v146", "v147", "v148", "v149", "v150", "v151", "v152", "v153", "v154", "v155", "v156", "v157", "v158", "v159", "v160", "v161", "v162", "v163", "v164", "v165", "v166", "v167", "v168", "v169", "v170", "v171", "v172", "v173", "v174", "v175", "v176", "v177", "v178", "v179", "v180", "v181", "v182", "v183", "v184", "v185", "v186", "v187", "v188", "v189", "v190", "v192", "v193", "v194", "v195", "v196", "v197", "v198", "v199", "v200", "v201", "v202", "v203", "v204", "v205", "v206", "v207", "v208", "v209", "v210", "v211", "v212", "v213", "v214", "v215", "v216", "v217", "v218", "v219", "v220", "v221", "v222", "v223", "v224", "v225", "v226", "v227", "v228", "v229", "v230", "v231", "v232", "v233", "v234", "v235", "v236", "v237", "v238", "v239", "v240", "v241", "v242", "v243", "v244", "v245", "v246", "v247", "v248", "v249", "v250", "v251", "v252", "v253", "v254", "v255", "s38", "s39", "s40", "s41", "s42", "s43", "s44", "s45", "s46", "s47", "s48", "s49", "s50", "s51", "s52", "s53", "s54", "s55", "s56", "s57", "s58", "s59", "s60", "s61", "s62", "s63", "s64", "s65", "s66", "s67", "s68", "s69", "s70", "s71", "s72", "s73", "s74", "s75", "s76", "s77", "s78", "s79", "s80", "s81", "s82", "s83", "s84", "s85", "s86", "s87", "s88", "s89", "s90", "s91", "s92", "s93", "s94", "s95", "s96", "s97", "s98", "s99", "vcc", "scc", "memory", "m0", "a0", "a1", "a2", "a3", "a4", "a5", "a6", "a7", "a8", "a9", "a10", "a11", "a12", "a13", "a14", "a15", "a16", "a17", "a18", "a19", "a20", "a21", "a22", "a23", "a24", "a25", "a26", "a27", "a28", "a29", "a30", "a31", "a32", "a33", "a34", "a35", "a36", "a37", "a38", "a39", "a40", "a41", "a42", "a43", "a44", "a45", "a46", "a47", "a48", "a49", "a50", "a51", "a52", "a53", "a54", "a55", "a56", "a57", "a58", "a59", "a60", "a61", "a62", "a63", "a64", "a65", "a66", "a67", "a68", "a69", "a70", "a71", "a72", "a73", "a74", "a75", "a76", "a77", "a78", "a79", "a80", "a81", "a82", "a83", "a84", "a85", "a86", "a87", "a88", "a89", "a90", "a91", "a92", "a93", "a94", "a95", "a96", "a97", "a98", "a99", "a100", "a101", "a102", "a103", "a104", "a105", "a106", "a107", "a108", "a109", "a110", "a111", "a112", "a113", "a114", "a115", "a116", "a117", "a118", "a119", "a120", "a121", "a122", "a123", "a124", "a125", "a126", "a127", "a128", "a129", "a130", "a131", "a132", "a133", "a134", "a135", "a136", "a137", "a138", "a139", "a140", "a141", "a142", "a143", "a144", "a145", "a146", "a147", "a148", "a149", "a150", "a151", "a152", "a153", "a154", "a155", "a156", "a157", "a158", "a159", "a160", "a161", "a162", "a163", "a164", "a165", "a166", "a167", "a168", "a169", "a170", "a171", "a172", "a173", "a174", "a175", "a176", "a177", "a178", "a179", "a180", "a181", "a182", "a183", "a184", "a185", "a186", "a187", "a188", "a189", "a190", "a191", "a192", "a193", "a194", "a195", "a196", "a197", "a198", "a199", "a200", "a201", "a202", "a203", "a204", "a205", "a206", "a207", "a208", "a209", "a210", "a211", "a212", "a213", "a214", "a215", "a216", "a217", "a218", "a219", "a220", "a221", "a222", "a223", "a224", "a225", "a226", "a227", "a228", "a229", "a230", "a231", "a232", "a233", "a234", "a235", "a236", "a237", "a238", "a239", "a240", "a241", "a242", "a243", "a244", "a245", "a246", "a247", "a248", "a249", "a250", "a251", "a252", "a253", "a254", "a255");
    __builtin_unreachable();
}

DI u32 naps(u32 n, u32 t0);
DI void role_vh(const Params& p, rsrc_t rs, int id, int wave, u32 t0) {
}

DI void role_wvb(const Params& p, rsrc_t rs, int h, int wave) {
    const int lane = lane_id(), tid = wave * 64 + lane;
    u32x32 ad = {}; xaddr(ad, tid, lane); dense_stage_addr(ad, tid, wave, lane);
    ad[18] = (2 * wave + ((lane >> 4) & 1)) * 32768 + h * 1024 + (lane >> 5) * 256 + (lane & 15) * 16;
    ad[19] = 81920 + wave * 2048 + lane * 16;
    ad[23] = 81920 + lane * 16;
    asm volatile(
        "s_mov_b32 s60, %[r0]\n"
        "s_mov_b32 s61, %[r1]\n"
        "s_mov_b32 s62, 0x0f000000\n"
        "s_mov_b32 s63, 0x00020000\n"
        "s_mov_b32 s67, %[wave]\n"
        "s_mov_b32 s58, %[h]\n"
        "s_mov_b32 s59, %[ts]\n"
        "s_mov_b32 s66, 0x100000\n"
        "s_mov_b32 s64, 0\n"
        "s_lshl_b32 s71, s67, 10\n"
        "s_mov_b32 s72, %[x0]\n"
        "s_mov_b32 s73, %[x1]\n"
        "s_mov_b32 s74, 2621440\n"
        "s_mov_b32 s75, 0x00020000\n"
        "s_mov_b32 s70, 0\n"
        "s_mov_b32 s68, 4026531840\n"
        "s_mov_b32 s69, 4026531840\n"
        "s_mov_b32 s76, 4026531840\n"
        "s_mov_b32 s70, 40960\n"
        "s_mov_b32 s80, %[a0]\n"
        "s_mov_b32 s81, %[a1]\n"
        "s_mov_b32 s82, 5242880\n"
        "s_mov_b32 s83, 0x00020000\n"
        "s_mov_b32 s84, %[b0]\n"
        "s_mov_b32 s85, %[b1]\n"
        "s_mov_b32 s86, s82\n"
        "s_mov_b32 s87, 0x00020000\n"
        "s_lshl_b32 s78, s58, 8\n"
        "s_mul_i32 s79, s58, 163840\n"
        "s_add_u32 s88, %[hs], 4333568\n"
        "s_add_u32 s89, %[hs], 4325376\n"
        "s_add_u32 s90, %[hs], 4305920\n"
        "buffer_load_dword v188, off, s[60:63], s88 sc1\n"
        "s_add_u32 m0, s71, 40960\n"
        "s_add_u32 s65, s78, 0\n"
        "buffer_load_dwordx4 v24, s[80:83], s65 offen lds\n"
        "s_add_u32 m0, s71, 45056\n"
        "s_add_u32 s65, s78, 131072\n"
        "buffer_load_dwordx4 v24, s[80:83], s65 offen lds\n"
        "s_add_u32 m0, s71, 49152\n"
        "s_add_u32 s65, s78, 262144\n"
        "buffer_load_dwordx4 v24, s[80:83], s65 offen lds\n"
        "s_add_u32 m0, s71, 53248\n"
        "s_add_u32 s65, s78, 393216\n"
        "buffer_load_dwordx4 v24, s[80:83], s65 offen lds\n"
        "s_add_u32 m0, s71, 57344\n"
        "s_add_u32 s65, s78, 524288\n"
        "buffer_load_dwordx4 v24, s[80:83], s65 offen lds\n"
        "s_add_u32 m0, s71, 61440\n"
        "s_add_u32 s65, s78, 655360\n"
        "buffer_load_dwordx4 v24, s[80:83], s65 offen lds\n"
        "s_add_u32 m0, s71, 65536\n"
        "s_add_u32 s65, s78, 786432\n"
        "buffer_load_dwordx4 v24, s[80:83], s65 offen lds\n"
        "s_add_u32 m0, s71, 69632\n"
        "s_add_u32 s65, s78, 917504\n"
        "buffer_load_dwordx4 v24, s[80:83], s65 offen lds\n"
        "s_add_u32 m0, s71, 73728\n"
        "s_add_u32 s65, s78, 1048576\n"
        "buffer_load_dwordx4 v24, s[80:83], s65 offen lds\n"
        "s_add_u32 m0, s71, 77824\n"
        "s_add_u32 s65, s78, 1179648\n"
        "buffer_load_dwordx4 v24, s[80:83], s65 offen lds\n"
        "s_add_u32 m0, s71, 81920\n"
        "s_add_u32 s65, s78, 1310720\n"
        "buffer_load_dwordx4 v24, s[80:83], s65 offen lds\n"
        "s_add_u32 m0, s71, 86016\n"
        "s_add_u32 s65, s78, 1441792\n"
        "buffer_load_dwordx4 v24, s[80:83], s65 offen lds\n"
        "s_add_u32 m0, s71, 90112\n"
        "s_add_u32 s65, s78, 1572864\n"
        "buffer_load_dwordx4 v24, s[80:83], s65 offen lds\n"
        "s_add_u32 m0, s71, 94208\n"
        "s_add_u32 s65, s78, 1703936\n"
        "buffer_load_dwordx4 v24, s[80:83], s65 offen lds\n"
        "s_add_u32 m0, s71, 98304\n"
        "s_add_u32 s65, s78, 1835008\n"
        "buffer_load_dwordx4 v24, s[80:83], s65 offen lds\n"
        "s_add_u32 m0, s71, 102400\n"
        "s_add_u32 s65, s78, 1966080\n"
        "buffer_load_dwordx4 v24, s[80:83], s65 offen lds\n"
        "s_add_u32 m0, s71, 0\n"
        "s_mov_b32 s65, 0\n"
        "buffer_load_dwordx4 v8, s[72:75], s65 offen lds\n"
        "s_add_u32 m0, s71, 4096\n"
        "s_mov_b32 s65, 0\n"
        "buffer_load_dwordx4 v9, s[72:75], s65 offen lds\n"
        "s_add_u32 m0, s71, 8192\n"
        "s_mov_b32 s65, 0\n"
        "buffer_load_dwordx4 v10, s[72:75], s65 offen lds\n"
        "s_add_u32 m0, s71, 12288\n"
        "s_mov_b32 s65, 0\n"
        "buffer_load_dwordx4 v11, s[72:75], s65 offen lds\n"
        "s_add_u32 m0, s71, 16384\n"
        "s_mov_b32 s65, 0\n"
        "buffer_load_dwordx4 v12, s[72:75], s65 offen lds\n"
        "s_add_u32 m0, s71, 20480\n"
        "s_mov_b32 s65, 0\n"
        "buffer_load_dwordx4 v13, s[72:75], s65 offen lds\n"
        "s_add_u32 m0, s71, 24576\n"
        "s_mov_b32 s65, 0\n"
        "buffer_load_dwordx4 v14, s[72:75], s65 offen lds\n"
        "s_add_u32 m0, s71, 28672\n"
        "s_mov_b32 s65, 0\n"
        "buffer_load_dwordx4 v15, s[72:75], s65 offen lds\n"
        "s_add_u32 m0, s71, 32768\n"
        "s_mov_b32 s65, 0\n"
        "buffer_load_dwordx4 v16, s[72:75], s65 offen lds\n"
        "s_add_u32 m0, s71, 36864\n"
        "s_mov_b32 s65, 0\n"
        "buffer_load_dwordx4 v17, s[72:75], s65 offen lds\n"
        "s_add_u32 m0, s71, 106496\n"
        "s_add_u32 s65, s78, 2097152\n"
        "buffer_load_dwordx4 v24, s[80:83], s65 offen lds\n"
        "s_add_u32 m0, s71, 110592\n"
        "s_add_u32 s65, s78, 2228224\n"
        "buffer_load_dwordx4 v24, s[80:83], s65 offen lds\n"
        "s_add_u32 m0, s71, 114688\n"
        "s_add_u32 s65, s78, 2359296\n"
        "buffer_load_dwordx4 v24, s[80:83], s65 offen lds\n"
        "s_add_u32 m0, s71, 118784\n"
        "s_add_u32 s65, s78, 2490368\n"
        "buffer_load_dwordx4 v24, s[80:83], s65 offen lds\n"
        "s_add_u32 m0, s71, 122880\n"
        "s_add_u32 s65, s78, 2621440\n"
        "buffer_load_dwordx4 v24, s[80:83], s65 offen lds\n"
        "s_add_u32 m0, s71, 126976\n"
        "s_add_u32 s65, s78, 2752512\n"
        "buffer_load_dwordx4 v24, s[80:83], s65 offen lds\n"
        "s_add_u32 m0, s71, 131072\n"
        "s_add_u32 s65, s78, 2883584\n"
        "buffer_load_dwordx4 v24, s[80:83], s65 offen lds\n"
        "s_add_u32 m0, s71, 135168\n"
        "s_add_u32 s65, s78, 3014656\n"
        "buffer_load_dwordx4 v24, s[80:83], s65 offen lds\n"
        "v_add_u32 v28, 40960, v26\n"
        "v_add_u32 v29, 40960, v27\n"
        "s_waitcnt vmcnt(8)\n"
        "s_barrier\n"
        "ds_read_b64_tr_b16 a[0:1], v28 offset:0\n"
        "ds_read_b64_tr_b16 a[2:3], v28 offset:1024\n"
        "ds_read_b64_tr_b16 a[4:5], v28 offset:4096\n"
        "ds_read_b64_tr_b16 a[6:7], v28 offset:5120\n"
        "ds_read_b64_tr_b16 a[8:9], v28 offset:8192\n"
        "ds_read_b64_tr_b16 a[10:11], v28 offset:9216\n"
        "ds_read_b64_tr_b16 a[12:13], v28 offset:12288\n"
        "ds_read_b64_tr_b16 a[14:15], v28 offset:13312\n"
        "ds_read_b64_tr_b16 a[16:17], v28 offset:16384\n"
        "ds_read_b64_tr_b16 a[18:19], v28 offset:17408\n"
        "ds_read_b64_tr_b16 a[20:21], v28 offset:20480\n"
        "ds_read_b64_tr_b16 a[22:23], v28 offset:21504\n"
        "ds_read_b64_tr_b16 a[24:25], v28 offset:24576\n"
        "ds_read_b64_tr_b16 a[26:27], v28 offset:25600\n"
        "ds_read_b64_tr_b16 a[28:29], v28 offset:28672\n"
        "ds_read_b64_tr_b16 a[30:31], v28 offset:29696\n"
        "s_waitcnt lgkmcnt(0)\n"
        "s_barrier\n"
        "s_add_u32 m0, s71, 40960\n"
        "s_add_u32 s65, s78, 3145728\n"
        "buffer_load_dwordx4 v24, s[80:83], s65 offen lds\n"
        "v_add_u32 v188, 1, v188\n"
        "s_mov_b64 exec, 1\n"
        "buffer_store_dword v188, off, s[60:63], s89 sc1\n"
        "s_mov_b64 exec, -1\n"
        "ds_read_b128 v[32:35], v0 offset:0\n"
        "ds_read_b128 v[36:39], v1 offset:0\n"
        "ds_read_b128 v[40:43], v2 offset:0\n"
        "ds_read_b128 v[44:47], v3 offset:0\n"
        "ds_read_b128 v[48:51], v4 offset:0\n"
        "ds_read_b128 v[52:55], v5 offset:0\n"
        "ds_read_b128 v[56:59], v6 offset:0\n"
        "ds_read_b128 v[60:63], v7 offset:0\n"
        "s_add_u32 m0, s71, 45056\n"
        "s_add_u32 s65, s78, 3276800\n"
        "buffer_load_dwordx4 v24, s[80:83], s65 offen lds\n"
        "s_add_u32 m0, s71, 49152\n"
        "s_add_u32 s65, s78, 3407872\n"
        "buffer_load_dwordx4 v24, s[80:83], s65 offen lds\n"
        "s_waitcnt lgkmcnt(7)\n"
        "v_mfma_f32_32x32x16_bf16 v[64:79], a[0:3], v[32:35], 0\n"
        "s_add_u32 m0, s71, 53248\n"
        "s_add_u32 s65, s78, 3538944\n"
        "buffer_load_dwordx4 v24, s[80:83], s65 offen lds\n"
        "s_waitcnt lgkmcnt(6)\n"
        "v_mfma_f32_32x32x16_bf16 v[64:79], a[4:7], v[36:39], v[64:79]\n"
        "s_add_u32 m0, s71, 57344\n"
        "s_add_u32 s65, s78, 3670016\n"
        "buffer_load_dwordx4 v24, s[80:83], s65 offen lds\n"
        "s_waitcnt lgkmcnt(5)\n"
        "v_mfma_f32_32x32x16_bf16 v[64:79], a[8:11], v[40:43], v[64:79]\n"
        "s_add_u32 m0, s71, 61440\n"
        "s_add_u32 s65, s78, 3801088\n"
        "buffer_load_dwordx4 v24, s[80:83], s65 offen lds\n"
        "s_waitcnt lgkmcnt(4)\n"
        "v_mfma_f32_32x32x16_bf16 v[64:79], a[12:15], v[44:47], v[64:79]\n"
        "s_waitcnt lgkmcnt(3)\n"
        "v_mfma_f32_32x32x16_bf16 v[64:79], a[16:19], v[48:51], v[64:79]\n"
        "s_add_u32 m0, s71, 65536\n"
        "s_add_u32 s65, s78, 3932160\n"
        "buffer_load_dwordx4 v24, s[80:83], s65 offen lds\n"
        "s_waitcnt lgkmcnt(2)\n"
        "v_mfma_f32_32x32x16_bf16 v[64:79], a[20:23], v[52:55], v[64:79]\n"
        "s_add_u32 m0, s71, 69632\n"
        "s_add_u32 s65, s78, 4063232\n"
        "buffer_load_dwordx4 v24, s[80:83], s65 offen lds\n"
        "s_waitcnt lgkmcnt(1)\n"
        "v_mfma_f32_32x32x16_bf16 v[64:79], a[24:27], v[56:59], v[64:79]\n"
        "s_waitcnt lgkmcnt(0)\n"
        "v_mfma_f32_32x32x16_bf16 v[64:79], a[28:31], v[60:63], v[64:79]\n"
        "v_add_u32 v28, 73728, v26\n"
        "v_add_u32 v29, 73728, v27\n"
        "s_waitcnt vmcnt(27)\n"
        "s_barrier\n"
        "ds_read_b64_tr_b16 a[32:33], v28 offset:0\n"
        "ds_read_b64_tr_b16 a[34:35], v28 offset:1024\n"
        "ds_read_b64_tr_b16 a[36:37], v28 offset:4096\n"
        "ds_read_b64_tr_b16 a[38:39], v28 offset:5120\n"
        "ds_read_b64_tr_b16 a[40:41], v28 offset:8192\n"
        "ds_read_b64_tr_b16 a[42:43], v28 offset:9216\n"
        "ds_read_b64_tr_b16 a[44:45], v28 offset:12288\n"
        "ds_read_b64_tr_b16 a[46:47], v28 offset:13312\n"
        "ds_read_b64_tr_b16 a[48:49], v28 offset:16384\n"
        "ds_read_b64_tr_b16 a[50:51], v28 offset:17408\n"
        "ds_read_b64_tr_b16 a[52:53], v28 offset:20480\n"
        "ds_read_b64_tr_b16 a[54:55], v28 offset:21504\n"
        "ds_read_b64_tr_b16 a[56:57], v28 offset:24576\n"
        "ds_read_b64_tr_b16 a[58:59], v28 offset:25600\n"
        "ds_read_b64_tr_b16 a[60:61], v28 offset:28672\n"
        "ds_read_b64_tr_b16 a[62:63], v28 offset:29696\n"
        "s_waitcnt lgkmcnt(0)\n"
        "s_barrier\n"
        "s_add_u32 m0, s71, 73728\n"
        "s_add_u32 s65, s78, 4194304\n"
        "buffer_load_dwordx4 v24, s[80:83], s65 offen lds\n"
        "ds_read_b128 v[32:35], v0 offset:256\n"
        "ds_read_b128 v[36:39], v1 offset:256\n"
        "ds_read_b128 v[40:43], v2 offset:256\n"
        "ds_read_b128 v[44:47], v3 offset:256\n"
        "ds_read_b128 v[48:51], v4 offset:256\n"
        "ds_read_b128 v[52:55], v5 offset:256\n"
        "ds_read_b128 v[56:59], v6 offset:256\n"
        "ds_read_b128 v[60:63], v7 offset:256\n"
        "s_add_u32 m0, s71, 77824\n"
        "s_add_u32 s65, s78, 4325376\n"
        "buffer_load_dwordx4 v24, s[80:83], s65 offen lds\n"
        "s_add_u32 m0, s71, 81920\n"
        "s_add_u32 s65, s78, 4456448\n"
        "buffer_load_dwordx4 v24, s[80:83], s65 offen lds\n"
        "s_waitcnt lgkmcnt(7)\n"
        "v_mfma_f32_32x32x16_bf16 v[64:79], a[32:35], v[32:35], v[64:79]\n"
        "s_add_u32 m0, s71, 86016\n"
        "s_add_u32 s65, s78, 4587520\n"
        "buffer_load_dwordx4 v24, s[80:83], s65 offen lds\n"
        "s_waitcnt lgkmcnt(6)\n"
        "v_mfma_f32_32x32x16_bf16 v[64:79], a[36:39], v[36:39], v[64:79]\n"
        "s_add_u32 m0, s71, 90112\n"
        "s_add_u32 s65, s78, 4718592\n"
        "buffer_load_dwordx4 v24, s[80:83], s65 offen lds\n"
        "s_waitcnt lgkmcnt(5)\n"
        "v_mfma_f32_32x32x16_bf16 v[64:79], a[40:43], v[40:43], v[64:79]\n"
        "s_add_u32 m0, s71, 94208\n"
        "s_add_u32 s65, s78, 4849664\n"
        "buffer_load_dwordx4 v24, s[80:83], s65 offen lds\n"
        "s_waitcnt lgkmcnt(4)\n"
        "v_mfma_f32_32x32x16_bf16 v[64:79], a[44:47], v[44:47], v[64:79]\n"
        "s_waitcnt lgkmcnt(3)\n"
        "v_mfma_f32_32x32x16_bf16 v[64:79], a[48:51], v[48:51], v[64:79]\n"
        "s_add_u32 m0, s71, 98304\n"
        "s_add_u32 s65, s78, 4980736\n"
        "buffer_load_dwordx4 v24, s[80:83], s65 offen lds\n"
        "s_waitcnt lgkmcnt(2)\n"
        "v_mfma_f32_32x32x16_bf16 v[64:79], a[52:55], v[52:55], v[64:79]\n"
        "s_add_u32 m0, s71, 102400\n"
        "s_add_u32 s65, s78, 5111808\n"
        "buffer_load_dwordx4 v24, s[80:83], s65 offen lds\n"
        "s_waitcnt lgkmcnt(1)\n"
        "v_mfma_f32_32x32x16_bf16 v[64:79], a[56:59], v[56:59], v[64:79]\n"
        "s_waitcnt lgkmcnt(0)\n"
        "v_mfma_f32_32x32x16_bf16 v[64:79], a[60:63], v[60:63], v[64:79]\n"
        "v_add_u32 v28, 106496, v26\n"
        "v_add_u32 v29, 106496, v27\n"
        "s_waitcnt vmcnt(17)\n"
        "s_barrier\n"
        "ds_read_b64_tr_b16 a[64:65], v28 offset:0\n"
        "ds_read_b64_tr_b16 a[66:67], v28 offset:1024\n"
        "ds_read_b64_tr_b16 a[68:69], v28 offset:4096\n"
        "ds_read_b64_tr_b16 a[70:71], v28 offset:5120\n"
        "ds_read_b64_tr_b16 a[72:73], v28 offset:8192\n"
        "ds_read_b64_tr_b16 a[74:75], v28 offset:9216\n"
        "ds_read_b64_tr_b16 a[76:77], v28 offset:12288\n"
        "ds_read_b64_tr_b16 a[78:79], v28 offset:13312\n"
        "ds_read_b64_tr_b16 a[80:81], v28 offset:16384\n"
        "ds_read_b64_tr_b16 a[82:83], v28 offset:17408\n"
        "ds_read_b64_tr_b16 a[84:85], v28 offset:20480\n"
        "ds_read_b64_tr_b16 a[86:87], v28 offset:21504\n"
        "ds_read_b64_tr_b16 a[88:89], v28 offset:24576\n"
        "ds_read_b64_tr_b16 a[90:91], v28 offset:25600\n"
        "ds_read_b64_tr_b16 a[92:93], v28 offset:28672\n"
        "ds_read_b64_tr_b16 a[94:95], v28 offset:29696\n"
        "s_waitcnt lgkmcnt(0)\n"
        "s_barrier\n"
        "s_add_u32 m0, s71, 106496\n"
        "s_add_u32 s65, s79, 0\n"
        "buffer_load_dwordx4 v25, s[84:87], s65 offen lds\n"
        "ds_read_b128 v[32:35], v0 offset:512\n"
        "ds_read_b128 v[36:39], v1 offset:512\n"
        "ds_read_b128 v[40:43], v2 offset:512\n"
        "ds_read_b128 v[44:47], v3 offset:512\n"
        "ds_read_b128 v[48:51], v4 offset:512\n"
        "ds_read_b128 v[52:55], v5 offset:512\n"
        "ds_read_b128 v[56:59], v6 offset:512\n"
        "ds_read_b128 v[60:63], v7 offset:512\n"
        "s_add_u32 m0, s71, 110592\n"
        "s_add_u32 s65, s79, 20480\n"
        "buffer_load_dwordx4 v25, s[84:87], s65 offen lds\n"
        "s_add_u32 m0, s71, 114688\n"
        "s_add_u32 s65, s79, 40960\n"
        "buffer_load_dwordx4 v25, s[84:87], s65 offen lds\n"
        "s_waitcnt lgkmcnt(7)\n"
        "v_mfma_f32_32x32x16_bf16 v[64:79], a[64:67], v[32:35], v[64:79]\n"
        "s_add_u32 m0, s71, 118784\n"
        "s_add_u32 s65, s79, 61440\n"
        "buffer_load_dwordx4 v25, s[84:87], s65 offen lds\n"
        "s_waitcnt lgkmcnt(6)\n"
        "v_mfma_f32_32x32x16_bf16 v[64:79], a[68:71], v[36:39], v[64:79]\n"
        "s_add_u32 m0, s71, 122880\n"
        "s_add_u32 s65, s79, 81920\n"
        "buffer_load_dwordx4 v25, s[84:87], s65 offen lds\n"
        "s_waitcnt lgkmcnt(5)\n"
        "v_mfma_f32_32x32x16_bf16 v[64:79], a[72:75], v[40:43], v[64:79]\n"
        "s_add_u32 m0, s71, 126976\n"
        "s_add_u32 s65, s79, 102400\n"
        "buffer_load_dwordx4 v25, s[84:87], s65 offen lds\n"
        "s_waitcnt lgkmcnt(4)\n"
        "v_mfma_f32_32x32x16_bf16 v[64:79], a[76:79], v[44:47], v[64:79]\n"
        "s_waitcnt lgkmcnt(3)\n"
        "v_mfma_f32_32x32x16_bf16 v[64:79], a[80:83], v[48:51], v[64:79]\n"
        "s_add_u32 m0, s71, 131072\n"
        "s_add_u32 s65, s79, 122880\n"
        "buffer_load_dwordx4 v25, s[84:87], s65 offen lds\n"
        "s_waitcnt lgkmcnt(2)\n"
        "v_mfma_f32_32x32x16_bf16 v[64:79], a[84:87], v[52:55], v[64:79]\n"
        "s_add_u32 m0, s71, 135168\n"
        "s_add_u32 s65, s79, 143360\n"
        "buffer_load_dwordx4 v25, s[84:87], s65 offen lds\n"
        "s_waitcnt lgkmcnt(1)\n"
        "v_mfma_f32_32x32x16_bf16 v[64:79], a[88:91], v[56:59], v[64:79]\n"
        "s_waitcnt lgkmcnt(0)\n"
        "v_mfma_f32_32x32x16_bf16 v[64:79], a[92:95], v[60:63], v[64:79]\n"
        "v_add_u32 v28, 40960, v26\n"
        "v_add_u32 v29, 40960, v27\n"
        "s_waitcnt vmcnt(16)\n"
        "s_barrier\n"
        "ds_read_b64_tr_b16 a[96:97], v28 offset:0\n"
        "ds_read_b64_tr_b16 a[98:99], v28 offset:1024\n"
        "ds_read_b64_tr_b16 a[100:101], v28 offset:4096\n"
        "ds_read_b64_tr_b16 a[102:103], v28 offset:5120\n"
        "ds_read_b64_tr_b16 a[104:105], v28 offset:8192\n"
        "ds_read_b64_tr_b16 a[106:107], v28 offset:9216\n"
        "ds_read_b64_tr_b16 a[108:109], v28 offset:12288\n"
        "ds_read_b64_tr_b16 a[110:111], v28 offset:13312\n"
        "ds_read_b64_tr_b16 a[112:113], v28 offset:16384\n"
        "ds_read_b64_tr_b16 a[114:115], v28 offset:17408\n"
        "ds_read_b64_tr_b16 a[116:117], v28 offset:20480\n"
        "ds_read_b64_tr_b16 a[118:119], v28 offset:21504\n"
        "ds_read_b64_tr_b16 a[120:121], v28 offset:24576\n"
        "ds_read_b64_tr_b16 a[122:123], v28 offset:25600\n"
        "ds_read_b64_tr_b16 a[124:125], v28 offset:28672\n"
        "ds_read_b64_tr_b16 a[126:127], v28 offset:29696\n"
        "s_waitcnt lgkmcnt(0)\n"
        "s_barrier\n"
        "s_add_u32 m0, s71, 40960\n"
        "s_add_u32 s65, s79, 256\n"
        "buffer_load_dwordx4 v25, s[84:87], s65 offen lds\n"
        "ds_read_b128 v[32:35], v0 offset:768\n"
        "ds_read_b128 v[36:39], v1 offset:768\n"
        "ds_read_b128 v[40:43], v2 offset:768\n"
        "ds_read_b128 v[44:47], v3 offset:768\n"
        "ds_read_b128 v[48:51], v4 offset:768\n"
        "ds_read_b128 v[52:55], v5 offset:768\n"
        "ds_read_b128 v[56:59], v6 offset:768\n"
        "ds_read_b128 v[60:63], v7 offset:768\n"
        "s_add_u32 m0, s71, 45056\n"
        "s_add_u32 s65, s79, 20736\n"
        "buffer_load_dwordx4 v25, s[84:87], s65 offen lds\n"
        "s_add_u32 m0, s71, 49152\n"
        "s_add_u32 s65, s79, 41216\n"
        "buffer_load_dwordx4 v25, s[84:87], s65 offen lds\n"
        "s_waitcnt lgkmcnt(7)\n"
        "v_mfma_f32_32x32x16_bf16 v[64:79], a[96:99], v[32:35], v[64:79]\n"
        "s_add_u32 m0, s71, 53248\n"
        "s_add_u32 s65, s79, 61696\n"
        "buffer_load_dwordx4 v25, s[84:87], s65 offen lds\n"
        "s_waitcnt lgkmcnt(6)\n"
        "v_mfma_f32_32x32x16_bf16 v[64:79], a[100:103], v[36:39], v[64:79]\n"
        "s_add_u32 m0, s71, 57344\n"
        "s_add_u32 s65, s79, 82176\n"
        "buffer_load_dwordx4 v25, s[84:87], s65 offen lds\n"
        "s_waitcnt lgkmcnt(5)\n"
        "v_mfma_f32_32x32x16_bf16 v[64:79], a[104:107], v[40:43], v[64:79]\n"
        "s_add_u32 m0, s71, 61440\n"
        "s_add_u32 s65, s79, 102656\n"
        "buffer_load_dwordx4 v25, s[84:87], s65 offen lds\n"
        "s_waitcnt lgkmcnt(4)\n"
        "v_mfma_f32_32x32x16_bf16 v[64:79], a[108:111], v[44:47], v[64:79]\n"
        "s_waitcnt lgkmcnt(3)\n"
        "v_mfma_f32_32x32x16_bf16 v[64:79], a[112:115], v[48:51], v[64:79]\n"
        "s_add_u32 m0, s71, 65536\n"
        "s_add_u32 s65, s79, 123136\n"
        "buffer_load_dwordx4 v25, s[84:87], s65 offen lds\n"
        "s_waitcnt lgkmcnt(2)\n"
        "v_mfma_f32_32x32x16_bf16 v[64:79], a[116:119], v[52:55], v[64:79]\n"
        "s_add_u32 m0, s71, 69632\n"
        "s_add_u32 s65, s79, 143616\n"
        "buffer_load_dwordx4 v25, s[84:87], s65 offen lds\n"
        "s_waitcnt lgkmcnt(1)\n"
        "v_mfma_f32_32x32x16_bf16 v[64:79], a[120:123], v[56:59], v[64:79]\n"
        "s_waitcnt lgkmcnt(0)\n"
        "v_mfma_f32_32x32x16_bf16 v[64:79], a[124:127], v[60:63], v[64:79]\n"
        "v_add_u32 v28, 73728, v26\n"
        "v_add_u32 v29, 73728, v27\n"
        "s_waitcnt vmcnt(16)\n"
        "s_barrier\n"
        "ds_read_b64_tr_b16 a[128:129], v28 offset:0\n"
        "ds_read_b64_tr_b16 a[130:131], v28 offset:1024\n"
        "ds_read_b64_tr_b16 a[132:133], v28 offset:4096\n"
        "ds_read_b64_tr_b16 a[134:135], v28 offset:5120\n"
        "ds_read_b64_tr_b16 a[136:137], v28 offset:8192\n"
        "ds_read_b64_tr_b16 a[138:139], v28 offset:9216\n"
        "ds_read_b64_tr_b16 a[140:141], v28 offset:12288\n"
        "ds_read_b64_tr_b16 a[142:143], v28 offset:13312\n"
        "ds_read_b64_tr_b16 a[144:145], v28 offset:16384\n"
        "ds_read_b64_tr_b16 a[146:147], v28 offset:17408\n"
        "ds_read_b64_tr_b16 a[148:149], v28 offset:20480\n"
        "ds_read_b64_tr_b16 a[150:151], v28 offset:21504\n"
        "ds_read_b64_tr_b16 a[152:153], v28 offset:24576\n"
        "ds_read_b64_tr_b16 a[154:155], v28 offset:25600\n"
        "ds_read_b64_tr_b16 a[156:157], v28 offset:28672\n"
        "ds_read_b64_tr_b16 a[158:159], v28 offset:29696\n"
        "s_waitcnt lgkmcnt(0)\n"
        "s_barrier\n"
        "s_add_u32 m0, s71, 73728\n"
        "s_add_u32 s65, s79, 512\n"
        "buffer_load_dwordx4 v25, s[84:87], s65 offen lds\n"
        "ds_read_b128 v[32:35], v0 offset:1024\n"
        "ds_read_b128 v[36:39], v1 offset:1024\n"
        "ds_read_b128 v[40:43], v2 offset:1024\n"
        "ds_read_b128 v[44:47], v3 offset:1024\n"
        "ds_read_b128 v[48:51], v4 offset:1024\n"
        "ds_read_b128 v[52:55], v5 offset:1024\n"
        "ds_read_b128 v[56:59], v6 offset:1024\n"
        "ds_read_b128 v[60:63], v7 offset:1024\n"
        "s_add_u32 m0, s71, 77824\n"
        "s_add_u32 s65, s79, 20992\n"
        "buffer_load_dwordx4 v25, s[84:87], s65 offen lds\n"
        "s_add_u32 m0, s71, 81920\n"
        "s_add_u32 s65, s79, 41472\n"
        "buffer_load_dwordx4 v25, s[84:87], s65 offen lds\n"
        "s_waitcnt lgkmcnt(7)\n"
        "v_mfma_f32_32x32x16_bf16 v[64:79], a[128:131], v[32:35], v[64:79]\n"
        "s_add_u32 m0, s71, 86016\n"
        "s_add_u32 s65, s79, 61952\n"
        "buffer_load_dwordx4 v25, s[84:87], s65 offen lds\n"
        "s_waitcnt lgkmcnt(6)\n"
        "v_mfma_f32_32x32x16_bf16 v[64:79], a[132:135], v[36:39], v[64:79]\n"
        "s_add_u32 m0, s71, 90112\n"
        "s_add_u32 s65, s79, 82432\n"
        "buffer_load_dwordx4 v25, s[84:87], s65 offen lds\n"
        "s_waitcnt lgkmcnt(5)\n"
        "v_mfma_f32_32x32x16_bf16 v[64:79], a[136:139], v[40:43], v[64:79]\n"
        "s_add_u32 m0, s71, 94208\n"
        "s_add_u32 s65, s79, 102912\n"
        "buffer_load_dwordx4 v25, s[84:87], s65 offen lds\n"
        "s_waitcnt lgkmcnt(4)\n"
        "v_mfma_f32_32x32x16_bf16 v[64:79], a[140:143], v[44:47], v[64:79]\n"
        "s_waitcnt lgkmcnt(3)\n"
        "v_mfma_f32_32x32x16_bf16 v[64:79], a[144:147], v[48:51], v[64:79]\n"
        "s_add_u32 m0, s71, 98304\n"
        "s_add_u32 s65, s79, 123392\n"
        "buffer_load_dwordx4 v25, s[84:87], s65 offen lds\n"
        "s_waitcnt lgkmcnt(2)\n"
        "v_mfma_f32_32x32x16_bf16 v[64:79], a[148:151], v[52:55], v[64:79]\n"
        "s_add_u32 m0, s71, 102400\n"
        "s_add_u32 s65, s79, 143872\n"
        "buffer_load_dwordx4 v25, s[84:87], s65 offen lds\n"
        "s_waitcnt lgkmcnt(1)\n"
        "v_mfma_f32_32x32x16_bf16 v[64:79], a[152:155], v[56:59], v[64:79]\n"
        "s_waitcnt lgkmcnt(0)\n"
        "v_mfma_f32_32x32x16_bf16 v[64:79], a[156:159], v[60:63], v[64:79]\n"
        "v_add_u32 v28, 106496, v26\n"
        "v_add_u32 v29, 106496, v27\n"
        "s_waitcnt vmcnt(16)\n"
        "s_barrier\n"
        "ds_read_b64_tr_b16 a[160:161], v29 offset:0\n"
        "ds_read_b64_tr_b16 a[162:163], v29 offset:2048\n"
        "ds_read_b64_tr_b16 a[164:165], v29 offset:4096\n"
        "ds_read_b64_tr_b16 a[166:167], v29 offset:6144\n"
        "ds_read_b64_tr_b16 a[168:169], v29 offset:8192\n"
        "ds_read_b64_tr_b16 a[170:171], v29 offset:10240\n"
        "ds_read_b64_tr_b16 a[172:173], v29 offset:12288\n"
        "ds_read_b64_tr_b16 a[174:175], v29 offset:14336\n"
        "ds_read_b64_tr_b16 a[176:177], v29 offset:16384\n"
        "ds_read_b64_tr_b16 a[178:179], v29 offset:18432\n"
        "ds_read_b64_tr_b16 a[180:181], v29 offset:20480\n"
        "ds_read_b64_tr_b16 a[182:183], v29 offset:22528\n"
        "ds_read_b64_tr_b16 a[184:185], v29 offset:24576\n"
        "ds_read_b64_tr_b16 a[186:187], v29 offset:26624\n"
        "ds_read_b64_tr_b16 a[188:189], v29 offset:28672\n"
        "ds_read_b64_tr_b16 a[190:191], v29 offset:30720\n"
        "s_waitcnt lgkmcnt(0)\n"
        "s_barrier\n"
        "s_add_u32 m0, s71, 106496\n"
        "s_add_u32 s65, s79, 768\n"
        "buffer_load_dwordx4 v25, s[84:87], s65 offen lds\n"
        "v_subrev_u32 v30, 81920, v19\n"
        "v_subrev_u32 v31, 81920, v23\n"
        "v_cvt_pk_bf16_f32 v144, v64, v65\n"
        "v_cvt_pk_bf16_f32 v145, v66, v67\n"
        "v_cvt_pk_bf16_f32 v146, v68, v69\n"
        "v_cvt_pk_bf16_f32 v147, v70, v71\n"
        "ds_write_b128 v30, v[144:147]\n"
        "v_cvt_pk_bf16_f32 v148, v72, v73\n"
        "v_cvt_pk_bf16_f32 v149, v74, v75\n"
        "v_cvt_pk_bf16_f32 v150, v76, v77\n"
        "v_cvt_pk_bf16_f32 v151, v78, v79\n"
        "ds_write_b128 v30, v[148:151] offset:1024\n"
        "s_waitcnt lgkmcnt(0)\n"
        "s_barrier\n"
        "ds_read_b128 v[112:115], v31 offset:0\n"
        "ds_read_b128 v[116:119], v31 offset:1024\n"
        "ds_read_b128 v[120:123], v31 offset:2048\n"
        "ds_read_b128 v[124:127], v31 offset:3072\n"
        "ds_read_b128 v[128:131], v31 offset:4096\n"
        "ds_read_b128 v[132:135], v31 offset:5120\n"
        "ds_read_b128 v[136:139], v31 offset:6144\n"
        "ds_read_b128 v[140:143], v31 offset:7168\n"
        "s_add_u32 m0, s71, 110592\n"
        "s_add_u32 s65, s79, 21248\n"
        "buffer_load_dwordx4 v25, s[84:87], s65 offen lds\n"
        "s_add_u32 m0, s71, 114688\n"
        "s_add_u32 s65, s79, 41728\n"
        "buffer_load_dwordx4 v25, s[84:87], s65 offen lds\n"
        "s_waitcnt lgkmcnt(0)\n"
        "v_mfma_f32_32x32x16_bf16 v[80:95], v[112:115], a[160:163], 0\n"
        "s_add_u32 m0, s71, 118784\n"
        "s_add_u32 s65, s79, 62208\n"
        "buffer_load_dwordx4 v25, s[84:87], s65 offen lds\n"
        "v_mfma_f32_32x32x16_bf16 v[80:95], v[116:119], a[164:167], v[80:95]\n"
        "s_add_u32 m0, s71, 122880\n"
        "s_add_u32 s65, s79, 82688\n"
        "buffer_load_dwordx4 v25, s[84:87], s65 offen lds\n"
        "v_mfma_f32_32x32x16_bf16 v[80:95], v[120:123], a[168:171], v[80:95]\n"
        "s_add_u32 m0, s71, 126976\n"
        "s_add_u32 s65, s79, 103168\n"
        "buffer_load_dwordx4 v25, s[84:87], s65 offen lds\n"
        "v_mfma_f32_32x32x16_bf16 v[80:95], v[124:127], a[172:175], v[80:95]\n"
        "v_mfma_f32_32x32x16_bf16 v[80:95], v[128:131], a[176:179], v[80:95]\n"
        "s_add_u32 m0, s71, 131072\n"
        "s_add_u32 s65, s79, 123648\n"
        "buffer_load_dwordx4 v25, s[84:87], s65 offen lds\n"
        "v_mfma_f32_32x32x16_bf16 v[80:95], v[132:135], a[180:183], v[80:95]\n"
        "s_add_u32 m0, s71, 135168\n"
        "s_add_u32 s65, s79, 144128\n"
        "buffer_load_dwordx4 v25, s[84:87], s65 offen lds\n"
        "v_mfma_f32_32x32x16_bf16 v[80:95], v[136:139], a[184:187], v[80:95]\n"
        "v_mfma_f32_32x32x16_bf16 v[80:95], v[140:143], a[188:191], v[80:95]\n"
        "s_nop 7\n"
        "s_nop 3\n"
        "v_cvt_pk_bf16_f32 v160, v80, v81\n"
        "v_cvt_pk_bf16_f32 v161, v82, v83\n"
        "v_cvt_pk_bf16_f32 v162, v84, v85\n"
        "v_cvt_pk_bf16_f32 v163, v86, v87\n"
        "v_cvt_pk_bf16_f32 v164, v88, v89\n"
        "v_cvt_pk_bf16_f32 v165, v90, v91\n"
        "v_cvt_pk_bf16_f32 v166, v92, v93\n"
        "v_cvt_pk_bf16_f32 v167, v94, v95\n"
        "v_add_u32 v28, 40960, v26\n"
        "v_add_u32 v29, 40960, v27\n"
        "s_waitcnt vmcnt(16)\n"
        "s_barrier\n"
        "ds_read_b64_tr_b16 a[192:193], v29 offset:0\n"
        "ds_read_b64_tr_b16 a[194:195], v29 offset:2048\n"
        "ds_read_b64_tr_b16 a[196:197], v29 offset:4096\n"
        "ds_read_b64_tr_b16 a[198:199], v29 offset:6144\n"
        "ds_read_b64_tr_b16 a[200:201], v29 offset:8192\n"
        "ds_read_b64_tr_b16 a[202:203], v29 offset:10240\n"
        "ds_read_b64_tr_b16 a[204:205], v29 offset:12288\n"
        "ds_read_b64_tr_b16 a[206:207], v29 offset:14336\n"
        "ds_read_b64_tr_b16 a[208:209], v29 offset:16384\n"
        "ds_read_b64_tr_b16 a[210:211], v29 offset:18432\n"
        "ds_read_b64_tr_b16 a[212:213], v29 offset:20480\n"
        "ds_read_b64_tr_b16 a[214:215], v29 offset:22528\n"
        "ds_read_b64_tr_b16 a[216:217], v29 offset:24576\n"
        "ds_read_b64_tr_b16 a[218:219], v29 offset:26624\n"
        "ds_read_b64_tr_b16 a[220:221], v29 offset:28672\n"
        "ds_read_b64_tr_b16 a[222:223], v29 offset:30720\n"
        "s_waitcnt lgkmcnt(0)\n"
        "s_barrier\n"
        "s_add_u32 m0, s71, 40960\n"
        "s_add_u32 s65, s79, 1024\n"
        "buffer_load_dwordx4 v25, s[84:87], s65 offen lds\n"
        "v_mfma_f32_32x32x16_bf16 v[96:111], v[112:115], a[192:195], 0\n"
        "s_add_u32 m0, s71, 45056\n"
        "s_add_u32 s65, s79, 21504\n"
        "buffer_load_dwordx4 v25, s[84:87], s65 offen lds\n"
        "v_mfma_f32_32x32x16_bf16 v[96:111], v[116:119], a[196:199], v[96:111]\n"
        "s_add_u32 m0, s71, 49152\n"
        "s_add_u32 s65, s79, 41984\n"
        "buffer_load_dwordx4 v25, s[84:87], s65 offen lds\n"
        "v_mfma_f32_32x32x16_bf16 v[96:111], v[120:123], a[200:203], v[96:111]\n"
        "s_add_u32 m0, s71, 53248\n"
        "s_add_u32 s65, s79, 62464\n"
        "buffer_load_dwordx4 v25, s[84:87], s65 offen lds\n"
        "v_mfma_f32_32x32x16_bf16 v[96:111], v[124:127], a[204:207], v[96:111]\n"
        "s_add_u32 m0, s71, 57344\n"
        "s_add_u32 s65, s79, 82944\n"
        "buffer_load_dwordx4 v25, s[84:87], s65 offen lds\n"
        "v_mfma_f32_32x32x16_bf16 v[96:111], v[128:131], a[208:211], v[96:111]\n"
        "s_add_u32 m0, s71, 61440\n"
        "s_add_u32 s65, s79, 103424\n"
        "buffer_load_dwordx4 v25, s[84:87], s65 offen lds\n"
        "v_mfma_f32_32x32x16_bf16 v[96:111], v[132:135], a[212:215], v[96:111]\n"
        "s_add_u32 m0, s71, 65536\n"
        "s_add_u32 s65, s79, 123904\n"
        "buffer_load_dwordx4 v25, s[84:87], s65 offen lds\n"
        "v_mfma_f32_32x32x16_bf16 v[96:111], v[136:139], a[216:219], v[96:111]\n"
        "s_add_u32 m0, s71, 69632\n"
        "s_add_u32 s65, s79, 144384\n"
        "buffer_load_dwordx4 v25, s[84:87], s65 offen lds\n"
        "v_mfma_f32_32x32x16_bf16 v[96:111], v[140:143], a[220:223], v[96:111]\n"
        "s_nop 7\n"
        "s_nop 3\n"
        "v_cvt_pk_bf16_f32 v168, v96, v97\n"
        "v_cvt_pk_bf16_f32 v169, v98, v99\n"
        "v_cvt_pk_bf16_f32 v170, v100, v101\n"
        "v_cvt_pk_bf16_f32 v171, v102, v103\n"
        "v_cvt_pk_bf16_f32 v172, v104, v105\n"
        "v_cvt_pk_bf16_f32 v173, v106, v107\n"
        "v_cvt_pk_bf16_f32 v174, v108, v109\n"
        "v_cvt_pk_bf16_f32 v175, v110, v111\n"
        "v_add_u32 v28, 73728, v26\n"
        "v_add_u32 v29, 73728, v27\n"
        "s_waitcnt vmcnt(16)\n"
        "s_barrier\n"
        "ds_read_b64_tr_b16 a[224:225], v29 offset:0\n"
        "ds_read_b64_tr_b16 a[226:227], v29 offset:2048\n"
        "ds_read_b64_tr_b16 a[228:229], v29 offset:4096\n"
        "ds_read_b64_tr_b16 a[230:231], v29 offset:6144\n"
        "ds_read_b64_tr_b16 a[232:233], v29 offset:8192\n"
        "ds_read_b64_tr_b16 a[234:235], v29 offset:10240\n"
        "ds_read_b64_tr_b16 a[236:237], v29 offset:12288\n"
        "ds_read_b64_tr_b16 a[238:239], v29 offset:14336\n"
        "ds_read_b64_tr_b16 a[240:241], v29 offset:16384\n"
        "ds_read_b64_tr_b16 a[242:243], v29 offset:18432\n"
        "ds_read_b64_tr_b16 a[244:245], v29 offset:20480\n"
        "ds_read_b64_tr_b16 a[246:247], v29 offset:22528\n"
        "ds_read_b64_tr_b16 a[248:249], v29 offset:24576\n"
        "ds_read_b64_tr_b16 a[250:251], v29 offset:26624\n"
        "ds_read_b64_tr_b16 a[252:253], v29 offset:28672\n"
        "ds_read_b64_tr_b16 a[254:255], v29 offset:30720\n"
        "s_waitcnt lgkmcnt(0)\n"
        "s_barrier\n"
        "v_mfma_f32_32x32x16_bf16 v[80:95], v[112:115], a[224:227], 0\n"
        "v_mfma_f32_32x32x16_bf16 v[80:95], v[116:119], a[228:231], v[80:95]\n"
        "v_mfma_f32_32x32x16_bf16 v[80:95], v[120:123], a[232:235], v[80:95]\n"
        "v_mfma_f32_32x32x16_bf16 v[80:95], v[124:127], a[236:239], v[80:95]\n"
        "v_mfma_f32_32x32x16_bf16 v[80:95], v[128:131], a[240:243], v[80:95]\n"
        "v_mfma_f32_32x32x16_bf16 v[80:95], v[132:135], a[244:247], v[80:95]\n"
        "v_mfma_f32_32x32x16_bf16 v[80:95], v[136:139], a[248:251], v[80:95]\n"
        "v_mfma_f32_32x32x16_bf16 v[80:95], v[140:143], a[252:255], v[80:95]\n"
        "s_mov_b32 s68, 55574528\n"
        "buffer_store_dwordx4 v[160:163], v18, s[60:63], s68 offen sc1\n"
        "buffer_store_dwordx4 v[164:167], v18, s[60:63], s68 offen offset:512 sc1\n"
        "s_mov_b32 s68, 55836672\n"
        "buffer_store_dwordx4 v[168:171], v18, s[60:63], s68 offen sc1\n"
        "buffer_store_dwordx4 v[172:175], v18, s[60:63], s68 offen offset:512 sc1\n"
        "s_mov_b32 s68, 56098816\n"
        "s_nop 7\n"
        "s_nop 3\n"
        "v_cvt_pk_bf16_f32 v144, v80, v81\n"
        "v_cvt_pk_bf16_f32 v145, v82, v83\n"
        "v_cvt_pk_bf16_f32 v146, v84, v85\n"
        "v_cvt_pk_bf16_f32 v147, v86, v87\n"
        "buffer_store_dwordx4 v[144:147], v18, s[60:63], s68 offen sc1\n"
        "v_cvt_pk_bf16_f32 v148, v88, v89\n"
        "v_cvt_pk_bf16_f32 v149, v90, v91\n"
        "v_cvt_pk_bf16_f32 v150, v92, v93\n"
        "v_cvt_pk_bf16_f32 v151, v94, v95\n"
        "buffer_store_dwordx4 v[148:151], v18, s[60:63], s68 offen offset:512 sc1\n"
        "v_add_u32 v28, 106496, v26\n"
        "v_add_u32 v29, 106496, v27\n"
        "s_waitcnt vmcnt(14)\n"
        "s_barrier\n"
        "ds_read_b64_tr_b16 v[192:193], v29 offset:0\n"
        "ds_read_b64_tr_b16 v[194:195], v29 offset:2048\n"
        "ds_read_b64_tr_b16 v[196:197], v29 offset:4096\n"
        "ds_read_b64_tr_b16 v[198:199], v29 offset:6144\n"
        "ds_read_b64_tr_b16 v[200:201], v29 offset:8192\n"
        "ds_read_b64_tr_b16 v[202:203], v29 offset:10240\n"
        "ds_read_b64_tr_b16 v[204:205], v29 offset:12288\n"
        "ds_read_b64_tr_b16 v[206:207], v29 offset:14336\n"
        "ds_read_b64_tr_b16 v[208:209], v29 offset:16384\n"
        "ds_read_b64_tr_b16 v[210:211], v29 offset:18432\n"
        "ds_read_b64_tr_b16 v[212:213], v29 offset:20480\n"
        "ds_read_b64_tr_b16 v[214:215], v29 offset:22528\n"
        "ds_read_b64_tr_b16 v[216:217], v29 offset:24576\n"
        "ds_read_b64_tr_b16 v[218:219], v29 offset:26624\n"
        "ds_read_b64_tr_b16 v[220:221], v29 offset:28672\n"
        "ds_read_b64_tr_b16 v[222:223], v29 offset:30720\n"
        "s_waitcnt lgkmcnt(0)\n"
        "s_barrier\n"
        "buffer_load_dword v189, off, s[60:63], s88 sc1\n"
        "v_mfma_f32_32x32x16_bf16 v[96:111], v[112:115], v[192:195], 0\n"
        "v_mfma_f32_32x32x16_bf16 v[96:111], v[116:119], v[196:199], v[96:111]\n"
        "v_mfma_f32_32x32x16_bf16 v[96:111], v[120:123], v[200:203], v[96:111]\n"
        "v_mfma_f32_32x32x16_bf16 v[96:111], v[124:127], v[204:207], v[96:111]\n"
        "v_mfma_f32_32x32x16_bf16 v[96:111], v[128:131], v[208:211], v[96:111]\n"
        "v_mfma_f32_32x32x16_bf16 v[96:111], v[132:135], v[212:215], v[96:111]\n"
        "v_mfma_f32_32x32x16_bf16 v[96:111], v[136:139], v[216:219], v[96:111]\n"
        "v_mfma_f32_32x32x16_bf16 v[96:111], v[140:143], v[220:223], v[96:111]\n"
        "s_mov_b32 s68, 56360960\n"
        "s_nop 7\n"
        "s_nop 3\n"
        "v_cvt_pk_bf16_f32 v152, v96, v97\n"
        "v_cvt_pk_bf16_f32 v153, v98, v99\n"
        "v_cvt_pk_bf16_f32 v154, v100, v101\n"
        "v_cvt_pk_bf16_f32 v155, v102, v103\n"
        "buffer_store_dwordx4 v[152:155], v18, s[60:63], s68 offen sc1\n"
        "v_cvt_pk_bf16_f32 v156, v104, v105\n"
        "v_cvt_pk_bf16_f32 v157, v106, v107\n"
        "v_cvt_pk_bf16_f32 v158, v108, v109\n"
        "v_cvt_pk_bf16_f32 v159, v110, v111\n"
        "buffer_store_dwordx4 v[156:159], v18, s[60:63], s68 offen offset:512 sc1\n"
        "v_add_u32 v28, 40960, v26\n"
        "v_add_u32 v29, 40960, v27\n"
        "s_waitcnt vmcnt(9)\n"
        "s_barrier\n"
        "ds_read_b64_tr_b16 v[224:225], v29 offset:0\n"
        "ds_read_b64_tr_b16 v[226:227], v29 offset:2048\n"
        "ds_read_b64_tr_b16 v[228:229], v29 offset:4096\n"
        "ds_read_b64_tr_b16 v[230:231], v29 offset:6144\n"
        "ds_read_b64_tr_b16 v[232:233], v29 offset:8192\n"
        "ds_read_b64_tr_b16 v[234:235], v29 offset:10240\n"
        "ds_read_b64_tr_b16 v[236:237], v29 offset:12288\n"
        "ds_read_b64_tr_b16 v[238:239], v29 offset:14336\n"
        "ds_read_b64_tr_b16 v[240:241], v29 offset:16384\n"
        "ds_read_b64_tr_b16 v[242:243], v29 offset:18432\n"
        "ds_read_b64_tr_b16 v[244:245], v29 offset:20480\n"
        "ds_read_b64_tr_b16 v[246:247], v29 offset:22528\n"
        "ds_read_b64_tr_b16 v[248:249], v29 offset:24576\n"
        "ds_read_b64_tr_b16 v[250:251], v29 offset:26624\n"
        "ds_read_b64_tr_b16 v[252:253], v29 offset:28672\n"
        "ds_read_b64_tr_b16 v[254:255], v29 offset:30720\n"
        "s_waitcnt lgkmcnt(0)\n"
        "s_barrier\n"
        "s_add_u32 m0, s71, 40960\n"
        "s_nop 0\n"
        "buffer_load_dwordx4 v8, s[72:75], s70 offen lds\n"
        "s_add_u32 m0, s71, 45056\n"
        "s_nop 0\n"
        "buffer_load_dwordx4 v9, s[72:75], s70 offen lds\n"
        "s_add_u32 m0, s71, 49152\n"
        "s_nop 0\n"
        "buffer_load_dwordx4 v10, s[72:75], s70 offen lds\n"
        "s_add_u32 m0, s71, 53248\n"
        "s_nop 0\n"
        "buffer_load_dwordx4 v11, s[72:75], s70 offen lds\n"
        "s_add_u32 m0, s71, 57344\n"
        "s_nop 0\n"
        "buffer_load_dwordx4 v12, s[72:75], s70 offen lds\n"
        "s_add_u32 m0, s71, 61440\n"
        "s_nop 0\n"
        "buffer_load_dwordx4 v13, s[72:75], s70 offen lds\n"
        "s_add_u32 m0, s71, 65536\n"
        "s_nop 0\n"
        "buffer_load_dwordx4 v14, s[72:75], s70 offen lds\n"
        "s_add_u32 m0, s71, 69632\n"
        "s_nop 0\n"
        "buffer_load_dwordx4 v15, s[72:75], s70 offen lds\n"
        "s_add_u32 m0, s71, 73728\n"
        "s_nop 0\n"
        "buffer_load_dwordx4 v16, s[72:75], s70 offen lds\n"
        "s_add_u32 m0, s71, 77824\n"
        "s_nop 0\n"
        "buffer_load_dwordx4 v17, s[72:75], s70 offen lds\n"
        "v_mfma_f32_32x32x16_bf16 v[80:95], v[112:115], v[224:227], 0\n"
        "v_mfma_f32_32x32x16_bf16 v[80:95], v[116:119], v[228:231], v[80:95]\n"
        "v_mfma_f32_32x32x16_bf16 v[80:95], v[120:123], v[232:235], v[80:95]\n"
        "v_mfma_f32_32x32x16_bf16 v[80:95], v[124:127], v[236:239], v[80:95]\n"
        "v_mfma_f32_32x32x16_bf16 v[80:95], v[128:131], v[240:243], v[80:95]\n"
        "v_mfma_f32_32x32x16_bf16 v[80:95], v[132:135], v[244:247], v[80:95]\n"
        "v_mfma_f32_32x32x16_bf16 v[80:95], v[136:139], v[248:251], v[80:95]\n"
        "v_mfma_f32_32x32x16_bf16 v[80:95], v[140:143], v[252:255], v[80:95]\n"
        "s_mov_b32 s68, 56623104\n"
        "s_nop 7\n"
        "s_nop 3\n"
        "v_cvt_pk_bf16_f32 v144, v80, v81\n"
        "v_cvt_pk_bf16_f32 v145, v82, v83\n"
        "v_cvt_pk_bf16_f32 v146, v84, v85\n"
        "v_cvt_pk_bf16_f32 v147, v86, v87\n"
        "buffer_store_dwordx4 v[144:147], v18, s[60:63], s68 offen sc1\n"
        "v_cvt_pk_bf16_f32 v148, v88, v89\n"
        "v_cvt_pk_bf16_f32 v149, v90, v91\n"
        "v_cvt_pk_bf16_f32 v150, v92, v93\n"
        "v_cvt_pk_bf16_f32 v151, v94, v95\n"
        "buffer_store_dwordx4 v[148:151], v18, s[60:63], s68 offen offset:512 sc1\n"
        "s_waitcnt vmcnt(14)\n"
        "v_cmp_ne_u32 vcc, v188, v189\n"
        "s_cbranch_vccz HS806_vob\n"
        "HS806_voa:\n"
        "buffer_load_dword v189, off, s[60:63], s88 sc1\n"
        "s_waitcnt vmcnt(0)\n"
        "v_cmp_ne_u32 vcc, v188, v189\n"
        "s_cbranch_vccz HS806_vob\n"
        "s_sleep 1\n"
        "s_cmp_eq_u32 s66, 0\n"
        "s_cbranch_scc1 HS806_vob\n"
        "s_sub_u32 s66, s66, 1\n"
        "s_branch HS806_voa\n"
        "HS806_vob:\n"
        "s_mov_b64 exec, 1\n"
        "v_mov_b32 v189, 0\n"
        "buffer_store_dword v189, off, s[60:63], s90 sc1\n"
        "s_mov_b64 exec, -1\n"
        "s_lshl_b32 s77, s58, 2\n"
        "s_add_u32 s77, s77, s67\n"
        "s_lshl_b32 s77, s77, 2\n"
        "s_add_u32 s77, s77, 4227072\n"
        "s_waitcnt vmcnt(0)\n"
        "buffer_store_dword v20, v21, s[60:63], s77 offen offset:0 sc1\n"
        "s_barrier\n"
        "ds_read_b128 v[32:35], v0 offset:40960\n"
        "ds_read_b128 v[36:39], v1 offset:40960\n"
        "ds_read_b128 v[40:43], v2 offset:40960\n"
        "ds_read_b128 v[44:47], v3 offset:40960\n"
        "ds_read_b128 v[48:51], v4 offset:40960\n"
        "ds_read_b128 v[52:55], v5 offset:40960\n"
        "s_mov_b32 s68, 4026531840\n"
        "s_mov_b32 s76, 4026531840\n"
        "s_mov_b32 s70, 81920\n"
        "s_mov_b32 s64, 1\n"
        "s_branch VB_p1\n"
        "VB_p1:\n"
        "s_waitcnt lgkmcnt(5)\n"
        "v_mfma_f32_32x32x16_bf16 v[160:175], a[0:3], v[32:35], 0\n"
        "ds_read_b128 v[56:59], v6 offset:40960\n"
        "s_waitcnt lgkmcnt(5)\n"
        "v_mfma_f32_32x32x16_bf16 v[160:175], a[4:7], v[36:39], v[160:175]\n"
        "ds_read_b128 v[60:63], v7 offset:40960\n"
        "s_waitcnt lgkmcnt(5)\n"
        "v_mfma_f32_32x32x16_bf16 v[160:175], a[8:11], v[40:43], v[160:175]\n"
        "ds_read_b128 v[32:35], v0 offset:41216\n"
        "s_add_u32 m0, s71, 0\n"
        "s_nop 0\n"
        "buffer_load_dwordx4 v8, s[72:75], s70 offen lds\n"
        "s_waitcnt lgkmcnt(5)\n"
        "v_mfma_f32_32x32x16_bf16 v[160:175], a[12:15], v[44:47], v[160:175]\n"
        "ds_read_b128 v[36:39], v1 offset:41216\n"
        "s_add_u32 m0, s71, 4096\n"
        "s_nop 0\n"
        "buffer_load_dwordx4 v9, s[72:75], s70 offen lds\n"
        "s_waitcnt lgkmcnt(5)\n"
        "v_mfma_f32_32x32x16_bf16 v[160:175], a[16:19], v[48:51], v[160:175]\n"
        "ds_read_b128 v[40:43], v2 offset:41216\n"
        "s_add_u32 m0, s71, 8192\n"
        "s_nop 0\n"
        "buffer_load_dwordx4 v10, s[72:75], s70 offen lds\n"
        "s_waitcnt lgkmcnt(5)\n"
        "v_mfma_f32_32x32x16_bf16 v[160:175], a[20:23], v[52:55], v[160:175]\n"
        "ds_read_b128 v[44:47], v3 offset:41216\n"
        "s_add_u32 m0, s71, 12288\n"
        "s_nop 0\n"
        "buffer_load_dwordx4 v11, s[72:75], s70 offen lds\n"
        "s_waitcnt lgkmcnt(5)\n"
        "v_mfma_f32_32x32x16_bf16 v[160:175], a[24:27], v[56:59], v[160:175]\n"
        "ds_read_b128 v[48:51], v4 offset:41216\n"
        "s_add_u32 m0, s71, 16384\n"
        "s_nop 0\n"
        "buffer_load_dwordx4 v12, s[72:75], s70 offen lds\n"
        "s_waitcnt lgkmcnt(5)\n"
        "v_mfma_f32_32x32x16_bf16 v[160:175], a[28:31], v[60:63], v[160:175]\n"
        "ds_read_b128 v[52:55], v5 offset:41216\n"
        "s_add_u32 m0, s71, 20480\n"
        "s_nop 0\n"
        "buffer_load_dwordx4 v13, s[72:75], s70 offen lds\n"
        "s_waitcnt lgkmcnt(5)\n"
        "v_mfma_f32_32x32x16_bf16 v[160:175], a[32:35], v[32:35], v[160:175]\n"
        "ds_read_b128 v[56:59], v6 offset:41216\n"
        "s_add_u32 m0, s71, 24576\n"
        "s_nop 0\n"
        "buffer_load_dwordx4 v14, s[72:75], s70 offen lds\n"
        "s_waitcnt lgkmcnt(5)\n"
        "v_mfma_f32_32x32x16_bf16 v[160:175], a[36:39], v[36:39], v[160:175]\n"
        "ds_read_b128 v[60:63], v7 offset:41216\n"
        "s_add_u32 m0, s71, 28672\n"
        "s_nop 0\n"
        "buffer_load_dwordx4 v15, s[72:75], s70 offen lds\n"
        "s_waitcnt lgkmcnt(5)\n"
        "v_mfma_f32_32x32x16_bf16 v[160:175], a[40:43], v[40:43], v[160:175]\n"
        "ds_read_b128 v[32:35], v0 offset:41472\n"
        "s_add_u32 m0, s71, 32768\n"
        "s_nop 0\n"
        "buffer_load_dwordx4 v16, s[72:75], s70 offen lds\n"
        "s_waitcnt lgkmcnt(5)\n"
        "v_mfma_f32_32x32x16_bf16 v[160:175], a[44:47], v[44:47], v[160:175]\n"
        "ds_read_b128 v[36:39], v1 offset:41472\n"
        "s_add_u32 m0, s71, 36864\n"
        "s_nop 0\n"
        "buffer_load_dwordx4 v17, s[72:75], s70 offen lds\n"
        "s_waitcnt lgkmcnt(5)\n"
        "v_mfma_f32_32x32x16_bf16 v[160:175], a[48:51], v[48:51], v[160:175]\n"
        "ds_read_b128 v[40:43], v2 offset:41472\n"
        "s_waitcnt lgkmcnt(5)\n"
        "v_mfma_f32_32x32x16_bf16 v[160:175], a[52:55], v[52:55], v[160:175]\n"
        "ds_read_b128 v[44:47], v3 offset:41472\n"
        "s_waitcnt lgkmcnt(5)\n"
        "v_mfma_f32_32x32x16_bf16 v[160:175], a[56:59], v[56:59], v[160:175]\n"
        "ds_read_b128 v[48:51], v4 offset:41472\n"
        "s_waitcnt lgkmcnt(5)\n"
        "v_mfma_f32_32x32x16_bf16 v[160:175], a[60:63], v[60:63], v[160:175]\n"
        "ds_read_b128 v[52:55], v5 offset:41472\n"
        "s_waitcnt lgkmcnt(5)\n"
        "v_mfma_f32_32x32x16_bf16 v[160:175], a[64:67], v[32:35], v[160:175]\n"
        "ds_read_b128 v[56:59], v6 offset:41472\n"
        "s_waitcnt lgkmcnt(5)\n"
        "v_mfma_f32_32x32x16_bf16 v[160:175], a[68:71], v[36:39], v[160:175]\n"
        "ds_read_b128 v[60:63], v7 offset:41472\n"
        "s_waitcnt lgkmcnt(5)\n"
        "v_mfma_f32_32x32x16_bf16 v[160:175], a[72:75], v[40:43], v[160:175]\n"
        "ds_read_b128 v[32:35], v0 offset:41728\n"
        "s_waitcnt lgkmcnt(5)\n"
        "v_mfma_f32_32x32x16_bf16 v[160:175], a[76:79], v[44:47], v[160:175]\n"
        "ds_read_b128 v[36:39], v1 offset:41728\n"
        "s_waitcnt lgkmcnt(5)\n"
        "v_mfma_f32_32x32x16_bf16 v[160:175], a[80:83], v[48:51], v[160:175]\n"
        "ds_read_b128 v[40:43], v2 offset:41728\n"
        "s_waitcnt lgkmcnt(5)\n"
        "v_mfma_f32_32x32x16_bf16 v[160:175], a[84:87], v[52:55], v[160:175]\n"
        "ds_read_b128 v[44:47], v3 offset:41728\n"
        "s_waitcnt lgkmcnt(5)\n"
        "v_mfma_f32_32x32x16_bf16 v[160:175], a[88:91], v[56:59], v[160:175]\n"
        "ds_read_b128 v[48:51], v4 offset:41728\n"
        "s_waitcnt lgkmcnt(5)\n"
        "v_mfma_f32_32x32x16_bf16 v[160:175], a[92:95], v[60:63], v[160:175]\n"
        "ds_read_b128 v[52:55], v5 offset:41728\n"
        "s_waitcnt lgkmcnt(5)\n"
        "v_mfma_f32_32x32x16_bf16 v[160:175], a[96:99], v[32:35], v[160:175]\n"
        "ds_read_b128 v[56:59], v6 offset:41728\n"
        "s_waitcnt lgkmcnt(5)\n"
        "v_mfma_f32_32x32x16_bf16 v[160:175], a[100:103], v[36:39], v[160:175]\n"
        "ds_read_b128 v[60:63], v7 offset:41728\n"
        "s_waitcnt lgkmcnt(5)\n"
        "v_mfma_f32_32x32x16_bf16 v[160:175], a[104:107], v[40:43], v[160:175]\n"
        "ds_read_b128 v[32:35], v0 offset:41984\n"
        "s_waitcnt lgkmcnt(5)\n"
        "v_mfma_f32_32x32x16_bf16 v[160:175], a[108:111], v[44:47], v[160:175]\n"
        "ds_read_b128 v[36:39], v1 offset:41984\n"
        "s_waitcnt lgkmcnt(5)\n"
        "v_mfma_f32_32x32x16_bf16 v[160:175], a[112:115], v[48:51], v[160:175]\n"
        "ds_read_b128 v[40:43], v2 offset:41984\n"
        "s_waitcnt lgkmcnt(5)\n"
        "v_mfma_f32_32x32x16_bf16 v[160:175], a[116:119], v[52:55], v[160:175]\n"
        "ds_read_b128 v[44:47], v3 offset:41984\n"
        "s_waitcnt lgkmcnt(5)\n"
        "v_mfma_f32_32x32x16_bf16 v[160:175], a[120:123], v[56:59], v[160:175]\n"
        "ds_read_b128 v[48:51], v4 offset:41984\n"
        "s_waitcnt lgkmcnt(5)\n"
        "v_mfma_f32_32x32x16_bf16 v[160:175], a[124:127], v[60:63], v[160:175]\n"
        "ds_read_b128 v[52:55], v5 offset:41984\n"
        "s_waitcnt lgkmcnt(5)\n"
        "v_mfma_f32_32x32x16_bf16 v[160:175], a[128:131], v[32:35], v[160:175]\n"
        "ds_read_b128 v[56:59], v6 offset:41984\n"
        "s_waitcnt lgkmcnt(5)\n"
        "v_mfma_f32_32x32x16_bf16 v[160:175], a[132:135], v[36:39], v[160:175]\n"
        "ds_read_b128 v[60:63], v7 offset:41984\n"
        "s_waitcnt vmcnt(0)\n"
        "s_barrier\n"
        "s_waitcnt lgkmcnt(5)\n"
        "v_mfma_f32_32x32x16_bf16 v[160:175], a[136:139], v[40:43], v[160:175]\n"
        "ds_read_b128 v[32:35], v0 offset:0\n"
        "s_waitcnt lgkmcnt(5)\n"
        "v_mfma_f32_32x32x16_bf16 v[160:175], a[140:143], v[44:47], v[160:175]\n"
        "ds_read_b128 v[36:39], v1 offset:0\n"
        "s_waitcnt lgkmcnt(5)\n"
        "v_mfma_f32_32x32x16_bf16 v[160:175], a[144:147], v[48:51], v[160:175]\n"
        "ds_read_b128 v[40:43], v2 offset:0\n"
        "s_waitcnt lgkmcnt(5)\n"
        "v_mfma_f32_32x32x16_bf16 v[160:175], a[148:151], v[52:55], v[160:175]\n"
        "ds_read_b128 v[44:47], v3 offset:0\n"
        "s_waitcnt lgkmcnt(5)\n"
        "v_mfma_f32_32x32x16_bf16 v[160:175], a[152:155], v[56:59], v[160:175]\n"
        "ds_read_b128 v[48:51], v4 offset:0\n"
        "s_waitcnt lgkmcnt(5)\n"
        "v_mfma_f32_32x32x16_bf16 v[160:175], a[156:159], v[60:63], v[160:175]\n"
        "ds_read_b128 v[52:55], v5 offset:0\n"
        "s_add_u32 s70, s70, 40960\n"
        "VB_loop:\n"
        "s_waitcnt lgkmcnt(5)\n"
        "v_mfma_f32_32x32x16_bf16 v[64:79], a[0:3], v[32:35], 0\n"
        "ds_read_b128 v[56:59], v6 offset:0\n"
        "s_mul_i32 s78, s64, 1310720\n"
        "s_add_u32 s78, s78, 55574528\n"
        "s_waitcnt lgkmcnt(5)\n"
        "v_mfma_f32_32x32x16_bf16 v[64:79], a[4:7], v[36:39], v[64:79]\n"
        "ds_read_b128 v[60:63], v7 offset:0\n"
        "v_cvt_pk_bf16_f32 v176, v160, v161\n"
        "v_cvt_pk_bf16_f32 v177, v162, v163\n"
        "v_cvt_pk_bf16_f32 v178, v164, v165\n"
        "v_cvt_pk_bf16_f32 v179, v166, v167\n"
        "ds_write_b128 v19, v[176:179] offset:0\n"
        "s_waitcnt lgkmcnt(6)\n"
        "v_mfma_f32_32x32x16_bf16 v[64:79], a[8:11], v[40:43], v[64:79]\n"
        "ds_read_b128 v[32:35], v0 offset:256\n"
        "v_cvt_pk_bf16_f32 v180, v168, v169\n"
        "v_cvt_pk_bf16_f32 v181, v170, v171\n"
        "v_cvt_pk_bf16_f32 v182, v172, v173\n"
        "v_cvt_pk_bf16_f32 v183, v174, v175\n"
        "ds_write_b128 v19, v[180:183] offset:1024\n"
        "s_waitcnt lgkmcnt(7)\n"
        "v_mfma_f32_32x32x16_bf16 v[64:79], a[12:15], v[44:47], v[64:79]\n"
        "ds_read_b128 v[36:39], v1 offset:256\n"
        "s_waitcnt lgkmcnt(7)\n"
        "v_mfma_f32_32x32x16_bf16 v[64:79], a[16:19], v[48:51], v[64:79]\n"
        "ds_read_b128 v[40:43], v2 offset:256\n"
        "s_waitcnt lgkmcnt(2)\n"
        "s_barrier\n"
        "ds_read_b128 v[112:115], v23 offset:0\n"
        "ds_read_b128 v[116:119], v23 offset:1024\n"
        "s_waitcnt lgkmcnt(9)\n"
        "v_mfma_f32_32x32x16_bf16 v[64:79], a[20:23], v[52:55], v[64:79]\n"
        "ds_read_b128 v[44:47], v3 offset:256\n"
        "ds_read_b128 v[120:123], v23 offset:2048\n"
        "ds_read_b128 v[124:127], v23 offset:3072\n"
        "s_waitcnt lgkmcnt(11)\n"
        "v_mfma_f32_32x32x16_bf16 v[64:79], a[24:27], v[56:59], v[64:79]\n"
        "ds_read_b128 v[48:51], v4 offset:256\n"
        "ds_read_b128 v[128:131], v23 offset:4096\n"
        "ds_read_b128 v[132:135], v23 offset:5120\n"
        "s_waitcnt lgkmcnt(13)\n"
        "v_mfma_f32_32x32x16_bf16 v[64:79], a[28:31], v[60:63], v[64:79]\n"
        "ds_read_b128 v[52:55], v5 offset:256\n"
        "ds_read_b128 v[136:139], v23 offset:6144\n"
        "ds_read_b128 v[140:143], v23 offset:7168\n"
        "s_waitcnt lgkmcnt(14)\n"
        "v_mfma_f32_32x32x16_bf16 v[64:79], a[32:35], v[32:35], v[64:79]\n"
        "ds_read_b128 v[56:59], v6 offset:256\n"
        "s_waitcnt lgkmcnt(13)\n"
        "v_mfma_f32_32x32x16_bf16 v[64:79], a[36:39], v[36:39], v[64:79]\n"
        "ds_read_b128 v[60:63], v7 offset:256\n"
        "s_waitcnt lgkmcnt(12)\n"
        "v_mfma_f32_32x32x16_bf16 v[80:95], v[112:115], a[160:163], 0\n"
        "s_add_u32 m0, s71, 40960\n"
        "s_nop 0\n"
        "buffer_load_dwordx4 v8, s[72:75], s70 offen lds\n"
        "s_waitcnt lgkmcnt(11)\n"
        "v_mfma_f32_32x32x16_bf16 v[80:95], v[116:119], a[164:167], v[80:95]\n"
        "s_add_u32 m0, s71, 45056\n"
        "s_nop 0\n"
        "buffer_load_dwordx4 v9, s[72:75], s70 offen lds\n"
        "s_waitcnt lgkmcnt(13)\n"
        "v_mfma_f32_32x32x16_bf16 v[64:79], a[40:43], v[40:43], v[64:79]\n"
        "ds_read_b128 v[32:35], v0 offset:512\n"
        "s_add_u32 m0, s71, 49152\n"
        "s_nop 0\n"
        "buffer_load_dwordx4 v10, s[72:75], s70 offen lds\n"
        "s_waitcnt lgkmcnt(10)\n"
        "v_mfma_f32_32x32x16_bf16 v[80:95], v[120:123], a[168:171], v[80:95]\n"
        "s_add_u32 m0, s71, 53248\n"
        "s_nop 0\n"
        "buffer_load_dwordx4 v11, s[72:75], s70 offen lds\n"
        "s_waitcnt lgkmcnt(9)\n"
        "v_mfma_f32_32x32x16_bf16 v[80:95], v[124:127], a[172:175], v[80:95]\n"
        "s_add_u32 m0, s71, 57344\n"
        "s_nop 0\n"
        "buffer_load_dwordx4 v12, s[72:75], s70 offen lds\n"
        "s_waitcnt lgkmcnt(11)\n"
        "v_mfma_f32_32x32x16_bf16 v[64:79], a[44:47], v[44:47], v[64:79]\n"
        "ds_read_b128 v[36:39], v1 offset:512\n"
        "s_add_u32 m0, s71, 61440\n"
        "s_nop 0\n"
        "buffer_load_dwordx4 v13, s[72:75], s70 offen lds\n"
        "s_waitcnt lgkmcnt(8)\n"
        "v_mfma_f32_32x32x16_bf16 v[80:95], v[128:131], a[176:179], v[80:95]\n"
        "s_add_u32 m0, s71, 65536\n"
        "s_nop 0\n"
        "buffer_load_dwordx4 v14, s[72:75], s70 offen lds\n"
        "s_waitcnt lgkmcnt(7)\n"
        "v_mfma_f32_32x32x16_bf16 v[80:95], v[132:135], a[180:183], v[80:95]\n"
        "s_add_u32 m0, s71, 69632\n"
        "s_nop 0\n"
        "buffer_load_dwordx4 v15, s[72:75], s70 offen lds\n"
        "s_waitcnt lgkmcnt(9)\n"
        "v_mfma_f32_32x32x16_bf16 v[64:79], a[48:51], v[48:51], v[64:79]\n"
        "ds_read_b128 v[40:43], v2 offset:512\n"
        "s_add_u32 m0, s71, 73728\n"
        "s_nop 0\n"
        "buffer_load_dwordx4 v16, s[72:75], s70 offen lds\n"
        "s_waitcnt lgkmcnt(6)\n"
        "v_mfma_f32_32x32x16_bf16 v[80:95], v[136:139], a[184:187], v[80:95]\n"
        "s_add_u32 m0, s71, 77824\n"
        "s_nop 0\n"
        "buffer_load_dwordx4 v17, s[72:75], s70 offen lds\n"
        "s_waitcnt lgkmcnt(5)\n"
        "v_mfma_f32_32x32x16_bf16 v[80:95], v[140:143], a[188:191], v[80:95]\n"
        "s_waitcnt lgkmcnt(7)\n"
        "v_mfma_f32_32x32x16_bf16 v[64:79], a[52:55], v[52:55], v[64:79]\n"
        "ds_read_b128 v[44:47], v3 offset:512\n"
        "v_mfma_f32_32x32x16_bf16 v[96:111], v[112:115], a[192:195], 0\n"
        "s_waitcnt vmcnt(10)\n"
        "buffer_store_dword v20, v21, s[60:63], s69 offen sc1\n"
        "v_cvt_pk_bf16_f32 v144, v80, v81\n"
        "v_cvt_pk_bf16_f32 v145, v82, v83\n"
        "v_cvt_pk_bf16_f32 v146, v84, v85\n"
        "v_mfma_f32_32x32x16_bf16 v[96:111], v[116:119], a[196:199], v[96:111]\n"
        "v_cvt_pk_bf16_f32 v147, v86, v87\n"
        "buffer_store_dwordx4 v[144:147], v18, s[60:63], s78 offen sc1\n"
        "v_cvt_pk_bf16_f32 v148, v88, v89\n"
        "s_waitcnt lgkmcnt(5)\n"
        "v_mfma_f32_32x32x16_bf16 v[64:79], a[56:59], v[56:59], v[64:79]\n"
        "ds_read_b128 v[48:51], v4 offset:512\n"
        "v_cvt_pk_bf16_f32 v149, v90, v91\n"
        "v_cvt_pk_bf16_f32 v150, v92, v93\n"
        "v_cvt_pk_bf16_f32 v151, v94, v95\n"
        "v_mfma_f32_32x32x16_bf16 v[96:111], v[120:123], a[200:203], v[96:111]\n"
        "buffer_store_dwordx4 v[148:151], v18, s[60:63], s78 offen offset:512 sc1\n"
        "s_add_u32 s78, s78, 262144\n"
        "v_mfma_f32_32x32x16_bf16 v[96:111], v[124:127], a[204:207], v[96:111]\n"
        "s_waitcnt lgkmcnt(5)\n"
        "v_mfma_f32_32x32x16_bf16 v[64:79], a[60:63], v[60:63], v[64:79]\n"
        "ds_read_b128 v[52:55], v5 offset:512\n"
        "v_mfma_f32_32x32x16_bf16 v[96:111], v[128:131], a[208:211], v[96:111]\n"
        "v_mfma_f32_32x32x16_bf16 v[96:111], v[132:135], a[212:215], v[96:111]\n"
        "s_waitcnt lgkmcnt(5)\n"
        "v_mfma_f32_32x32x16_bf16 v[64:79], a[64:67], v[32:35], v[64:79]\n"
        "ds_read_b128 v[56:59], v6 offset:512\n"
        "v_mfma_f32_32x32x16_bf16 v[96:111], v[136:139], a[216:219], v[96:111]\n"
        "v_mfma_f32_32x32x16_bf16 v[96:111], v[140:143], a[220:223], v[96:111]\n"
        "s_waitcnt lgkmcnt(5)\n"
        "v_mfma_f32_32x32x16_bf16 v[64:79], a[68:71], v[36:39], v[64:79]\n"
        "ds_read_b128 v[60:63], v7 offset:512\n"
        "v_mfma_f32_32x32x16_bf16 v[80:95], v[112:115], a[224:227], 0\n"
        "v_cvt_pk_bf16_f32 v152, v96, v97\n"
        "v_cvt_pk_bf16_f32 v153, v98, v99\n"
        "v_cvt_pk_bf16_f32 v154, v100, v101\n"
        "v_mfma_f32_32x32x16_bf16 v[80:95], v[116:119], a[228:231], v[80:95]\n"
        "v_cvt_pk_bf16_f32 v155, v102, v103\n"
        "buffer_store_dwordx4 v[152:155], v18, s[60:63], s78 offen sc1\n"
        "v_cvt_pk_bf16_f32 v156, v104, v105\n"
        "s_waitcnt lgkmcnt(5)\n"
        "v_mfma_f32_32x32x16_bf16 v[64:79], a[72:75], v[40:43], v[64:79]\n"
        "ds_read_b128 v[32:35], v0 offset:768\n"
        "v_cvt_pk_bf16_f32 v157, v106, v107\n"
        "v_cvt_pk_bf16_f32 v158, v108, v109\n"
        "v_cvt_pk_bf16_f32 v159, v110, v111\n"
        "v_mfma_f32_32x32x16_bf16 v[80:95], v[120:123], a[232:235], v[80:95]\n"
        "buffer_store_dwordx4 v[156:159], v18, s[60:63], s78 offen offset:512 sc1\n"
        "s_add_u32 s78, s78, 262144\n"
        "v_mfma_f32_32x32x16_bf16 v[80:95], v[124:127], a[236:239], v[80:95]\n"
        "s_waitcnt lgkmcnt(5)\n"
        "v_mfma_f32_32x32x16_bf16 v[64:79], a[76:79], v[44:47], v[64:79]\n"
        "ds_read_b128 v[36:39], v1 offset:768\n"
        "v_mfma_f32_32x32x16_bf16 v[80:95], v[128:131], a[240:243], v[80:95]\n"
        "v_mfma_f32_32x32x16_bf16 v[80:95], v[132:135], a[244:247], v[80:95]\n"
        "s_waitcnt lgkmcnt(5)\n"
        "v_mfma_f32_32x32x16_bf16 v[64:79], a[80:83], v[48:51], v[64:79]\n"
        "ds_read_b128 v[40:43], v2 offset:768\n"
        "v_mfma_f32_32x32x16_bf16 v[80:95], v[136:139], a[248:251], v[80:95]\n"
        "v_mfma_f32_32x32x16_bf16 v[80:95], v[140:143], a[252:255], v[80:95]\n"
        "s_waitcnt lgkmcnt(5)\n"
        "v_mfma_f32_32x32x16_bf16 v[64:79], a[84:87], v[52:55], v[64:79]\n"
        "ds_read_b128 v[44:47], v3 offset:768\n"
        "v_mfma_f32_32x32x16_bf16 v[96:111], v[112:115], v[192:195], 0\n"
        "v_cvt_pk_bf16_f32 v144, v80, v81\n"
        "v_cvt_pk_bf16_f32 v145, v82, v83\n"
        "v_cvt_pk_bf16_f32 v146, v84, v85\n"
        "v_mfma_f32_32x32x16_bf16 v[96:111], v[116:119], v[196:199], v[96:111]\n"
        "v_cvt_pk_bf16_f32 v147, v86, v87\n"
        "buffer_store_dwordx4 v[144:147], v18, s[60:63], s78 offen sc1\n"
        "v_cvt_pk_bf16_f32 v148, v88, v89\n"
        "s_waitcnt lgkmcnt(5)\n"
        "v_mfma_f32_32x32x16_bf16 v[64:79], a[88:91], v[56:59], v[64:79]\n"
        "ds_read_b128 v[48:51], v4 offset:768\n"
        "v_cvt_pk_bf16_f32 v149, v90, v91\n"
        "v_cvt_pk_bf16_f32 v150, v92, v93\n"
        "v_cvt_pk_bf16_f32 v151, v94, v95\n"
        "v_mfma_f32_32x32x16_bf16 v[96:111], v[120:123], v[200:203], v[96:111]\n"
        "buffer_store_dwordx4 v[148:151], v18, s[60:63], s78 offen offset:512 sc1\n"
        "s_add_u32 s78, s78, 262144\n"
        "v_mfma_f32_32x32x16_bf16 v[96:111], v[124:127], v[204:207], v[96:111]\n"
        "s_waitcnt lgkmcnt(5)\n"
        "v_mfma_f32_32x32x16_bf16 v[64:79], a[92:95], v[60:63], v[64:79]\n"
        "ds_read_b128 v[52:55], v5 offset:768\n"
        "v_mfma_f32_32x32x16_bf16 v[96:111], v[128:131], v[208:211], v[96:111]\n"
        "v_mfma_f32_32x32x16_bf16 v[96:111], v[132:135], v[212:215], v[96:111]\n"
        "s_waitcnt lgkmcnt(5)\n"
        "v_mfma_f32_32x32x16_bf16 v[64:79], a[96:99], v[32:35], v[64:79]\n"
        "ds_read_b128 v[56:59], v6 offset:768\n"
        "v_mfma_f32_32x32x16_bf16 v[96:111], v[136:139], v[216:219], v[96:111]\n"
        "v_mfma_f32_32x32x16_bf16 v[96:111], v[140:143], v[220:223], v[96:111]\n"
        "s_waitcnt lgkmcnt(5)\n"
        "v_mfma_f32_32x32x16_bf16 v[64:79], a[100:103], v[36:39], v[64:79]\n"
        "ds_read_b128 v[60:63], v7 offset:768\n"
        "v_mfma_f32_32x32x16_bf16 v[80:95], v[112:115], v[224:227], 0\n"
        "v_cvt_pk_bf16_f32 v152, v96, v97\n"
        "v_cvt_pk_bf16_f32 v153, v98, v99\n"
        "v_cvt_pk_bf16_f32 v154, v100, v101\n"
        "v_mfma_f32_32x32x16_bf16 v[80:95], v[116:119], v[228:231], v[80:95]\n"
        "v_cvt_pk_bf16_f32 v155, v102, v103\n"
        "buffer_store_dwordx4 v[152:155], v18, s[60:63], s78 offen sc1\n"
        "v_cvt_pk_bf16_f32 v156, v104, v105\n"
        "s_waitcnt lgkmcnt(5)\n"
        "v_mfma_f32_32x32x16_bf16 v[64:79], a[104:107], v[40:43], v[64:79]\n"
        "ds_read_b128 v[32:35], v0 offset:1024\n"
        "v_cvt_pk_bf16_f32 v157, v106, v107\n"
        "v_cvt_pk_bf16_f32 v158, v108, v109\n"
        "v_cvt_pk_bf16_f32 v159, v110, v111\n"
        "v_mfma_f32_32x32x16_bf16 v[80:95], v[120:123], v[232:235], v[80:95]\n"
        "buffer_store_dwordx4 v[156:159], v18, s[60:63], s78 offen offset:512 sc1\n"
        "s_add_u32 s78, s78, 262144\n"
        "v_mfma_f32_32x32x16_bf16 v[80:95], v[124:127], v[236:239], v[80:95]\n"
        "s_waitcnt lgkmcnt(5)\n"
        "v_mfma_f32_32x32x16_bf16 v[64:79], a[108:111], v[44:47], v[64:79]\n"
        "ds_read_b128 v[36:39], v1 offset:1024\n"
        "v_mfma_f32_32x32x16_bf16 v[80:95], v[128:131], v[240:243], v[80:95]\n"
        "v_mfma_f32_32x32x16_bf16 v[80:95], v[132:135], v[244:247], v[80:95]\n"
        "s_waitcnt lgkmcnt(5)\n"
        "v_mfma_f32_32x32x16_bf16 v[64:79], a[112:115], v[48:51], v[64:79]\n"
        "ds_read_b128 v[40:43], v2 offset:1024\n"
        "v_mfma_f32_32x32x16_bf16 v[80:95], v[136:139], v[248:251], v[80:95]\n"
        "v_mfma_f32_32x32x16_bf16 v[80:95], v[140:143], v[252:255], v[80:95]\n"
        "s_waitcnt lgkmcnt(5)\n"
        "v_mfma_f32_32x32x16_bf16 v[64:79], a[116:119], v[52:55], v[64:79]\n"
        "ds_read_b128 v[44:47], v3 offset:1024\n"
        "s_waitcnt lgkmcnt(5)\n"
        "v_mfma_f32_32x32x16_bf16 v[64:79], a[120:123], v[56:59], v[64:79]\n"
        "ds_read_b128 v[48:51], v4 offset:1024\n"
        "v_cvt_pk_bf16_f32 v144, v80, v81\n"
        "v_cvt_pk_bf16_f32 v145, v82, v83\n"
        "v_cvt_pk_bf16_f32 v146, v84, v85\n"
        "s_waitcnt lgkmcnt(5)\n"
        "v_mfma_f32_32x32x16_bf16 v[64:79], a[124:127], v[60:63], v[64:79]\n"
        "ds_read_b128 v[52:55], v5 offset:1024\n"
        "v_cvt_pk_bf16_f32 v147, v86, v87\n"
        "buffer_store_dwordx4 v[144:147], v18, s[60:63], s78 offen sc1\n"
        "v_cvt_pk_bf16_f32 v148, v88, v89\n"
        "s_waitcnt lgkmcnt(5)\n"
        "v_mfma_f32_32x32x16_bf16 v[64:79], a[128:131], v[32:35], v[64:79]\n"
        "ds_read_b128 v[56:59], v6 offset:1024\n"
        "v_cvt_pk_bf16_f32 v149, v90, v91\n"
        "v_cvt_pk_bf16_f32 v150, v92, v93\n"
        "v_cvt_pk_bf16_f32 v151, v94, v95\n"
        "s_waitcnt lgkmcnt(5)\n"
        "v_mfma_f32_32x32x16_bf16 v[64:79], a[132:135], v[36:39], v[64:79]\n"
        "ds_read_b128 v[60:63], v7 offset:1024\n"
        "buffer_store_dwordx4 v[148:151], v18, s[60:63], s78 offen offset:512 sc1\n"
        "s_add_u32 s78, s78, 262144\n"
        "s_waitcnt vmcnt(11)\n"
        "s_barrier\n"
        "s_waitcnt lgkmcnt(5)\n"
        "v_mfma_f32_32x32x16_bf16 v[64:79], a[136:139], v[40:43], v[64:79]\n"
        "ds_read_b128 v[32:35], v0 offset:40960\n"
        "s_waitcnt lgkmcnt(5)\n"
        "v_mfma_f32_32x32x16_bf16 v[64:79], a[140:143], v[44:47], v[64:79]\n"
        "ds_read_b128 v[36:39], v1 offset:40960\n"
        "s_waitcnt lgkmcnt(5)\n"
        "v_mfma_f32_32x32x16_bf16 v[64:79], a[144:147], v[48:51], v[64:79]\n"
        "ds_read_b128 v[40:43], v2 offset:40960\n"
        "s_waitcnt lgkmcnt(5)\n"
        "v_mfma_f32_32x32x16_bf16 v[64:79], a[148:151], v[52:55], v[64:79]\n"
        "ds_read_b128 v[44:47], v3 offset:40960\n"
        "s_waitcnt lgkmcnt(5)\n"
        "v_mfma_f32_32x32x16_bf16 v[64:79], a[152:155], v[56:59], v[64:79]\n"
        "ds_read_b128 v[48:51], v4 offset:40960\n"
        "s_waitcnt lgkmcnt(5)\n"
        "v_mfma_f32_32x32x16_bf16 v[64:79], a[156:159], v[60:63], v[64:79]\n"
        "ds_read_b128 v[52:55], v5 offset:40960\n"
        "s_mul_i32 s68, s64, 0\n"
        "s_add_u32 s68, s68, 0\n"
        "s_mul_i32 s69, s64, 512\n"
        "s_add_u32 s69, s69, s77\n"
        "s_add_u32 s70, s70, 40960\n"
        "s_add_u32 s64, s64, 1\n"
        "s_cmp_lt_u32 s64, 64\n"
        "s_cbranch_scc0 VB_end\n"
        "s_waitcnt lgkmcnt(5)\n"
        "v_mfma_f32_32x32x16_bf16 v[160:175], a[0:3], v[32:35], 0\n"
        "ds_read_b128 v[56:59], v6 offset:40960\n"
        "s_mul_i32 s78, s64, 1310720\n"
        "s_add_u32 s78, s78, 55574528\n"
        "s_waitcnt lgkmcnt(5)\n"
        "v_mfma_f32_32x32x16_bf16 v[160:175], a[4:7], v[36:39], v[160:175]\n"
        "ds_read_b128 v[60:63], v7 offset:40960\n"
        "v_cvt_pk_bf16_f32 v176, v64, v65\n"
        "v_cvt_pk_bf16_f32 v177, v66, v67\n"
        "v_cvt_pk_bf16_f32 v178, v68, v69\n"
        "v_cvt_pk_bf16_f32 v179, v70, v71\n"
        "ds_write_b128 v19, v[176:179] offset:8192\n"
        "s_waitcnt lgkmcnt(6)\n"
        "v_mfma_f32_32x32x16_bf16 v[160:175], a[8:11], v[40:43], v[160:175]\n"
        "ds_read_b128 v[32:35], v0 offset:41216\n"
        "v_cvt_pk_bf16_f32 v180, v72, v73\n"
        "v_cvt_pk_bf16_f32 v181, v74, v75\n"
        "v_cvt_pk_bf16_f32 v182, v76, v77\n"
        "v_cvt_pk_bf16_f32 v183, v78, v79\n"
        "ds_write_b128 v19, v[180:183] offset:9216\n"
        "s_waitcnt lgkmcnt(7)\n"
        "v_mfma_f32_32x32x16_bf16 v[160:175], a[12:15], v[44:47], v[160:175]\n"
        "ds_read_b128 v[36:39], v1 offset:41216\n"
        "s_waitcnt lgkmcnt(7)\n"
        "v_mfma_f32_32x32x16_bf16 v[160:175], a[16:19], v[48:51], v[160:175]\n"
        "ds_read_b128 v[40:43], v2 offset:41216\n"
        "s_waitcnt lgkmcnt(2)\n"
        "s_barrier\n"
        "ds_read_b128 v[112:115], v23 offset:8192\n"
        "ds_read_b128 v[116:119], v23 offset:9216\n"
        "s_waitcnt lgkmcnt(9)\n"
        "v_mfma_f32_32x32x16_bf16 v[160:175], a[20:23], v[52:55], v[160:175]\n"
        "ds_read_b128 v[44:47], v3 offset:41216\n"
        "ds_read_b128 v[120:123], v23 offset:10240\n"
        "ds_read_b128 v[124:127], v23 offset:11264\n"
        "s_waitcnt lgkmcnt(11)\n"
        "v_mfma_f32_32x32x16_bf16 v[160:175], a[24:27], v[56:59], v[160:175]\n"
        "ds_read_b128 v[48:51], v4 offset:41216\n"
        "ds_read_b128 v[128:131], v23 offset:12288\n"
        "ds_read_b128 v[132:135], v23 offset:13312\n"
        "s_waitcnt lgkmcnt(13)\n"
        "v_mfma_f32_32x32x16_bf16 v[160:175], a[28:31], v[60:63], v[160:175]\n"
        "ds_read_b128 v[52:55], v5 offset:41216\n"
        "ds_read_b128 v[136:139], v23 offset:14336\n"
        "ds_read_b128 v[140:143], v23 offset:15360\n"
        "s_waitcnt lgkmcnt(14)\n"
        "v_mfma_f32_32x32x16_bf16 v[160:175], a[32:35], v[32:35], v[160:175]\n"
        "ds_read_b128 v[56:59], v6 offset:41216\n"
        "s_waitcnt lgkmcnt(13)\n"
        "v_mfma_f32_32x32x16_bf16 v[160:175], a[36:39], v[36:39], v[160:175]\n"
        "ds_read_b128 v[60:63], v7 offset:41216\n"
        "s_waitcnt lgkmcnt(12)\n"
        "v_mfma_f32_32x32x16_bf16 v[80:95], v[112:115], a[160:163], 0\n"
        "s_add_u32 m0, s71, 0\n"
        "s_nop 0\n"
        "buffer_load_dwordx4 v8, s[72:75], s70 offen lds\n"
        "s_waitcnt lgkmcnt(11)\n"
        "v_mfma_f32_32x32x16_bf16 v[80:95], v[116:119], a[164:167], v[80:95]\n"
        "s_add_u32 m0, s71, 4096\n"
        "s_nop 0\n"
        "buffer_load_dwordx4 v9, s[72:75], s70 offen lds\n"
        "s_waitcnt lgkmcnt(13)\n"
        "v_mfma_f32_32x32x16_bf16 v[160:175], a[40:43], v[40:43], v[160:175]\n"
        "ds_read_b128 v[32:35], v0 offset:41472\n"
        "s_add_u32 m0, s71, 8192\n"
        "s_nop 0\n"
        "buffer_load_dwordx4 v10, s[72:75], s70 offen lds\n"
        "s_waitcnt lgkmcnt(10)\n"
        "v_mfma_f32_32x32x16_bf16 v[80:95], v[120:123], a[168:171], v[80:95]\n"
        "s_add_u32 m0, s71, 12288\n"
        "s_nop 0\n"
        "buffer_load_dwordx4 v11, s[72:75], s70 offen lds\n"
        "s_waitcnt lgkmcnt(9)\n"
        "v_mfma_f32_32x32x16_bf16 v[80:95], v[124:127], a[172:175], v[80:95]\n"
        "s_add_u32 m0, s71, 16384\n"
        "s_nop 0\n"
        "buffer_load_dwordx4 v12, s[72:75], s70 offen lds\n"
        "s_waitcnt lgkmcnt(11)\n"
        "v_mfma_f32_32x32x16_bf16 v[160:175], a[44:47], v[44:47], v[160:175]\n"
        "ds_read_b128 v[36:39], v1 offset:41472\n"
        "s_add_u32 m0, s71, 20480\n"
        "s_nop 0\n"
        "buffer_load_dwordx4 v13, s[72:75], s70 offen lds\n"
        "s_waitcnt lgkmcnt(8)\n"
        "v_mfma_f32_32x32x16_bf16 v[80:95], v[128:131], a[176:179], v[80:95]\n"
        "s_add_u32 m0, s71, 24576\n"
        "s_nop 0\n"
        "buffer_load_dwordx4 v14, s[72:75], s70 offen lds\n"
        "s_waitcnt lgkmcnt(7)\n"
        "v_mfma_f32_32x32x16_bf16 v[80:95], v[132:135], a[180:183], v[80:95]\n"
        "s_add_u32 m0, s71, 28672\n"
        "s_nop 0\n"
        "buffer_load_dwordx4 v15, s[72:75], s70 offen lds\n"
        "s_waitcnt lgkmcnt(9)\n"
        "v_mfma_f32_32x32x16_bf16 v[160:175], a[48:51], v[48:51], v[160:175]\n"
        "ds_read_b128 v[40:43], v2 offset:41472\n"
        "s_add_u32 m0, s71, 32768\n"
        "s_nop 0\n"
        "buffer_load_dwordx4 v16, s[72:75], s70 offen lds\n"
        "s_waitcnt lgkmcnt(6)\n"
        "v_mfma_f32_32x32x16_bf16 v[80:95], v[136:139], a[184:187], v[80:95]\n"
        "s_add_u32 m0, s71, 36864\n"
        "s_nop 0\n"
        "buffer_load_dwordx4 v17, s[72:75], s70 offen lds\n"
        "s_waitcnt lgkmcnt(5)\n"
        "v_mfma_f32_32x32x16_bf16 v[80:95], v[140:143], a[188:191], v[80:95]\n"
        "s_waitcnt lgkmcnt(7)\n"
        "v_mfma_f32_32x32x16_bf16 v[160:175], a[52:55], v[52:55], v[160:175]\n"
        "ds_read_b128 v[44:47], v3 offset:41472\n"
        "v_mfma_f32_32x32x16_bf16 v[96:111], v[112:115], a[192:195], 0\n"
        "s_waitcnt vmcnt(10)\n"
        "buffer_store_dword v20, v21, s[60:63], s69 offen sc1\n"
        "v_cvt_pk_bf16_f32 v144, v80, v81\n"
        "v_cvt_pk_bf16_f32 v145, v82, v83\n"
        "v_cvt_pk_bf16_f32 v146, v84, v85\n"
        "v_mfma_f32_32x32x16_bf16 v[96:111], v[116:119], a[196:199], v[96:111]\n"
        "v_cvt_pk_bf16_f32 v147, v86, v87\n"
        "buffer_store_dwordx4 v[144:147], v18, s[60:63], s78 offen sc1\n"
        "v_cvt_pk_bf16_f32 v148, v88, v89\n"
        "s_waitcnt lgkmcnt(5)\n"
        "v_mfma_f32_32x32x16_bf16 v[160:175], a[56:59], v[56:59], v[160:175]\n"
        "ds_read_b128 v[48:51], v4 offset:41472\n"
        "v_cvt_pk_bf16_f32 v149, v90, v91\n"
        "v_cvt_pk_bf16_f32 v150, v92, v93\n"
        "v_cvt_pk_bf16_f32 v151, v94, v95\n"
        "v_mfma_f32_32x32x16_bf16 v[96:111], v[120:123], a[200:203], v[96:111]\n"
        "buffer_store_dwordx4 v[148:151], v18, s[60:63], s78 offen offset:512 sc1\n"
        "s_add_u32 s78, s78, 262144\n"
        "v_mfma_f32_32x32x16_bf16 v[96:111], v[124:127], a[204:207], v[96:111]\n"
        "s_waitcnt lgkmcnt(5)\n"
        "v_mfma_f32_32x32x16_bf16 v[160:175], a[60:63], v[60:63], v[160:175]\n"
        "ds_read_b128 v[52:55], v5 offset:41472\n"
        "v_mfma_f32_32x32x16_bf16 v[96:111], v[128:131], a[208:211], v[96:111]\n"
        "v_mfma_f32_32x32x16_bf16 v[96:111], v[132:135], a[212:215], v[96:111]\n"
        "s_waitcnt lgkmcnt(5)\n"
        "v_mfma_f32_32x32x16_bf16 v[160:175], a[64:67], v[32:35], v[160:175]\n"
        "ds_read_b128 v[56:59], v6 offset:41472\n"
        "v_mfma_f32_32x32x16_bf16 v[96:111], v[136:139], a[216:219], v[96:111]\n"
        "v_mfma_f32_32x32x16_bf16 v[96:111], v[140:143], a[220:223], v[96:111]\n"
        "s_waitcnt lgkmcnt(5)\n"
        "v_mfma_f32_32x32x16_bf16 v[160:175], a[68:71], v[36:39], v[160:175]\n"
        "ds_read_b128 v[60:63], v7 offset:41472\n"
        "v_mfma_f32_32x32x16_bf16 v[80:95], v[112:115], a[224:227], 0\n"
        "v_cvt_pk_bf16_f32 v152, v96, v97\n"
        "v_cvt_pk_bf16_f32 v153, v98, v99\n"
        "v_cvt_pk_bf16_f32 v154, v100, v101\n"
        "v_mfma_f32_32x32x16_bf16 v[80:95], v[116:119], a[228:231], v[80:95]\n"
        "v_cvt_pk_bf16_f32 v155, v102, v103\n"
        "buffer_store_dwordx4 v[152:155], v18, s[60:63], s78 offen sc1\n"
        "v_cvt_pk_bf16_f32 v156, v104, v105\n"
        "s_waitcnt lgkmcnt(5)\n"
        "v_mfma_f32_32x32x16_bf16 v[160:175], a[72:75], v[40:43], v[160:175]\n"
        "ds_read_b128 v[32:35], v0 offset:41728\n"
        "v_cvt_pk_bf16_f32 v157, v106, v107\n"
        "v_cvt_pk_bf16_f32 v158, v108, v109\n"
        "v_cvt_pk_bf16_f32 v159, v110, v111\n"
        "v_mfma_f32_32x32x16_bf16 v[80:95], v[120:123], a[232:235], v[80:95]\n"
        "buffer_store_dwordx4 v[156:159], v18, s[60:63], s78 offen offset:512 sc1\n"
        "s_add_u32 s78, s78, 262144\n"
        "v_mfma_f32_32x32x16_bf16 v[80:95], v[124:127], a[236:239], v[80:95]\n"
        "s_waitcnt lgkmcnt(5)\n"
        "v_mfma_f32_32x32x16_bf16 v[160:175], a[76:79], v[44:47], v[160:175]\n"
        "ds_read_b128 v[36:39], v1 offset:41728\n"
        "v_mfma_f32_32x32x16_bf16 v[80:95], v[128:131], a[240:243], v[80:95]\n"
        "v_mfma_f32_32x32x16_bf16 v[80:95], v[132:135], a[244:247], v[80:95]\n"
        "s_waitcnt lgkmcnt(5)\n"
        "v_mfma_f32_32x32x16_bf16 v[160:175], a[80:83], v[48:51], v[160:175]\n"
        "ds_read_b128 v[40:43], v2 offset:41728\n"
        "v_mfma_f32_32x32x16_bf16 v[80:95], v[136:139], a[248:251], v[80:95]\n"
        "v_mfma_f32_32x32x16_bf16 v[80:95], v[140:143], a[252:255], v[80:95]\n"
        "s_waitcnt lgkmcnt(5)\n"
        "v_mfma_f32_32x32x16_bf16 v[160:175], a[84:87], v[52:55], v[160:175]\n"
        "ds_read_b128 v[44:47], v3 offset:41728\n"
        "v_mfma_f32_32x32x16_bf16 v[96:111], v[112:115], v[192:195], 0\n"
        "v_cvt_pk_bf16_f32 v144, v80, v81\n"
        "v_cvt_pk_bf16_f32 v145, v82, v83\n"
        "v_cvt_pk_bf16_f32 v146, v84, v85\n"
        "v_mfma_f32_32x32x16_bf16 v[96:111], v[116:119], v[196:199], v[96:111]\n"
        "v_cvt_pk_bf16_f32 v147, v86, v87\n"
        "buffer_store_dwordx4 v[144:147], v18, s[60:63], s78 offen sc1\n"
        "v_cvt_pk_bf16_f32 v148, v88, v89\n"
        "s_waitcnt lgkmcnt(5)\n"
        "v_mfma_f32_32x32x16_bf16 v[160:175], a[88:91], v[56:59], v[160:175]\n"
        "ds_read_b128 v[48:51], v4 offset:41728\n"
        "v_cvt_pk_bf16_f32 v149, v90, v91\n"
        "v_cvt_pk_bf16_f32 v150, v92, v93\n"
        "v_cvt_pk_bf16_f32 v151, v94, v95\n"
        "v_mfma_f32_32x32x16_bf16 v[96:111], v[120:123], v[200:203], v[96:111]\n"
        "buffer_store_dwordx4 v[148:151], v18, s[60:63], s78 offen offset:512 sc1\n"
        "s_add_u32 s78, s78, 262144\n"
        "v_mfma_f32_32x32x16_bf16 v[96:111], v[124:127], v[204:207], v[96:111]\n"
        "s_waitcnt lgkmcnt(5)\n"
        "v_mfma_f32_32x32x16_bf16 v[160:175], a[92:95], v[60:63], v[160:175]\n"
        "ds_read_b128 v[52:55], v5 offset:41728\n"
        "v_mfma_f32_32x32x16_bf16 v[96:111], v[128:131], v[208:211], v[96:111]\n"
        "v_mfma_f32_32x32x16_bf16 v[96:111], v[132:135], v[212:215], v[96:111]\n"
        "s_waitcnt lgkmcnt(5)\n"
        "v_mfma_f32_32x32x16_bf16 v[160:175], a[96:99], v[32:35], v[160:175]\n"
        "ds_read_b128 v[56:59], v6 offset:41728\n"
        "v_mfma_f32_32x32x16_bf16 v[96:111], v[136:139], v[216:219], v[96:111]\n"
        "v_mfma_f32_32x32x16_bf16 v[96:111], v[140:143], v[220:223], v[96:111]\n"
        "s_waitcnt lgkmcnt(5)\n"
        "v_mfma_f32_32x32x16_bf16 v[160:175], a[100:103], v[36:39], v[160:175]\n"
        "ds_read_b128 v[60:63], v7 offset:41728\n"
        "v_mfma_f32_32x32x16_bf16 v[80:95], v[112:115], v[224:227], 0\n"
        "v_cvt_pk_bf16_f32 v152, v96, v97\n"
        "v_cvt_pk_bf16_f32 v153, v98, v99\n"
        "v_cvt_pk_bf16_f32 v154, v100, v101\n"
        "v_mfma_f32_32x32x16_bf16 v[80:95], v[116:119], v[228:231], v[80:95]\n"
        "v_cvt_pk_bf16_f32 v155, v102, v103\n"
        "buffer_store_dwordx4 v[152:155], v18, s[60:63], s78 offen sc1\n"
        "v_cvt_pk_bf16_f32 v156, v104, v105\n"
        "s_waitcnt lgkmcnt(5)\n"
        "v_mfma_f32_32x32x16_bf16 v[160:175], a[104:107], v[40:43], v[160:175]\n"
        "ds_read_b128 v[32:35], v0 offset:41984\n"
        "v_cvt_pk_bf16_f32 v157, v106, v107\n"
        "v_cvt_pk_bf16_f32 v158, v108, v109\n"
        "v_cvt_pk_bf16_f32 v159, v110, v111\n"
        "v_mfma_f32_32x32x16_bf16 v[80:95], v[120:123], v[232:235], v[80:95]\n"
        "buffer_store_dwordx4 v[156:159], v18, s[60:63], s78 offen offset:512 sc1\n"
        "s_add_u32 s78, s78, 262144\n"
        "v_mfma_f32_32x32x16_bf16 v[80:95], v[124:127], v[236:239], v[80:95]\n"
        "s_waitcnt lgkmcnt(5)\n"
        "v_mfma_f32_32x32x16_bf16 v[160:175], a[108:111], v[44:47], v[160:175]\n"
        "ds_read_b128 v[36:39], v1 offset:41984\n"
        "v_mfma_f32_32x32x16_bf16 v[80:95], v[128:131], v[240:243], v[80:95]\n"
        "v_mfma_f32_32x32x16_bf16 v[80:95], v[132:135], v[244:247], v[80:95]\n"
        "s_waitcnt lgkmcnt(5)\n"
        "v_mfma_f32_32x32x16_bf16 v[160:175], a[112:115], v[48:51], v[160:175]\n"
        "ds_read_b128 v[40:43], v2 offset:41984\n"
        "v_mfma_f32_32x32x16_bf16 v[80:95], v[136:139], v[248:251], v[80:95]\n"
        "v_mfma_f32_32x32x16_bf16 v[80:95], v[140:143], v[252:255], v[80:95]\n"
        "s_waitcnt lgkmcnt(5)\n"
        "v_mfma_f32_32x32x16_bf16 v[160:175], a[116:119], v[52:55], v[160:175]\n"
        "ds_read_b128 v[44:47], v3 offset:41984\n"
        "s_waitcnt lgkmcnt(5)\n"
        "v_mfma_f32_32x32x16_bf16 v[160:175], a[120:123], v[56:59], v[160:175]\n"
        "ds_read_b128 v[48:51], v4 offset:41984\n"
        "v_cvt_pk_bf16_f32 v144, v80, v81\n"
        "v_cvt_pk_bf16_f32 v145, v82, v83\n"
        "v_cvt_pk_bf16_f32 v146, v84, v85\n"
        "s_waitcnt lgkmcnt(5)\n"
        "v_mfma_f32_32x32x16_bf16 v[160:175], a[124:127], v[60:63], v[160:175]\n"
        "ds_read_b128 v[52:55], v5 offset:41984\n"
        "v_cvt_pk_bf16_f32 v147, v86, v87\n"
        "buffer_store_dwordx4 v[144:147], v18, s[60:63], s78 offen sc1\n"
        "v_cvt_pk_bf16_f32 v148, v88, v89\n"
        "s_waitcnt lgkmcnt(5)\n"
        "v_mfma_f32_32x32x16_bf16 v[160:175], a[128:131], v[32:35], v[160:175]\n"
        "ds_read_b128 v[56:59], v6 offset:41984\n"
        "v_cvt_pk_bf16_f32 v149, v90, v91\n"
        "v_cvt_pk_bf16_f32 v150, v92, v93\n"
        "v_cvt_pk_bf16_f32 v151, v94, v95\n"
        "s_waitcnt lgkmcnt(5)\n"
        "v_mfma_f32_32x32x16_bf16 v[160:175], a[132:135], v[36:39], v[160:175]\n"
        "ds_read_b128 v[60:63], v7 offset:41984\n"
        "buffer_store_dwordx4 v[148:151], v18, s[60:63], s78 offen offset:512 sc1\n"
        "s_add_u32 s78, s78, 262144\n"
        "s_waitcnt vmcnt(11)\n"
        "s_barrier\n"
        "s_waitcnt lgkmcnt(5)\n"
        "v_mfma_f32_32x32x16_bf16 v[160:175], a[136:139], v[40:43], v[160:175]\n"
        "ds_read_b128 v[32:35], v0 offset:0\n"
        "s_waitcnt lgkmcnt(5)\n"
        "v_mfma_f32_32x32x16_bf16 v[160:175], a[140:143], v[44:47], v[160:175]\n"
        "ds_read_b128 v[36:39], v1 offset:0\n"
        "s_waitcnt lgkmcnt(5)\n"
        "v_mfma_f32_32x32x16_bf16 v[160:175], a[144:147], v[48:51], v[160:175]\n"
        "ds_read_b128 v[40:43], v2 offset:0\n"
        "s_waitcnt lgkmcnt(5)\n"
        "v_mfma_f32_32x32x16_bf16 v[160:175], a[148:151], v[52:55], v[160:175]\n"
        "ds_read_b128 v[44:47], v3 offset:0\n"
        "s_waitcnt lgkmcnt(5)\n"
        "v_mfma_f32_32x32x16_bf16 v[160:175], a[152:155], v[56:59], v[160:175]\n"
        "ds_read_b128 v[48:51], v4 offset:0\n"
        "s_waitcnt lgkmcnt(5)\n"
        "v_mfma_f32_32x32x16_bf16 v[160:175], a[156:159], v[60:63], v[160:175]\n"
        "ds_read_b128 v[52:55], v5 offset:0\n"
        "s_mul_i32 s68, s64, 0\n"
        "s_add_u32 s68, s68, 0\n"
        "s_mul_i32 s69, s64, 512\n"
        "s_add_u32 s69, s69, s77\n"
        "s_add_u32 s70, s70, 40960\n"
        "s_add_u32 s64, s64, 1\n"
        "s_cmp_lt_u32 s64, 64\n"
        "s_cbranch_scc1 VB_loop\n"
        "VB_end:\n"
        "s_waitcnt vmcnt(0) lgkmcnt(0)\n"
        "buffer_store_dword v20, v21, s[60:63], s69 offen sc1\n"
        "s_endpgm\n"
        ROLE_OPS(p.wv, p.wo) : "v32", "v33", "v34", "v35", "v36", "v37", "v38", "v39", "v40", "v41", "v42", "v43", "v44", "v45", "v46", "v47", "v48", "v49", "v50", "v51", "v52", "v53", "v54", "v55", "v56", "v57", "v58", "v59", "v60", "v61", "v62", "v63", "v64", "v65", "v66", "v67", "v68", "v69", "v70", "v71", "v72", "v73", "v74", "v75", "v76", "v77", "v78", "v79", "v80", "v81", "v82", "v83", "v84", "v85", "v86", "v87", "v88", "v89", "v90", "v91", "v92", "v93", "v94", "v95", "v96", "v97", "v98", "v99", "v100", "v101", "v102", "v103", "v104", "v105", "v106", "v107", "v108", "v109", "v110", "v111", "v112", "v113", "v114", "v115", "v116", "v117", "v118", "v119", "v120", "v121", "v122", "v123", "v124", "v125", "v126", "v127", "v128", "v129", "v130", "v131", "v132", "v133", "v134", "v135", "v136", "v137", "v138", "v139", "v140", "v141", "v142", "v143", "v144", "v145", "v146", "v147", "v148", "v149", "v150", "v151", "v152", "v153", "v154", "v155", "v156", "v157", "v158", "v159", "v160", "v161", "v162", "v163", "v164", "v165", "v166", "v167", "v168", "v169", "v170", "v171", "v172", "v173", "v174", "v175", "v176", "v177", "v178", "v179", "v180", "v181", "v182", "v183", "v184", "v185", "v186", "v187", "v188", "v189", "v190", "v192", "v193", "v194", "v195", "v196", "v197", "v198", "v199", "v200", "v201", "v202", "v203", "v204", "v205", "v206", "v207", "v208", "v209", "v210", "v211", "v212", "v213", "v214", "v215", "v216", "v217", "v218", "v219", "v220", "v221", "v222", "v223", "v224", "v225", "v226", "v227", "v228", "v229", "v230", "v231", "v232", "v233", "v234", "v235", "v236", "v237", "v238", "v239", "v240", "v241", "v242", "v243", "v244", "v245", "v246", "v247", "v248", "v249", "v250", "v251", "v252", "v253", "v254", "v255", "s38", "s39", "s40", "s41", "s42", "s43", "s44", "s45", "s46", "s47", "s48", "s49", "s50", "s51", "s52", "s53", "s54", "s55", "s56", "s57", "s58", "s59", "s60", "s61", "s62", "s63", "s64", "s65", "s66", "s67", "s68", "s69", "s70", "s71", "s72", "s73", "s74", "s75", "s76", "s77", "s78", "s79", "s80", "s81", "s82", "s83", "s84", "s85", "s86", "s87", "s88", "s89", "s90", "s91", "s92", "s93", "s94", "s95", "s96", "s97", "s98", "s99", "vcc", "scc", "memory", "m0", "a0", "a1", "a2", "a3", "a4", "a5", "a6", "a7", "a8", "a9", "a10", "a11", "a12", "a13", "a14", "a15", "a16", "a17", "a18", "a19", "a20", "a21", "a22", "a23", "a24", "a25", "a26", "a27", "a28", "a29", "a30", "a31", "a32", "a33", "a34", "a35", "a36", "a37", "a38", "a39", "a40", "a41", "a42", "a43", "a44", "a45", "a46", "a47", "a48", "a49", "a50", "a51", "a52", "a53", "a54", "a55", "a56", "a57", "a58", "a59", "a60", "a61", "a62", "a63", "a64", "a65", "a66", "a67", "a68", "a69", "a70", "a71", "a72", "a73", "a74", "a75", "a76", "a77", "a78", "a79", "a80", "a81", "a82", "a83", "a84", "a85", "a86", "a87", "a88", "a89", "a90", "a91", "a92", "a93", "a94", "a95", "a96", "a97", "a98", "a99", "a100", "a101", "a102", "a103", "a104", "a105", "a106", "a107", "a108", "a109", "a110", "a111", "a112", "a113", "a114", "a115", "a116", "a117", "a118", "a119", "a120", "a121", "a122", "a123", "a124", "a125", "a126", "a127", "a128", "a129", "a130", "a131", "a132", "a133", "a134", "a135", "a136", "a137", "a138", "a139", "a140", "a141", "a142", "a143", "a144", "a145", "a146", "a147", "a148", "a149", "a150", "a151", "a152", "a153", "a154", "a155", "a156", "a157", "a158", "a159", "a160", "a161", "a162", "a163", "a164", "a165", "a166", "a167", "a168", "a169", "a170", "a171", "a172", "a173", "a174", "a175", "a176", "a177", "a178", "a179", "a180", "a181", "a182", "a183", "a184", "a185", "a186", "a187", "a188", "a189", "a190", "a191", "a192", "a193", "a194", "a195", "a196", "a197", "a198", "a199", "a200", "a201", "a202", "a203", "a204", "a205", "a206", "a207", "a208", "a209", "a210", "a211", "a212", "a213", "a214", "a215", "a216", "a217", "a218", "a219", "a220", "a221", "a222", "a223", "a224", "a225", "a226", "a227", "a228", "a229", "a230", "a231", "a232", "a233", "a234", "a235", "a236", "a237", "a238", "a239", "a240", "a241", "a242", "a243", "a244", "a245", "a246", "a247", "a248", "a249", "a250", "a251", "a252", "a253", "a254", "a255");
    __builtin_unreachable();
}

DI u32 naps(u32 n, u32 t0) { const int r = (int)n - (int)(((u32)__builtin_amdgcn_s_memrealtime() - t0) / 46u); return r > 0 ? r : 0; }
DI void role_f1(const Params& p, rsrc_t rs, int h, int wave, u32 t0) {
    const int lane = lane_id(), tid = wave * 64 + lane;
    u32x32 ad = {}; xaddr(ad, tid, lane);
    {
        const u32 r = tid >> 3, f = 4 * ((r >> 1) & 1);
        ad[24] = r * 3456 + (((tid & 7) ^ f) << 4); ad[25] = r * 3456 + (((tid & 7) ^ f ^ 2) << 4);
        const u32 rho = (lane >> 2) & 3, c4 = lane & 3, cb = (lane >> 4) & 1;
        ad[26] = cb * 16384 + (8 * (lane >> 5) + rho) * 128 + (((2 * wave + (c4 >> 1)) ^ (4 * (rho >> 1)) ^ (2 * cb)) << 4) + (c4 & 1) * 8;
    }
    ad[18] = (4 * h + wave) * 1024 + lane * 16;
    if (0) h |= naps(p.tune[4] & 255, t0) << 8;
    asm volatile(
        "s_mov_b32 s60, %[r0]\n"
        "s_mov_b32 s61, %[r1]\n"
        "s_mov_b32 s62, 0x0f000000\n"
        "s_mov_b32 s63, 0x00020000\n"
        "s_mov_b32 s67, %[wave]\n"
        "s_mov_b32 s58, %[h]\n"
        "s_mov_b32 s59, %[ts]\n"
        "s_mov_b32 s66, 0x100000\n"
        "s_mov_b32 s64, 0\n"
        "s_lshl_b32 s71, s67, 10\n"
        "s_add_u32 s88, %[hs], 4333568\n"
        "s_add_u32 s89, %[hs], 4325376\n"
        "s_add_u32 s90, %[hs], 4305920\n"
        "buffer_load_dword v188, off, s[60:63], s88 sc1\n"
        "s_waitcnt vmcnt(0)\n"
        "v_add_u32 v188, 1, v188\n"
        "s_mov_b64 exec, 1\n"
        "buffer_store_dword v188, off, s[60:63], s89 sc1\n"
        "s_mov_b64 exec, -1\n"
        "s_and_b32 s65, %[t4], 255\n"
        "s_add_u32 s65, s65, 1\n"
        "F1_nap:\n"
        "s_sub_u32 s65, s65, 1\n"
        "s_cmp_eq_u32 s65, 0\n"
        "s_cbranch_scc1 F1_napd\n"
        "s_sleep 16\n"
        "s_branch F1_nap\n"
        "F1_napd:\n"
        "s_mov_b32 s72, %[x0]\n"
        "s_mov_b32 s73, %[x1]\n"
        "s_mov_b32 s74, 2621440\n"
        "s_mov_b32 s75, 0x00020000\n"
        "s_mov_b32 s70, 0\n"
        "s_mov_b32 s68, 4026531840\n"
        "s_mov_b32 s69, 4026531840\n"
        "s_mov_b32 s76, 4026531840\n"
        "s_mov_b32 s70, 40960\n"
        "s_mov_b32 s80, %[a0]\n"
        "s_mov_b32 s81, %[a1]\n"
        "s_mov_b32 s82, 2211840\n"
        "s_mov_b32 s83, 0x00020000\n"
        "s_mov_b32 s84, %[b0]\n"
        "s_mov_b32 s85, %[b1]\n"
        "s_mov_b32 s86, s82\n"
        "s_mov_b32 s87, 0x00020000\n"
        "s_lshl_b32 s78, s58, 7\n"
        "s_add_u32 m0, s71, 40960\n"
        "s_add_u32 s65, s78, 0\n"
        "buffer_load_dwordx4 v24, s[80:83], s65 offen lds\n"
        "s_add_u32 m0, s71, 45056\n"
        "s_add_u32 s65, s78, 110592\n"
        "buffer_load_dwordx4 v24, s[80:83], s65 offen lds\n"
        "s_add_u32 m0, s71, 49152\n"
        "s_add_u32 s65, s78, 221184\n"
        "buffer_load_dwordx4 v24, s[80:83], s65 offen lds\n"
        "s_add_u32 m0, s71, 53248\n"
        "s_add_u32 s65, s78, 331776\n"
        "buffer_load_dwordx4 v24, s[80:83], s65 offen lds\n"
        "s_add_u32 m0, s71, 57344\n"
        "s_add_u32 s65, s78, 0\n"
        "buffer_load_dwordx4 v25, s[84:87], s65 offen lds\n"
        "s_add_u32 m0, s71, 61440\n"
        "s_add_u32 s65, s78, 110592\n"
        "buffer_load_dwordx4 v25, s[84:87], s65 offen lds\n"
        "s_add_u32 m0, s71, 65536\n"
        "s_add_u32 s65, s78, 221184\n"
        "buffer_load_dwordx4 v25, s[84:87], s65 offen lds\n"
        "s_add_u32 m0, s71, 69632\n"
        "s_add_u32 s65, s78, 331776\n"
        "buffer_load_dwordx4 v25, s[84:87], s65 offen lds\n"
        "s_add_u32 m0, s71, 73728\n"
        "s_add_u32 s65, s78, 442368\n"
        "buffer_load_dwordx4 v24, s[80:83], s65 offen lds\n"
        "s_add_u32 m0, s71, 77824\n"
        "s_add_u32 s65, s78, 552960\n"
        "buffer_load_dwordx4 v24, s[80:83], s65 offen lds\n"
        "s_add_u32 m0, s71, 81920\n"
        "s_add_u32 s65, s78, 663552\n"
        "buffer_load_dwordx4 v24, s[80:83], s65 offen lds\n"
        "s_add_u32 m0, s71, 86016\n"
        "s_add_u32 s65, s78, 774144\n"
        "buffer_load_dwordx4 v24, s[80:83], s65 offen lds\n"
        "s_add_u32 m0, s71, 90112\n"
        "s_add_u32 s65, s78, 442368\n"
        "buffer_load_dwordx4 v25, s[84:87], s65 offen lds\n"
        "s_add_u32 m0, s71, 94208\n"
        "s_add_u32 s65, s78, 552960\n"
        "buffer_load_dwordx4 v25, s[84:87], s65 offen lds\n"
        "s_add_u32 m0, s71, 98304\n"
        "s_add_u32 s65, s78, 663552\n"
        "buffer_load_dwordx4 v25, s[84:87], s65 offen lds\n"
        "s_add_u32 m0, s71, 102400\n"
        "s_add_u32 s65, s78, 774144\n"
        "buffer_load_dwordx4 v25, s[84:87], s65 offen lds\n"
        "s_add_u32 m0, s71, 0\n"
        "s_mov_b32 s65, 0\n"
        "buffer_load_dwordx4 v8, s[72:75], s65 offen lds\n"
        "s_add_u32 m0, s71, 4096\n"
        "s_mov_b32 s65, 0\n"
        "buffer_load_dwordx4 v9, s[72:75], s65 offen lds\n"
        "s_add_u32 m0, s71, 8192\n"
        "s_mov_b32 s65, 0\n"
        "buffer_load_dwordx4 v10, s[72:75], s65 offen lds\n"
        "s_add_u32 m0, s71, 12288\n"
        "s_mov_b32 s65, 0\n"
        "buffer_load_dwordx4 v11, s[72:75], s65 offen lds\n"
        "s_add_u32 m0, s71, 16384\n"
        "s_mov_b32 s65, 0\n"
        "buffer_load_dwordx4 v12, s[72:75], s65 offen lds\n"
        "s_add_u32 m0, s71, 20480\n"
        "s_mov_b32 s65, 0\n"
        "buffer_load_dwordx4 v13, s[72:75], s65 offen lds\n"
        "s_add_u32 m0, s71, 24576\n"
        "s_mov_b32 s65, 0\n"
        "buffer_load_dwordx4 v14, s[72:75], s65 offen lds\n"
        "s_add_u32 m0, s71, 28672\n"
        "s_mov_b32 s65, 0\n"
        "buffer_load_dwordx4 v15, s[72:75], s65 offen lds\n"
        "s_add_u32 m0, s71, 32768\n"
        "s_mov_b32 s65, 0\n"
        "buffer_load_dwordx4 v16, s[72:75], s65 offen lds\n"
        "s_add_u32 m0, s71, 36864\n"
        "s_mov_b32 s65, 0\n"
        "buffer_load_dwordx4 v17, s[72:75], s65 offen lds\n"
        "s_add_u32 m0, s71, 106496\n"
        "s_add_u32 s65, s78, 884736\n"
        "buffer_load_dwordx4 v24, s[80:83], s65 offen lds\n"
        "s_add_u32 m0, s71, 110592\n"
        "s_add_u32 s65, s78, 995328\n"
        "buffer_load_dwordx4 v24, s[80:83], s65 offen lds\n"
        "s_add_u32 m0, s71, 114688\n"
        "s_add_u32 s65, s78, 1105920\n"
        "buffer_load_dwordx4 v24, s[80:83], s65 offen lds\n"
        "s_add_u32 m0, s71, 118784\n"
        "s_add_u32 s65, s78, 1216512\n"
        "buffer_load_dwordx4 v24, s[80:83], s65 offen lds\n"
        "s_add_u32 m0, s71, 122880\n"
        "s_add_u32 s65, s78, 884736\n"
        "buffer_load_dwordx4 v25, s[84:87], s65 offen lds\n"
        "s_add_u32 m0, s71, 126976\n"
        "s_add_u32 s65, s78, 995328\n"
        "buffer_load_dwordx4 v25, s[84:87], s65 offen lds\n"
        "s_add_u32 m0, s71, 131072\n"
        "s_add_u32 s65, s78, 1105920\n"
        "buffer_load_dwordx4 v25, s[84:87], s65 offen lds\n"
        "s_add_u32 m0, s71, 135168\n"
        "s_add_u32 s65, s78, 1216512\n"
        "buffer_load_dwordx4 v25, s[84:87], s65 offen lds\n"
        "v_add_u32 v28, 40960, v26\n"
        "v_add_u32 v29, 40960, v27\n"
        "s_waitcnt vmcnt(8)\n"
        "s_barrier\n"
        "ds_read_b64_tr_b16 a[0:1], v28 offset:0\n"
        "ds_read_b64_tr_b16 a[2:3], v28 offset:512\n"
        "ds_read_b64_tr_b16 a[4:5], v28 offset:2048\n"
        "ds_read_b64_tr_b16 a[6:7], v28 offset:2560\n"
        "ds_read_b64_tr_b16 a[8:9], v28 offset:4096\n"
        "ds_read_b64_tr_b16 a[10:11], v28 offset:4608\n"
        "ds_read_b64_tr_b16 a[12:13], v28 offset:6144\n"
        "ds_read_b64_tr_b16 a[14:15], v28 offset:6656\n"
        "ds_read_b64_tr_b16 a[16:17], v28 offset:8192\n"
        "ds_read_b64_tr_b16 a[18:19], v28 offset:8704\n"
        "ds_read_b64_tr_b16 a[20:21], v28 offset:10240\n"
        "ds_read_b64_tr_b16 a[22:23], v28 offset:10752\n"
        "ds_read_b64_tr_b16 a[24:25], v28 offset:12288\n"
        "ds_read_b64_tr_b16 a[26:27], v28 offset:12800\n"
        "ds_read_b64_tr_b16 a[28:29], v28 offset:14336\n"
        "ds_read_b64_tr_b16 a[30:31], v28 offset:14848\n"
        "s_waitcnt lgkmcnt(0)\n"
        "s_barrier\n"
        "s_add_u32 m0, s71, 40960\n"
        "s_add_u32 s65, s78, 1327104\n"
        "buffer_load_dwordx4 v24, s[80:83], s65 offen lds\n"
        "ds_read_b128 v[32:35], v0 offset:0\n"
        "ds_read_b128 v[36:39], v1 offset:0\n"
        "ds_read_b128 v[40:43], v2 offset:0\n"
        "ds_read_b128 v[44:47], v3 offset:0\n"
        "ds_read_b128 v[48:51], v4 offset:0\n"
        "ds_read_b128 v[52:55], v5 offset:0\n"
        "ds_read_b128 v[56:59], v6 offset:0\n"
        "ds_read_b128 v[60:63], v7 offset:0\n"
        "s_add_u32 m0, s71, 45056\n"
        "s_add_u32 s65, s78, 1437696\n"
        "buffer_load_dwordx4 v24, s[80:83], s65 offen lds\n"
        "s_add_u32 m0, s71, 49152\n"
        "s_add_u32 s65, s78, 1548288\n"
        "buffer_load_dwordx4 v24, s[80:83], s65 offen lds\n"
        "s_waitcnt lgkmcnt(7)\n"
        "v_mfma_f32_32x32x16_bf16 v[64:79], a[0:3], v[32:35], 0\n"
        "s_add_u32 m0, s71, 53248\n"
        "s_add_u32 s65, s78, 1658880\n"
        "buffer_load_dwordx4 v24, s[80:83], s65 offen lds\n"
        "s_waitcnt lgkmcnt(6)\n"
        "v_mfma_f32_32x32x16_bf16 v[64:79], a[4:7], v[36:39], v[64:79]\n"
        "s_add_u32 m0, s71, 57344\n"
        "s_add_u32 s65, s78, 1327104\n"
        "buffer_load_dwordx4 v25, s[84:87], s65 offen lds\n"
        "s_waitcnt lgkmcnt(5)\n"
        "v_mfma_f32_32x32x16_bf16 v[64:79], a[8:11], v[40:43], v[64:79]\n"
        "s_add_u32 m0, s71, 61440\n"
        "s_add_u32 s65, s78, 1437696\n"
        "buffer_load_dwordx4 v25, s[84:87], s65 offen lds\n"
        "s_waitcnt lgkmcnt(4)\n"
        "v_mfma_f32_32x32x16_bf16 v[64:79], a[12:15], v[44:47], v[64:79]\n"
        "s_waitcnt lgkmcnt(3)\n"
        "v_mfma_f32_32x32x16_bf16 v[64:79], a[16:19], v[48:51], v[64:79]\n"
        "s_add_u32 m0, s71, 65536\n"
        "s_add_u32 s65, s78, 1548288\n"
        "buffer_load_dwordx4 v25, s[84:87], s65 offen lds\n"
        "s_waitcnt lgkmcnt(2)\n"
        "v_mfma_f32_32x32x16_bf16 v[64:79], a[20:23], v[52:55], v[64:79]\n"
        "s_add_u32 m0, s71, 69632\n"
        "s_add_u32 s65, s78, 1658880\n"
        "buffer_load_dwordx4 v25, s[84:87], s65 offen lds\n"
        "s_waitcnt lgkmcnt(1)\n"
        "v_mfma_f32_32x32x16_bf16 v[64:79], a[24:27], v[56:59], v[64:79]\n"
        "s_waitcnt lgkmcnt(0)\n"
        "v_mfma_f32_32x32x16_bf16 v[64:79], a[28:31], v[60:63], v[64:79]\n"
        "v_add_u32 v28, 73728, v26\n"
        "v_add_u32 v29, 73728, v27\n"
        "s_waitcnt vmcnt(26)\n"
        "s_barrier\n"
        "ds_read_b64_tr_b16 a[32:33], v28 offset:0\n"
        "ds_read_b64_tr_b16 a[34:35], v28 offset:512\n"
        "ds_read_b64_tr_b16 a[36:37], v28 offset:2048\n"
        "ds_read_b64_tr_b16 a[38:39], v28 offset:2560\n"
        "ds_read_b64_tr_b16 a[40:41], v28 offset:4096\n"
        "ds_read_b64_tr_b16 a[42:43], v28 offset:4608\n"
        "ds_read_b64_tr_b16 a[44:45], v28 offset:6144\n"
        "ds_read_b64_tr_b16 a[46:47], v28 offset:6656\n"
        "ds_read_b64_tr_b16 a[48:49], v28 offset:8192\n"
        "ds_read_b64_tr_b16 a[50:51], v28 offset:8704\n"
        "ds_read_b64_tr_b16 a[52:53], v28 offset:10240\n"
        "ds_read_b64_tr_b16 a[54:55], v28 offset:10752\n"
        "ds_read_b64_tr_b16 a[56:57], v28 offset:12288\n"
        "ds_read_b64_tr_b16 a[58:59], v28 offset:12800\n"
        "ds_read_b64_tr_b16 a[60:61], v28 offset:14336\n"
        "ds_read_b64_tr_b16 a[62:63], v28 offset:14848\n"
        "s_waitcnt lgkmcnt(0)\n"
        "s_barrier\n"
        "s_add_u32 m0, s71, 73728\n"
        "s_add_u32 s65, s78, 1769472\n"
        "buffer_load_dwordx4 v24, s[80:83], s65 offen lds\n"
        "ds_read_b128 v[32:35], v0 offset:256\n"
        "ds_read_b128 v[36:39], v1 offset:256\n"
        "ds_read_b128 v[40:43], v2 offset:256\n"
        "ds_read_b128 v[44:47], v3 offset:256\n"
        "ds_read_b128 v[48:51], v4 offset:256\n"
        "ds_read_b128 v[52:55], v5 offset:256\n"
        "ds_read_b128 v[56:59], v6 offset:256\n"
        "ds_read_b128 v[60:63], v7 offset:256\n"
        "s_add_u32 m0, s71, 77824\n"
        "s_add_u32 s65, s78, 1880064\n"
        "buffer_load_dwordx4 v24, s[80:83], s65 offen lds\n"
        "s_add_u32 m0, s71, 81920\n"
        "s_add_u32 s65, s78, 1990656\n"
        "buffer_load_dwordx4 v24, s[80:83], s65 offen lds\n"
        "s_waitcnt lgkmcnt(7)\n"
        "v_mfma_f32_32x32x16_bf16 v[64:79], a[32:35], v[32:35], v[64:79]\n"
        "s_add_u32 m0, s71, 86016\n"
        "s_add_u32 s65, s78, 2101248\n"
        "buffer_load_dwordx4 v24, s[80:83], s65 offen lds\n"
        "s_waitcnt lgkmcnt(6)\n"
        "v_mfma_f32_32x32x16_bf16 v[64:79], a[36:39], v[36:39], v[64:79]\n"
        "s_add_u32 m0, s71, 90112\n"
        "s_add_u32 s65, s78, 1769472\n"
        "buffer_load_dwordx4 v25, s[84:87], s65 offen lds\n"
        "s_waitcnt lgkmcnt(5)\n"
        "v_mfma_f32_32x32x16_bf16 v[64:79], a[40:43], v[40:43], v[64:79]\n"
        "s_add_u32 m0, s71, 94208\n"
        "s_add_u32 s65, s78, 1880064\n"
        "buffer_load_dwordx4 v25, s[84:87], s65 offen lds\n"
        "s_waitcnt lgkmcnt(4)\n"
        "v_mfma_f32_32x32x16_bf16 v[64:79], a[44:47], v[44:47], v[64:79]\n"
        "s_waitcnt lgkmcnt(3)\n"
        "v_mfma_f32_32x32x16_bf16 v[64:79], a[48:51], v[48:51], v[64:79]\n"
        "s_add_u32 m0, s71, 98304\n"
        "s_add_u32 s65, s78, 1990656\n"
        "buffer_load_dwordx4 v25, s[84:87], s65 offen lds\n"
        "s_waitcnt lgkmcnt(2)\n"
        "v_mfma_f32_32x32x16_bf16 v[64:79], a[52:55], v[52:55], v[64:79]\n"
        "s_add_u32 m0, s71, 102400\n"
        "s_add_u32 s65, s78, 2101248\n"
        "buffer_load_dwordx4 v25, s[84:87], s65 offen lds\n"
        "s_waitcnt lgkmcnt(1)\n"
        "v_mfma_f32_32x32x16_bf16 v[64:79], a[56:59], v[56:59], v[64:79]\n"
        "s_waitcnt lgkmcnt(0)\n"
        "v_mfma_f32_32x32x16_bf16 v[64:79], a[60:63], v[60:63], v[64:79]\n"
        "v_add_u32 v28, 106496, v26\n"
        "v_add_u32 v29, 106496, v27\n"
        "s_waitcnt vmcnt(16)\n"
        "s_barrier\n"
        "ds_read_b64_tr_b16 a[64:65], v28 offset:0\n"
        "ds_read_b64_tr_b16 a[66:67], v28 offset:512\n"
        "ds_read_b64_tr_b16 a[68:69], v28 offset:2048\n"
        "ds_read_b64_tr_b16 a[70:71], v28 offset:2560\n"
        "ds_read_b64_tr_b16 a[72:73], v28 offset:4096\n"
        "ds_read_b64_tr_b16 a[74:75], v28 offset:4608\n"
        "ds_read_b64_tr_b16 a[76:77], v28 offset:6144\n"
        "ds_read_b64_tr_b16 a[78:79], v28 offset:6656\n"
        "ds_read_b64_tr_b16 a[80:81], v28 offset:8192\n"
        "ds_read_b64_tr_b16 a[82:83], v28 offset:8704\n"
        "ds_read_b64_tr_b16 a[84:85], v28 offset:10240\n"
        "ds_read_b64_tr_b16 a[86:87], v28 offset:10752\n"
        "ds_read_b64_tr_b16 a[88:89], v28 offset:12288\n"
        "ds_read_b64_tr_b16 a[90:91], v28 offset:12800\n"
        "ds_read_b64_tr_b16 a[92:93], v28 offset:14336\n"
        "ds_read_b64_tr_b16 a[94:95], v28 offset:14848\n"
        "s_waitcnt lgkmcnt(0)\n"
        "s_barrier\n"
        "ds_read_b128 v[32:35], v0 offset:512\n"
        "ds_read_b128 v[36:39], v1 offset:512\n"
        "ds_read_b128 v[40:43], v2 offset:512\n"
        "ds_read_b128 v[44:47], v3 offset:512\n"
        "ds_read_b128 v[48:51], v4 offset:512\n"
        "ds_read_b128 v[52:55], v5 offset:512\n"
        "ds_read_b128 v[56:59], v6 offset:512\n"
        "ds_read_b128 v[60:63], v7 offset:512\n"
        "s_waitcnt lgkmcnt(7)\n"
        "v_mfma_f32_32x32x16_bf16 v[64:79], a[64:67], v[32:35], v[64:79]\n"
        "s_waitcnt lgkmcnt(6)\n"
        "v_mfma_f32_32x32x16_bf16 v[64:79], a[68:71], v[36:39], v[64:79]\n"
        "s_waitcnt lgkmcnt(5)\n"
        "v_mfma_f32_32x32x16_bf16 v[64:79], a[72:75], v[40:43], v[64:79]\n"
        "s_waitcnt lgkmcnt(4)\n"
        "v_mfma_f32_32x32x16_bf16 v[64:79], a[76:79], v[44:47], v[64:79]\n"
        "s_waitcnt lgkmcnt(3)\n"
        "v_mfma_f32_32x32x16_bf16 v[64:79], a[80:83], v[48:51], v[64:79]\n"
        "s_waitcnt lgkmcnt(2)\n"
        "v_mfma_f32_32x32x16_bf16 v[64:79], a[84:87], v[52:55], v[64:79]\n"
        "s_waitcnt lgkmcnt(1)\n"
        "v_mfma_f32_32x32x16_bf16 v[64:79], a[88:91], v[56:59], v[64:79]\n"
        "s_waitcnt lgkmcnt(0)\n"
        "v_mfma_f32_32x32x16_bf16 v[64:79], a[92:95], v[60:63], v[64:79]\n"
        "v_add_u32 v28, 40960, v26\n"
        "v_add_u32 v29, 40960, v27\n"
        "s_waitcnt vmcnt(8)\n"
        "s_barrier\n"
        "ds_read_b64_tr_b16 a[96:97], v28 offset:0\n"
        "ds_read_b64_tr_b16 a[98:99], v28 offset:512\n"
        "ds_read_b64_tr_b16 a[100:101], v28 offset:2048\n"
        "ds_read_b64_tr_b16 a[102:103], v28 offset:2560\n"
        "ds_read_b64_tr_b16 a[104:105], v28 offset:4096\n"
        "ds_read_b64_tr_b16 a[106:107], v28 offset:4608\n"
        "ds_read_b64_tr_b16 a[108:109], v28 offset:6144\n"
        "ds_read_b64_tr_b16 a[110:111], v28 offset:6656\n"
        "ds_read_b64_tr_b16 a[112:113], v28 offset:8192\n"
        "ds_read_b64_tr_b16 a[114:115], v28 offset:8704\n"
        "ds_read_b64_tr_b16 a[116:117], v28 offset:10240\n"
        "ds_read_b64_tr_b16 a[118:119], v28 offset:10752\n"
        "ds_read_b64_tr_b16 a[120:121], v28 offset:12288\n"
        "ds_read_b64_tr_b16 a[122:123], v28 offset:12800\n"
        "ds_read_b64_tr_b16 a[124:125], v28 offset:14336\n"
        "ds_read_b64_tr_b16 a[126:127], v28 offset:14848\n"
        "s_waitcnt lgkmcnt(0)\n"
        "s_barrier\n"
        "buffer_load_dword v189, off, s[60:63], s88 sc1\n"
        "ds_read_b128 v[32:35], v0 offset:768\n"
        "ds_read_b128 v[36:39], v1 offset:768\n"
        "ds_read_b128 v[40:43], v2 offset:768\n"
        "ds_read_b128 v[44:47], v3 offset:768\n"
        "ds_read_b128 v[48:51], v4 offset:768\n"
        "ds_read_b128 v[52:55], v5 offset:768\n"
        "ds_read_b128 v[56:59], v6 offset:768\n"
        "ds_read_b128 v[60:63], v7 offset:768\n"
        "s_waitcnt lgkmcnt(7)\n"
        "v_mfma_f32_32x32x16_bf16 v[64:79], a[96:99], v[32:35], v[64:79]\n"
        "s_waitcnt lgkmcnt(6)\n"
        "v_mfma_f32_32x32x16_bf16 v[64:79], a[100:103], v[36:39], v[64:79]\n"
        "s_waitcnt lgkmcnt(5)\n"
        "v_mfma_f32_32x32x16_bf16 v[64:79], a[104:107], v[40:43], v[64:79]\n"
        "s_waitcnt lgkmcnt(4)\n"
        "v_mfma_f32_32x32x16_bf16 v[64:79], a[108:111], v[44:47], v[64:79]\n"
        "s_waitcnt lgkmcnt(3)\n"
        "v_mfma_f32_32x32x16_bf16 v[64:79], a[112:115], v[48:51], v[64:79]\n"
        "s_waitcnt lgkmcnt(2)\n"
        "v_mfma_f32_32x32x16_bf16 v[64:79], a[116:119], v[52:55], v[64:79]\n"
        "s_waitcnt lgkmcnt(1)\n"
        "v_mfma_f32_32x32x16_bf16 v[64:79], a[120:123], v[56:59], v[64:79]\n"
        "s_waitcnt lgkmcnt(0)\n"
        "v_mfma_f32_32x32x16_bf16 v[64:79], a[124:127], v[60:63], v[64:79]\n"
        "v_add_u32 v28, 73728, v26\n"
        "v_add_u32 v29, 73728, v27\n"
        "s_waitcnt vmcnt(1)\n"
        "s_barrier\n"
        "ds_read_b64_tr_b16 a[128:129], v28 offset:0\n"
        "ds_read_b64_tr_b16 a[130:131], v28 offset:512\n"
        "ds_read_b64_tr_b16 a[132:133], v28 offset:2048\n"
        "ds_read_b64_tr_b16 a[134:135], v28 offset:2560\n"
        "ds_read_b64_tr_b16 a[136:137], v28 offset:4096\n"
        "ds_read_b64_tr_b16 a[138:139], v28 offset:4608\n"
        "ds_read_b64_tr_b16 a[140:141], v28 offset:6144\n"
        "ds_read_b64_tr_b16 a[142:143], v28 offset:6656\n"
        "ds_read_b64_tr_b16 a[144:145], v28 offset:8192\n"
        "ds_read_b64_tr_b16 a[146:147], v28 offset:8704\n"
        "ds_read_b64_tr_b16 a[148:149], v28 offset:10240\n"
        "ds_read_b64_tr_b16 a[150:151], v28 offset:10752\n"
        "ds_read_b64_tr_b16 a[152:153], v28 offset:12288\n"
        "ds_read_b64_tr_b16 a[154:155], v28 offset:12800\n"
        "ds_read_b64_tr_b16 a[156:157], v28 offset:14336\n"
        "ds_read_b64_tr_b16 a[158:159], v28 offset:14848\n"
        "s_waitcnt lgkmcnt(0)\n"
        "s_barrier\n"
        "s_add_u32 m0, s71, 40960\n"
        "s_nop 0\n"
        "buffer_load_dwordx4 v8, s[72:75], s70 offen lds\n"
        "s_add_u32 m0, s71, 45056\n"
        "s_nop 0\n"
        "buffer_load_dwordx4 v9, s[72:75], s70 offen lds\n"
        "s_add_u32 m0, s71, 49152\n"
        "s_nop 0\n"
        "buffer_load_dwordx4 v10, s[72:75], s70 offen lds\n"
        "s_add_u32 m0, s71, 53248\n"
        "s_nop 0\n"
        "buffer_load_dwordx4 v11, s[72:75], s70 offen lds\n"
        "s_add_u32 m0, s71, 57344\n"
        "s_nop 0\n"
        "buffer_load_dwordx4 v12, s[72:75], s70 offen lds\n"
        "s_add_u32 m0, s71, 61440\n"
        "s_nop 0\n"
        "buffer_load_dwordx4 v13, s[72:75], s70 offen lds\n"
        "s_add_u32 m0, s71, 65536\n"
        "s_nop 0\n"
        "buffer_load_dwordx4 v14, s[72:75], s70 offen lds\n"
        "s_add_u32 m0, s71, 69632\n"
        "s_nop 0\n"
        "buffer_load_dwordx4 v15, s[72:75], s70 offen lds\n"
        "s_add_u32 m0, s71, 73728\n"
        "s_nop 0\n"
        "buffer_load_dwordx4 v16, s[72:75], s70 offen lds\n"
        "s_add_u32 m0, s71, 77824\n"
        "s_nop 0\n"
        "buffer_load_dwordx4 v17, s[72:75], s70 offen lds\n"
        "ds_read_b128 v[32:35], v0 offset:1024\n"
        "ds_read_b128 v[36:39], v1 offset:1024\n"
        "ds_read_b128 v[40:43], v2 offset:1024\n"
        "ds_read_b128 v[44:47], v3 offset:1024\n"
        "ds_read_b128 v[48:51], v4 offset:1024\n"
        "ds_read_b128 v[52:55], v5 offset:1024\n"
        "ds_read_b128 v[56:59], v6 offset:1024\n"
        "ds_read_b128 v[60:63], v7 offset:1024\n"
        "s_waitcnt lgkmcnt(7)\n"
        "v_mfma_f32_32x32x16_bf16 v[64:79], a[128:131], v[32:35], v[64:79]\n"
        "s_waitcnt lgkmcnt(6)\n"
        "v_mfma_f32_32x32x16_bf16 v[64:79], a[132:135], v[36:39], v[64:79]\n"
        "s_waitcnt lgkmcnt(5)\n"
        "v_mfma_f32_32x32x16_bf16 v[64:79], a[136:139], v[40:43], v[64:79]\n"
        "s_waitcnt lgkmcnt(4)\n"
        "v_mfma_f32_32x32x16_bf16 v[64:79], a[140:143], v[44:47], v[64:79]\n"
        "s_waitcnt lgkmcnt(3)\n"
        "v_mfma_f32_32x32x16_bf16 v[64:79], a[144:147], v[48:51], v[64:79]\n"
        "s_waitcnt lgkmcnt(2)\n"
        "v_mfma_f32_32x32x16_bf16 v[64:79], a[148:151], v[52:55], v[64:79]\n"
        "s_waitcnt lgkmcnt(1)\n"
        "v_mfma_f32_32x32x16_bf16 v[64:79], a[152:155], v[56:59], v[64:79]\n"
        "s_waitcnt lgkmcnt(0)\n"
        "v_mfma_f32_32x32x16_bf16 v[64:79], a[156:159], v[60:63], v[64:79]\n"
        "s_waitcnt vmcnt(10)\n"
        "v_cmp_ne_u32 vcc, v188, v189\n"
        "s_cbranch_vccz HS457_f1b\n"
        "HS457_f1a:\n"
        "buffer_load_dword v189, off, s[60:63], s88 sc1\n"
        "s_waitcnt vmcnt(0)\n"
        "v_cmp_ne_u32 vcc, v188, v189\n"
        "s_cbranch_vccz HS457_f1b\n"
        "s_sleep 1\n"
        "s_cmp_eq_u32 s66, 0\n"
        "s_cbranch_scc1 HS457_f1b\n"
        "s_sub_u32 s66, s66, 1\n"
        "s_branch HS457_f1a\n"
        "HS457_f1b:\n"
        "s_mov_b64 exec, 1\n"
        "v_mov_b32 v189, 0\n"
        "buffer_store_dword v189, off, s[60:63], s90 sc1\n"
        "s_mov_b64 exec, -1\n"
        "s_lshl_b32 s77, s58, 2\n"
        "s_add_u32 s77, s77, s67\n"
        "s_lshl_b32 s77, s77, 2\n"
        "s_add_u32 s77, s77, 4259840\n"
        "s_mov_b32 s68, 45088768\n"
        "s_nop 7\n"
        "v_cvt_pk_bf16_f32 v128, v64, v65\n"
        "v_cvt_pk_bf16_f32 v129, v72, v73\n"
        "v_lshlrev_b32 v130, 16, v128\n"
        "v_and_b32 v131, 0xffff0000, v128\n"
        "v_mul_f32 v132, 0xbfb8aa3b, v130\n"
        "v_mul_f32 v133, 0xbfb8aa3b, v131\n"
        "v_exp_f32 v132, v132\n"
        "v_exp_f32 v133, v133\n"
        "v_lshlrev_b32 v134, 16, v129\n"
        "v_add_f32 v132, 1.0, v132\n"
        "v_add_f32 v133, 1.0, v133\n"
        "v_rcp_f32 v132, v132\n"
        "v_rcp_f32 v133, v133\n"
        "v_and_b32 v135, 0xffff0000, v129\n"
        "v_mul_f32 v130, v130, v132\n"
        "v_mul_f32 v131, v131, v133\n"
        "v_cvt_pk_bf16_f32 v130, v130, v131\n"
        "v_and_b32 v131, 0xffff0000, v130\n"
        "v_lshlrev_b32 v130, 16, v130\n"
        "v_mul_f32 v130, v130, v134\n"
        "v_mul_f32 v131, v131, v135\n"
        "v_cvt_pk_bf16_f32 v140, v130, v131\n"
        "v_cvt_pk_bf16_f32 v128, v66, v67\n"
        "v_cvt_pk_bf16_f32 v129, v74, v75\n"
        "v_lshlrev_b32 v130, 16, v128\n"
        "v_and_b32 v131, 0xffff0000, v128\n"
        "v_mul_f32 v132, 0xbfb8aa3b, v130\n"
        "v_mul_f32 v133, 0xbfb8aa3b, v131\n"
        "v_exp_f32 v132, v132\n"
        "v_exp_f32 v133, v133\n"
        "v_lshlrev_b32 v134, 16, v129\n"
        "v_add_f32 v132, 1.0, v132\n"
        "v_add_f32 v133, 1.0, v133\n"
        "v_rcp_f32 v132, v132\n"
        "v_rcp_f32 v133, v133\n"
        "v_and_b32 v135, 0xffff0000, v129\n"
        "v_mul_f32 v130, v130, v132\n"
        "v_mul_f32 v131, v131, v133\n"
        "v_cvt_pk_bf16_f32 v130, v130, v131\n"
        "v_and_b32 v131, 0xffff0000, v130\n"
        "v_lshlrev_b32 v130, 16, v130\n"
        "v_mul_f32 v130, v130, v134\n"
        "v_mul_f32 v131, v131, v135\n"
        "v_cvt_pk_bf16_f32 v141, v130, v131\n"
        "v_cvt_pk_bf16_f32 v128, v68, v69\n"
        "v_cvt_pk_bf16_f32 v129, v76, v77\n"
        "v_lshlrev_b32 v130, 16, v128\n"
        "v_and_b32 v131, 0xffff0000, v128\n"
        "v_mul_f32 v132, 0xbfb8aa3b, v130\n"
        "v_mul_f32 v133, 0xbfb8aa3b, v131\n"
        "v_exp_f32 v132, v132\n"
        "v_exp_f32 v133, v133\n"
        "v_lshlrev_b32 v134, 16, v129\n"
        "v_add_f32 v132, 1.0, v132\n"
        "v_add_f32 v133, 1.0, v133\n"
        "v_rcp_f32 v132, v132\n"
        "v_rcp_f32 v133, v133\n"
        "v_and_b32 v135, 0xffff0000, v129\n"
        "v_mul_f32 v130, v130, v132\n"
        "v_mul_f32 v131, v131, v133\n"
        "v_cvt_pk_bf16_f32 v130, v130, v131\n"
        "v_and_b32 v131, 0xffff0000, v130\n"
        "v_lshlrev_b32 v130, 16, v130\n"
        "v_mul_f32 v130, v130, v134\n"
        "v_mul_f32 v131, v131, v135\n"
        "v_cvt_pk_bf16_f32 v142, v130, v131\n"
        "v_cvt_pk_bf16_f32 v128, v70, v71\n"
        "v_cvt_pk_bf16_f32 v129, v78, v79\n"
        "v_lshlrev_b32 v130, 16, v128\n"
        "v_and_b32 v131, 0xffff0000, v128\n"
        "v_mul_f32 v132, 0xbfb8aa3b, v130\n"
        "v_mul_f32 v133, 0xbfb8aa3b, v131\n"
        "v_exp_f32 v132, v132\n"
        "v_exp_f32 v133, v133\n"
        "v_lshlrev_b32 v134, 16, v129\n"
        "v_add_f32 v132, 1.0, v132\n"
        "v_add_f32 v133, 1.0, v133\n"
        "v_rcp_f32 v132, v132\n"
        "v_rcp_f32 v133, v133\n"
        "v_and_b32 v135, 0xffff0000, v129\n"
        "v_mul_f32 v130, v130, v132\n"
        "v_mul_f32 v131, v131, v133\n"
        "v_cvt_pk_bf16_f32 v130, v130, v131\n"
        "v_and_b32 v131, 0xffff0000, v130\n"
        "v_lshlrev_b32 v130, 16, v130\n"
        "v_mul_f32 v130, v130, v134\n"
        "v_mul_f32 v131, v131, v135\n"
        "v_cvt_pk_bf16_f32 v143, v130, v131\n"
        "buffer_store_dwordx4 v[140:143], v18, s[60:63], s68 offen sc1\n"
        "s_waitcnt vmcnt(0)\n"
        "buffer_store_dword v20, v21, s[60:63], s77 offen offset:0 sc1\n"
        "s_barrier\n"
        "ds_read_b128 v[32:35], v0 offset:40960\n"
        "ds_read_b128 v[36:39], v1 offset:40960\n"
        "ds_read_b128 v[40:43], v2 offset:40960\n"
        "ds_read_b128 v[44:47], v3 offset:40960\n"
        "ds_read_b128 v[48:51], v4 offset:40960\n"
        "ds_read_b128 v[52:55], v5 offset:40960\n"
        "s_mov_b32 s68, 4026531840\n"
        "s_mov_b32 s76, 4026531840\n"
        "s_mov_b32 s70, 81920\n"
        "s_mov_b32 s64, 1\n"
        "s_branch F1_p1\n"
        "F1_loop:\n"
        "s_waitcnt lgkmcnt(5)\n"
        "v_mfma_f32_32x32x16_bf16 v[64:79], a[0:3], v[32:35], 0\n"
        "ds_read_b128 v[56:59], v6 offset:0\n"
        "s_waitcnt lgkmcnt(5)\n"
        "v_mfma_f32_32x32x16_bf16 v[64:79], a[4:7], v[36:39], v[64:79]\n"
        "ds_read_b128 v[60:63], v7 offset:0\n"
        "s_waitcnt lgkmcnt(5)\n"
        "v_mfma_f32_32x32x16_bf16 v[64:79], a[8:11], v[40:43], v[64:79]\n"
        "ds_read_b128 v[32:35], v0 offset:256\n"
        "v_cvt_pk_bf16_f32 v128, v80, v81\n"
        "v_cvt_pk_bf16_f32 v129, v88, v89\n"
        "v_lshlrev_b32 v130, 16, v128\n"
        "v_and_b32 v131, 0xffff0000, v128\n"
        "s_add_u32 m0, s71, 40960\n"
        "s_nop 0\n"
        "buffer_load_dwordx4 v8, s[72:75], s70 offen lds\n"
        "s_waitcnt lgkmcnt(5)\n"
        "v_mfma_f32_32x32x16_bf16 v[64:79], a[12:15], v[44:47], v[64:79]\n"
        "ds_read_b128 v[36:39], v1 offset:256\n"
        "v_mul_f32 v132, 0xbfb8aa3b, v130\n"
        "v_mul_f32 v133, 0xbfb8aa3b, v131\n"
        "v_exp_f32 v132, v132\n"
        "v_exp_f32 v133, v133\n"
        "s_waitcnt lgkmcnt(5)\n"
        "v_mfma_f32_32x32x16_bf16 v[64:79], a[16:19], v[48:51], v[64:79]\n"
        "ds_read_b128 v[40:43], v2 offset:256\n"
        "v_lshlrev_b32 v134, 16, v129\n"
        "v_add_f32 v132, 1.0, v132\n"
        "v_add_f32 v133, 1.0, v133\n"
        "v_rcp_f32 v132, v132\n"
        "s_add_u32 m0, s71, 45056\n"
        "s_nop 0\n"
        "buffer_load_dwordx4 v9, s[72:75], s70 offen lds\n"
        "s_waitcnt lgkmcnt(5)\n"
        "v_mfma_f32_32x32x16_bf16 v[64:79], a[20:23], v[52:55], v[64:79]\n"
        "ds_read_b128 v[44:47], v3 offset:256\n"
        "v_rcp_f32 v133, v133\n"
        "v_and_b32 v135, 0xffff0000, v129\n"
        "v_mul_f32 v130, v130, v132\n"
        "v_mul_f32 v131, v131, v133\n"
        "s_waitcnt lgkmcnt(5)\n"
        "v_mfma_f32_32x32x16_bf16 v[64:79], a[24:27], v[56:59], v[64:79]\n"
        "ds_read_b128 v[48:51], v4 offset:256\n"
        "v_cvt_pk_bf16_f32 v130, v130, v131\n"
        "v_and_b32 v131, 0xffff0000, v130\n"
        "v_lshlrev_b32 v130, 16, v130\n"
        "v_mul_f32 v130, v130, v134\n"
        "s_add_u32 m0, s71, 49152\n"
        "s_nop 0\n"
        "buffer_load_dwordx4 v10, s[72:75], s70 offen lds\n"
        "s_waitcnt lgkmcnt(5)\n"
        "v_mfma_f32_32x32x16_bf16 v[64:79], a[28:31], v[60:63], v[64:79]\n"
        "ds_read_b128 v[52:55], v5 offset:256\n"
        "v_mul_f32 v131, v131, v135\n"
        "v_cvt_pk_bf16_f32 v140, v130, v131\n"
        "v_cvt_pk_bf16_f32 v128, v82, v83\n"
        "v_cvt_pk_bf16_f32 v129, v90, v91\n"
        "s_waitcnt lgkmcnt(5)\n"
        "v_mfma_f32_32x32x16_bf16 v[64:79], a[32:35], v[32:35], v[64:79]\n"
        "ds_read_b128 v[56:59], v6 offset:256\n"
        "v_lshlrev_b32 v130, 16, v128\n"
        "v_and_b32 v131, 0xffff0000, v128\n"
        "v_mul_f32 v132, 0xbfb8aa3b, v130\n"
        "v_mul_f32 v133, 0xbfb8aa3b, v131\n"
        "s_add_u32 m0, s71, 53248\n"
        "s_nop 0\n"
        "buffer_load_dwordx4 v11, s[72:75], s70 offen lds\n"
        "s_waitcnt lgkmcnt(5)\n"
        "v_mfma_f32_32x32x16_bf16 v[64:79], a[36:39], v[36:39], v[64:79]\n"
        "ds_read_b128 v[60:63], v7 offset:256\n"
        "v_exp_f32 v132, v132\n"
        "v_exp_f32 v133, v133\n"
        "v_lshlrev_b32 v134, 16, v129\n"
        "v_add_f32 v132, 1.0, v132\n"
        "s_waitcnt lgkmcnt(5)\n"
        "v_mfma_f32_32x32x16_bf16 v[64:79], a[40:43], v[40:43], v[64:79]\n"
        "ds_read_b128 v[32:35], v0 offset:512\n"
        "v_add_f32 v133, 1.0, v133\n"
        "v_rcp_f32 v132, v132\n"
        "v_rcp_f32 v133, v133\n"
        "v_and_b32 v135, 0xffff0000, v129\n"
        "s_add_u32 m0, s71, 57344\n"
        "s_nop 0\n"
        "buffer_load_dwordx4 v12, s[72:75], s70 offen lds\n"
        "s_waitcnt lgkmcnt(5)\n"
        "v_mfma_f32_32x32x16_bf16 v[64:79], a[44:47], v[44:47], v[64:79]\n"
        "ds_read_b128 v[36:39], v1 offset:512\n"
        "v_mul_f32 v130, v130, v132\n"
        "v_mul_f32 v131, v131, v133\n"
        "v_cvt_pk_bf16_f32 v130, v130, v131\n"
        "v_and_b32 v131, 0xffff0000, v130\n"
        "s_waitcnt lgkmcnt(5)\n"
        "v_mfma_f32_32x32x16_bf16 v[64:79], a[48:51], v[48:51], v[64:79]\n"
        "ds_read_b128 v[40:43], v2 offset:512\n"
        "v_lshlrev_b32 v130, 16, v130\n"
        "v_mul_f32 v130, v130, v134\n"
        "v_mul_f32 v131, v131, v135\n"
        "v_cvt_pk_bf16_f32 v141, v130, v131\n"
        "s_add_u32 m0, s71, 61440\n"
        "s_nop 0\n"
        "buffer_load_dwordx4 v13, s[72:75], s70 offen lds\n"
        "s_waitcnt lgkmcnt(5)\n"
        "v_mfma_f32_32x32x16_bf16 v[64:79], a[52:55], v[52:55], v[64:79]\n"
        "ds_read_b128 v[44:47], v3 offset:512\n"
        "v_cvt_pk_bf16_f32 v128, v84, v85\n"
        "v_cvt_pk_bf16_f32 v129, v92, v93\n"
        "v_lshlrev_b32 v130, 16, v128\n"
        "v_and_b32 v131, 0xffff0000, v128\n"
        "s_waitcnt lgkmcnt(5)\n"
        "v_mfma_f32_32x32x16_bf16 v[64:79], a[56:59], v[56:59], v[64:79]\n"
        "ds_read_b128 v[48:51], v4 offset:512\n"
        "v_mul_f32 v132, 0xbfb8aa3b, v130\n"
        "v_mul_f32 v133, 0xbfb8aa3b, v131\n"
        "v_exp_f32 v132, v132\n"
        "v_exp_f32 v133, v133\n"
        "s_add_u32 m0, s71, 65536\n"
        "s_nop 0\n"
        "buffer_load_dwordx4 v14, s[72:75], s70 offen lds\n"
        "s_waitcnt lgkmcnt(5)\n"
        "v_mfma_f32_32x32x16_bf16 v[64:79], a[60:63], v[60:63], v[64:79]\n"
        "ds_read_b128 v[52:55], v5 offset:512\n"
        "v_lshlrev_b32 v134, 16, v129\n"
        "v_add_f32 v132, 1.0, v132\n"
        "v_add_f32 v133, 1.0, v133\n"
        "v_rcp_f32 v132, v132\n"
        "s_waitcnt lgkmcnt(5)\n"
        "v_mfma_f32_32x32x16_bf16 v[64:79], a[64:67], v[32:35], v[64:79]\n"
        "ds_read_b128 v[56:59], v6 offset:512\n"
        "v_rcp_f32 v133, v133\n"
        "v_and_b32 v135, 0xffff0000, v129\n"
        "v_mul_f32 v130, v130, v132\n"
        "v_mul_f32 v131, v131, v133\n"
        "s_add_u32 m0, s71, 69632\n"
        "s_nop 0\n"
        "buffer_load_dwordx4 v15, s[72:75], s70 offen lds\n"
        "s_waitcnt lgkmcnt(5)\n"
        "v_mfma_f32_32x32x16_bf16 v[64:79], a[68:71], v[36:39], v[64:79]\n"
        "ds_read_b128 v[60:63], v7 offset:512\n"
        "v_cvt_pk_bf16_f32 v130, v130, v131\n"
        "v_and_b32 v131, 0xffff0000, v130\n"
        "v_lshlrev_b32 v130, 16, v130\n"
        "v_mul_f32 v130, v130, v134\n"
        "s_waitcnt lgkmcnt(5)\n"
        "v_mfma_f32_32x32x16_bf16 v[64:79], a[72:75], v[40:43], v[64:79]\n"
        "ds_read_b128 v[32:35], v0 offset:768\n"
        "v_mul_f32 v131, v131, v135\n"
        "v_cvt_pk_bf16_f32 v142, v130, v131\n"
        "v_cvt_pk_bf16_f32 v128, v86, v87\n"
        "v_cvt_pk_bf16_f32 v129, v94, v95\n"
        "s_add_u32 m0, s71, 73728\n"
        "s_nop 0\n"
        "buffer_load_dwordx4 v16, s[72:75], s70 offen lds\n"
        "s_waitcnt lgkmcnt(5)\n"
        "v_mfma_f32_32x32x16_bf16 v[64:79], a[76:79], v[44:47], v[64:79]\n"
        "ds_read_b128 v[36:39], v1 offset:768\n"
        "v_lshlrev_b32 v130, 16, v128\n"
        "v_and_b32 v131, 0xffff0000, v128\n"
        "v_mul_f32 v132, 0xbfb8aa3b, v130\n"
        "v_mul_f32 v133, 0xbfb8aa3b, v131\n"
        "s_waitcnt lgkmcnt(5)\n"
        "v_mfma_f32_32x32x16_bf16 v[64:79], a[80:83], v[48:51], v[64:79]\n"
        "ds_read_b128 v[40:43], v2 offset:768\n"
        "v_exp_f32 v132, v132\n"
        "v_exp_f32 v133, v133\n"
        "v_lshlrev_b32 v134, 16, v129\n"
        "v_add_f32 v132, 1.0, v132\n"
        "s_add_u32 m0, s71, 77824\n"
        "s_nop 0\n"
        "buffer_load_dwordx4 v17, s[72:75], s70 offen lds\n"
        "s_waitcnt lgkmcnt(5)\n"
        "v_mfma_f32_32x32x16_bf16 v[64:79], a[84:87], v[52:55], v[64:79]\n"
        "ds_read_b128 v[44:47], v3 offset:768\n"
        "v_add_f32 v133, 1.0, v133\n"
        "v_rcp_f32 v132, v132\n"
        "v_rcp_f32 v133, v133\n"
        "v_and_b32 v135, 0xffff0000, v129\n"
        "s_waitcnt lgkmcnt(5)\n"
        "v_mfma_f32_32x32x16_bf16 v[64:79], a[88:91], v[56:59], v[64:79]\n"
        "ds_read_b128 v[48:51], v4 offset:768\n"
        "v_mul_f32 v130, v130, v132\n"
        "v_mul_f32 v131, v131, v133\n"
        "v_cvt_pk_bf16_f32 v130, v130, v131\n"
        "v_and_b32 v131, 0xffff0000, v130\n"
        "s_waitcnt lgkmcnt(5)\n"
        "v_mfma_f32_32x32x16_bf16 v[64:79], a[92:95], v[60:63], v[64:79]\n"
        "ds_read_b128 v[52:55], v5 offset:768\n"
        "v_lshlrev_b32 v130, 16, v130\n"
        "v_mul_f32 v130, v130, v134\n"
        "v_mul_f32 v131, v131, v135\n"
        "v_cvt_pk_bf16_f32 v143, v130, v131\n"
        "s_waitcnt lgkmcnt(5)\n"
        "v_mfma_f32_32x32x16_bf16 v[64:79], a[96:99], v[32:35], v[64:79]\n"
        "ds_read_b128 v[56:59], v6 offset:768\n"
        "buffer_store_dwordx4 v[140:143], v18, s[60:63], s68 offen sc1\n"
        "s_waitcnt lgkmcnt(5)\n"
        "v_mfma_f32_32x32x16_bf16 v[64:79], a[100:103], v[36:39], v[64:79]\n"
        "ds_read_b128 v[60:63], v7 offset:768\n"
        "s_waitcnt lgkmcnt(5)\n"
        "v_mfma_f32_32x32x16_bf16 v[64:79], a[104:107], v[40:43], v[64:79]\n"
        "ds_read_b128 v[32:35], v0 offset:1024\n"
        "s_waitcnt lgkmcnt(5)\n"
        "v_mfma_f32_32x32x16_bf16 v[64:79], a[108:111], v[44:47], v[64:79]\n"
        "ds_read_b128 v[36:39], v1 offset:1024\n"
        "s_waitcnt lgkmcnt(5)\n"
        "v_mfma_f32_32x32x16_bf16 v[64:79], a[112:115], v[48:51], v[64:79]\n"
        "ds_read_b128 v[40:43], v2 offset:1024\n"
        "s_waitcnt lgkmcnt(5)\n"
        "v_mfma_f32_32x32x16_bf16 v[64:79], a[116:119], v[52:55], v[64:79]\n"
        "ds_read_b128 v[44:47], v3 offset:1024\n"
        "s_waitcnt lgkmcnt(5)\n"
        "v_mfma_f32_32x32x16_bf16 v[64:79], a[120:123], v[56:59], v[64:79]\n"
        "ds_read_b128 v[48:51], v4 offset:1024\n"
        "s_waitcnt lgkmcnt(5)\n"
        "v_mfma_f32_32x32x16_bf16 v[64:79], a[124:127], v[60:63], v[64:79]\n"
        "ds_read_b128 v[52:55], v5 offset:1024\n"
        "s_waitcnt lgkmcnt(5)\n"
        "v_mfma_f32_32x32x16_bf16 v[64:79], a[128:131], v[32:35], v[64:79]\n"
        "ds_read_b128 v[56:59], v6 offset:1024\n"
        "s_waitcnt lgkmcnt(5)\n"
        "v_mfma_f32_32x32x16_bf16 v[64:79], a[132:135], v[36:39], v[64:79]\n"
        "ds_read_b128 v[60:63], v7 offset:1024\n"
        "s_waitcnt lgkmcnt(5)\n"
        "v_mfma_f32_32x32x16_bf16 v[64:79], a[136:139], v[40:43], v[64:79]\n"
        "s_waitcnt lgkmcnt(4)\n"
        "v_mfma_f32_32x32x16_bf16 v[64:79], a[140:143], v[44:47], v[64:79]\n"
        "s_waitcnt vmcnt(0) lgkmcnt(0)\n"
        "s_barrier\n"
        "buffer_store_dword v20, v21, s[60:63], s69 offen sc1\n"
        "v_mfma_f32_32x32x16_bf16 v[64:79], a[144:147], v[48:51], v[64:79]\n"
        "ds_read_b128 v[32:35], v0 offset:40960\n"
        "ds_read_b128 v[36:39], v1 offset:40960\n"
        "ds_read_b128 v[40:43], v2 offset:40960\n"
        "v_mfma_f32_32x32x16_bf16 v[64:79], a[148:151], v[52:55], v[64:79]\n"
        "ds_read_b128 v[44:47], v3 offset:40960\n"
        "v_mfma_f32_32x32x16_bf16 v[64:79], a[152:155], v[56:59], v[64:79]\n"
        "ds_read_b128 v[48:51], v4 offset:40960\n"
        "v_mfma_f32_32x32x16_bf16 v[64:79], a[156:159], v[60:63], v[64:79]\n"
        "ds_read_b128 v[52:55], v5 offset:40960\n"
        "s_mul_i32 s68, s64, 110592\n"
        "s_add_u32 s68, s68, 45088768\n"
        "s_mul_i32 s69, s64, 512\n"
        "s_add_u32 s69, s69, s77\n"
        "s_add_u32 s70, s70, 40960\n"
        "s_add_u32 s64, s64, 1\n"
        "F1_p1:\n"
        "s_waitcnt lgkmcnt(5)\n"
        "v_mfma_f32_32x32x16_bf16 v[80:95], a[0:3], v[32:35], 0\n"
        "ds_read_b128 v[56:59], v6 offset:40960\n"
        "s_waitcnt lgkmcnt(5)\n"
        "v_mfma_f32_32x32x16_bf16 v[80:95], a[4:7], v[36:39], v[80:95]\n"
        "ds_read_b128 v[60:63], v7 offset:40960\n"
        "s_waitcnt lgkmcnt(5)\n"
        "v_mfma_f32_32x32x16_bf16 v[80:95], a[8:11], v[40:43], v[80:95]\n"
        "ds_read_b128 v[32:35], v0 offset:41216\n"
        "v_cvt_pk_bf16_f32 v128, v64, v65\n"
        "v_cvt_pk_bf16_f32 v129, v72, v73\n"
        "v_lshlrev_b32 v130, 16, v128\n"
        "v_and_b32 v131, 0xffff0000, v128\n"
        "s_add_u32 m0, s71, 0\n"
        "s_nop 0\n"
        "buffer_load_dwordx4 v8, s[72:75], s70 offen lds\n"
        "s_waitcnt lgkmcnt(5)\n"
        "v_mfma_f32_32x32x16_bf16 v[80:95], a[12:15], v[44:47], v[80:95]\n"
        "ds_read_b128 v[36:39], v1 offset:41216\n"
        "v_mul_f32 v132, 0xbfb8aa3b, v130\n"
        "v_mul_f32 v133, 0xbfb8aa3b, v131\n"
        "v_exp_f32 v132, v132\n"
        "v_exp_f32 v133, v133\n"
        "s_waitcnt lgkmcnt(5)\n"
        "v_mfma_f32_32x32x16_bf16 v[80:95], a[16:19], v[48:51], v[80:95]\n"
        "ds_read_b128 v[40:43], v2 offset:41216\n"
        "v_lshlrev_b32 v134, 16, v129\n"
        "v_add_f32 v132, 1.0, v132\n"
        "v_add_f32 v133, 1.0, v133\n"
        "v_rcp_f32 v132, v132\n"
        "s_add_u32 m0, s71, 4096\n"
        "s_nop 0\n"
        "buffer_load_dwordx4 v9, s[72:75], s70 offen lds\n"
        "s_waitcnt lgkmcnt(5)\n"
        "v_mfma_f32_32x32x16_bf16 v[80:95], a[20:23], v[52:55], v[80:95]\n"
        "ds_read_b128 v[44:47], v3 offset:41216\n"
        "v_rcp_f32 v133, v133\n"
        "v_and_b32 v135, 0xffff0000, v129\n"
        "v_mul_f32 v130, v130, v132\n"
        "v_mul_f32 v131, v131, v133\n"
        "s_waitcnt lgkmcnt(5)\n"
        "v_mfma_f32_32x32x16_bf16 v[80:95], a[24:27], v[56:59], v[80:95]\n"
        "ds_read_b128 v[48:51], v4 offset:41216\n"
        "v_cvt_pk_bf16_f32 v130, v130, v131\n"
        "v_and_b32 v131, 0xffff0000, v130\n"
        "v_lshlrev_b32 v130, 16, v130\n"
        "v_mul_f32 v130, v130, v134\n"
        "s_add_u32 m0, s71, 8192\n"
        "s_nop 0\n"
        "buffer_load_dwordx4 v10, s[72:75], s70 offen lds\n"
        "s_waitcnt lgkmcnt(5)\n"
        "v_mfma_f32_32x32x16_bf16 v[80:95], a[28:31], v[60:63], v[80:95]\n"
        "ds_read_b128 v[52:55], v5 offset:41216\n"
        "v_mul_f32 v131, v131, v135\n"
        "v_cvt_pk_bf16_f32 v140, v130, v131\n"
        "v_cvt_pk_bf16_f32 v128, v66, v67\n"
        "v_cvt_pk_bf16_f32 v129, v74, v75\n"
        "s_waitcnt lgkmcnt(5)\n"
        "v_mfma_f32_32x32x16_bf16 v[80:95], a[32:35], v[32:35], v[80:95]\n"
        "ds_read_b128 v[56:59], v6 offset:41216\n"
        "v_lshlrev_b32 v130, 16, v128\n"
        "v_and_b32 v131, 0xffff0000, v128\n"
        "v_mul_f32 v132, 0xbfb8aa3b, v130\n"
        "v_mul_f32 v133, 0xbfb8aa3b, v131\n"
        "s_add_u32 m0, s71, 12288\n"
        "s_nop 0\n"
        "buffer_load_dwordx4 v11, s[72:75], s70 offen lds\n"
        "s_waitcnt lgkmcnt(5)\n"
        "v_mfma_f32_32x32x16_bf16 v[80:95], a[36:39], v[36:39], v[80:95]\n"
        "ds_read_b128 v[60:63], v7 offset:41216\n"
        "v_exp_f32 v132, v132\n"
        "v_exp_f32 v133, v133\n"
        "v_lshlrev_b32 v134, 16, v129\n"
        "v_add_f32 v132, 1.0, v132\n"
        "s_waitcnt lgkmcnt(5)\n"
        "v_mfma_f32_32x32x16_bf16 v[80:95], a[40:43], v[40:43], v[80:95]\n"
        "ds_read_b128 v[32:35], v0 offset:41472\n"
        "v_add_f32 v133, 1.0, v133\n"
        "v_rcp_f32 v132, v132\n"
        "v_rcp_f32 v133, v133\n"
        "v_and_b32 v135, 0xffff0000, v129\n"
        "s_add_u32 m0, s71, 16384\n"
        "s_nop 0\n"
        "buffer_load_dwordx4 v12, s[72:75], s70 offen lds\n"
        "s_waitcnt lgkmcnt(5)\n"
        "v_mfma_f32_32x32x16_bf16 v[80:95], a[44:47], v[44:47], v[80:95]\n"
        "ds_read_b128 v[36:39], v1 offset:41472\n"
        "v_mul_f32 v130, v130, v132\n"
        "v_mul_f32 v131, v131, v133\n"
        "v_cvt_pk_bf16_f32 v130, v130, v131\n"
        "v_and_b32 v131, 0xffff0000, v130\n"
        "s_waitcnt lgkmcnt(5)\n"
        "v_mfma_f32_32x32x16_bf16 v[80:95], a[48:51], v[48:51], v[80:95]\n"
        "ds_read_b128 v[40:43], v2 offset:41472\n"
        "v_lshlrev_b32 v130, 16, v130\n"
        "v_mul_f32 v130, v130, v134\n"
        "v_mul_f32 v131, v131, v135\n"
        "v_cvt_pk_bf16_f32 v141, v130, v131\n"
        "s_add_u32 m0, s71, 20480\n"
        "s_nop 0\n"
        "buffer_load_dwordx4 v13, s[72:75], s70 offen lds\n"
        "s_waitcnt lgkmcnt(5)\n"
        "v_mfma_f32_32x32x16_bf16 v[80:95], a[52:55], v[52:55], v[80:95]\n"
        "ds_read_b128 v[44:47], v3 offset:41472\n"
        "v_cvt_pk_bf16_f32 v128, v68, v69\n"
        "v_cvt_pk_bf16_f32 v129, v76, v77\n"
        "v_lshlrev_b32 v130, 16, v128\n"
        "v_and_b32 v131, 0xffff0000, v128\n"
        "s_waitcnt lgkmcnt(5)\n"
        "v_mfma_f32_32x32x16_bf16 v[80:95], a[56:59], v[56:59], v[80:95]\n"
        "ds_read_b128 v[48:51], v4 offset:41472\n"
        "v_mul_f32 v132, 0xbfb8aa3b, v130\n"
        "v_mul_f32 v133, 0xbfb8aa3b, v131\n"
        "v_exp_f32 v132, v132\n"
        "v_exp_f32 v133, v133\n"
        "s_add_u32 m0, s71, 24576\n"
        "s_nop 0\n"
        "buffer_load_dwordx4 v14, s[72:75], s70 offen lds\n"
        "s_waitcnt lgkmcnt(5)\n"
        "v_mfma_f32_32x32x16_bf16 v[80:95], a[60:63], v[60:63], v[80:95]\n"
        "ds_read_b128 v[52:55], v5 offset:41472\n"
        "v_lshlrev_b32 v134, 16, v129\n"
        "v_add_f32 v132, 1.0, v132\n"
        "v_add_f32 v133, 1.0, v133\n"
        "v_rcp_f32 v132, v132\n"
        "s_waitcnt lgkmcnt(5)\n"
        "v_mfma_f32_32x32x16_bf16 v[80:95], a[64:67], v[32:35], v[80:95]\n"
        "ds_read_b128 v[56:59], v6 offset:41472\n"
        "v_rcp_f32 v133, v133\n"
        "v_and_b32 v135, 0xffff0000, v129\n"
        "v_mul_f32 v130, v130, v132\n"
        "v_mul_f32 v131, v131, v133\n"
        "s_add_u32 m0, s71, 28672\n"
        "s_nop 0\n"
        "buffer_load_dwordx4 v15, s[72:75], s70 offen lds\n"
        "s_waitcnt lgkmcnt(5)\n"
        "v_mfma_f32_32x32x16_bf16 v[80:95], a[68:71], v[36:39], v[80:95]\n"
        "ds_read_b128 v[60:63], v7 offset:41472\n"
        "v_cvt_pk_bf16_f32 v130, v130, v131\n"
        "v_and_b32 v131, 0xffff0000, v130\n"
        "v_lshlrev_b32 v130, 16, v130\n"
        "v_mul_f32 v130, v130, v134\n"
        "s_waitcnt lgkmcnt(5)\n"
        "v_mfma_f32_32x32x16_bf16 v[80:95], a[72:75], v[40:43], v[80:95]\n"
        "ds_read_b128 v[32:35], v0 offset:41728\n"
        "v_mul_f32 v131, v131, v135\n"
        "v_cvt_pk_bf16_f32 v142, v130, v131\n"
        "v_cvt_pk_bf16_f32 v128, v70, v71\n"
        "v_cvt_pk_bf16_f32 v129, v78, v79\n"
        "s_add_u32 m0, s71, 32768\n"
        "s_nop 0\n"
        "buffer_load_dwordx4 v16, s[72:75], s70 offen lds\n"
        "s_waitcnt lgkmcnt(5)\n"
        "v_mfma_f32_32x32x16_bf16 v[80:95], a[76:79], v[44:47], v[80:95]\n"
        "ds_read_b128 v[36:39], v1 offset:41728\n"
        "v_lshlrev_b32 v130, 16, v128\n"
        "v_and_b32 v131, 0xffff0000, v128\n"
        "v_mul_f32 v132, 0xbfb8aa3b, v130\n"
        "v_mul_f32 v133, 0xbfb8aa3b, v131\n"
        "s_waitcnt lgkmcnt(5)\n"
        "v_mfma_f32_32x32x16_bf16 v[80:95], a[80:83], v[48:51], v[80:95]\n"
        "ds_read_b128 v[40:43], v2 offset:41728\n"
        "v_exp_f32 v132, v132\n"
        "v_exp_f32 v133, v133\n"
        "v_lshlrev_b32 v134, 16, v129\n"
        "v_add_f32 v132, 1.0, v132\n"
        "s_add_u32 m0, s71, 36864\n"
        "s_nop 0\n"
        "buffer_load_dwordx4 v17, s[72:75], s70 offen lds\n"
        "s_waitcnt lgkmcnt(5)\n"
        "v_mfma_f32_32x32x16_bf16 v[80:95], a[84:87], v[52:55], v[80:95]\n"
        "ds_read_b128 v[44:47], v3 offset:41728\n"
        "v_add_f32 v133, 1.0, v133\n"
        "v_rcp_f32 v132, v132\n"
        "v_rcp_f32 v133, v133\n"
        "v_and_b32 v135, 0xffff0000, v129\n"
        "s_waitcnt lgkmcnt(5)\n"
        "v_mfma_f32_32x32x16_bf16 v[80:95], a[88:91], v[56:59], v[80:95]\n"
        "ds_read_b128 v[48:51], v4 offset:41728\n"
        "v_mul_f32 v130, v130, v132\n"
        "v_mul_f32 v131, v131, v133\n"
        "v_cvt_pk_bf16_f32 v130, v130, v131\n"
        "v_and_b32 v131, 0xffff0000, v130\n"
        "s_waitcnt lgkmcnt(5)\n"
        "v_mfma_f32_32x32x16_bf16 v[80:95], a[92:95], v[60:63], v[80:95]\n"
        "ds_read_b128 v[52:55], v5 offset:41728\n"
        "v_lshlrev_b32 v130, 16, v130\n"
        "v_mul_f32 v130, v130, v134\n"
        "v_mul_f32 v131, v131, v135\n"
        "v_cvt_pk_bf16_f32 v143, v130, v131\n"
        "s_waitcnt lgkmcnt(5)\n"
        "v_mfma_f32_32x32x16_bf16 v[80:95], a[96:99], v[32:35], v[80:95]\n"
        "ds_read_b128 v[56:59], v6 offset:41728\n"
        "buffer_store_dwordx4 v[140:143], v18, s[60:63], s68 offen sc1\n"
        "s_waitcnt lgkmcnt(5)\n"
        "v_mfma_f32_32x32x16_bf16 v[80:95], a[100:103], v[36:39], v[80:95]\n"
        "ds_read_b128 v[60:63], v7 offset:41728\n"
        "s_waitcnt lgkmcnt(5)\n"
        "v_mfma_f32_32x32x16_bf16 v[80:95], a[104:107], v[40:43], v[80:95]\n"
        "ds_read_b128 v[32:35], v0 offset:41984\n"
        "s_waitcnt lgkmcnt(5)\n"
        "v_mfma_f32_32x32x16_bf16 v[80:95], a[108:111], v[44:47], v[80:95]\n"
        "ds_read_b128 v[36:39], v1 offset:41984\n"
        "s_waitcnt lgkmcnt(5)\n"
        "v_mfma_f32_32x32x16_bf16 v[80:95], a[112:115], v[48:51], v[80:95]\n"
        "ds_read_b128 v[40:43], v2 offset:41984\n"
        "s_waitcnt lgkmcnt(5)\n"
        "v_mfma_f32_32x32x16_bf16 v[80:95], a[116:119], v[52:55], v[80:95]\n"
        "ds_read_b128 v[44:47], v3 offset:41984\n"
        "s_waitcnt lgkmcnt(5)\n"
        "v_mfma_f32_32x32x16_bf16 v[80:95], a[120:123], v[56:59], v[80:95]\n"
        "ds_read_b128 v[48:51], v4 offset:41984\n"
        "s_waitcnt lgkmcnt(5)\n"
        "v_mfma_f32_32x32x16_bf16 v[80:95], a[124:127], v[60:63], v[80:95]\n"
        "ds_read_b128 v[52:55], v5 offset:41984\n"
        "s_waitcnt lgkmcnt(5)\n"
        "v_mfma_f32_32x32x16_bf16 v[80:95], a[128:131], v[32:35], v[80:95]\n"
        "ds_read_b128 v[56:59], v6 offset:41984\n"
        "s_waitcnt lgkmcnt(5)\n"
        "v_mfma_f32_32x32x16_bf16 v[80:95], a[132:135], v[36:39], v[80:95]\n"
        "ds_read_b128 v[60:63], v7 offset:41984\n"
        "s_waitcnt lgkmcnt(5)\n"
        "v_mfma_f32_32x32x16_bf16 v[80:95], a[136:139], v[40:43], v[80:95]\n"
        "s_waitcnt lgkmcnt(4)\n"
        "v_mfma_f32_32x32x16_bf16 v[80:95], a[140:143], v[44:47], v[80:95]\n"
        "s_waitcnt vmcnt(0) lgkmcnt(0)\n"
        "s_barrier\n"
        "buffer_store_dword v20, v21, s[60:63], s69 offen sc1\n"
        "v_mfma_f32_32x32x16_bf16 v[80:95], a[144:147], v[48:51], v[80:95]\n"
        "ds_read_b128 v[32:35], v0 offset:0\n"
        "ds_read_b128 v[36:39], v1 offset:0\n"
        "ds_read_b128 v[40:43], v2 offset:0\n"
        "v_mfma_f32_32x32x16_bf16 v[80:95], a[148:151], v[52:55], v[80:95]\n"
        "ds_read_b128 v[44:47], v3 offset:0\n"
        "v_mfma_f32_32x32x16_bf16 v[80:95], a[152:155], v[56:59], v[80:95]\n"
        "ds_read_b128 v[48:51], v4 offset:0\n"
        "v_mfma_f32_32x32x16_bf16 v[80:95], a[156:159], v[60:63], v[80:95]\n"
        "ds_read_b128 v[52:55], v5 offset:0\n"
        "s_mul_i32 s68, s64, 110592\n"
        "s_add_u32 s68, s68, 45088768\n"
        "s_mul_i32 s69, s64, 512\n"
        "s_add_u32 s69, s69, s77\n"
        "s_add_u32 s70, s70, 40960\n"
        "s_add_u32 s64, s64, 1\n"
        "s_cmp_lt_u32 s64, 64\n"
        "s_cbranch_scc1 F1_loop\n"
        "s_nop 7\n"
        "s_nop 7\n"
        "v_cvt_pk_bf16_f32 v128, v80, v81\n"
        "v_cvt_pk_bf16_f32 v129, v88, v89\n"
        "v_lshlrev_b32 v130, 16, v128\n"
        "v_and_b32 v131, 0xffff0000, v128\n"
        "v_mul_f32 v132, 0xbfb8aa3b, v130\n"
        "v_mul_f32 v133, 0xbfb8aa3b, v131\n"
        "v_exp_f32 v132, v132\n"
        "v_exp_f32 v133, v133\n"
        "v_lshlrev_b32 v134, 16, v129\n"
        "v_add_f32 v132, 1.0, v132\n"
        "v_add_f32 v133, 1.0, v133\n"
        "v_rcp_f32 v132, v132\n"
        "v_rcp_f32 v133, v133\n"
        "v_and_b32 v135, 0xffff0000, v129\n"
        "v_mul_f32 v130, v130, v132\n"
        "v_mul_f32 v131, v131, v133\n"
        "v_cvt_pk_bf16_f32 v130, v130, v131\n"
        "v_and_b32 v131, 0xffff0000, v130\n"
        "v_lshlrev_b32 v130, 16, v130\n"
        "v_mul_f32 v130, v130, v134\n"
        "v_mul_f32 v131, v131, v135\n"
        "v_cvt_pk_bf16_f32 v140, v130, v131\n"
        "v_cvt_pk_bf16_f32 v128, v82, v83\n"
        "v_cvt_pk_bf16_f32 v129, v90, v91\n"
        "v_lshlrev_b32 v130, 16, v128\n"
        "v_and_b32 v131, 0xffff0000, v128\n"
        "v_mul_f32 v132, 0xbfb8aa3b, v130\n"
        "v_mul_f32 v133, 0xbfb8aa3b, v131\n"
        "v_exp_f32 v132, v132\n"
        "v_exp_f32 v133, v133\n"
        "v_lshlrev_b32 v134, 16, v129\n"
        "v_add_f32 v132, 1.0, v132\n"
        "v_add_f32 v133, 1.0, v133\n"
        "v_rcp_f32 v132, v132\n"
        "v_rcp_f32 v133, v133\n"
        "v_and_b32 v135, 0xffff0000, v129\n"
        "v_mul_f32 v130, v130, v132\n"
        "v_mul_f32 v131, v131, v133\n"
        "v_cvt_pk_bf16_f32 v130, v130, v131\n"
        "v_and_b32 v131, 0xffff0000, v130\n"
        "v_lshlrev_b32 v130, 16, v130\n"
        "v_mul_f32 v130, v130, v134\n"
        "v_mul_f32 v131, v131, v135\n"
        "v_cvt_pk_bf16_f32 v141, v130, v131\n"
        "v_cvt_pk_bf16_f32 v128, v84, v85\n"
        "v_cvt_pk_bf16_f32 v129, v92, v93\n"
        "v_lshlrev_b32 v130, 16, v128\n"
        "v_and_b32 v131, 0xffff0000, v128\n"
        "v_mul_f32 v132, 0xbfb8aa3b, v130\n"
        "v_mul_f32 v133, 0xbfb8aa3b, v131\n"
        "v_exp_f32 v132, v132\n"
        "v_exp_f32 v133, v133\n"
        "v_lshlrev_b32 v134, 16, v129\n"
        "v_add_f32 v132, 1.0, v132\n"
        "v_add_f32 v133, 1.0, v133\n"
        "v_rcp_f32 v132, v132\n"
        "v_rcp_f32 v133, v133\n"
        "v_and_b32 v135, 0xffff0000, v129\n"
        "v_mul_f32 v130, v130, v132\n"
        "v_mul_f32 v131, v131, v133\n"
        "v_cvt_pk_bf16_f32 v130, v130, v131\n"
        "v_and_b32 v131, 0xffff0000, v130\n"
        "v_lshlrev_b32 v130, 16, v130\n"
        "v_mul_f32 v130, v130, v134\n"
        "v_mul_f32 v131, v131, v135\n"
        "v_cvt_pk_bf16_f32 v142, v130, v131\n"
        "v_cvt_pk_bf16_f32 v128, v86, v87\n"
        "v_cvt_pk_bf16_f32 v129, v94, v95\n"
        "v_lshlrev_b32 v130, 16, v128\n"
        "v_and_b32 v131, 0xffff0000, v128\n"
        "v_mul_f32 v132, 0xbfb8aa3b, v130\n"
        "v_mul_f32 v133, 0xbfb8aa3b, v131\n"
        "v_exp_f32 v132, v132\n"
        "v_exp_f32 v133, v133\n"
        "v_lshlrev_b32 v134, 16, v129\n"
        "v_add_f32 v132, 1.0, v132\n"
        "v_add_f32 v133, 1.0, v133\n"
        "v_rcp_f32 v132, v132\n"
        "v_rcp_f32 v133, v133\n"
        "v_and_b32 v135, 0xffff0000, v129\n"
        "v_mul_f32 v130, v130, v132\n"
        "v_mul_f32 v131, v131, v133\n"
        "v_cvt_pk_bf16_f32 v130, v130, v131\n"
        "v_and_b32 v131, 0xffff0000, v130\n"
        "v_lshlrev_b32 v130, 16, v130\n"
        "v_mul_f32 v130, v130, v134\n"
        "v_mul_f32 v131, v131, v135\n"
        "v_cvt_pk_bf16_f32 v143, v130, v131\n"
        "buffer_store_dwordx4 v[140:143], v18, s[60:63], s68 offen sc1\n"
        "s_waitcnt vmcnt(0)\n"
        "buffer_store_dword v20, v21, s[60:63], s69 offen sc1\n"
        "s_endpgm\n"
        ROLE_OPS(p.w1, p.w3) : "v32", "v33", "v34", "v35", "v36", "v37", "v38", "v39", "v40", "v41", "v42", "v43", "v44", "v45", "v46", "v47", "v48", "v49", "v50", "v51", "v52", "v53", "v54", "v55", "v56", "v57", "v58", "v59", "v60", "v61", "v62", "v63", "v64", "v65", "v66", "v67", "v68", "v69", "v70", "v71", "v72", "v73", "v74", "v75", "v76", "v77", "v78", "v79", "v80", "v81", "v82", "v83", "v84", "v85", "v86", "v87", "v88", "v89", "v90", "v91", "v92", "v93", "v94", "v95", "v96", "v97", "v98", "v99", "v100", "v101", "v102", "v103", "v104", "v105", "v106", "v107", "v108", "v109", "v110", "v111", "v112", "v113", "v114", "v115", "v116", "v117", "v118", "v119", "v120", "v121", "v122", "v123", "v124", "v125", "v126", "v127", "v128", "v129", "v130", "v131", "v132", "v133", "v134", "v135", "v136", "v137", "v138", "v139", "v140", "v141", "v142", "v143", "v144", "v145", "v146", "v147", "v148", "v149", "v150", "v151", "v152", "v153", "v154", "v155", "v156", "v157", "v158", "v159", "v160", "v161", "v162", "v163", "v164", "v165", "v166", "v167", "v168", "v169", "v170", "v171", "v172", "v173", "v174", "v175", "v176", "v177", "v178", "v179", "v180", "v181", "v182", "v183", "v184", "v185", "v186", "v187", "v188", "v189", "v190", "v192", "v193", "v194", "v195", "v196", "v197", "v198", "v199", "v200", "v201", "v202", "v203", "v204", "v205", "v206", "v207", "v208", "v209", "v210", "v211", "v212", "v213", "v214", "v215", "v216", "v217", "v218", "v219", "v220", "v221", "v222", "v223", "v224", "v225", "v226", "v227", "v228", "v229", "v230", "v231", "v232", "v233", "v234", "v235", "v236", "v237", "v238", "v239", "v240", "v241", "v242", "v243", "v244", "v245", "v246", "v247", "v248", "v249", "v250", "v251", "v252", "v253", "v254", "v255", "s38", "s39", "s40", "s41", "s42", "s43", "s44", "s45", "s46", "s47", "s48", "s49", "s50", "s51", "s52", "s53", "s54", "s55", "s56", "s57", "s58", "s59", "s60", "s61", "s62", "s63", "s64", "s65", "s66", "s67", "s68", "s69", "s70", "s71", "s72", "s73", "s74", "s75", "s76", "s77", "s78", "s79", "s80", "s81", "s82", "s83", "s84", "s85", "s86", "s87", "s88", "s89", "s90", "s91", "s92", "s93", "s94", "s95", "s96", "s97", "s98", "s99", "vcc", "scc", "memory", "m0", "a0", "a1", "a2", "a3", "a4", "a5", "a6", "a7", "a8", "a9", "a10", "a11", "a12", "a13", "a14", "a15", "a16", "a17", "a18", "a19", "a20", "a21", "a22", "a23", "a24", "a25", "a26", "a27", "a28", "a29", "a30", "a31", "a32", "a33", "a34", "a35", "a36", "a37", "a38", "a39", "a40", "a41", "a42", "a43", "a44", "a45", "a46", "a47", "a48", "a49", "a50", "a51", "a52", "a53", "a54", "a55", "a56", "a57", "a58", "a59", "a60", "a61", "a62", "a63", "a64", "a65", "a66", "a67", "a68", "a69", "a70", "a71", "a72", "a73", "a74", "a75", "a76", "a77", "a78", "a79", "a80", "a81", "a82", "a83", "a84", "a85", "a86", "a87", "a88", "a89", "a90", "a91", "a92", "a93", "a94", "a95", "a96", "a97", "a98", "a99", "a100", "a101", "a102", "a103", "a104", "a105", "a106", "a107", "a108", "a109", "a110", "a111", "a112", "a113", "a114", "a115", "a116", "a117", "a118", "a119", "a120", "a121", "a122", "a123", "a124", "a125", "a126", "a127", "a128", "a129", "a130", "a131", "a132", "a133", "a134", "a135", "a136", "a137", "a138", "a139", "a140", "a141", "a142", "a143", "a144", "a145", "a146", "a147", "a148", "a149", "a150", "a151", "a152", "a153", "a154", "a155", "a156", "a157", "a158", "a159", "a160", "a161", "a162", "a163", "a164", "a165", "a166", "a167", "a168", "a169", "a170", "a171", "a172", "a173", "a174", "a175", "a176", "a177", "a178", "a179", "a180", "a181", "a182", "a183", "a184", "a185", "a186", "a187", "a188", "a189", "a190", "a191", "a192", "a193", "a194", "a195", "a196", "a197", "a198", "a199", "a200", "a201", "a202", "a203", "a204", "a205", "a206", "a207", "a208", "a209", "a210", "a211", "a212", "a213", "a214", "a215", "a216", "a217", "a218", "a219", "a220", "a221", "a222", "a223", "a224", "a225", "a226", "a227", "a228", "a229", "a230", "a231", "a232", "a233", "a234", "a235", "a236", "a237", "a238", "a239", "a240", "a241", "a242", "a243", "a244", "a245", "a246", "a247", "a248", "a249", "a250", "a251", "a252", "a253", "a254", "a255");
    __builtin_unreachable();
}

DI void role_f2(const Params& p, rsrc_t rs, int id, int wave, u32 t0) {
    const int lane = lane_id(), tid = wave * 64 + lane, n = id % 10;
    bool dead = false;
    const u32 ask = hello_ask(rs, wave, lane);
    { const int s = 79; STAMP(0); }
    for (u32 i = 0, e = 0 ? naps(p.tune[5], t0) : p.tune[5]; i < e; ++i) __builtin_amdgcn_s_sleep(16);
    { const int s = 79; STAMP(1); }
    u4 A[2][27];
    {
        const u16* src = p.w2 + 64 * n + (tid & 7) * 8;
        const u32 r0 = tid >> 3;
        const u32 lw = r0 * 128 + (((tid & 7) ^ (4 * ((r0 >> 1) & 1))) << 4);
        const u32 rho = (lane >> 2) & 3, c4 = lane & 3;
        u32 ra[2];
#pragma unroll
        for (int u = 0; u < 2; ++u) ra[u] = (wave * 64 + 4 * (lane >> 5) + rho) * 128 + (((4 * u + 2 * ((lane >> 4) & 1) + (c4 >> 1)) ^ (4 * (rho >> 1))) << 4) + (c4 & 1) * 8;
        stage<7>([&](int c, int i) { u32 rr = 64 * c + 32 * (i & 1) + r0; rr = rr < 431 ? rr : 431; return (const u4*)(src + (size_t)(432 * (i >> 1) + rr) * 640); },
                 [&](int i) { return lw + i * 4096; },
                 [&](int c, u32 base) {
#pragma unroll
                     for (int kk = 0; kk < 4; ++kk)
#pragma unroll
                         for (int u = 0; u < 2; ++u)
                             if (4 * c + kk < 27) { u32 a = base + ra[u] + kk * 2048; A[u][4 * c + kk] = cat(trr(a), trr(a + 1024)); PIN_A(A[u][4 * c + kk]); }
                 });
    }
    hello_wait(rs, wave, lane, ask, dead);
    { const int s = 79; STAMP(2); }
    for (int s = id / 10; s < 64; s += 2) {
        CSTAMP(1);
        wait_flags<1>(rs, F_H + s * 512 + wave * 108, 27, lane, dead);
        CSTAMP(2);
        u4 hb[27];
#pragma unroll
        for (int k = 0; k < 27; ++k) hb[k] = ldc(rs, (27 * wave + k) * 1024 + lane * 16, HF + s * 110592);
        SB();
        f16v d[2] = {};
#pragma unroll
        for (int k = 0; k < 27; ++k) { d[0] = m32(A[0][k], hb[k], d[0]); d[1] = m32(A[1][k], hb[k], d[1]); }
        CSTAMP(3);
#pragma unroll
        for (int u = 0; u < 2; ++u)
#pragma unroll
            for (int i = 0; i < 4; ++i) *(lds_f4)((((wave * 2 + u) * 4 + i) * 64 + lane) * 16) = (f4){d[u][4 * i], d[u][4 * i + 1], d[u][4 * i + 2], d[u][4 * i + 3]};
        BAR();
        f4 r[2];
#pragma unroll
        for (int z = 0; z < 2; ++z) {
            r[z] = *(lds_f4)(((2 * wave + z) * 64 + lane) * 16);
#pragma unroll
            for (int w = 1; w < 4; ++w) r[z] += *(lds_f4)(((w * 8 + 2 * wave + z) * 64 + lane) * 16);
        }
        stc((u4){pk(r[0][0], r[0][1]), pk(r[0][2], r[0][3]), pk(r[1][0], r[1][1]), pk(r[1][2], r[1][3])}, rs, (4 * n + wave) * 1024 + lane * 16, BF + s * 40960);
        CSTAMP(4);
        DRAIN(); BAR();
        if (tid == 0) stc1(0, rs, F_F + (s * 16 + n) * 4, 0);
        STAMP(0);
    }
}

DI void role_g(const Params& p, rsrc_t rs, int gi, int wave) {
    const int lane = lane_id();
    bool dead = false;
    hello_wait(rs, wave, lane, hello_ask(rs, wave, lane), dead);
    u4 w[10];
#pragma unroll
    for (int i = 0; i < 10; ++i) w[i] = *(const u4*)(p.gw + ((lane & 7) + 8 * i) * 8);
    for (int s = gi; s < 64; s += 2) {
        const int tok = s * 32 + 8 * wave + (lane >> 3);
        u4 x[10];
#pragma unroll
        for (int i = 0; i < 10; ++i) x[i] = *(const u4*)(p.x + (size_t)tok * 640 + ((lane & 7) + 8 * i) * 8);
        float a = 0;
#pragma unroll
        for (int i = 0; i < 10; ++i)
#pragma unroll
            for (int j = 0; j < 4; ++j) { u32 t = pk(lo2f(x[i][j]) * lo2f(w[i][j]), hi2f(x[i][j]) * hi2f(w[i][j])); a += lo2f(t) + hi2f(t); }
        a += __shfl_xor(a, 1); a += __shfl_xor(a, 2); a += __shfl_xor(a, 4);
        a = rbf(a);
        float gt = rbf(__builtin_amdgcn_rcpf(1.f + __builtin_amdgcn_exp2f(-1.4426950408889634f * a)));
        if ((lane & 7) == 0) stc1(nos(__float_as_uint(gt)), rs, F_G + tok * 4, 0);
    }
}

DI int role_of(int b, int& id) {
    const int xcd = b & 7, k = b >> 3;
    int role = -1; id = 0;
    if (xcd >= 4) {
        int j = xcd - 4 + 4 * k;
        if (j < 32) { role = 3; id = skew(j); } else if (j < 64) { role = 4; id = skew(j) - 32; } else if (j < 91) { role = 5; id = j - 64; } else if (j < 91 + 10 * 2) { role = 6; id = j - 91; } else if (0) { role = j < 127 ? -2 : 11; id = j - 111; } else if (j < 93 + 10 * 2) { role = 7; id = j - 91 - 10 * 2; }
        else role = 11;
    } else if (0) {
        if (k < 16) { role = k & 1; id = (k >> 1) + 8 * xcd; } else if (k < 26) { role = 2; id = xcd * 10 + k - 16; } else if (k < 31) { role = 9; id = xcd * 5 + k - 26; } else role = xcd == 2 ? 8 : 10;
    } else if (xcd < 2) { role = k & 1; id = (k >> 1) + 16 * xcd; }
    else if (k < 20) { role = 2; id = (xcd - 2) * 20 + k; }
    else if (k < 30) { role = 9; id = (xcd - 2) * 10 + k - 20; }
    else if (b == 255 - 5) role = 8;
    else if (0 && b == 242) role = -1;
    else if (0 && xcd == 3) { role = 7; id = k - 30; }
    else role = 10;
    return role;
}

DI void role_janitor(rsrc_t rs, int wave) {
    const int lane = lane_id(), tid = wave * 64 + lane;
    for (u32 o = tid * 16; o < FLG_BYTES; o += 4096) stc((u4)(SENT), rs, o, FLG);
    DRAIN(); BAR();
    int id; const int r = role_of(tid, id);
    const bool idle = r == 8 || r >= 10;
    for (int i = 0; i <= SPIN; ++i) {
        FENCE();
        const u4 m = ldc(rs, tid * 16, HS_M), d = ldc(rs, tid * 16, F_D);
        if (__all(idle || mx4(d) != SENT)) break;
        stc(m, rs, tid * 16, HS_R);
        __builtin_amdgcn_s_sleep(2);
    }
}

__global__ void __launch_bounds__(256, 1) fused(Params p) {
    const int wave = __builtin_amdgcn_readfirstlane(threadIdx.x >> 6);
    const rsrc_t rs = __builtin_amdgcn_make_buffer_rsrc(p.ws, 0, 0x0F000000, 0x00020000);
    int id; const int role = role_of(blockIdx.x, id);
    const u32 t0 = (0 && role >= 5) || role == -2 ? (u32)__builtin_amdgcn_s_memrealtime() : 0;
    if (0 || 0) SB();
    if (3 && (role == 11 || role == -2) && (blockIdx.x >> 3) >= 32 - 3) {
        const u4* src = (const u4*)p.x + (31 - (blockIdx.x >> 3)) * 2560 + threadIdx.x;
        u4 v[10];
#pragma unroll
        for (int i = 0; i < 10; ++i) v[i] = 0 ? __builtin_nontemporal_load(src + i * 256) : src[i * 256];
#pragma unroll
        for (int i = 0; i < 10; ++i) asm volatile("" :: "v"(v[i]));
    }
    if ((role >= 5 || role == -2) && (blockIdx.x & 7) >= 4) warm_slab(p, (blockIdx.x & 7) - 4 + 4 * (blockIdx.x >> 3) - 64);
    if (role == 0) role_r1(p, rs, id, wave);
    else if (role == 1) role_ru(p, rs, id, wave);
    else if (role == 2) role_r2(p, rs, id, wave);
    else if (role == 3) role_wqk(p, rs, id, wave);
    else if (role == 4) role_wvb(p, rs, id, wave);
    else if (role == 5) role_f1(p, rs, id, wave, t0);
    else if (role == 6) role_f2(p, rs, id, wave, t0);
    else if (role == 7) role_g(p, rs, id, wave);
    else if (role == 8) role_janitor(rs, wave);
    else if (role == 10) warm_helper(p, wave);
    else if (role == 9) role_h(p, rs, id, wave);
    else if (role == -1) role_cal(rs, wave);
    else if (role == -2) role_vh(p, rs, id, wave, t0);
}

extern "C" void kernel_launch(void* const* d_in, const int* in_sizes, int n_in, void* d_out, int out_size, void* d_ws, size_t ws_size, hipStream_t stream) {
    Params p;
    p.x = (const u16*)d_in[0]; p.mem0 = (const u16*)d_in[1]; p.wq = (const u16*)d_in[2]; p.wk = (const u16*)d_in[3]; p.wv = (const u16*)d_in[4]; p.wo = (const u16*)d_in[5];
    p.w1 = (const u16*)d_in[8]; p.w3 = (const u16*)d_in[9]; p.w2 = (const u16*)d_in[10]; p.gw = (const u16*)d_in[11];
    p.out = (u16*)d_out;
    p.ws = (char*)(((size_t)d_ws + (2u << 20) - 1) & ~(size_t)((2u << 20) - 1));
    const u32 tune[10] = {245, 245, 100u, 210, 33284, 6, 850, 850, 1000, 650};
    for (int i = 0; i < 10; ++i) p.tune[i] = tune[i];
    (void)hipFuncSetAttribute((const void*)fused, hipFuncAttributeMaxDynamicSharedMemorySize, LDS_BYTES);
    hipLaunchKernelGGL(fused, dim3(256), dim3(256), LDS_BYTES, stream, p);
}
```
